```python
import torch

def _softmax(x, axis=-1):
    return torch.softmax(x, dim=axis)
def _gelu(x):
    return torch.nn.functional.gelu(x, approximate="tanh")
def _silu(x):
    return torch.nn.functional.silu(x)
def _sigmoid(x):
    return torch.sigmoid(x)
def _layernorm(x, axis=-1, eps=1e-5):
    m = x.mean(dim=axis, keepdim=True)
    v = x.var(dim=axis, keepdim=True, unbiased=False)
    return (x - m) / torch.sqrt(v + eps)
def _scatter_add(tgt, idx, src, axis=0):
    return tgt.clone().index_add_(axis, idx, src)
def _segment_sum(src, seg, n):
    out = torch.zeros((n,) + src.shape[1:], dtype=src.dtype)
    return out.index_add_(0, seg, src)
def _pad(x, pw):
    flat = tuple(v for pair in reversed(pw) for v in pair)
    return torch.nn.functional.pad(x, flat)

kernel_name = 'sequential_preLN__linear_kernel_hedgehog__moe_arch_mod__prompt'

def setup_inputs():

    _w = (lambda a: (lambda t: t.to(torch.bfloat16) if t.is_floating_point() else t)(torch.as_tensor(a)))
    def _A_setup():
        B, S, D, H = 1, 2048, 1024, 32
        Dh = D // H
        return {
            "x": _w((lambda *s: torch.randn(*s, dtype=torch.bfloat16) * 0.05)(B, S, D)),
            "wqkv": _w((lambda *s: torch.randn(*s, dtype=torch.bfloat16) * 0.2)(D, 3 * D)),
            "w_phi": _w((lambda *s: torch.randn(*s, dtype=torch.bfloat16) * 0.2)(H, Dh, Dh) * (Dh ** -0.5)),
            "wo": _w((lambda *s: torch.randn(*s, dtype=torch.bfloat16) * 0.05)(D, D)),
        }
    _A_in = _A_setup()

    def _B_setup():
        B, S, D, F, C = 1, 2048, 1024, 3584, 1
        return {
            "x": _w((lambda *s: torch.randn(*s, dtype=torch.bfloat16) * 0.05)(B, S, D)),
            "w_r": _w((lambda *s: torch.randn(*s, dtype=torch.bfloat16) * 0.1)(D)),
            "w_gate": _w((lambda *s: torch.randn(*s, dtype=torch.bfloat16) * 0.05)(D, F)),
            "w_up": _w((lambda *s: torch.randn(*s, dtype=torch.bfloat16) * 0.05)(D, F)),
            "w_down": _w((lambda *s: torch.randn(*s, dtype=torch.bfloat16) * 0.05)(F, D)),
        }
    _B_in = _B_setup()

    return {
        "A_x": _A_in["x"],
        "A_wqkv": _A_in["wqkv"],
        "A_w_phi": _A_in["w_phi"],
        "A_wo": _A_in["wo"],
        "B_x": _B_in["x"],
        "B_w_r": _B_in["w_r"],
        "B_w_gate": _B_in["w_gate"],
        "B_w_up": _B_in["w_up"],
        "B_w_down": _B_in["w_down"],
    }

def reference(A_x, A_wqkv, A_w_phi, A_wo, B_x, B_w_r, B_w_gate, B_w_up, B_w_down):
    def _A_ref(x, wqkv, w_phi, wo):
        B, S, D = x.shape
        H = 32; Dh = D // H
        qkv = (lambda x, *d: x.permute(*d))((x @ wqkv).reshape(B, S, 3, H, Dh), 2, 0, 3, 1, 4)
        q, k, v = qkv[0], qkv[1], qkv[2]
        pq = torch.einsum("bhsd,hdf->bhsf", q, w_phi)
        pk = torch.einsum("bhsd,hdf->bhsf", k, w_phi)
        phi_q = torch.concatenate([_softmax(pq, axis=-1), _softmax(-pq, axis=-1)], axis=-1)
        phi_k = torch.concatenate([_softmax(pk, axis=-1), _softmax(-pk, axis=-1)], axis=-1)
        kv = torch.einsum("bhsf,bhsd->bhfd", phi_k, v)
        z  = torch.einsum("bhsf->bhf", phi_k)
        num = torch.einsum("bhqf,bhfd->bhqd", phi_q, kv)
        den = torch.einsum("bhqf,bhf->bhq", phi_q, z)[..., None] + 1e-6
        o = num / den
        return (lambda x, *d: x.permute(*d))(o, 0, 2, 1, 3).reshape(B, S, D) @ wo

    def _B_ref(x, w_r, w_gate, w_up, w_down):
        B, S, D = x.shape
        C = 1
        r = x @ w_r
        idx = torch.argsort(-r, axis=-1)[:, :C]
        idx_e = (lambda x, n, axis: torch.repeat_interleave(x, n, dim=axis))(idx.reshape(B, C, 1), D, axis=2)
        x_sel = (lambda x, i, axis: torch.take_along_dim(x, i, dim=axis))(x, idx_e, axis=1)
        g = _silu(x_sel @ w_gate)
        u = x_sel @ w_up
        y = (g * u) @ w_down
        r_sel = _sigmoid((lambda x, i, axis: torch.take_along_dim(x, i, dim=axis))(r, idx, axis=1))
        y = y * r_sel.reshape(B, C, 1)
        b_off = (torch.arange(B) * S).reshape(B, 1)
        flat_idx = (idx + b_off).reshape(B * C)
        zero = (x * 0.0).reshape(B * S, D)
        upd = _scatter_add(zero, flat_idx, y.reshape(B * C, D), axis=0)
        return x + upd.reshape(B, S, D)

    x = A_x
    h = x + _A_ref(_layernorm(x, axis=-1), A_wqkv, A_w_phi, A_wo)
    h2 = h + _B_ref(_layernorm(h, axis=-1), B_w_r, B_w_gate, B_w_up, B_w_down)
    return h2
```

```cpp
#include <hip/hip_runtime.h>
#include <stdint.h>
#include <utility>

typedef unsigned short u16;
typedef unsigned long long u64;
typedef short bf16x8 __attribute__((ext_vector_type(8)));
typedef short s16x4 __attribute__((ext_vector_type(4)));
typedef float f32x4 __attribute__((ext_vector_type(4)));
typedef float f32x2 __attribute__((ext_vector_type(2)));
typedef __bf16 bf16x2 __attribute__((ext_vector_type(2)));
typedef unsigned v4u __attribute__((ext_vector_type(4)));
typedef unsigned v2u __attribute__((ext_vector_type(2)));
#define LAS __attribute__((address_space(3)))
#define GAS __attribute__((address_space(1)))
#define CAS __attribute__((address_space(4)))
typedef GAS u64 gu64;
typedef GAS unsigned gu32;
typedef __amdgpu_buffer_rsrc_t rsrc_t;
#define DI __device__ __forceinline__
#define RLX_AGENT __ATOMIC_RELAXED, __HIP_MEMORY_SCOPE_AGENT
#define BAR() __builtin_amdgcn_s_barrier()
#define SCHED() __builtin_amdgcn_sched_barrier(0)
#define SGB(m, n) __builtin_amdgcn_sched_group_barrier((m), (n), 0)
#define WAITL0() asm volatile("s_waitcnt lgkmcnt(0)" ::: "memory")
#define WL0() __builtin_amdgcn_s_waitcnt(0xc07f)
#define MFMA(a, b, c) __builtin_amdgcn_mfma_f32_16x16x32_bf16((a), (b), (c), 0, 0, 0)
#define ORDER_BEHIND_POLL() __builtin_amdgcn_fence(__ATOMIC_ACQUIRE, "wavefront")
#define AUX_SC1 16
template <int N> DI void waitv() { asm volatile("s_waitcnt vmcnt(%0)" ::"i"(N) : "memory"); }

#define WS_EPOCH 0
#define WS_G0 0x1000
#define WS_G1 0x8000
#define WS_F4 0x78000
#define WS_GC 0x70000
#define WS_G3 0x80000
#define WS_GZ 0xC0000
#define SENT 0xFFFFFFFFu
#define WS_TL 0x100000
#define WS_G5 0x100000
#define WS_G8 0x180000
#define WS_G9 0x1A0000
#define WS_GK 0x1B4000
#define WS_C 0x1C0000
#define WS_P2 0x200000
#define P2_STRIDE 0x2180
#define WS_P4 0x700000
#define WS_USED 0xB00000
#define WS_STAMP 0x1000000

#define STAGE_B 40960
#define LDS_BYTES 163840
#define L_WO 32768
#define L_MB 161792
#define L_WPHI (L_MB - 8192)
#define L_SD (L_MB + 512)
#define L_CS (L_MB + 1024)
#define L_V L_WO
#define L_KH (L_WO + 65536)
#define L_XB 0
#define L_STASH (L_WO + 1024)
#define L_RED 4096

#define SL4 18
#define SL5 13
#define SL6 12
#define SL7 10
#define CT2 0
#define DL 0
#define DLY(n) do { if (DL == (n)) { SCHED(); __builtin_amdgcn_s_sleep(16); SCHED(); } } while (0)
#define OT 1
#define L_OUT (L_WO + 8192)
#define XT 0
#define SL3 12
#define SL2 12
#define NL 1
#define ZL 1
#define EO 0
#define LO 0
#define LS 0
#define OA 16
#define H1 5
#define H2 7
#define BC 1
#define SL3H SL3
#define RC 0
#define RH 0
#define MB 0
#define L_FL (L_XB + 32768 - 16)
#define R1 3
#define SL1 2
#define EF 0
#define ES 0
#define F0 6
#define F1 12
#define DA 3
#define DS 16
#define DP 0
#define RS1 0
#define RS2 0
#define SPIN_CAP 20000u
#define W_AUX 2
#define LOG2E 1.4426950408889634f

DI unsigned rfl(unsigned x) { return __builtin_amdgcn_readfirstlane(x); }
DI float bf2f(unsigned u) { return __uint_as_float(u << 16); }
DI float bflo(unsigned u) { return __uint_as_float(u << 16); }
DI float bfhi(unsigned u) { return __uint_as_float(u & 0xffff0000u); }
DI unsigned pk2(float lo, float hi) { f32x2 v = {lo, hi}; return __builtin_bit_cast(unsigned, __builtin_convertvector(v, bf16x2)); }
DI unsigned nsent(unsigned x) { return __builtin_elementwise_min(x, SENT - 1); }
DI unsigned umx(unsigned a, unsigned b) { return __builtin_elementwise_max(a, b); }
DI float rbf(float f) { return bflo(pk2(f, f) & 0xffffu); }
DI bf16x8 mk8(unsigned a, unsigned b, unsigned c, unsigned d) { v4u v = {a, b, c, d}; return __builtin_bit_cast(bf16x8, v); }
DI void dma16(unsigned ldsaddr, unsigned voff, rsrc_t rs, unsigned soff) {
    asm volatile("s_mov_b32 m0, %0\n\ts_nop 0\n\tbuffer_load_dwordx4 %1, %2, %3 offen lds" ::"s"(ldsaddr), "v"(voff), "s"(rs), "s"(soff) : "memory");
}
DI void dma16x4(unsigned ldsaddr, const unsigned* v, rsrc_t rs, unsigned soff) {
    asm volatile("s_mov_b32 m0, %0\n\ts_nop 0\n\tbuffer_load_dwordx4 %1, %5, %6 offen lds\n\tbuffer_load_dwordx4 %2, %5, %6 offen offset:1024 lds\n\t"
                 "buffer_load_dwordx4 %3, %5, %6 offen offset:2048 lds\n\tbuffer_load_dwordx4 %4, %5, %6 offen offset:3072 lds"
                 ::"s"(ldsaddr), "v"(v[0]), "v"(v[1]), "v"(v[2]), "v"(v[3]), "s"(rs), "s"(soff) : "memory");
}
DI void touch(unsigned voff, rsrc_t rs, unsigned soff) { asm volatile("buffer_load_dword v255, %0, %1, %2 offen" ::"v"(voff), "s"(rs), "s"(soff) : "memory", "v255"); }
DI v4u ld16_asm(unsigned voff, rsrc_t rs, unsigned soff) {
    v4u r; asm volatile("buffer_load_dwordx4 %0, %1, %2, %3 offen" : "=v"(r) : "v"(voff), "s"(rs), "s"(soff) : "memory"); return r;
}
#define PIN(x) asm volatile("" : "+v"(x))
DI s16x4 tr_read(unsigned a) { return __builtin_amdgcn_ds_read_tr16_b64_v4i16((LAS s16x4*)(a)); }
DI bf16x8 cat8(s16x4 a, s16x4 b) { return __builtin_shufflevector(a, b, 0, 1, 2, 3, 4, 5, 6, 7); }
DI bf16x8 lds128(unsigned a) { return *(const LAS bf16x8*)(a); }
DI f32x4 ldsf4(unsigned a) { return *(const LAS f32x4*)(a); }
DI float ldsf(unsigned a) { return *(const LAS float*)(a); }
DI unsigned ldsu(unsigned a) { return *(const LAS unsigned*)(a); }
DI void stsf4(unsigned a, f32x4 v) { *(LAS f32x4*)(a) = v; }
DI void stsf(unsigned a, float v) { *(LAS float*)(a) = v; }
DI void stsu(unsigned a, unsigned v) { *(LAS unsigned*)(a) = v; }
template <int C> DI float dppf(float x) { return __uint_as_float(__builtin_amdgcn_update_dpp(0, __float_as_uint(x), C, 0xf, 0xf, true)); }
template <int C> DI unsigned dppu(unsigned x) { return __builtin_amdgcn_update_dpp(0, x, C, 0xf, 0xf, true); }
DI float max2f(float a, float b) { float r; asm("v_max_f32_e32 %0, %1, %2" : "=v"(r) : "v"(a), "v"(b)); return r; }
DI float max3f(float a, float b, float c) { float r; asm("v_max3_f32 %0, %1, %2, %3" : "=v"(r) : "v"(a), "v"(b), "v"(c)); return r; }
DI float row_sum(float x) { x += dppf<0x128>(x); x += dppf<0x124>(x); x += dppf<0x122>(x); x += dppf<0x121>(x); return x; }
DI float xrow_sum(float x) {
    auto a = __builtin_amdgcn_permlane16_swap(__float_as_uint(x), __float_as_uint(x), false, false);
    x = __uint_as_float(a[0]) + __uint_as_float(a[1]);
    auto b = __builtin_amdgcn_permlane32_swap(__float_as_uint(x), __float_as_uint(x), false, false);
    return __uint_as_float(b[0]) + __uint_as_float(b[1]);
}
DI float wave_sum(float x) { return xrow_sum(row_sum(x)); }
DI unsigned wave_maxu(unsigned x) {
    x = umx(x, dppu<0x128>(x)); x = umx(x, dppu<0x124>(x)); x = umx(x, dppu<0x122>(x)); x = umx(x, dppu<0x121>(x));
    auto a = __builtin_amdgcn_permlane16_swap(x, x, false, false);
    x = umx(a[0], a[1]);
    auto b = __builtin_amdgcn_permlane32_swap(x, x, false, false);
    return umx(b[0], b[1]);
}
DI float nmax3f(float a, float b, float c) { float r; asm("v_max3_f32 %0, -%1, -%2, -%3" : "=v"(r) : "v"(a), "v"(b), "v"(c)); return r; }
DI float nmax2f(float a, float b) { float r; asm("v_max_f32_e64 %0, -%1, -%2" : "=v"(r) : "v"(a), "v"(b)); return r; }
DI float mx2(float a, float b) { return __builtin_amdgcn_fmed3f(a, b, __builtin_inff()); }
template <bool MAX> DI float xrow_scatter(float a, float b, float c, float d) {
    const auto s = __builtin_amdgcn_permlane16_swap(__float_as_uint(a), __float_as_uint(b), false, false);
    const auto t = __builtin_amdgcn_permlane16_swap(__float_as_uint(c), __float_as_uint(d), false, false);
    const float u = MAX ? mx2(__uint_as_float(s[0]), __uint_as_float(s[1])) : __uint_as_float(s[0]) + __uint_as_float(s[1]);
    const float v = MAX ? mx2(__uint_as_float(t[0]), __uint_as_float(t[1])) : __uint_as_float(t[0]) + __uint_as_float(t[1]);
    const auto r = __builtin_amdgcn_permlane32_swap(__float_as_uint(u), __float_as_uint(v), false, false);
    return MAX ? mx2(__uint_as_float(r[0]), __uint_as_float(r[1])) : __uint_as_float(r[0]) + __uint_as_float(r[1]);
}
DI void xrow_gather(float w, float (&o)[4]) {
    const auto r = __builtin_amdgcn_permlane32_swap(__float_as_uint(w), __float_as_uint(w), false, false);
    const auto s = __builtin_amdgcn_permlane16_swap(r[0], r[0], false, false);
    const auto t = __builtin_amdgcn_permlane16_swap(r[1], r[1], false, false);
    o[0] = __uint_as_float(s[0]); o[1] = __uint_as_float(s[1]); o[2] = __uint_as_float(t[0]); o[3] = __uint_as_float(t[1]);
}
struct NoHook { DI void operator()(int, float&) const {} DI void operator()(int, float&, float&) const {} };
template <bool MUL, class H = NoHook> DI void softmax2(const f32x4 (&p)[2][2], float rs0, float rs1, bool hi, bf16x8 (&out)[4], H hook = H()) {
    float m[4];
#pragma unroll
    for (int i = 0; i < 2; i++) {
        const f32x4 &p0 = p[i][0], &p1 = p[i][1];
        m[2 * i] = max3f(max3f(p0[0], p0[1], p0[2]), max3f(p0[3], p1[0], p1[1]), max2f(p1[2], p1[3]));
        m[2 * i + 1] = max3f(nmax3f(p0[0], p0[1], p0[2]), nmax3f(p0[3], p1[0], p1[1]), nmax2f(p1[2], p1[3]));
    }
    asm volatile("s_nop 1" : "+v"(m[0]), "+v"(m[1]), "+v"(m[2]), "+v"(m[3]));
    hook(1, m[3]);
    const float rsr = hi ? rs1 : rs0;
    float a[4];
    xrow_gather(xrow_scatter<true>(m[0], m[1], m[2], m[3]) * (rsr * -LOG2E), a);
    f32x2 e[4][4];
    float s[4];
#pragma unroll
    for (int k = 0; k < 4; k++) {
        if (k == 2) hook(2, a[2], s[1]);
        const float sc = ((k >> 1) ? rs1 : rs0) * ((k & 1) ? -LOG2E : LOG2E);
#pragma unroll
        for (int j = 0; j < 4; j++) {
            const f32x4& q = p[k >> 1][j >> 1];
            const f32x2 t = __builtin_elementwise_fma((f32x2){q[2 * (j & 1)], q[2 * (j & 1) + 1]}, (f32x2){sc, sc}, (f32x2){a[k], a[k]});
            e[k][j] = (f32x2){__builtin_amdgcn_exp2f(t[0]), __builtin_amdgcn_exp2f(t[1])};
        }
        const f32x2 h = (e[k][0] + e[k][1]) + (e[k][2] + e[k][3]);
        s[k] = h[0] + h[1];
        if (k == 3) hook(3, s[k]);
    }
    float iv = __builtin_amdgcn_rcpf(xrow_scatter<false>(s[0], s[1], s[2], s[3]));
    if (MUL) iv *= rsr;
    xrow_gather(iv, a);
#pragma unroll
    for (int k = 0; k < 4; k++) {
        if (k == 2) hook(4, a[2]);
        const f32x2 c = {a[k], a[k]};
        const f32x2 x0 = e[k][0] * c, x1 = e[k][1] * c, x2 = e[k][2] * c, x3 = e[k][3] * c;
        out[k] = mk8(pk2(x0[0], x0[1]), pk2(x1[0], x1[1]), pk2(x2[0], x2[1]), pk2(x3[0], x3[1]));
    }
}
DI float dot2(unsigned a, unsigned b, float c) {
    return __builtin_amdgcn_fdot2_f32_bf16(__builtin_bit_cast(bf16x2, a), __builtin_bit_cast(bf16x2, b), c, false);
}
DI void st_gran(char* ws, unsigned off, unsigned tag, unsigned val) {
    __hip_atomic_store((gu64*)(ws + off), ((u64)tag << 32) | val, RLX_AGENT);
}
DI v2u ldg(rsrc_t rs, unsigned voff, unsigned soff) { return __builtin_amdgcn_raw_buffer_load_b64(rs, voff, soff, AUX_SC1); }
#define RARE(c) __builtin_expect(!!(c), 0)
#define FRESH() asm volatile("" ::: "memory")
template <int N> DI void sweep(rsrc_t rs, unsigned voff, unsigned soff, unsigned S, unsigned tag, unsigned (&v)[N]) {
#pragma nounroll
    for (unsigned spins = 0;;) {
        unsigned bad = 0;
        FRESH();
#pragma unroll
        for (int k = 0; k < N; ++k) { const v2u x = ldg(rs, voff, soff + k * S); v[k] = x[0]; bad |= x[1] ^ tag; }
        if (__all(bad == 0)) break;
        if (++spins > SPIN_CAP) break;
        __builtin_amdgcn_s_sleep(1);
    }
}
template <int A, int B> DI unsigned budget(unsigned& spins) {
    unsigned r;
    asm volatile("s_add_i32 %0, %0, 1\n\ts_cmpk_lt_u32 %0, %2\n\ts_cselect_b32 %1, %3, %4" : "+s"(spins), "=s"(r) : "i"(SPIN_CAP), "i"(A), "i"(B) : "scc");
    SCHED();
    return r;
}
DI unsigned wmax(unsigned x) { return x; }
DI unsigned wmax(v2u x) { return umx(x[0], x[1]); }
DI unsigned wmax(v4u x) { return umx(umx(x[0], x[1]), umx(x[2], x[3])); }
template <int N, int SL = 0, class T, class F> DI void sweepz(F ld, T (&v)[N]) {
#pragma nounroll
    for (unsigned spins = 0;;) {
        FRESH();
        unsigned m = 0;
#pragma unroll
        for (int k = 0; k < N; ++k) v[k] = ld(k);
        const unsigned lim = budget<-2, -1>(spins);
#pragma unroll
        for (int k = 0; k < N; ++k) m = umx(m, wmax(v[k]));
        if (!__any(m > lim)) break;
        if (SL) __builtin_amdgcn_s_sleep(SL);
    }
    return;
#pragma nounroll
    for (unsigned spins = 0;;) {
        FRESH();
        unsigned m = 0;
#pragma unroll
        for (int k = 0; k < N; ++k) v[k] = ld(k);
#pragma unroll
        for (int k = 0; k < N; ++k) m = umx(m, wmax(v[k]));
        if (__all(m != SENT)) break;
        if (++spins > SPIN_CAP) break;
        if (SL) __builtin_amdgcn_s_sleep(SL);
    }
}
template <int N, int SL, class T, class F, class G> DI void sweepn(F ld, T (&v)[N], G use) {
    unsigned spins = 0;
    bool again;
#pragma nounroll
    do {
        FRESH();
#pragma unroll
        for (int k = 0; k < N; ++k) v[k] = ld(k);
        const int m = budget<3, 0>(spins);
        again = __any(__builtin_amdgcn_classf(use(), m));
    } while (again);
    return;
    FRESH();
#pragma unroll
    for (int k = 0; k < N; ++k) v[k] = ld(k);
    const float r = use();
    if (RARE(__any(r != r))) { sweepz<N, SL>(ld, v); use(); }
}
DI unsigned f2s(float x) { return nsent(__float_as_uint(x)); }
#define OOB 0x4000000u
#define LD16(v, s) __builtin_amdgcn_raw_buffer_load_b128(rsWS, (v), (s), AUX_SC1)
#define LD4(v, s) __builtin_amdgcn_raw_buffer_load_b32(rsWS, (v), (s), AUX_SC1)
#define ST16(x, v, s) __builtin_amdgcn_raw_buffer_store_b128((x), rsWS, (v), (s), AUX_SC1)
#define ST4(x, v, s) __builtin_amdgcn_raw_buffer_store_b32((x), rsWS, (v), (s), AUX_SC1)
#define SLEEP(n) __builtin_amdgcn_s_sleep(n)
DI void poll16(rsrc_t rs, unsigned off, unsigned tag, int l) {
    unsigned spins = 0, lim, bad;
#pragma nounroll
    do {
        FRESH();
        const v2u x = ldg(rs, (l & 15) * 8, off);
        lim = budget<0, -1>(spins);
        bad = (x[1] ^ tag) | (x[0] ^ tag ^ 0x5bd1e995u);
    } while (__any(bad > lim));
    ORDER_BEHIND_POLL();
    return;
#pragma nounroll
    for (unsigned spins = 0; spins < SPIN_CAP; spins++) {
        FRESH();
        const v2u x = ldg(rs, (l & 15) * 8, off);
        if (__all(x[1] == tag && x[0] == (tag ^ 0x5bd1e995u))) break;
    }
    ORDER_BEHIND_POLL();
}
#define STAMPR(i, role)
#define STAMP(i)
#define STAMPW(i, ww)

#define ARR_DECL
#define KATTR
#define ARR_MARK(i)
#define ARR_END()
template <class F, int... Is> DI void static_for_impl(F&& f, std::integer_sequence<int, Is...>) { (f(std::integral_constant<int, Is>{}), ...); }
template <int N, class F> DI void static_for(F&& f) { static_for_impl(f, std::make_integer_sequence<int, N>{}); }

DI s16x4 bq(unsigned b0, unsigned b1, int j, int i) {
    const int ni = (j + 2) % 6;
    return tr_read(((ni & 1) ? b1 : b0) + (ni >> 1) * 8192 + (i >> 1) * 4096 + (i & 1) * 512);
}
template <class F> DI void group(f32x4 (&acc)[4][6], const bf16x8 (&A)[2][4], const bf16x8 (&B)[2], int j, F gap) {
    const int ni = (j + 2) % 6;
#pragma unroll
    for (int i = 0; i < 8; i++) {
        const int mi = i >> 1, ks = i & 1;
        if (ni < 4) asm volatile("v_mfma_f32_16x16x32_bf16 %0, %1, %2, %0" : "+v"(acc[mi][ni]) : "v"(B[ks]), "v"(A[ks][mi]));
        else asm volatile("v_mfma_f32_16x16x32_bf16 %0, %1, %2, %0" : "+v"(acc[mi][ni]) : "v"(A[ks][mi]), "v"(B[ks]));
        gap(i);
        SCHED();
    }
}
DI void img_write2(const unsigned (&ia)[4], const bf16x8 (&f)[4]) {
#pragma unroll
    for (int k = 0; k < 4; k++) {
        const v4u u = __builtin_bit_cast(v4u, f[k]);
        *(LAS v2u*)(ia[2 * (k & 1)] + (k >> 1) * 2048) = (v2u){u[0], u[1]};
        *(LAS v2u*)(ia[2 * (k & 1) + 1] + (k >> 1) * 2048) = (v2u){u[2], u[3]};
    }
}
DI float lds_bf(unsigned a) { return bf2f(*(const LAS u16*)(a)); }
#define MFMA_SETTLE2(p) asm volatile("s_nop 7\n\ts_nop 1" : "+v"(p[0][0]), "+v"(p[0][1]), "+v"(p[1][0]), "+v"(p[1][1]))

__global__ KATTR __launch_bounds__(512, 2) void fused_kernel(const u16* __restrict__ X, const u16* __restrict__ WQKV, const u16* __restrict__ WPHI,
                                                       const u16* __restrict__ WO, const u16* __restrict__ WR, const u16* __restrict__ WG,
                                                       const u16* __restrict__ WU, const u16* __restrict__ WD, u16* __restrict__ OUT,
                                                       char* __restrict__ ws) {
    extern __shared__ __attribute__((aligned(16))) char lds[];
    u64 pc0; unsigned clen;
    const int tid = threadIdx.x, l = tid & 63;
    asm volatile("s_getpc_b64 %0\n.Lfk_beg:\n\ts_lshr_b32 %1, %3, 3\n\tv_and_b32 v255, 0x1e0, %2\n\tv_add_lshl_u32 v255, v255, %1, 7\n\ts_mov_b32 %1, .Lfk_end-.Lfk_beg\n\t"
                 "v_min_u32 v255, %1, v255\n\tglobal_load_dword v255, v255, %0" : "=&s"(pc0), "=&s"(clen) : "v"(tid), "s"(blockIdx.x) : "v255", "scc");
    SCHED();
    const unsigned w = rfl(tid >> 6);
    const unsigned bid = blockIdx.x, xcd = bid & 7, idx = bid >> 3;
    const unsigned rt = (xcd & 1) * 8 + (idx & 7), hp = (xcd >> 1) * 4 + (idx >> 3);
    const unsigned i16 = idx & 15, rt2 = xcd * 2 + (idx >> 4), ct = i16 == 4 ? 11 : i16 == 11 ? 4 : i16;
    const unsigned hq = (hp + 4) & 15;
    const unsigned rot = (rt >> 2) * 4;
    const unsigned rot2 = (2 * (rt2 + ct)) & 31;
    const unsigned ldsb = (unsigned)(size_t)lds;
    const int g = l >> 4, q4 = (l & 15) >> 2, p4 = l & 3, t16 = l & 15;
    const unsigned kr = l >> 3;
    asm volatile("" ::"s"(X), "s"(WQKV), "s"(WPHI), "s"(WO), "s"(WR), "s"(WG), "s"(WU), "s"(WD), "s"(OUT), "s"(ws));
    unsigned tag;
#define READ_TAG() do { SCHED(); tag = *(const volatile CAS unsigned*)(ws + WS_EPOCH) + 1u; asm volatile("" : "+s"(tag)); } while (0)
    const rsrc_t rsWS = __builtin_amdgcn_make_buffer_rsrc((void*)ws, 0, WS_USED, 0x00020000);
    const rsrc_t rsX = __builtin_amdgcn_make_buffer_rsrc((void*)X, 0, 2048 * 1024 * 2, 0x00020000);
    const rsrc_t rsW = __builtin_amdgcn_make_buffer_rsrc((void*)WQKV, 0, 1024 * 3072 * 2, 0x00020000);
    const unsigned vA = (kr * 1024 + (((l & 7) ^ kr) * 8)) * 2;
    const unsigned vB0 = (kr * 3072 + (((l & 7) ^ (((kr >> 1) & 1) << 1)) * 8)) * 2;
    const unsigned vB1 = (kr * 3072 + (((l & 7) ^ ((((kr >> 1) & 1) | 2) << 1)) * 8)) * 2;
    const unsigned srow1 = rt * 128 + hp * 8 + (w & 3) * 2;
    v4u xr[4];
    if (w < 4) {
#pragma unroll
        for (int i = 0; i < 4; i++) xr[i] = ld16_asm(l * 16, rsX, srow1 * 2048 + i * 1024);
    }
    {
        const unsigned vBw = (w & 1) ? vB1 : vB0;
#pragma unroll
        for (int i = 0; i < 2; i++) dma16(ldsb + (w * 2 + i) * 1024, vA, rsX, (rt * 128 + w * 16 + i * 8) * 2048 + rot * 128);
#pragma unroll
        for (int i = 0; i < 3; i++) dma16(ldsb + 16384 + i * 8192 + w * 1024, vBw, rsW, (rot * 64 + w * 8) * 6144 + (i ? hp : hq) * 128 + i * 2048);
    }
    const f32x4 zero4 = {0.f, 0.f, 0.f, 0.f};
    ARR_DECL;
    const bf16x8 ones = mk8(0x3F803F80u, 0x3F803F80u, 0x3F803F80u, 0x3F803F80u);
    const unsigned lw = w & 3;
    const bool ffn = (ct & 7) != 3;
    const unsigned fb = !ffn ? 255u : rt2 * 14 + (i16 < 3 ? i16 : i16 - 2);
    const unsigned cg = (fb + 5) & 15, kc = fb >> 4;
    const unsigned kq = fb / 56, cg1 = fb % 56, cg1u = (cg1 + 3) % 56;
    v4u wreg[24];
    STAMPW(0, 0); STAMPW(0, 4);

    const rsrc_t rsWO = __builtin_amdgcn_make_buffer_rsrc((void*)WO, 0, 1024 * 1024 * 2, 0x00020000);
    const unsigned vWO = kr * 2048 + (((l & 7) ^ (((kr >> 1) & 3) << 1)) * 16);
    unsigned vwo[4];
    auto wo_so = [&](unsigned b) { return (((2 * b & 30) + (b >> 4) + rot2) & 31) * 65536 + ct * 128; };
    const unsigned fw = (((t16 >> 1) & 3) << 2) | (((t16 >> 3) & 1) << 1) | (t16 & 1);
    unsigned ad;
    unsigned ia[4];
    bf16x8 sdv[4];
    auto pre_addr = [&]() {
#pragma unroll
        for (int k = 0; k < 4; k++) { ia[k] = ldsb + L_XB + lw * 8192 + (w >> 2) * 4096 + t16 * 128 + (((4 * k + g) ^ fw) * 8); PIN(ia[k]); }
        const unsigned rr = 4 * g + q4, fr = (((rr >> 1) & 3) << 2) | (((rr >> 3) & 1) << 1) | (rr & 1);
        ad = ldsb + L_XB + (w & 1) * 16384 + rr * 128 + ((((w >> 1) * 4 + p4) ^ fr) * 8);
        PIN(ad);
    };
    auto sd_reads = [&]() {
        unsigned sa = ldsb + L_SD + g * 8;
        PIN(sa);
#pragma unroll
        for (int s = 0; s < 4; s++) {
            const v2u a = *(const LAS v2u*)(sa + s * 64), b = *(const LAS v2u*)(sa + s * 64 + 32);
            sdv[s] = mk8(a[0], a[1], b[0], b[1]);
        }
    };
    bf16x8 v0[4], v1[4];
    auto v_reads = [&]() {
        unsigned vd = ldsb + L_V + (w & 1) * 8192 + l * 16;
        PIN(vd);
#pragma unroll
        for (int s = 0; s < 4; s++) { v0[s] = lds128(vd + s * 2048); v1[s] = lds128(vd + s * 2048 + 1024); }
    };
    auto kv_tile = [&](auto hook6) {
        const unsigned h2 = w & 1, ft = w >> 1;
        WAITL0();
        ARR_MARK(18);
        BAR();
        SCHED();
        bf16x8 pf[4];
#pragma unroll
        for (int s = 0; s < 4; s++) pf[s] = cat8(tr_read(ad + s * 4096), tr_read(ad + s * 4096 + 2048));
        SCHED();
        hook6();
        SCHED();
        f32x4 k0 = zero4, k1 = zero4, kz = zero4;
#pragma unroll
        for (int s = 0; s < 4; s++) { k0 = MFMA(pf[s], v0[s], k0); k1 = MFMA(pf[s], v1[s], k1); }
#pragma unroll
        for (int s = 0; s < 4; s++) kz = MFMA(pf[s], sdv[s], kz);
        const unsigned pbase = WS_P2 + (hp * 16 + rt) * P2_STRIDE;
        const v4u o0 = {nsent(pk2(k0[0], k0[1])), nsent(pk2(k0[2], k0[3])), nsent(pk2(k1[0], k1[1])), nsent(pk2(k1[2], k1[3]))};
        DLY(11);
        __builtin_amdgcn_raw_buffer_store_b128(o0, rsWS, l * 16, pbase + h2 * 4096 + ft * 1024, AUX_SC1);
        asm volatile("s_nop 1" :: "v"(o0));
        __builtin_amdgcn_raw_buffer_store_b64((v2u){nsent(pk2(kz[0], kz[1])), nsent(pk2(kz[2], kz[3]))}, rsWS, t16 == 0 ? g * 8 : OOB,
                                              pbase + 8192 + h2 * 128 + ft * 32, AUX_SC1);
        SCHED();
        ARR_MARK(19);
        BAR();
    };

    const bool zjob = lw == 0 && (rt & 7) == 0;
    v4u pp[4];
    auto pp_ld = [&](int k) {
        return LD16(kr * P2_STRIDE + (l & 7) * 16, WS_P2 + (hp * 16 + (k & 1) * 8) * P2_STRIDE +
                    (k < 2 ? (rt >> 2) * 2048 + (lw & 1) * 1024 + (rt & 3) * 256 + (lw >> 1) * 128 : 8192 + (rt >> 3) * 128 + (zjob ? 0u : OOB)));
    };
    auto kv_reduce = [&]() {
        DLY(12);
        SLEEP(SL1);
        sweepz<4, RS1>(pp_ld, pp);
        DLY(13);
        STAMPW(25, 0);
        auto red = [&](const v4u* p) {
            float lo = xrow_scatter<false>(bflo(p[0][0]) + bflo(p[1][0]), bflo(p[0][1]) + bflo(p[1][1]), bflo(p[0][2]) + bflo(p[1][2]), bflo(p[0][3]) + bflo(p[1][3]));
            float hi = xrow_scatter<false>(bfhi(p[0][0]) + bfhi(p[1][0]), bfhi(p[0][1]) + bfhi(p[1][1]), bfhi(p[0][2]) + bfhi(p[1][2]), bfhi(p[0][3]) + bfhi(p[1][3]));
            lo += dppf<0x128>(lo); hi += dppf<0x128>(hi);
            return nsent(pk2(lo, hi));
        };
        __builtin_amdgcn_raw_buffer_store_b32(red(pp), rsWS, (l & 8) ? OOB : (l & 7) * 16 + g * 4,
                                              WS_G3 + (hp * 4 + (rt >> 2)) * 2048 + (lw & 1) * 1024 + (rt & 3) * 256 + (lw >> 1) * 128, AUX_SC1);
        if (RARE(zjob))
            __builtin_amdgcn_raw_buffer_store_b32(red(pp + 2), rsWS, (l & 8) ? OOB : ((l >> 2) & 1) * 64 + ((l & 1) * 2 + (g >> 1)) * 16 + ((l >> 1) & 1) * 8 + (g & 1) * 4,
                                                  WS_GZ + (hp * 2 + (rt >> 3)) * 128, AUX_SC1);
    };

    const unsigned half = w >> 2;
    const unsigned R2 = rt2 * 128 + lw * 32 + half * 16 + t16;

    rsrc_t rsD = __builtin_amdgcn_make_buffer_rsrc((void*)WD, 0, 1024 * 3584 * 2, 0x00020000);
    unsigned vD, sD;
    auto dloads = [&](int i0, int i1) {
#pragma unroll
        for (int i = i0; i < i1; i++)
            wreg[16 + i] = __builtin_amdgcn_raw_buffer_load_b128(rsD, vD, sD + i * 2048, W_AUX);
        FRESH();
    };
    v4u wp1[2][8];
    auto perm1 = [&]() {
#pragma unroll
        for (int n = 0; n < 16; n++) PIN(wreg[n]);
#pragma unroll
        for (int ip = 0; ip < 4; ip++)
#pragma unroll
            for (int c = 0; c < 4; c++) {
#pragma unroll
                for (int m = 0; m < 2; m++) {
                    const unsigned a = wreg[m * 8 + 2 * ip][c], b = wreg[m * 8 + 2 * ip + 1][c];
                    wp1[m][2 * c][ip] = __builtin_amdgcn_perm(b, a, 0x05040100);
                    wp1[m][2 * c + 1][ip] = __builtin_amdgcn_perm(b, a, 0x07060302);
                }
            }
#pragma unroll
        for (int c = 0; c < 8; c++) { PIN(wp1[0][c]); PIN(wp1[1][c]); }
    };
    v4u xres[2], wrv[2];
    auto res_loads = [&]() {
#pragma unroll
        for (int i = 0; i < 2; i++) {
            if (XT) dma16(ldsb + L_XB + w * 4096 + i * 1024, kr * 2048 + (((l & 7) ^ kr) * 16), rsX, (rt2 * 128 + lw * 32 + half * 16 + i * 8) * 2048 + ct * 128);
            else xres[i] = *(const v4u*)(X + (size_t)R2 * 1024 + ct * 64 + i * 32 + 8 * g);
            wrv[i] = *(const v4u*)(WR + ct * 64 + i * 32 + 8 * g);
        }
        FRESH();
    };

    if (w < 4) {
        const unsigned wm = w & 1, hh = w >> 1;
        f32x4 acc[4][6];
        bf16x8 wf[2], wfq[2];
        float mbt[4], rst[4], csv[2];
        f32x4 mbv[4], csq[2], csk[2];
        unsigned vgc = WS_GC + (l < 32 ? hp : hq) * 256 + (l & 31) * 8;
        PIN(vgc);
        v2u gc;
#pragma unroll
        for (int i = 0; i < 4; i++)
#pragma unroll
            for (int j = 0; j < 6; j++) { acc[i][j] = zero4; PIN(acc[i][j]); }
        {
            const unsigned aoff0 = ldsb + (wm * 64 + t16) * 128 + (((0 + g) ^ (l & 7)) * 16);
            const unsigned aoff1 = ldsb + (wm * 64 + t16) * 128 + (((4 + g) ^ (l & 7)) * 16);
            const unsigned sk = ((q4 >> 1) & 1) | ((g & 1) << 1);
            const unsigned boffA = ldsb + 16384 + (8 * g + q4) * 128 + (((2 * hh) ^ sk) * 32) + p4 * 8;
            const unsigned boffB = ldsb + 16384 + (8 * g + q4) * 128 + (((2 * hh + 1) ^ sk) * 32) + p4 * 8;
            bf16x8 A[2][4], B[3][2];
            s16x4 lo;
            unsigned a0[3], a1[3], bA[3], bB[3];
#pragma unroll
            for (int i = 0; i < 3; i++) {
                a0[i] = aoff0 + i * STAGE_B; a1[i] = aoff1 + i * STAGE_B; bA[i] = boffA + i * STAGE_B; bB[i] = boffB + i * STAGE_B;
                PIN(a0[i]); PIN(a1[i]); PIN(bA[i]); PIN(bB[i]);
            }
            unsigned aT = ldsb + L_MB + (wm * 64 + t16) * 4, aS = ldsb + L_SD + (wm * 64 + t16) * 2, aM = ldsb + L_MB + wm * 256 + g * 16;
            unsigned aC = ldsb + L_CS + hh * 128 + g * 16, aV = ldsb + L_CS + (128 + hh * 32 + t16) * 4;
            unsigned aW = ldsb + L_WPHI + hh * 2048 + (4 * g + q4) * 64 + p4 * 8, aK = ldsb + L_KH + w * 2048 + l * 16;
            PIN(aT); PIN(aS); PIN(aM); PIN(aC); PIN(aV); PIN(aW); PIN(aK);
            pre_addr();
            READ_TAG();
            waitv<5>();
            PIN(xr[0]); PIN(xr[1]); PIN(xr[2]); PIN(xr[3]);
            {
                float s[2], q[2], x0[2];
#pragma unroll
                for (int rr = 0; rr < 2; rr++) {
                    x0[rr] = bflo(rfl(xr[2 * rr][0]));
                    s[rr] = 0.f; q[rr] = 0.f;
#pragma unroll
                    for (int i = 0; i < 4; i++) {
                        const float a = bflo(xr[2 * rr][i]) - x0[rr], b = bfhi(xr[2 * rr][i]) - x0[rr];
                        const float c = bflo(xr[2 * rr + 1][i]) - x0[rr], d = bfhi(xr[2 * rr + 1][i]) - x0[rr];
                        s[rr] += (a + b) + (c + d);
                        q[rr] += (a * a + b * b) + (c * c + d * d);
                    }
                }
                const float sq = row_sum(xrow_scatter<false>(s[0], s[1], q[0], q[1]));
#define RLF(x, n) __uint_as_float(__builtin_amdgcn_readlane(__float_as_uint(x), (n)))
                const float r0 = RLF(sq, 0), r1 = RLF(sq, 16), r2 = RLF(sq, 32), r3 = RLF(sq, 48);
                const float sm = (l ? r1 : r0) * (1.0f / 1024.0f), qm = (l ? r3 : r2) * (1.0f / 1024.0f);
                const float var = fmaxf(qm - sm * sm, 0.f);
                if (l < 2) st_gran(ws, WS_G0 + (srow1 + l) * 8, tag, pk2((l ? x0[1] : x0[0]) + sm, sqrtf(rbf(rbf(var) + 1e-5f))));
            }
            waitv<1>();
            WAITL0();
            ARR_MARK(0);
            BAR();
            SCHED();
            STAMPW(1, 0);
            auto rdB = [&](int u, int j, int i) {
                if (i & 1) B[j % 3][i >> 1] = cat8(lo, bq(bA[u], bB[u], j, i)); else lo = bq(bA[u], bB[u], j, i);
            };
            auto rdA = [&](int u, int i) { A[i & 1][i >> 1] = lds128(((i & 1) ? a1[u] : a0[u]) + (i >> 1) * 2048); };
            auto head = [&](int u) {
                group(acc, A, B[0], 0, [&](int i) { rdB(u, 1 + (i >> 2), i & 3); });
                group(acc, A, B[1], 1, [&](int i) { if (i < 4) rdB(u, 3, i); });
                group(acc, A, B[2], 2, [&](int i) { if (i < 4) rdB(u, 4, i); });
                group(acc, A, B[0], 3, [&](int i) { if (i < 4) rdB(u, 5, i); });
                WL0();
            };
#pragma unroll
            for (int i = 0; i < 4; i++) rdB(0, 0, i);
#pragma unroll
            for (int i = 0; i < 8; i++) rdA(0, i);
            SCHED();
#pragma nounroll
            for (unsigned t = 0; t < 5; t++) {
#pragma unroll
                for (int u = 0; u < 3; u++) {
                    head(u);
                    ARR_MARK(1 + t * 3 + u);
                    BAR();
                    SCHED();
                    group(acc, A, B[1], 4, [&](int i) { if (i < 4) rdB((u + 1) % 3, 0, i); });
                    group(acc, A, B[2], 5, [&](int i) { rdA((u + 1) % 3, i); });
                }
            }
            unsigned kw[4][4];
            f32x2 kx;
            auto kpiece = [&](int n) {
                if (n > 0) { kw[((n - 1 >> 2) + 2) & 3][(n - 1) & 3] = pk2(kx[0], kx[1]); PIN(kw[((n - 1 >> 2) + 2) & 3][(n - 1) & 3]); }
                if (n < 16) {
                    const int mi = ((n >> 2) + 2) & 3, j = n & 3;
                    const f32x4 &c = acc[mi][2 + (j >> 1)], &s_ = csk[j >> 1];
                    float x0 = __builtin_fmaf(mbt[mi], s_[2 * (j & 1)], c[2 * (j & 1)]); PIN(x0);
                    float x1 = __builtin_fmaf(mbt[mi], s_[2 * (j & 1) + 1], c[2 * (j & 1) + 1]); PIN(x1);
                    kx = (f32x2){x0, x1};
                }
                if (n == 5 || n == 9) *(LAS v4u*)(aK + (n == 9) * 1024) = (v4u){kw[2 + (n == 9)][0], kw[2 + (n == 9)][1], kw[2 + (n == 9)][2], kw[2 + (n == 9)][3]};
            };
            group(acc, A, B[0], 0, [&](int i) { rdB(0, 1 + (i >> 2), i & 3); });
            group(acc, A, B[1], 1, [&](int i) {
                if (i < 4) { rdB(0, 3, i); mbt[(i + 2) & 3] = ldsf(aT + ((i + 2) & 3) * 64); }
                else if (i < 6) csk[i - 4] = ldsf4(aC + 256 + (i - 4) * 64);
                else wf[i - 6] = cat8(tr_read(aW + (i - 6) * 32), tr_read(aW + (i - 6) * 32 + 1024));
            });
            group(acc, A, B[2], 2, [&](int i) { if (i < 4) rdB(0, 4, i); else mbv[i - 4] = ldsf4(aM + (i - 4) * 64); kpiece(i); });
            group(acc, A, B[0], 3, [&](int i) { if (i < 4) rdB(0, 5, i); if (i == 2 || i == 3) csv[i - 2] = ldsf(aV + (i - 2) * 64); kpiece(8 + i); });
            DLY(10);
            WL0();
            ARR_MARK(16);
            BAR();
            SCHED();
            kpiece(16);
            f32x4 p[2][2];
#pragma unroll
            for (int i = 0; i < 4; i++) p[i >> 1][i & 1] = MFMA(wf[i & 1], mk8(kw[i >> 1][0], kw[i >> 1][1], kw[i >> 1][2], kw[i >> 1][3]), zero4);
            SCHED();
            STAMPW(2, 0);
#pragma unroll
            for (int mi = 0; mi < 4; mi++) rst[mi] = lds_bf(aS + mi * 32);
            gc = ldg(rsWS, vgc, 0);
#pragma unroll
            for (int a = 0; a < 2; a++)
#pragma unroll
                for (int dt = 0; dt < 2; dt++) {
                    const f32x4 x0 = acc[2 * a][4 + dt] + mbv[2 * a] * csv[dt], x1 = acc[2 * a + 1][4 + dt] + mbv[2 * a + 1] * csv[dt];
                    *(LAS v4u*)(aK + (L_V - L_KH) + w * 2048 + (a * 2 + dt) * 1024) = (v4u){pk2(x0[0], x0[1]), pk2(x0[2], x0[3]), pk2(x1[0], x1[1]), pk2(x1[2], x1[3])};
                }
            SCHED();
            rst[0] = __builtin_amdgcn_rcpf(rst[0]); rst[1] = __builtin_amdgcn_rcpf(rst[1]);
            bf16x8 f[4];
            struct HookC {
                decltype(v_reads)& vr; decltype(sd_reads)& sr;
                DI void operator()(int k, float& x) const { if (k == 1) { PIN(x); WAITL0(); ARR_MARK(17); BAR(); PIN(x); } if (k == 3) { PIN(x); vr(); sr(); PIN(x); } }
                DI void operator()(int, float&, float&) const {}
            };
            softmax2<true>(p, rst[0], rst[1], g >= 2, f, HookC{v_reads, sd_reads});
            img_write2(ia, f);
#pragma unroll
            for (int ft = 0; ft < 2; ft++) wfq[ft] = cat8(tr_read(aW + ft * 32 + 4096), tr_read(aW + ft * 32 + 5120));
            csq[0] = ldsf4(aC); csq[1] = ldsf4(aC + 64);
            STAMPW(3, 0);
            kv_tile([]() {});
            STAMPW(4, 0);
            SCHED();
            if (RC == 2) { res_loads(); SCHED(); }
            group(acc, A, B[1], 4, [&](int i) { if (i == 2 || i == 3) { rst[i] = __builtin_amdgcn_rcpf(rst[i]); PIN(rst[i]); } });
            group(acc, A, B[2], 5, [&](int) {});
            asm volatile("s_nop 7\n\ts_nop 1");
            SCHED();
        }
        bf16x8 phq[2][4];
        v4u kg[6];
        auto kg_ld = [&](int k) { return k < 4 ? LD16(l * 16, WS_G3 + (hq * 2 + hh) * 4096 + k * 1024) : LD16(g * 16, WS_GZ + (hq * 2 + hh) * 128 + (k - 4) * 64); };
        {
            f32x4 pq[2][2][2];
#pragma unroll
            for (int mi = 0; mi < 4; mi++) {
                const f32x4 x0 = acc[mi][0] + mbt[mi] * csq[0], x1 = acc[mi][1] + mbt[mi] * csq[1];
                const bf16x8 qf = mk8(pk2(x0[0], x0[1]), pk2(x0[2], x0[3]), pk2(x1[0], x1[1]), pk2(x1[2], x1[3]));
                pq[mi >> 1][mi & 1][0] = MFMA(wfq[0], qf, zero4); pq[mi >> 1][mi & 1][1] = MFMA(wfq[1], qf, zero4);
            }
            MFMA_SETTLE2(pq[0]); MFMA_SETTLE2(pq[1]);
            if (RARE(!__all(gc[1] == tag))) { unsigned c[1]; sweep<1>(rsWS, vgc, 0, 0, tag, c); }
            ORDER_BEHIND_POLL();
            softmax2<false>(pq[0], rst[0], rst[1], g >= 2, phq[0]);
#pragma unroll
            for (int i = 0; i < 4; i++) PIN(phq[0][i]);
            SCHED();
            kv_reduce();
            SCHED();
            if (RC == 0) res_loads();
            SCHED();
            BAR();
            SCHED();
            softmax2<false>(pq[1], rst[2], rst[3], g >= 2, phq[1]);
#pragma unroll
            for (int i = 0; i < 4; i++) { PIN(phq[0][i]); PIN(phq[1][i]); }
        }
        STAMPW(5, 0);
        STAMPW(26, 0);
        v2u fv = {tag ^ 0x5bd1e995u, tag};
        unsigned sF = WS_F4 + ((rt * 4 + w) * 16 + hq) * 8, vF = l == 0 ? 0 : OOB;
        {
            unsigned vO = l * 16, sO = WS_P4 + ((rt * 8 + wm * 4) * 32 + hq * 2 + hh) * 1024;
            asm volatile("" : "+s"(sO), "+s"(sF));
            PIN(fv); PIN(vF);
            DLY(14);
            SLEEP(SL2);
            sweepz<6, RS2>(kg_ld, kg);
            if (BC == 0) { SCHED(); BAR(); SCHED(); }
            STAMPW(6, 0);
            bf16x8 kvA[2][2], zA[2];
            v4u ov[4];
#pragma unroll
            for (int s = 0; s < 2; s++) {
#pragma unroll
                for (int dt = 0; dt < 2; dt++) kvA[s][dt] = mk8(kg[2 * s][2 * dt], kg[2 * s][2 * dt + 1], kg[2 * s + 1][2 * dt], kg[2 * s + 1][2 * dt + 1]);
                zA[s] = __builtin_bit_cast(bf16x8, kg[4 + s]);
            }
#define PH(mi, s) phq[(mi) >> 1][2 * ((mi) & 1) + (s)]
            f32x4 dn[4], n0[4], n1[4];
            float id[4];
#pragma unroll
            for (int mi = 0; mi < 4; mi++) dn[mi] = MFMA(zA[0], PH(mi, 0), zero4);
#pragma unroll
            for (int mi = 0; mi < 4; mi++) dn[mi] = MFMA(zA[1], PH(mi, 1), dn[mi]);
            SCHED();
#pragma unroll
            for (int mi = 0; mi <= 4; mi++) {
                if (mi < 4) {
                    n0[mi] = MFMA(kvA[0][0], PH(mi, 0), zero4); n1[mi] = MFMA(kvA[0][1], PH(mi, 0), zero4);
                    n0[mi] = MFMA(kvA[1][0], PH(mi, 1), n0[mi]); n1[mi] = MFMA(kvA[1][1], PH(mi, 1), n1[mi]);
                    id[mi] = __builtin_amdgcn_rcpf(rbf(rbf(dn[mi][0]) + 1e-6f));
                }
                if (mi > 0) {
                    const f32x4 a = n0[mi - 1] * id[mi - 1], b = n1[mi - 1] * id[mi - 1];
                    ov[mi - 1] = (v4u){pk2(a[0], a[1]), pk2(a[2], a[3]), pk2(b[0], b[1]), pk2(b[2], b[3])};
                    ST16(ov[mi - 1], vO + (mi - 1) * 32768, sO);
                }
                if (mi < 4) { SGB(0x8, 1); SGB(0x2, 6); SGB(0x8, 1); SGB(0x2, 3); SGB(0x8, 1); SGB(0x2, 3); SGB(0x8, 1); SGB(0x2, 4); }
                SCHED();
            }
#pragma unroll
            for (int mi = 0; mi < 4; mi++) PIN(ov[mi]);
        }
        if (BC == 1) BAR();
        waitv<0>();
        __builtin_amdgcn_raw_buffer_store_b64(fv, rsWS, vF, sF, AUX_SC1);
        if (BC == 2) { SCHED(); BAR(); SCHED(); }
        if (RC == 1) { SCHED(); res_loads(); }
        STAMPW(7, 0);
    } else {
        const unsigned pw = lw & 1;
        const rsrc_t rsG = __builtin_amdgcn_make_buffer_rsrc((void*)WG, 0, 1024 * 3584 * 2, 0x00020000);
        const rsrc_t rsU = __builtin_amdgcn_make_buffer_rsrc((void*)WU, 0, 1024 * 3584 * 2, 0x00020000);
        unsigned vwl = kr * 57344 + (l & 7) * 16;
        unsigned sg = (kq * 256 + lw * 64) * 7168 + cg1 * 128, su = (kq * 256 + lw * 64) * 7168 + cg1u * 128;
        auto wloads = [&](int n0, int n1) {
#pragma unroll
            for (int n = n0; n < n1; n++)
                asm volatile("buffer_load_dwordx4 %0, %1, %2, %3 offen nt" : "=v"(wreg[n]) : "v"(vwl), "s"(n < 8 ? rsG : rsU), "s"((n < 8 ? sg : su) + (n & 7) * 7168));
        };
        auto repack = [&](unsigned gI) {
            const unsigned s2 = (2 * g + (q4 >> 1)) & 3;
            bf16x8 f[4];
#pragma unroll
            for (int nt = 0; nt < 4; nt++) {
                const unsigned a = ldsb + L_WO + gI * 4096 + (4 * g + q4) * 128 + ((((nt >> 1) * 2 + (p4 >> 1)) ^ s2) * 32) + (p4 & 1) * 16 + (nt & 1) * 8;
                f[nt] = cat8(tr_read(a), tr_read(a + 2048));
            }
            WAITL0();
#pragma unroll
            for (int nt = 0; nt < 4; nt++) *(LAS bf16x8*)(ldsb + L_WO + gI * 4096 + nt * 1024 + l * 16) = f[nt];
        };
        unsigned aK, aW, aS;
        auto k_pre = [&]() {
            pre_addr();
            aK = ldsb + L_KH + lw * 2048 + l * 16; aW = ldsb + L_WPHI + (lw >> 1) * 2048 + (4 * g + q4) * 64 + p4 * 8; aS = ldsb + L_SD + (pw * 64 + 32 + t16) * 2;
            PIN(aK); PIN(aW); PIN(aS);
        };
        auto kfeat = [&](auto hook) {
            bf16x8 wf[2];
            float rst[2];
#pragma unroll
            for (int ft = 0; ft < 2; ft++) {
                wf[ft] = cat8(tr_read(aW + ft * 32), tr_read(aW + ft * 32 + 1024));
                rst[ft] = __builtin_amdgcn_rcpf(lds_bf(aS + ft * 32));
            }
            sd_reads();
            PIN(wf[0]); PIN(wf[1]); PIN(rst[0]); PIN(rst[1]);
            SCHED();
            ARR_MARK(16);
            BAR();
            SCHED();
            const bf16x8 kf0 = lds128(aK), kf1 = lds128(aK + 1024);
            SCHED();
            hook(-1, rst[0]);
            hook(0, rst[0]);
            SCHED();
            f32x4 p[2][2];
            p[0][0] = MFMA(wf[0], kf0, zero4); p[0][1] = MFMA(wf[1], kf0, zero4);
            p[1][0] = MFMA(wf[0], kf1, zero4); p[1][1] = MFMA(wf[1], kf1, zero4);
            MFMA_SETTLE2(p);
            bf16x8 f[4];
            softmax2<true>(p, rst[0], rst[1], g >= 2, f, hook);
            img_write2(ia, f);
            SCHED();
            hook(5, rst[0]);
            SCHED();
            STAMPW(3, 4); STAMPW(3, 7);
            kv_tile([&]() { hook(6, rst[0]); });
            STAMPW(4, 4); STAMPW(4, 7);
        };
        auto wblk = [&](int k) {
            constexpr unsigned T1[8] = {0x19181716, 0x041c1b1a, 0x08070605, 0x0c0b0a09, 0x1413120d, 0x11100015, 0x1d030201, 0x1f1e0f0e};
            constexpr unsigned T2[8] = {0x19181716, 0x05041b1a, 0x09080706, 0x13120b0a, 0x11101514, 0x03020100, 0x1d1c0d0c, 0x1f1e0f0e};
            constexpr unsigned T3[8] = {0x19181716, 0x07060504, 0x13120908, 0x11101514, 0x03020100, 0x1b1a0b0a, 0x1d1c0d0c, 0x1f1e0f0e};
            return ((H2 == 5 ? T3[k] : H2 == 6 ? T2[k] : T1[k]) >> (8 * lw)) & 31;
        };
        auto wo_init = [&]() {
#pragma unroll
            for (int i = 0; i < 4; i++) { vwo[i] = vWO + i * (16384 - 1024); PIN(vwo[i]); }
        };
        unsigned hs[8];
        auto wl_pre = [&]() { asm volatile("" : "+s"(sg), "+s"(su)); PIN(vwl); };
        auto wo_pre = [&]() {
            wo_init();
#pragma unroll
            for (int k = 0; k < 8; k++) { hs[k] = wo_so(wblk(k)); asm volatile("" : "+s"(hs[k])); }
        };
        auto wo_rng = [&](int k0, int k1) {
#pragma unroll
            for (int k = k0; k < k1; k++) dma16x4(ldsb + L_WO + wblk(k) * 4096, vwo, rsWO, hs[k]);
        };
        bool early_far = false;
        auto pf = [&](int i) {
            if (i == 2 && early_far) return;
            if (i == 20) i = 2;
            if (i == 0) { wloads(0, F0); SCHED(); if (lw < 2) wo_pre(); SCHED(); }
            if (i < 3 && !ffn) wo_rng(i, i + 1);
            if (i == 1 || i == 2) wloads(i == 1 ? F0 : F1, i == 1 ? F1 : 16);
        };
        struct Hook {
            decltype(pf)& f; decltype(v_reads)& vr;
            DI void operator()(int k, float& x) const { PIN(x); if (k == 1) { ARR_MARK(17); BAR(); } if (k == 3) vr(); f(3 + k); PIN(x); }
            DI void operator()(int k, float& x, float& y) const { PIN(y); f(3 + k); PIN(x); }
        };
        auto tail16 = [&]() {
            kfeat(Hook{pf, v_reads});
            DLY(16);
            if (RH == 2) res_loads();
            if (ffn) wo_rng(0, 3);
            wo_rng(3, H1);
            static_for<R1>([&](auto kc_) {
                constexpr int k = decltype(kc_)::value;
                waitv<4 * (H1 - 1 - k)>();
                repack(wblk(k));
            });
            BAR();
            if (RH == 0) res_loads();
            wo_rng(H1, H2);
            static_for<H2 - R1>([&](auto kc_) {
                constexpr int k = decltype(kc_)::value + R1;
                waitv<4 * (H2 - 1 - k)>();
                repack(wblk(k));
            });
            if (!XT) { PIN(xres[0]); PIN(xres[1]); }
            PIN(wrv[0]); PIN(wrv[1]);
            perm1();
            SCHED();
            if (BC == 2) WAITL0();
            BAR();
            wo_rng(H2, 8);
        };
        auto vT23 = [&]() {
            const unsigned n = (idx * 2 + pw) * 64 + l, r = n / 12, c = n % 12;
            return idx < 24 ? ((r & 127) + (r >> 7) * 256 + 128) * 6144 + (c >> 2) * 2048 + (((xcd >> 1) * 4 + (c >> 2 ? 0 : 4) + (c & 3)) & 15) * 128 : OOB;
        };
        if (lw < 2) {
            const unsigned sA = (rt * 128 + lw * 64) * 2048, sB = lw * 32 * 6144 + hp * 128, sBq = lw * 32 * 6144 + hq * 128;
            unsigned buf = 1;
            unsigned va[4], vb[4];
#pragma unroll
            for (int i = 0; i < 4; i++) { va[i] = vA + i * (16384 - 1024); vb[i] = ((i & 1) ? vB1 : vB0) + i * (49152 - 1024); PIN(va[i]); PIN(vb[i]); }
            auto stage = [&](unsigned s, unsigned bf, int i0, int i1) {
                const unsigned kt = (s + rot) & 15;
                const unsigned lb = ldsb + bf * STAGE_B;
                const unsigned sa = sA + kt * 128, sb = kt * 393216;
                if (i0 <= 0 && 0 < i1) dma16x4(lb + lw * 8192, va, rsX, sa);
                if (i0 <= 1 && 1 < i1) dma16x4(lb + lw * 8192 + 4096, va, rsX, sa + 65536);
                if (i0 <= 2 && 2 < i1) dma16x4(lb + 16384 + 8192 + lw * 4096, vb, rsW, sb + sB + 2048);
                if (i0 <= 3 && 3 < i1) dma16x4(lb + 16384 + 16384 + lw * 4096, vb, rsW, sb + sB + 4096);
                if (i0 <= 4 && 4 < i1) dma16x4(lb + 16384 + lw * 4096, vb, rsW, sb + sBq);
            };
            stage(1, 1, 0, 4);
            SCHED();
            unsigned vt = vT23();
            PIN(vt);
            wl_pre(); k_pre();
            waitv<16>(); ARR_MARK(0); BAR();
            stage(1, 1, 4, 5);
            touch(vt, rsW, (xcd & 1) * 512 * 6144);
            waitv<1>(); ARR_MARK(1); BAR();
            stage(2, 2, 0, 5); stage(3, 0, 0, 3);
            waitv<12>(); ARR_MARK(2); BAR();
            stage(3, 0, 3, 5); stage(4, 1, 0, 4);
            waitv<16>(); ARR_MARK(3); BAR();
            stage(4, 1, 4, 5);
            buf = 2;
#pragma nounroll
            for (unsigned s = 5; s < 16; s++) {
                if (s > 5) { waitv<20>(); ARR_MARK(s - 2); BAR(); }
                stage(s, buf, 0, 5);
                buf = buf == 2 ? 0 : buf + 1;
            }
            waitv<20>(); ARR_MARK(14); BAR();
            pf(0);
            if (ffn) waitv<F0>(); else waitv<F0 + 4>();
            ARR_MARK(15); BAR();
            pf(1);
            tail16();
            READ_TAG();
        } else {
            SCHED();
            unsigned v3;
            const unsigned h3 = (xcd >> 1) * 4 + 3, kv3 = !(xcd & 2), wv = idx * 2 + pw;
            auto line3 = [&](unsigned per, unsigned rows, auto rowf) {
                const unsigned n = wv * (kv3 ? per : (per + 2) / 3) + l, r3 = kv3 ? n / 3 : n, c3 = kv3 ? n % 3 : 0;
                return (idx < 24 && l < (kv3 ? per : (per + 2) / 3) && r3 < rows) ? rowf(r3) * 6144 + c3 * 2048 + ((c3 ? h3 : h3 + 4) & 15) * 128 : OOB;
            };
            v3 = line3(32, 512, [](unsigned r) { return r; });
            touch(line3(16, 256, [](unsigned r) { return (r & 127) + (r >> 7) * 256; }), rsW, (xcd & 1) * 512 * 6144);
            {
                const rsrc_t rsP = __builtin_amdgcn_make_buffer_rsrc((void*)WPHI, 0, 32 * 1024 * 2, 0x00020000);
#pragma unroll
                for (int i = 0; i < 4; i++) dma16(ldsb + L_WPHI + pw * 4096 + i * 1024, l * 16, rsP, (pw ? hq : hp) * 4096 + i * 1024);
            }
            v4u sv = {SENT, SENT, SENT, SENT};
            unsigned vc4 = l < 16 ? WS_G3 + (hp * 4 + (rt >> 2)) * 2048 + pw * 1024 + (rt & 3) * 256 + l * 16
                         : l < 24 ? WS_P2 + (hp * 16 + rt) * P2_STRIDE + 8192 + pw * 128 + (l - 16) * 16
                         : (l < 32 && pw == 0 && (rt & 7) == 0) ? WS_GZ + (hp * 2 + (rt >> 3)) * 128 + (l - 24) * 16 : OOB;
            auto clears = [&](int i0, int i1) {
#pragma unroll
                for (int i = i0; i < i1; i++) {
                    if (i < 4) ST16(sv, l * 16, WS_P2 + (hp * 16 + rt) * P2_STRIDE + pw * 4096 + i * 1024);
                    if (i == 4) ST16(sv, vc4, 0);
                    if (i > 4) ST16(sv, l * 16, WS_TL + (bid * 4 + pw * 2 + i - 5) * 1024);
                }
                FRESH();
            };
            PIN(vc4); PIN(sv);
            unsigned ca = ldsb + 16384 + pw * 8192 + (8 * g + q4) * 128 + (((rt & 3) ^ (((q4 >> 1) & 1) | ((g & 1) << 1))) * 32) + p4 * 8;
            PIN(ca);
            bf16x8 cf[4];
            f32x4 cd[2] = {zero4, zero4};
            auto cs_read = [&](int st) {
#pragma unroll
                for (int i = 0; i < 4; i++) cf[i] = cat8(tr_read(ca + (st % 3) * STAGE_B + i * 4096), tr_read(ca + (st % 3) * STAGE_B + i * 4096 + 512));
                SCHED();
            };
            READ_TAG();
            waitv<5>();
            ARR_MARK(0);
            BAR();
            SCHED();
            cs_read(0);
            FRESH();
            const rsrc_t rsC = __builtin_amdgcn_make_buffer_rsrc((void*)pc0, 0, clen, 0x00020000);
            auto code_pf = [&]() { touch(l < 6 ? l * 128 : OOB, rsC, (idx * 2 + pw) * 768); };
            v4u gl[4];
            float s0 = 0.f, s1 = 0.f;
            unsigned bad;
            v2u gs;
            unsigned vgl = (l < 48 ? l : 0) * 16;
            unsigned aw0 = ldsb + L_CS + pw * 384 + (vgl >> 1), aw1 = ldsb + L_MB + (pw * 64 + l) * 4, aw2 = ldsb + L_SD + (pw * 64 + l) * 2;
            static_for<15>([&](auto tc) {
                constexpr int t = decltype(tc)::value;
                WAITL0();
                ARR_MARK(t + 1);
                BAR();
                SCHED();
                if constexpr (t < 4) {
                    __builtin_amdgcn_s_setprio(2);
#pragma unroll
                    for (int i = 0; i < 4; i++) cd[i & 1] = MFMA(ones, cf[(i & 1) * 2 + (i >> 1)], cd[i & 1]);
                    SCHED();
                    __builtin_amdgcn_s_setprio(0);
                    if constexpr (t < 3) cs_read(t + 1);
                    else __builtin_amdgcn_raw_buffer_store_b64((v2u){__float_as_uint(g ? cd[1][0] : cd[0][0]), tag}, rsWS, (g == 1 || g + pw == 0) ? g * 512 + t16 * 8 : OOB,
                                                               WS_G1 + ((hp * 4 + (rt >> 2)) * 192 + pw * 64 + (rt & 3) * 16) * 8, AUX_SC1);
                }
                if constexpr (t == 0) touch(v3, rsW, (~xcd & 1) * 512 * 6144);
                if constexpr (t == 12) code_pf();
                if constexpr (t == 8) {
                    waitv<0>();
                    __builtin_amdgcn_raw_buffer_store_b64((v2u){0u, tag}, rsWS, l == 0 ? 0 : OOB, WS_GC + ((hp * 2 + pw) * 16 + rt) * 8, AUX_SC1);
                }
                if constexpr (t == 8 || t == 9) {
#pragma unroll
                    for (int r = (t - 8) * 2; r < (t - 8) * 2 + 2; r++)
                        asm volatile("buffer_load_dwordx4 %0, %1, %2, %3 offen sc1" : "=v"(gl[r]) : "v"(vgl), "s"(rsWS), "s"(WS_G1 + ((hp * 4 + r) * 192 + pw * 96) * 8) : "memory");
                }
                if constexpr (t == 6) {
                    asm volatile("buffer_load_dwordx2 %0, %1, %2, %3 offen sc1" : "=v"(gs) : "v"(l * 8), "s"(rsWS), "s"(WS_G0 + (rt * 128 + pw * 64) * 8) : "memory");
                }
                if constexpr (t == 11) {
                    waitv<0>();
                    PIN(gs);
                    bad = gs[1] ^ tag;
#pragma unroll
                    for (int r = 0; r < 4; r++) {
                        PIN(gl[r]);
                        bad |= (gl[r][1] ^ tag) | (gl[r][3] ^ tag);
                        s0 += __uint_as_float(gl[r][0]); s1 += __uint_as_float(gl[r][2]);
                    }
                    PIN(s0); PIN(s1); PIN(bad);
                }
                if constexpr (t == 12) {
                    if (RARE(!__all(bad == 0))) {
                        s0 = 0.f; s1 = 0.f;
#pragma nounroll
                        for (unsigned r = 0; r < 4; r++) {
                            unsigned a[2];
                            sweep<2>(rsWS, vgl, WS_G1 + ((hp * 4 + r) * 192 + pw * 96) * 8, 8, tag, a);
                            s0 += __uint_as_float(a[0]); s1 += __uint_as_float(a[1]);
                        }
                        unsigned c[1];
                        sweep<1>(rsWS, l * 8, WS_G0 + (rt * 128 + pw * 64) * 8, 0, tag, c);
                        gs[0] = c[0];
                    }
                    *(LAS f32x2*)(aw0) = (f32x2){-s0, -s1};
                    stsf(aw1, bflo(gs[0]));
                    *(LAS u16*)(aw2) = (u16)(gs[0] >> 16);
                    SCHED();
                }
                if constexpr (t == 5) k_pre();
                if constexpr (t == 6) { wl_pre(); wo_pre(); }
                if constexpr (t == 7) { PIN(vgl); PIN(aw0); PIN(aw1); PIN(aw2); }
                clears(t < 7 ? t : 7, t < 7 ? t + 1 : 7);
                if constexpr (t == 13 - EF) { if (ES) SLEEP(ES); pf(0); }
                if constexpr (t == 14 - EF) pf(1);
                if constexpr (EF && t == 15 - EF) { pf(20); early_far = true; }
            });
            tail16();
        }
        WAITL0();
        STAMPW(7, 4); STAMPW(7, 7);
    }
    const unsigned sFl = WS_F4 + (rt2 * 4 + (lw >> 1) + 2 * half) * 128;
    ARR_MARK(20);
    if (BC != 2) BAR();
    ARR_END();
    const unsigned wb = ldsb + L_WO + half * 65536 + l * 16;
    bf16x8 bb[2][4];
#pragma unroll
    for (int nt = 0; nt < 4; nt++) { bb[0][nt] = lds128(wb + nt * 1024); PIN(bb[0][nt]); }
    if (XT) {
#pragma unroll
        for (int i = 0; i < 2; i++) { xres[i] = *(const LAS v4u*)(ldsb + L_XB + w * 4096 + t16 * 128 + (((i * 4 + g) ^ (l & 7)) * 16)); PIN(xres[i]); }
    }
    unsigned so[16], va0 = l * 16 + half * 32768, va1 = l * 16 + (half ^ 1) * 32768;
    unsigned aXw = ldsb + L_XB + w * 4096 + l * 16, aXr = ldsb + L_XB + (w ^ 4) * 4096 + l * 16, sG5 = WS_G5 + ((rt2 * 8 + lw * 2 + half) * 16 + ct) * 256;
    unsigned vG5 = g == q4 ? t16 * 16 : OOB;
#pragma unroll
    for (int st = 0; st < 16; st++) { so[st] = WS_P4 + (rt2 * 8 + lw * 2) * 32768 + ((2 * st + half + rot2) & 31) * 1024; asm volatile("" : "+s"(so[st])); }
    unsigned wt[2];
#pragma unroll
    for (int i = 0; i < 2; i++) { wt[i] = ldsb + L_WO + half * 65536 + (4 * g + q4) * 128 + (((i * 2 + (p4 >> 1)) ^ ((2 * g + (q4 >> 1)) & 3)) * 32) + (p4 & 1) * 16; PIN(wt[i]); }
    PIN(va0); PIN(va1); PIN(aXw); PIN(aXr); PIN(vG5);
    vD = kr * 16384 + (l & 7) * 16; sD = (kc * 256 + lw * 64) * 2048 + cg * 128;
    PIN(vD);
    asm volatile("" : "+s"(sG5), "+s"(sD), "+s"(rsD));
    auto code2 = [&]() {
        u64 pc1; unsigned cl1;
        asm volatile("s_getpc_b64 %0\n.Lfk_mid:\n\ts_mov_b32 %1, .Lfk_end-.Lfk_mid" : "=s"(pc1), "=s"(cl1));
        if (w == 0) touch(l < 4 ? l * 4096 : OOB, __builtin_amdgcn_make_buffer_rsrc((void*)pc1, 0, cl1, 0x00020000), idx * 128);
    };
    if (CT2 == 1) code2();
    if (half) SLEEP(SL3H); else SLEEP(SL3);
    DLY(18);
    poll16(rsWS, sFl, tag, l);
    DLY(19);
    FRESH();
    SCHED();
    STAMPW(8, 0); STAMPW(8, 4);
    unsigned swc;
    f32x4 acc2[2][4];
    bf16x8 sop[2];
    {
        auto aload = [&](int st, int m2) { return LD16(m2 ? va1 : va0, so[st]); };
        constexpr int PD = 2;
        v4u af[PD][2];
#pragma unroll
        for (int i = 0; i < PD; i++) {
#pragma unroll
            for (int m2 = 1; m2 >= 0; m2--) af[i][m2] = aload(i, m2);
            SCHED();
        }
#pragma unroll
        for (int nt = 0; nt < 4; nt++) {
            const unsigned x0 = xres[nt >> 1][2 * (nt & 1)], x1 = xres[nt >> 1][2 * (nt & 1) + 1];
            acc2[0][nt] = (f32x4){bflo(x0), bfhi(x0), bflo(x1), bfhi(x1)};
            acc2[1][nt] = zero4;
        }
        unsigned m0 = p4 == 0 ? 0x3F803F80u : 0u, m1 = p4 == 1 ? ~0u : 0u;
        PIN(m0); PIN(m1);
#pragma unroll
        for (int i = 0; i < 2; i++) {
            v4u u;
#pragma unroll
            for (int j = 0; j < 4; j++) u[j] = (wrv[i][j] & m1) | m0;
            sop[i] = __builtin_bit_cast(bf16x8, u);
            PIN(sop[i]);
        }
        {
            float sw = 0.f;
#pragma unroll
            for (int i = 0; i < 8; i++) sw = dot2(wrv[i >> 2][i & 3], 0x3F803F80u, sw);
            swc = f2s(xrow_sum(sw));
            PIN(swc);
        }
        SCHED();
#pragma unroll
        for (int st = 0; st < 16; st++) {
            if (st + 1 == 2 * H2) BAR();
            if (st + 1 < 16) {
#pragma unroll
                for (int nt = 0; nt < 4; nt++) {
                    if (st + 1 < 2 * H2) bb[(st + 1) % (2)][nt] = lds128(wb + (st + 1) * 4096 + nt * 1024);
                    else bb[(st + 1) % (2)][nt] = cat8(tr_read(wt[nt >> 1] + (st + 1) * 4096 + (nt & 1) * 8), tr_read(wt[nt >> 1] + (st + 1) * 4096 + (nt & 1) * 8 + 2048));
                }
            }
#pragma unroll
            for (int mm = 0; mm < 2; mm++) {
                const int m2 = mm ^ 1;
                const bf16x8 a = __builtin_bit_cast(bf16x8, af[st % PD][m2]);
#pragma unroll
                for (int nt = 0; nt < 4; nt++) acc2[m2][nt] = MFMA(bb[st % (2)][nt], a, acc2[m2][nt]);
            }
            if (st + PD < 16) {
#pragma unroll
                for (int m2 = 1; m2 >= 0; m2--) af[st % PD][m2] = aload(PD + st, m2);
            }
            if (st == DS) { SCHED(); if (half) dloads(0, DA); }
#pragma unroll
            for (int i = 0; i < (st + 1 < 2 * H2 ? 4 : 8); i++) { SGB(0x8, 1); SGB(0x100, 1); }
            SCHED();
        }
    }
    STAMPW(9, 0); STAMPW(9, 4);
#pragma unroll
    for (int nt = 0; nt < 4; nt++) stsf4(aXw + nt * 1024, acc2[1][nt]);
    SCHED();
    if (half && DS == 16 && DP == 0) dloads(0, DA);
    SCHED();
    WAITL0();
    if (half) __builtin_amdgcn_s_setprio(1);
    BAR();
    SCHED();
    v4u hpk[2];
    {
        f32x4 pa[4];
#pragma unroll
        for (int nt = 0; nt < 4; nt++) pa[nt] = ldsf4(aXr + nt * 1024);
        SCHED();
#pragma unroll
        for (int i = 0; i < 8; i++) {
            const f32x4 a = acc2[0][i >> 1] + pa[i >> 1];
            hpk[i >> 2][i & 3] = pk2(a[2 * (i & 1)], a[2 * (i & 1) + 1]);
        }
        const bf16x8 h0 = __builtin_bit_cast(bf16x8, hpk[0]), h1 = __builtin_bit_cast(bf16x8, hpk[1]);
        f32x4 gq = MFMA(h0, h0, zero4), sd = MFMA(sop[0], h0, zero4);
        gq = MFMA(h1, h1, gq); sd = MFMA(sop[1], h1, sd);
        float q0 = (l & 1) ? gq[1] : gq[0], q1 = (l & 1) ? gq[3] : gq[2];
        PIN(q0); PIN(q1);
        const v4u pv = {f2s(sd[0]), f2s((l & 2) ? q1 : q0), f2s(sd[1]), swc};
        DLY(20);
        ST16(pv, vG5, sG5);
        asm volatile("s_nop 1" :: "v"(pv));
    }
    __builtin_amdgcn_s_setprio(0);
    if (DP == 1) { SCHED(); if (half) dloads(0, DA); SCHED(); }
    STAMPW(10, 0); STAMPW(10, 4);
    v4u wp2[8];
    auto perm2 = [&]() {
#pragma unroll
        for (int i = 16; i < 24; i++) PIN(wreg[i]);
#pragma unroll
        for (int ip = 0; ip < 4; ip++)
#pragma unroll
            for (int c = 0; c < 4; c++) {
                const unsigned a = wreg[16 + 2 * ip][c], b = wreg[16 + 2 * ip + 1][c];
                wp2[2 * c][ip] = __builtin_amdgcn_perm(b, a, 0x05040100);
                wp2[2 * c + 1][ip] = __builtin_amdgcn_perm(b, a, 0x07060302);
            }
#pragma unroll
        for (int c = 0; c < 8; c++) PIN(wp2[c]);
    };
    const unsigned mt = rt2 * 8 + lw * 2 + half;
    v4u outv[2];
    unsigned kb;
    {
        v4u pv[4];
        unsigned sK = WS_GK + (ct * 128 + mt) * 4, vK = l == 0 ? 0 : OOB, rk = 2047u - R2;
        asm volatile("" : "+s"(sK));
        PIN(vK); PIN(rk);
        unsigned sS = WS_G5 + mt * 4096;
        asm volatile("" : "+s"(sS));
        if (CT2 == 2) code2();
        SLEEP(SL4);
        DLY(21);
        __builtin_amdgcn_s_setprio(1);
        float T[4];
        sweepn<4, 4>([&](int k) { return LD16(l * 16 + k * 1024, sS); }, pv, [&]() {
            const f32x4 ps = (__builtin_bit_cast(f32x4, pv[0]) + __builtin_bit_cast(f32x4, pv[1])) + (__builtin_bit_cast(f32x4, pv[2]) + __builtin_bit_cast(f32x4, pv[3]));
            const float r = xrow_scatter<false>(ps[0], ps[1], ps[2], ps[3]);
            xrow_gather(r, T);
            return r;
        });
        STAMPW(21, 0); STAMPW(21, 4);
        const float mean = T[0] * (1.0f / 1024.0f), var = fmaxf(T[1] * (1.0f / 1024.0f) - mean * mean, 0.f);
        const unsigned sg = pk2(mean, __builtin_amdgcn_sqrtf(rbf(rbf(var) + 1e-5f)));
        const float mb2 = bflo(sg), isd2 = __builtin_amdgcn_rcpf(bfhi(sg));
        const float rsc = (T[2] - mb2 * T[3]) * isd2;
        const unsigned rb = pk2(rsc, rsc);
        const unsigned key = ((rb ^ ((unsigned)((int)rb >> 31) | 0x80000000u)) & 0xffff0000u) | rk;
        kb = umx(key, dppu<0x128>(key));
        kb = umx(kb, dppu<0x124>(kb)); kb = umx(kb, dppu<0x122>(kb)); kb = umx(kb, dppu<0x121>(kb));
        DLY(22);
        ST4(kb, vK, sK);
        SCHED();
        __builtin_amdgcn_s_setprio(0);
        if (half) { dloads(DA, 8); SCHED(); }
        const bool cand = key == kb;
        v4u e[2];
#pragma unroll
        for (int i = 0; i < 8; i++) {
            const float h0 = bflo(hpk[i >> 2][i & 3]), h1 = bfhi(hpk[i >> 2][i & 3]);
            const unsigned d = pk2(h0 - mb2, h1 - mb2);
            e[i >> 2][i & 3] = nsent(pk2(bflo(d) * isd2, bfhi(d) * isd2));
        }
        DLY(24);
        ST16(e[0], cand ? g * 16 : OOB, WS_C + (mt * 16 + ct) * 128);
        ST16(e[1], cand ? g * 16 + 64 : OOB, WS_C + (mt * 16 + ct) * 128);
        SCHED();
        STAMPW(22, 0); STAMPW(22, 4);
#pragma unroll
        for (int i = 0; i < 8; i++)
            outv[i >> 2][i & 3] = pk2(bflo(hpk[i >> 2][i & 3]) + bflo(e[i >> 2][i & 3]), bfhi(hpk[i >> 2][i & 3]) + bfhi(e[i >> 2][i & 3]));
        if (cand) {
#pragma unroll
            for (int i = 0; i < 2; i++) { *(LAS v4u*)(ldsb + L_STASH + w * 256 + i * 64 + g * 16) = hpk[i]; *(LAS v4u*)(ldsb + L_STASH + w * 256 + 128 + i * 64 + g * 16) = e[i]; }
        }
        if (OT) {
            const unsigned ao = ldsb + L_OUT + w * 2048;
#pragma unroll
            for (int i = 0; i < 2; i++) *(LAS v4u*)(ao + t16 * 128 + (((i * 4 + g) ^ (l & 7)) * 16)) = outv[i];
            FRESH();
#pragma unroll
            for (int i = 0; i < 2; i++) outv[i] = *(const LAS v4u*)(ao + i * 1024 + kr * 128 + (((l & 7) ^ kr) * 16));
        }
        PIN(outv[0]); PIN(outv[1]);
    }
    const rsrc_t rsO = __builtin_amdgcn_make_buffer_rsrc((void*)OUT, 0, 2048 * 1024 * 2, 0x00020000);
    auto out_rows = [&](auto want) {
#pragma unroll
        for (int i = 0; i < 2; i++) {
            const unsigned R = mt * 16 + i * 8 + kr;
            __builtin_amdgcn_raw_buffer_store_b128(outv[i], rsO, want(2047u - R) ? R * 2048 + (l & 7) * 16 : OOB, ct * 128, OA);
        }
    };
    const bool early = EO == 1 || EO == 3 || (EO == 2 && !half) || (EO == 4 && w != 1);
    if (EO == 1 || (EO == 4 && w != 1) || ((EO == 2 || EO == 3) && !half)) out_rows([&](unsigned r) { return r != (kb & 2047u); });
    unsigned aG = ldsb + L_WO + 512;
    PIN(aG);
    unsigned vX = (2 * g + (l & 1)) * 16, aP = (2 * g + (l & 1)) * 32, aR1 = ldsb + L_RED + lw * 4352 + l * 16, vl4 = l * 4;
    unsigned aR2 = ldsb + L_RED + ((lw & 1) * 2 + ((l >> 2) & 1)) * 1088 + kr * 16 + p4 * 4;
    unsigned sX = WS_C + kq * 512 + lw * 128, sG8w = WS_G8 + ((kq * 2 + lw) * 3584 + (lw ? cg1u : cg1) * 64) * 4, sG8r = WS_G8 + (kc * 256 + lw * 64) * 4;
    unsigned sG9 = WS_G9 + (cg * 14 + kc) * 256;
    unsigned aR3 = aR1 + 17408 - lw * 2176;
    PIN(vX); PIN(aP); PIN(aR1); PIN(aR2); PIN(aR3); PIN(vl4);
    asm volatile("" : "+s"(sX), "+s"(sG8w), "+s"(sG8r), "+s"(sG9));
    SCHED();
    if (w == 1) {
        unsigned sK = WS_GK + (bid & 15) * 512, gk;
        asm volatile("" : "+s"(sK));
        SLEEP(SL5);
        DLY(23);
        unsigned spins = 0, lim;
#pragma nounroll
        do {
            FRESH();
            const v2u hb = ldg(rsWS, l * 8, sK);
            lim = budget<-2, -1>(spins);
            gk = wmax(hb);
        } while (__any(gk > lim));
        stsu(aG, wave_maxu(gk));
        if (0) {
            v2u hb[1];
            sweepz<1>([&](int) { return ldg(rsWS, l * 8, sK); }, hb);
            stsu(aG, wave_maxu(wmax(hb[0])));
        }
        STAMPR(23, 2);
    }
    if (half) { DLY(25); perm2(); if (EO == 3) out_rows([&](unsigned r) { return r != (kb & 2047u); }); }
    STAMPW(24, 7);
    WAITL0();
    if (half) __builtin_amdgcn_s_setprio(1);
    BAR();
    STAMPW(11, 0); STAMPW(11, 4);
    const unsigned gkv = ldsu(aG);
    auto store_out = [&]() {
        if (OT) {
            out_rows([&](unsigned r) { return r != (gkv & 2047u) && (!early || r == (kb & 2047u)); });
            return;
        }
        const unsigned vo = R2 != 2047u - (gkv & 2047u) ? R2 * 2048 + g * 16 : OOB;
#pragma unroll
        for (int i = 0; i < 2; i++)
            __builtin_amdgcn_raw_buffer_store_b128(outv[i], rsO, vo, ct * 128 + i * 64, AUX_SC1);
    };
    if (w < 4) {
        if (bid == 0 && tid == 0) __hip_atomic_store((gu32*)(ws + WS_EPOCH), tag, RLX_AGENT);
        if (LO == 1 || (LO == 2 && w < 2)) { BAR(); BAR(); if (LS) SLEEP(LS); }
        DLY(5);
        if (DL != 6) store_out();
        STAMPW(12, 0);
        return;
    }

    auto gemv = [&](const v4u& x, const v4u* wp, int n, unsigned a) {
        f32x4 d[16], o[4];
#pragma unroll
        for (int c = 0; c < n + 4; c++) {
            if (c < n) d[c] = MFMA(__builtin_bit_cast(bf16x8, x), __builtin_bit_cast(bf16x8, wp[c]), zero4);
            SCHED();
            if (c >= 4) {
                const int j = c - 4;
                float t = d[j][0] + dppf<0x128>(d[j][1]);
                asm volatile("" : "+v"(t) : "v"(d[j]));
                o[j >> 2][j & 3] = t;
                if ((j & 3) == 3) stsf4(a + (j >> 2) * 1088, o[j >> 2]);
            }
            SCHED();
        }
        return o[0][0];
    };
    auto sum4 = [&](unsigned a, unsigned stride) {
        float u = ldsf(a) + ldsf(a + stride), v = ldsf(a + 2 * stride) + ldsf(a + 3 * stride);
        PIN(u); PIN(v);
        return u + v;
    };
    if (ffn) {
        v4u xq[1];
        const unsigned vx = ((~gkv & 0x7f0u) << 7) + vX;
        sweepn<1, 0>([&](int) { return LD16(vx, sX); }, xq, [&]() { return gemv(xq[0], &wp1[0][0], 16, aR1); });
        STAMPW(13, 4);
    }
    DLY(2);
    WAITL0();
    BAR();
    if (ffn) {
        if (lw < 2) ST4(f2s(sum4(aR2, 4352)), vl4, sG8w);
        STAMPW(14, 4);
        unsigned pv[8];
        v4u ap;
        SLEEP(SL6);
        sweepn<8, 0>([&](int k) { return LD4(vl4, sG8r + k * 3584 * 4); }, pv, [&]() {
            const float gg = rbf((__uint_as_float(pv[0]) + __uint_as_float(pv[2])) + (__uint_as_float(pv[4]) + __uint_as_float(pv[6])));
            const float uu = rbf((__uint_as_float(pv[1]) + __uint_as_float(pv[3])) + (__uint_as_float(pv[5]) + __uint_as_float(pv[7])));
            const float act = rbf(gg * __builtin_amdgcn_rcpf(1.0f + __expf(-gg))) * uu;
            const unsigned apk = pk2(act, dppf<0xB1>(act));
#pragma unroll
            for (int ip = 0; ip < 4; ip++) ap[ip] = __builtin_amdgcn_ds_bpermute(aP + ip * 8, apk);
            return act;
        });
        STAMPW(15, 4);
        gemv(ap, wp2, 8, aR3);
    }
    WAITL0();
    BAR();
    if (lw != 0) { if (LS) SLEEP(LS); DLY(3); if (DL != 4) store_out(); return; }
    if (ffn) ST4(f2s(sum4(aR2 + 17408, 2176)), vl4, sG9);
    SCHED();
    store_out();
    STAMPW(16, 4);
    const unsigned gkey = rfl(gkv), srow = 2047u - (gkey & 2047u);
    if ((srow >> 7) == rt2) {
        const float rs = __uint_as_float(((gkey & 0x80000000u) ? gkey ^ 0x80000000u : ~gkey) & 0xffff0000u);
        float sig = rbf(1.0f / (1.0f + __expf(-rs)));
        const unsigned sb = ldsb + L_STASH + (((srow >> 5) & 3) + ((srow >> 4) & 1) * 4) * 256 + (t16 * 4 + g) * 2;
        float hc = lds_bf(sb), lc = lds_bf(sb + 128);
        unsigned vo = (t16 * 4 + g) * 2, so_ = srow * 2048 + ct * 128, vy = l < 32 ? l * 16 + 3072 : OOB, sY = WS_G9 + ct * 3584;
        PIN(sig); PIN(hc); PIN(lc); PIN(vo); PIN(vy);
        asm volatile("" : "+s"(so_), "+s"(sY));
        v4u yp[4];
        float y;
        SLEEP(SL7);
        sweepn<4, 0>([&](int k) { return LD16(k < 3 ? l * 16 + k * 1024 : vy, sY); }, yp, [&]() {
            const f32x4 ys = (__builtin_bit_cast(f32x4, yp[0]) + __builtin_bit_cast(f32x4, yp[1])) + (__builtin_bit_cast(f32x4, yp[2]) + __builtin_bit_cast(f32x4, yp[3]));
            y = xrow_scatter<false>(ys[0], ys[1], ys[2], ys[3]);
            return y;
        });
        DLY(1);
        __builtin_amdgcn_raw_buffer_store_b16((u16)(pk2(hc + rbf(lc + rbf(rbf(y) * sig)), 0.f) & 0xffffu), rsO, vo, so_, 0);
        STAMPW(17, 4);
    }
}
__device__ __attribute__((used, noinline)) void fk_tail() { asm volatile(".Lfk_end:"); }

extern "C" void kernel_launch(void* const* d_in, const int* in_sizes, int n_in, void* d_out, int out_size, void* d_ws,
                              size_t ws_size, hipStream_t stream) {
    (void)in_sizes; (void)n_in; (void)out_size; (void)ws_size;
    (void)hipFuncSetAttribute((const void*)fused_kernel, hipFuncAttributeMaxDynamicSharedMemorySize, LDS_BYTES);
    fused_kernel<<<256, 512, LDS_BYTES, stream>>>((const u16*)d_in[0], (const u16*)d_in[1], (const u16*)d_in[2], (const u16*)d_in[3],
                                                  (const u16*)d_in[5], (const u16*)d_in[6], (const u16*)d_in[7], (const u16*)d_in[8],
                                                  (u16*)d_out, (char*)d_ws);
}
```

```cpp
#include <hip/hip_runtime.h>
#include <stdint.h>
#include <utility>

typedef unsigned short u16;
typedef unsigned long long u64;
typedef short bf16x8 __attribute__((ext_vector_type(8)));
typedef short s16x4 __attribute__((ext_vector_type(4)));
typedef float f32x4 __attribute__((ext_vector_type(4)));
typedef float f32x2 __attribute__((ext_vector_type(2)));
typedef __bf16 bf16x2 __attribute__((ext_vector_type(2)));
typedef unsigned v4u __attribute__((ext_vector_type(4)));
typedef unsigned v2u __attribute__((ext_vector_type(2)));
#define LAS __attribute__((address_space(3)))
#define GAS __attribute__((address_space(1)))
#define CAS __attribute__((address_space(4)))
typedef GAS u64 gu64;
typedef GAS unsigned gu32;
typedef __amdgpu_buffer_rsrc_t rsrc_t;
#define DI __device__ __forceinline__
#define RLX_AGENT __ATOMIC_RELAXED, __HIP_MEMORY_SCOPE_AGENT
#define BAR() __builtin_amdgcn_s_barrier()
#define SCHED() __builtin_amdgcn_sched_barrier(0)
#define SGB(m, n) __builtin_amdgcn_sched_group_barrier((m), (n), 0)
#define WAITL0() asm volatile("s_waitcnt lgkmcnt(0)" ::: "memory")
#define WL0() __builtin_amdgcn_s_waitcnt(0xc07f)
#define MFMA(a, b, c) __builtin_amdgcn_mfma_f32_16x16x32_bf16((a), (b), (c), 0, 0, 0)
#define ORDER_BEHIND_POLL() __builtin_amdgcn_fence(__ATOMIC_ACQUIRE, "wavefront")
#define AUX_SC1 16
template <int N> DI void waitv() { asm volatile("s_waitcnt vmcnt(%0)" ::"i"(N) : "memory"); }

#define WS_EPOCH 0
#define WS_G0 0x1000
#define WS_G1 0x8000
#define WS_F4 0x78000
#define WS_GC 0x70000
#define WS_G3 0x80000
#define WS_GZ 0xC0000
#define SENT 0xFFFFFFFFu
#define WS_TL 0x100000
#define WS_G5 0x100000
#define WS_G8 0x180000
#define WS_G9 0x1A0000
#define WS_GK 0x1B4000
#define WS_C 0x1C0000
#define WS_P2 0x200000
#define P2_STRIDE 0x2180
#define WS_P4 0x700000
#define WS_USED 0xB00000
#define WS_STAMP 0x1000000

#define STAGE_B 40960
#define LDS_BYTES 163840
#define L_WO 32768
#define L_MB 161792
#define L_WPHI (L_MB - 8192)
#define L_SD (L_MB + 512)
#define L_CS (L_MB + 1024)
#define L_V L_WO
#define L_KH (L_WO + 65536)
#define L_XB 0
#define L_STASH (L_WO + 1024)
#define L_OUT (L_WO + 8192)
#define L_RED 4096

#define SL1 2
#define SL2 12
#define SL3 12
#define SL4 18
#define SL5 13
#define SL6 12
#define SL7 10
#define DL 0
#define H1 5
#define R1 3
#define PM 0x2110
#define Q1 3
#define Q2 3
#define SLN 0
#define KD 0
#define SL5H 4
#define DLY(n) do { if (DL == (n)) { SCHED(); __builtin_amdgcn_s_sleep(16); SCHED(); } } while (0)
#define SPIN_CAP 20000u
#define W_AUX 2
#define LOG2E 1.4426950408889634f

DI unsigned rfl(unsigned x) { return __builtin_amdgcn_readfirstlane(x); }
DI float bf2f(unsigned u) { return __uint_as_float(u << 16); }
DI float bflo(unsigned u) { return __uint_as_float(u << 16); }
DI float bfhi(unsigned u) { return __uint_as_float(u & 0xffff0000u); }
DI unsigned pk2(float lo, float hi) { f32x2 v = {lo, hi}; return __builtin_bit_cast(unsigned, __builtin_convertvector(v, bf16x2)); }
DI unsigned nsent(unsigned x) { return __builtin_elementwise_min(x, SENT - 1); }
DI unsigned umx(unsigned a, unsigned b) { return __builtin_elementwise_max(a, b); }
DI float rbf(float f) { return bflo(pk2(f, f) & 0xffffu); }
DI bf16x8 mk8(unsigned a, unsigned b, unsigned c, unsigned d) { v4u v = {a, b, c, d}; return __builtin_bit_cast(bf16x8, v); }
DI void dma16(unsigned ldsaddr, unsigned voff, rsrc_t rs, unsigned soff) {
    asm volatile("s_mov_b32 m0, %0\n\ts_nop 0\n\tbuffer_load_dwordx4 %1, %2, %3 offen lds" ::"s"(ldsaddr), "v"(voff), "s"(rs), "s"(soff) : "memory");
}
DI void dma16x4(unsigned ldsaddr, const unsigned* v, rsrc_t rs, unsigned soff) {
    asm volatile("s_mov_b32 m0, %0\n\ts_nop 0\n\tbuffer_load_dwordx4 %1, %5, %6 offen lds\n\tbuffer_load_dwordx4 %2, %5, %6 offen offset:1024 lds\n\t"
                 "buffer_load_dwordx4 %3, %5, %6 offen offset:2048 lds\n\tbuffer_load_dwordx4 %4, %5, %6 offen offset:3072 lds"
                 ::"s"(ldsaddr), "v"(v[0]), "v"(v[1]), "v"(v[2]), "v"(v[3]), "s"(rs), "s"(soff) : "memory");
}
DI void touch(unsigned voff, rsrc_t rs, unsigned soff) { asm volatile("buffer_load_dword v255, %0, %1, %2 offen" ::"v"(voff), "s"(rs), "s"(soff) : "memory", "v255"); }
DI v4u ld16_asm(unsigned voff, rsrc_t rs, unsigned soff) {
    v4u r; asm volatile("buffer_load_dwordx4 %0, %1, %2, %3 offen" : "=v"(r) : "v"(voff), "s"(rs), "s"(soff) : "memory"); return r;
}
#define PIN(x) asm volatile("" : "+v"(x))
DI s16x4 tr_read(unsigned a) { return __builtin_amdgcn_ds_read_tr16_b64_v4i16((LAS s16x4*)(a)); }
DI bf16x8 cat8(s16x4 a, s16x4 b) { return __builtin_shufflevector(a, b, 0, 1, 2, 3, 4, 5, 6, 7); }
DI bf16x8 lds128(unsigned a) { return *(const LAS bf16x8*)(a); }
DI f32x4 ldsf4(unsigned a) { return *(const LAS f32x4*)(a); }
DI float ldsf(unsigned a) { return *(const LAS float*)(a); }
DI unsigned ldsu(unsigned a) { return *(const LAS unsigned*)(a); }
DI void stsf4(unsigned a, f32x4 v) { *(LAS f32x4*)(a) = v; }
DI void stsf(unsigned a, float v) { *(LAS float*)(a) = v; }
DI void stsu(unsigned a, unsigned v) { *(LAS unsigned*)(a) = v; }
template <int C> DI float dppf(float x) { return __uint_as_float(__builtin_amdgcn_update_dpp(0, __float_as_uint(x), C, 0xf, 0xf, true)); }
template <int C> DI unsigned dppu(unsigned x) { return __builtin_amdgcn_update_dpp(0, x, C, 0xf, 0xf, true); }
DI float max2f(float a, float b) { float r; asm("v_max_f32_e32 %0, %1, %2" : "=v"(r) : "v"(a), "v"(b)); return r; }
DI float max3f(float a, float b, float c) { float r; asm("v_max3_f32 %0, %1, %2, %3" : "=v"(r) : "v"(a), "v"(b), "v"(c)); return r; }
DI float row_sum(float x) { x += dppf<0x128>(x); x += dppf<0x124>(x); x += dppf<0x122>(x); x += dppf<0x121>(x); return x; }
DI float xrow_sum(float x) {
    auto a = __builtin_amdgcn_permlane16_swap(__float_as_uint(x), __float_as_uint(x), false, false);
    x = __uint_as_float(a[0]) + __uint_as_float(a[1]);
    auto b = __builtin_amdgcn_permlane32_swap(__float_as_uint(x), __float_as_uint(x), false, false);
    return __uint_as_float(b[0]) + __uint_as_float(b[1]);
}
DI float wave_sum(float x) { return xrow_sum(row_sum(x)); }
DI unsigned wave_maxu(unsigned x) {
    x = umx(x, dppu<0x128>(x)); x = umx(x, dppu<0x124>(x)); x = umx(x, dppu<0x122>(x)); x = umx(x, dppu<0x121>(x));
    auto a = __builtin_amdgcn_permlane16_swap(x, x, false, false);
    x = umx(a[0], a[1]);
    auto b = __builtin_amdgcn_permlane32_swap(x, x, false, false);
    return umx(b[0], b[1]);
}
DI float nmax3f(float a, float b, float c) { float r; asm("v_max3_f32 %0, -%1, -%2, -%3" : "=v"(r) : "v"(a), "v"(b), "v"(c)); return r; }
DI float nmax2f(float a, float b) { float r; asm("v_max_f32_e64 %0, -%1, -%2" : "=v"(r) : "v"(a), "v"(b)); return r; }
DI float mx2(float a, float b) { return __builtin_amdgcn_fmed3f(a, b, __builtin_inff()); }
template <bool MAX> DI float xrow_scatter(float a, float b, float c, float d) {
    const auto s = __builtin_amdgcn_permlane16_swap(__float_as_uint(a), __float_as_uint(b), false, false);
    const auto t = __builtin_amdgcn_permlane16_swap(__float_as_uint(c), __float_as_uint(d), false, false);
    const float u = MAX ? mx2(__uint_as_float(s[0]), __uint_as_float(s[1])) : __uint_as_float(s[0]) + __uint_as_float(s[1]);
    const float v = MAX ? mx2(__uint_as_float(t[0]), __uint_as_float(t[1])) : __uint_as_float(t[0]) + __uint_as_float(t[1]);
    const auto r = __builtin_amdgcn_permlane32_swap(__float_as_uint(u), __float_as_uint(v), false, false);
    return MAX ? mx2(__uint_as_float(r[0]), __uint_as_float(r[1])) : __uint_as_float(r[0]) + __uint_as_float(r[1]);
}
DI void xrow_gather(float w, float (&o)[4]) {
    const auto r = __builtin_amdgcn_permlane32_swap(__float_as_uint(w), __float_as_uint(w), false, false);
    const auto s = __builtin_amdgcn_permlane16_swap(r[0], r[0], false, false);
    const auto t = __builtin_amdgcn_permlane16_swap(r[1], r[1], false, false);
    o[0] = __uint_as_float(s[0]); o[1] = __uint_as_float(s[1]); o[2] = __uint_as_float(t[0]); o[3] = __uint_as_float(t[1]);
}
struct NoHook { DI void operator()(int, float&) const {} DI void operator()(int, float&, float&) const {} };
template <bool MUL, class H = NoHook> DI void softmax2(const f32x4 (&p)[2][2], float rs0, float rs1, bool hi, bf16x8 (&out)[4], H hook = H()) {
    float m[4];
#pragma unroll
    for (int i = 0; i < 2; i++) {
        const f32x4 &p0 = p[i][0], &p1 = p[i][1];
        m[2 * i] = max3f(max3f(p0[0], p0[1], p0[2]), max3f(p0[3], p1[0], p1[1]), max2f(p1[2], p1[3]));
        m[2 * i + 1] = max3f(nmax3f(p0[0], p0[1], p0[2]), nmax3f(p0[3], p1[0], p1[1]), nmax2f(p1[2], p1[3]));
    }
    asm volatile("s_nop 1" : "+v"(m[0]), "+v"(m[1]), "+v"(m[2]), "+v"(m[3]));
    hook(1, m[3]);
    const float rsr = hi ? rs1 : rs0;
    float a[4];
    xrow_gather(xrow_scatter<true>(m[0], m[1], m[2], m[3]) * (rsr * -LOG2E), a);
    f32x2 e[4][4];
    float s[4];
#pragma unroll
    for (int k = 0; k < 4; k++) {
        if (k == 2) hook(2, a[2], s[1]);
        const float sc = ((k >> 1) ? rs1 : rs0) * ((k & 1) ? -LOG2E : LOG2E);
#pragma unroll
        for (int j = 0; j < 4; j++) {
            const f32x4& q = p[k >> 1][j >> 1];
            const f32x2 t = __builtin_elementwise_fma((f32x2){q[2 * (j & 1)], q[2 * (j & 1) + 1]}, (f32x2){sc, sc}, (f32x2){a[k], a[k]});
            e[k][j] = (f32x2){__builtin_amdgcn_exp2f(t[0]), __builtin_amdgcn_exp2f(t[1])};
        }
        const f32x2 h = (e[k][0] + e[k][1]) + (e[k][2] + e[k][3]);
        s[k] = h[0] + h[1];
        if (k == 3) hook(3, s[k]);
    }
    float iv = __builtin_amdgcn_rcpf(xrow_scatter<false>(s[0], s[1], s[2], s[3]));
    if (MUL) iv *= rsr;
    xrow_gather(iv, a);
#pragma unroll
    for (int k = 0; k < 4; k++) {
        if (k == 2) hook(4, a[2]);
        const f32x2 c = {a[k], a[k]};
        const f32x2 x0 = e[k][0] * c, x1 = e[k][1] * c, x2 = e[k][2] * c, x3 = e[k][3] * c;
        out[k] = mk8(pk2(x0[0], x0[1]), pk2(x1[0], x1[1]), pk2(x2[0], x2[1]), pk2(x3[0], x3[1]));
    }
}
DI float dot2(unsigned a, unsigned b, float c) {
    return __builtin_amdgcn_fdot2_f32_bf16(__builtin_bit_cast(bf16x2, a), __builtin_bit_cast(bf16x2, b), c, false);
}
DI void st_gran(char* ws, unsigned off, unsigned tag, unsigned val) {
    __hip_atomic_store((gu64*)(ws + off), ((u64)tag << 32) | val, RLX_AGENT);
}
DI v2u ldg(rsrc_t rs, unsigned voff, unsigned soff) { return __builtin_amdgcn_raw_buffer_load_b64(rs, voff, soff, AUX_SC1); }
#define RARE(c) __builtin_expect(!!(c), 0)
#define FRESH() asm volatile("" ::: "memory")
template <int N> DI void sweep(rsrc_t rs, unsigned voff, unsigned soff, unsigned S, unsigned tag, unsigned (&v)[N]) {
#pragma nounroll
    for (unsigned spins = 0;;) {
        unsigned bad = 0;
        FRESH();
#pragma unroll
        for (int k = 0; k < N; ++k) { const v2u x = ldg(rs, voff, soff + k * S); v[k] = x[0]; bad |= x[1] ^ tag; }
        if (__all(bad == 0)) break;
        if (++spins > SPIN_CAP) break;
        __builtin_amdgcn_s_sleep(1);
    }
}
template <int A, int B> DI unsigned budget(unsigned& spins) {
    unsigned r;
    asm volatile("s_add_i32 %0, %0, 1\n\ts_cmpk_lt_u32 %0, %2\n\ts_cselect_b32 %1, %3, %4" : "+s"(spins), "=s"(r) : "i"(SPIN_CAP), "i"(A), "i"(B) : "scc");
    SCHED();
    return r;
}
DI unsigned wmax(unsigned x) { return x; }
DI unsigned wmax(v2u x) { return umx(x[0], x[1]); }
DI unsigned wmax(v4u x) { return umx(umx(x[0], x[1]), umx(x[2], x[3])); }
template <int N, class T, class F> DI unsigned sweepz(F ld, T (&v)[N]) {
    unsigned spins = 0, lim, m;
#pragma nounroll
    do {
        FRESH();
#pragma unroll
        for (int k = 0; k < N; ++k) v[k] = ld(k);
        lim = budget<-2, -1>(spins);
        m = 0;
#pragma unroll
        for (int k = 0; k < N; ++k) m = umx(m, wmax(v[k]));
    } while (__any(m > lim));
    return m;
}
template <int N, class T, class F, class G> DI void sweepn(F ld, T (&v)[N], G use) {
    unsigned spins = 0;
    bool again;
#pragma nounroll
    do {
        FRESH();
#pragma unroll
        for (int k = 0; k < N; ++k) v[k] = ld(k);
        const int m = budget<3, 0>(spins);
        again = __any(__builtin_amdgcn_classf(use(), m));
    } while (again);
}
DI unsigned f2s(float x) { return nsent(__float_as_uint(x)); }
#define OOB 0x4000000u
#define LD16(v, s) __builtin_amdgcn_raw_buffer_load_b128(rsWS, (v), (s), AUX_SC1)
#define LD4(v, s) __builtin_amdgcn_raw_buffer_load_b32(rsWS, (v), (s), AUX_SC1)
#define ST16(x, v, s) __builtin_amdgcn_raw_buffer_store_b128((x), rsWS, (v), (s), AUX_SC1)
#define ST4(x, v, s) __builtin_amdgcn_raw_buffer_store_b32((x), rsWS, (v), (s), AUX_SC1)
#define SLEEP(n) __builtin_amdgcn_s_sleep(n)
DI void poll16(rsrc_t rs, unsigned off, unsigned tag, int l) {
    unsigned spins = 0, lim, bad;
#pragma nounroll
    do {
        FRESH();
        const v2u x = ldg(rs, (l & 15) * 8, off);
        lim = budget<0, -1>(spins);
        bad = (x[1] ^ tag) | (x[0] ^ tag ^ 0x5bd1e995u);
    } while (__any(bad > lim));
    ORDER_BEHIND_POLL();
}
#define STAMPR(i, role)
#define STAMP(i)
#define STAMPW(i, ww)

#define ARR_DECL
#define KATTR
#define ARR_MARK(i)
#define ARR_END()
template <class F, int... Is> DI void static_for_impl(F&& f, std::integer_sequence<int, Is...>) { (f(std::integral_constant<int, Is>{}), ...); }
template <int N, class F> DI void static_for(F&& f) { static_for_impl(f, std::make_integer_sequence<int, N>{}); }

DI s16x4 bq(unsigned b0, unsigned b1, int j, int i) {
    const int ni = (j + 2) % 6;
    return tr_read(((ni & 1) ? b1 : b0) + (ni >> 1) * 8192 + (i >> 1) * 4096 + (i & 1) * 512);
}
template <class F> DI void group(f32x4 (&acc)[4][6], const bf16x8 (&A)[2][4], const bf16x8 (&B)[2], int j, F gap) {
    const int ni = (j + 2) % 6;
#pragma unroll
    for (int i = 0; i < 8; i++) {
        const int mi = i >> 1, ks = i & 1;
        if (ni < 4) asm volatile("v_mfma_f32_16x16x32_bf16 %0, %1, %2, %0" : "+v"(acc[mi][ni]) : "v"(B[ks]), "v"(A[ks][mi]));
        else asm volatile("v_mfma_f32_16x16x32_bf16 %0, %1, %2, %0" : "+v"(acc[mi][ni]) : "v"(A[ks][mi]), "v"(B[ks]));
        gap(i);
        SCHED();
    }
}
DI void img_write2(const unsigned (&ia)[4], const bf16x8 (&f)[4]) {
#pragma unroll
    for (int k = 0; k < 4; k++) {
        const v4u u = __builtin_bit_cast(v4u, f[k]);
        *(LAS v2u*)(ia[2 * (k & 1)] + (k >> 1) * 2048) = (v2u){u[0], u[1]};
        *(LAS v2u*)(ia[2 * (k & 1) + 1] + (k >> 1) * 2048) = (v2u){u[2], u[3]};
    }
}
DI float lds_bf(unsigned a) { return bf2f(*(const LAS u16*)(a)); }
#define MFMA_SETTLE2(p) asm volatile("s_nop 7\n\ts_nop 1" : "+v"(p[0][0]), "+v"(p[0][1]), "+v"(p[1][0]), "+v"(p[1][1]))

__global__ KATTR __launch_bounds__(512, 2) void fused_kernel(const u16* __restrict__ X, const u16* __restrict__ WQKV, const u16* __restrict__ WPHI,
                                                       const u16* __restrict__ WO, const u16* __restrict__ WR, const u16* __restrict__ WG,
                                                       const u16* __restrict__ WU, const u16* __restrict__ WD, u16* __restrict__ OUT,
                                                       char* __restrict__ ws) {
    extern __shared__ __attribute__((aligned(16))) char lds[];
    u64 pc0; unsigned clen;
    const int tid = threadIdx.x, l = tid & 63;
    asm volatile("s_getpc_b64 %0\n.Lfk_beg:\n\ts_lshr_b32 %1, %3, 3\n\tv_and_b32 v255, 0x1e0, %2\n\tv_add_lshl_u32 v255, v255, %1, 7\n\ts_mov_b32 %1, .Lfk_end-.Lfk_beg\n\t"
                 "v_min_u32 v255, %1, v255\n\tglobal_load_dword v255, v255, %0" : "=&s"(pc0), "=&s"(clen) : "v"(tid), "s"(blockIdx.x) : "v255", "scc");
    SCHED();
    const unsigned w = rfl(tid >> 6);
    const unsigned bid = blockIdx.x, xcd = bid & 7, idx = bid >> 3;
    const unsigned rt = (xcd & 1) * 8 + (idx & 7), hp = (xcd >> 1) * 4 + (idx >> 3);
    const unsigned i16 = idx & 15, rt2 = xcd * 2 + (idx >> 4), ct = i16 == 4 ? 11 : i16 == 11 ? 4 : i16;
    const unsigned hq = (hp + 4) & 15;
    const unsigned rot = (rt >> 2) * 4;
    const unsigned rot2 = (2 * (rt2 + ct)) & 31;
    const unsigned ldsb = (unsigned)(size_t)lds;
    const int g = l >> 4, q4 = (l & 15) >> 2, p4 = l & 3, t16 = l & 15;
    const unsigned kr = l >> 3;
    asm volatile("" ::"s"(X), "s"(WQKV), "s"(WPHI), "s"(WO), "s"(WR), "s"(WG), "s"(WU), "s"(WD), "s"(OUT), "s"(ws));
    unsigned tag;
#define READ_TAG() do { SCHED(); tag = *(const volatile CAS unsigned*)(ws + WS_EPOCH) + 1u; asm volatile("" : "+s"(tag)); } while (0)
    const rsrc_t rsWS = __builtin_amdgcn_make_buffer_rsrc((void*)ws, 0, WS_USED, 0x00020000);
    const rsrc_t rsX = __builtin_amdgcn_make_buffer_rsrc((void*)X, 0, 2048 * 1024 * 2, 0x00020000);
    const rsrc_t rsW = __builtin_amdgcn_make_buffer_rsrc((void*)WQKV, 0, 1024 * 3072 * 2, 0x00020000);
    const unsigned vA = (kr * 1024 + (((l & 7) ^ kr) * 8)) * 2;
    const unsigned vB0 = (kr * 3072 + (((l & 7) ^ (((kr >> 1) & 1) << 1)) * 8)) * 2;
    const unsigned vB1 = (kr * 3072 + (((l & 7) ^ ((((kr >> 1) & 1) | 2) << 1)) * 8)) * 2;
    const unsigned srow1 = rt * 128 + hp * 8 + (w & 3) * 2;
    v4u xr[4];
    if (w < 4) {
#pragma unroll
        for (int i = 0; i < 4; i++) xr[i] = ld16_asm(l * 16, rsX, srow1 * 2048 + i * 1024);
    }
    {
        const unsigned vBw = (w & 1) ? vB1 : vB0;
#pragma unroll
        for (int i = 0; i < 2; i++) dma16(ldsb + (w * 2 + i) * 1024, vA, rsX, (rt * 128 + w * 16 + i * 8) * 2048 + rot * 128);
#pragma unroll
        for (int i = 0; i < 3; i++) dma16(ldsb + 16384 + i * 8192 + w * 1024, vBw, rsW, (rot * 64 + w * 8) * 6144 + (i ? hp : hq) * 128 + i * 2048);
    }
    const f32x4 zero4 = {0.f, 0.f, 0.f, 0.f};
    ARR_DECL;
    const bf16x8 ones = mk8(0x3F803F80u, 0x3F803F80u, 0x3F803F80u, 0x3F803F80u);
    const unsigned lw = w & 3;
    const bool ffn = (ct & 7) != 3;
    const unsigned fb = !ffn ? 255u : rt2 * 14 + (i16 < 3 ? i16 : i16 - 2);
    const unsigned cg = (fb + 5) & 15, kc = fb >> 4;
    const unsigned kq = fb / 56, cg1 = fb % 56, cg1u = (cg1 + 3) % 56;
    v4u wreg[24];
    STAMPW(0, 0); STAMPW(0, 4);

    const rsrc_t rsWO = __builtin_amdgcn_make_buffer_rsrc((void*)WO, 0, 1024 * 1024 * 2, 0x00020000);
    const unsigned vWO = kr * 2048 + (((l & 7) ^ (((kr >> 1) & 3) << 1)) * 16);
    unsigned vwo[4];
    auto wo_so = [&](unsigned b) { return (((2 * b & 30) + (b >> 4) + rot2) & 31) * 65536 + ct * 128; };
    const unsigned fw = (((t16 >> 1) & 3) << 2) | (((t16 >> 3) & 1) << 1) | (t16 & 1);
    unsigned ad;
    unsigned ia[4];
    bf16x8 sdv[4];
    auto pre_addr = [&]() {
#pragma unroll
        for (int k = 0; k < 4; k++) { ia[k] = ldsb + L_XB + lw * 8192 + (w >> 2) * 4096 + t16 * 128 + (((4 * k + g) ^ fw) * 8); PIN(ia[k]); }
        const unsigned rr = 4 * g + q4, fr = (((rr >> 1) & 3) << 2) | (((rr >> 3) & 1) << 1) | (rr & 1);
        ad = ldsb + L_XB + (w & 1) * 16384 + rr * 128 + ((((w >> 1) * 4 + p4) ^ fr) * 8);
        PIN(ad);
    };
    auto sd_reads = [&]() {
        unsigned sa = ldsb + L_SD + g * 8;
        PIN(sa);
#pragma unroll
        for (int s = 0; s < 4; s++) {
            const v2u a = *(const LAS v2u*)(sa + s * 64), b = *(const LAS v2u*)(sa + s * 64 + 32);
            sdv[s] = mk8(a[0], a[1], b[0], b[1]);
        }
    };
    bf16x8 v0[4], v1[4];
    auto v_reads = [&]() {
        unsigned vd = ldsb + L_V + (w & 1) * 8192 + l * 16;
        PIN(vd);
#pragma unroll
        for (int s = 0; s < 4; s++) { v0[s] = lds128(vd + s * 2048); v1[s] = lds128(vd + s * 2048 + 1024); }
    };
    auto kv_tile = [&](auto hook6) {
        const unsigned h2 = w & 1, ft = w >> 1;
        WAITL0();
        ARR_MARK(18);
        BAR();
        SCHED();
        bf16x8 pf[4];
#pragma unroll
        for (int s = 0; s < 4; s++) pf[s] = cat8(tr_read(ad + s * 4096), tr_read(ad + s * 4096 + 2048));
        SCHED();
        hook6();
        SCHED();
        f32x4 k0 = zero4, k1 = zero4, kz = zero4;
#pragma unroll
        for (int s = 0; s < 4; s++) { k0 = MFMA(pf[s], v0[s], k0); k1 = MFMA(pf[s], v1[s], k1); }
#pragma unroll
        for (int s = 0; s < 4; s++) kz = MFMA(pf[s], sdv[s], kz);
        const unsigned pbase = WS_P2 + (hp * 16 + rt) * P2_STRIDE;
        const v4u o0 = {nsent(pk2(k0[0], k0[1])), nsent(pk2(k0[2], k0[3])), nsent(pk2(k1[0], k1[1])), nsent(pk2(k1[2], k1[3]))};
        DLY(11);
        __builtin_amdgcn_raw_buffer_store_b128(o0, rsWS, l * 16, pbase + h2 * 4096 + ft * 1024, AUX_SC1);
        asm volatile("s_nop 1" :: "v"(o0));
        __builtin_amdgcn_raw_buffer_store_b64((v2u){nsent(pk2(kz[0], kz[1])), nsent(pk2(kz[2], kz[3]))}, rsWS, t16 == 0 ? g * 8 : OOB,
                                              pbase + 8192 + h2 * 128 + ft * 32, AUX_SC1);
        SCHED();
        ARR_MARK(19);
        BAR();
    };

    const bool zjob = lw == 0 && (rt & 7) == 0;
    v4u pp[4];
    auto pp_ld = [&](int k) {
        return LD16(kr * P2_STRIDE + (l & 7) * 16, WS_P2 + (hp * 16 + (k & 1) * 8) * P2_STRIDE +
                    (k < 2 ? (rt >> 2) * 2048 + (lw & 1) * 1024 + (rt & 3) * 256 + (lw >> 1) * 128 : 8192 + (rt >> 3) * 128 + (zjob ? 0u : OOB)));
    };
    auto kv_reduce = [&]() {
        DLY(12);
        SLEEP(SL1);
        sweepz<4>(pp_ld, pp);
        DLY(13);
        STAMPW(25, 0);
        auto red = [&](const v4u* p) {
            float lo = xrow_scatter<false>(bflo(p[0][0]) + bflo(p[1][0]), bflo(p[0][1]) + bflo(p[1][1]), bflo(p[0][2]) + bflo(p[1][2]), bflo(p[0][3]) + bflo(p[1][3]));
            float hi = xrow_scatter<false>(bfhi(p[0][0]) + bfhi(p[1][0]), bfhi(p[0][1]) + bfhi(p[1][1]), bfhi(p[0][2]) + bfhi(p[1][2]), bfhi(p[0][3]) + bfhi(p[1][3]));
            lo += dppf<0x128>(lo); hi += dppf<0x128>(hi);
            return nsent(pk2(lo, hi));
        };
        __builtin_amdgcn_raw_buffer_store_b32(red(pp), rsWS, (l & 8) ? OOB : (l & 7) * 16 + g * 4,
                                              WS_G3 + (hp * 4 + (rt >> 2)) * 2048 + (lw & 1) * 1024 + (rt & 3) * 256 + (lw >> 1) * 128, AUX_SC1);
        if (RARE(zjob))
            __builtin_amdgcn_raw_buffer_store_b32(red(pp + 2), rsWS, (l & 8) ? OOB : ((l >> 2) & 1) * 64 + ((l & 1) * 2 + (g >> 1)) * 16 + ((l >> 1) & 1) * 8 + (g & 1) * 4,
                                                  WS_GZ + (hp * 2 + (rt >> 3)) * 128, AUX_SC1);
    };

    const unsigned half = w >> 2;
    const unsigned R2 = rt2 * 128 + lw * 32 + half * 16 + t16;

    rsrc_t rsD = __builtin_amdgcn_make_buffer_rsrc((void*)WD, 0, 1024 * 3584 * 2, 0x00020000);
    unsigned vD, sD;
    auto dloads = [&](int i0, int i1) {
#pragma unroll
        for (int i = i0; i < i1; i++)
            wreg[16 + i] = __builtin_amdgcn_raw_buffer_load_b128(rsD, vD, sD + i * 2048, W_AUX);
        FRESH();
    };
    v4u wp1[2][8];
    auto perm1 = [&]() {
#pragma unroll
        for (int n = 0; n < 16; n++) PIN(wreg[n]);
#pragma unroll
        for (int ip = 0; ip < 4; ip++)
#pragma unroll
            for (int c = 0; c < 4; c++) {
#pragma unroll
                for (int m = 0; m < 2; m++) {
                    const unsigned a = wreg[m * 8 + 2 * ip][c], b = wreg[m * 8 + 2 * ip + 1][c];
                    wp1[m][2 * c][ip] = __builtin_amdgcn_perm(b, a, 0x05040100);
                    wp1[m][2 * c + 1][ip] = __builtin_amdgcn_perm(b, a, 0x07060302);
                }
            }
#pragma unroll
        for (int c = 0; c < 8; c++) { PIN(wp1[0][c]); PIN(wp1[1][c]); }
    };
    v4u xres[2], wrv[2];
    auto res_loads = [&]() {
#pragma unroll
        for (int i = 0; i < 2; i++) {
            xres[i] = *(const v4u*)(X + (size_t)R2 * 1024 + ct * 64 + i * 32 + 8 * g);
            wrv[i] = *(const v4u*)(WR + ct * 64 + i * 32 + 8 * g);
        }
        FRESH();
    };

    if (w < 4) {
        const unsigned wm = w & 1, hh = w >> 1;
        f32x4 acc[4][6];
        bf16x8 wf[2], wfq[2];
        float mbt[4], rst[4], csv[2];
        f32x4 mbv[4], csq[2], csk[2];
        unsigned vgc = WS_GC + (l < 32 ? hp : hq) * 256 + (l & 31) * 8;
        PIN(vgc);
        v2u gc;
#pragma unroll
        for (int i = 0; i < 4; i++)
#pragma unroll
            for (int j = 0; j < 6; j++) { acc[i][j] = zero4; PIN(acc[i][j]); }
        {
            const unsigned aoff0 = ldsb + (wm * 64 + t16) * 128 + (((0 + g) ^ (l & 7)) * 16);
            const unsigned aoff1 = ldsb + (wm * 64 + t16) * 128 + (((4 + g) ^ (l & 7)) * 16);
            const unsigned sk = ((q4 >> 1) & 1) | ((g & 1) << 1);
            const unsigned boffA = ldsb + 16384 + (8 * g + q4) * 128 + (((2 * hh) ^ sk) * 32) + p4 * 8;
            const unsigned boffB = ldsb + 16384 + (8 * g + q4) * 128 + (((2 * hh + 1) ^ sk) * 32) + p4 * 8;
            bf16x8 A[2][4], B[3][2];
            s16x4 lo;
            unsigned a0[3], a1[3], bA[3], bB[3];
#pragma unroll
            for (int i = 0; i < 3; i++) {
                a0[i] = aoff0 + i * STAGE_B; a1[i] = aoff1 + i * STAGE_B; bA[i] = boffA + i * STAGE_B; bB[i] = boffB + i * STAGE_B;
                PIN(a0[i]); PIN(a1[i]); PIN(bA[i]); PIN(bB[i]);
            }
            unsigned aT = ldsb + L_MB + (wm * 64 + t16) * 4, aS = ldsb + L_SD + (wm * 64 + t16) * 2, aM = ldsb + L_MB + wm * 256 + g * 16;
            unsigned aC = ldsb + L_CS + hh * 128 + g * 16, aV = ldsb + L_CS + (128 + hh * 32 + t16) * 4;
            unsigned aW = ldsb + L_WPHI + hh * 2048 + (4 * g + q4) * 64 + p4 * 8, aK = ldsb + L_KH + w * 2048 + l * 16;
            PIN(aT); PIN(aS); PIN(aM); PIN(aC); PIN(aV); PIN(aW); PIN(aK);
            pre_addr();
            READ_TAG();
            waitv<5>();
            PIN(xr[0]); PIN(xr[1]); PIN(xr[2]); PIN(xr[3]);
            {
                float s[2], q[2], x0[2];
#pragma unroll
                for (int rr = 0; rr < 2; rr++) {
                    x0[rr] = bflo(rfl(xr[2 * rr][0]));
                    s[rr] = 0.f; q[rr] = 0.f;
#pragma unroll
                    for (int i = 0; i < 4; i++) {
                        const float a = bflo(xr[2 * rr][i]) - x0[rr], b = bfhi(xr[2 * rr][i]) - x0[rr];
                        const float c = bflo(xr[2 * rr + 1][i]) - x0[rr], d = bfhi(xr[2 * rr + 1][i]) - x0[rr];
                        s[rr] += (a + b) + (c + d);
                        q[rr] += (a * a + b * b) + (c * c + d * d);
                    }
                }
                const float sq = row_sum(xrow_scatter<false>(s[0], s[1], q[0], q[1]));
#define RLF(x, n) __uint_as_float(__builtin_amdgcn_readlane(__float_as_uint(x), (n)))
                const float r0 = RLF(sq, 0), r1 = RLF(sq, 16), r2 = RLF(sq, 32), r3 = RLF(sq, 48);
                const float sm = (l ? r1 : r0) * (1.0f / 1024.0f), qm = (l ? r3 : r2) * (1.0f / 1024.0f);
                const float var = fmaxf(qm - sm * sm, 0.f);
                if (l < 2) st_gran(ws, WS_G0 + (srow1 + l) * 8, tag, pk2((l ? x0[1] : x0[0]) + sm, sqrtf(rbf(rbf(var) + 1e-5f))));
            }
            waitv<1>();
            WAITL0();
            ARR_MARK(0);
            BAR();
            SCHED();
            STAMPW(1, 0);
            auto rdB = [&](int u, int j, int i) {
                if (i & 1) B[j % 3][i >> 1] = cat8(lo, bq(bA[u], bB[u], j, i)); else lo = bq(bA[u], bB[u], j, i);
            };
            auto rdA = [&](int u, int i) { A[i & 1][i >> 1] = lds128(((i & 1) ? a1[u] : a0[u]) + (i >> 1) * 2048); };
            auto head = [&](int u) {
                group(acc, A, B[0], 0, [&](int i) { rdB(u, 1 + (i >> 2), i & 3); });
                group(acc, A, B[1], 1, [&](int i) { if (i < 4) rdB(u, 3, i); });
                group(acc, A, B[2], 2, [&](int i) { if (i < 4) rdB(u, 4, i); });
                group(acc, A, B[0], 3, [&](int i) { if (i < 4) rdB(u, 5, i); });
                WL0();
            };
#pragma unroll
            for (int i = 0; i < 4; i++) rdB(0, 0, i);
#pragma unroll
            for (int i = 0; i < 8; i++) rdA(0, i);
            SCHED();
#pragma nounroll
            for (unsigned t = 0; t < 5; t++) {
#pragma unroll
                for (int u = 0; u < 3; u++) {
                    head(u);
                    ARR_MARK(1 + t * 3 + u);
                    BAR();
                    SCHED();
                    group(acc, A, B[1], 4, [&](int i) { if (i < 4) rdB((u + 1) % 3, 0, i); });
                    group(acc, A, B[2], 5, [&](int i) { rdA((u + 1) % 3, i); });
                }
            }
            unsigned kw[4][4];
            f32x2 kx;
            auto kpiece = [&](int n) {
                if (n > 0) { kw[((n - 1 >> 2) + 2) & 3][(n - 1) & 3] = pk2(kx[0], kx[1]); PIN(kw[((n - 1 >> 2) + 2) & 3][(n - 1) & 3]); }
                if (n < 16) {
                    const int mi = ((n >> 2) + 2) & 3, j = n & 3;
                    const f32x4 &c = acc[mi][2 + (j >> 1)], &s_ = csk[j >> 1];
                    float x0 = __builtin_fmaf(mbt[mi], s_[2 * (j & 1)], c[2 * (j & 1)]); PIN(x0);
                    float x1 = __builtin_fmaf(mbt[mi], s_[2 * (j & 1) + 1], c[2 * (j & 1) + 1]); PIN(x1);
                    kx = (f32x2){x0, x1};
                }
                if (n == 5 || n == 9) *(LAS v4u*)(aK + (n == 9) * 1024) = (v4u){kw[2 + (n == 9)][0], kw[2 + (n == 9)][1], kw[2 + (n == 9)][2], kw[2 + (n == 9)][3]};
            };
            group(acc, A, B[0], 0, [&](int i) { rdB(0, 1 + (i >> 2), i & 3); });
            group(acc, A, B[1], 1, [&](int i) {
                if (i < 4) { rdB(0, 3, i); mbt[(i + 2) & 3] = ldsf(aT + ((i + 2) & 3) * 64); }
                else if (i < 6) csk[i - 4] = ldsf4(aC + 256 + (i - 4) * 64);
                else wf[i - 6] = cat8(tr_read(aW + (i - 6) * 32), tr_read(aW + (i - 6) * 32 + 1024));
            });
            group(acc, A, B[2], 2, [&](int i) { if (i < 4) rdB(0, 4, i); else mbv[i - 4] = ldsf4(aM + (i - 4) * 64); kpiece(i); });
            group(acc, A, B[0], 3, [&](int i) { if (i < 4) rdB(0, 5, i); if (i == 2 || i == 3) csv[i - 2] = ldsf(aV + (i - 2) * 64); kpiece(8 + i); });
            DLY(10);
            WL0();
            ARR_MARK(16);
            BAR();
            SCHED();
            kpiece(16);
            f32x4 p[2][2];
#pragma unroll
            for (int i = 0; i < 4; i++) p[i >> 1][i & 1] = MFMA(wf[i & 1], mk8(kw[i >> 1][0], kw[i >> 1][1], kw[i >> 1][2], kw[i >> 1][3]), zero4);
            SCHED();
            STAMPW(2, 0);
#pragma unroll
            for (int mi = 0; mi < 4; mi++) rst[mi] = lds_bf(aS + mi * 32);
            gc = ldg(rsWS, vgc, 0);
#pragma unroll
            for (int a = 0; a < 2; a++)
#pragma unroll
                for (int dt = 0; dt < 2; dt++) {
                    const f32x4 x0 = acc[2 * a][4 + dt] + mbv[2 * a] * csv[dt], x1 = acc[2 * a + 1][4 + dt] + mbv[2 * a + 1] * csv[dt];
                    *(LAS v4u*)(aK + (L_V - L_KH) + w * 2048 + (a * 2 + dt) * 1024) = (v4u){pk2(x0[0], x0[1]), pk2(x0[2], x0[3]), pk2(x1[0], x1[1]), pk2(x1[2], x1[3])};
                }
            SCHED();
            rst[0] = __builtin_amdgcn_rcpf(rst[0]); rst[1] = __builtin_amdgcn_rcpf(rst[1]);
            bf16x8 f[4];
            struct HookC {
                decltype(v_reads)& vr; decltype(sd_reads)& sr;
                DI void operator()(int k, float& x) const { if (k == 1) { PIN(x); WAITL0(); ARR_MARK(17); BAR(); PIN(x); } if (k == 3) { PIN(x); vr(); sr(); PIN(x); } }
                DI void operator()(int, float&, float&) const {}
            };
            softmax2<true>(p, rst[0], rst[1], g >= 2, f, HookC{v_reads, sd_reads});
            img_write2(ia, f);
#pragma unroll
            for (int ft = 0; ft < 2; ft++) wfq[ft] = cat8(tr_read(aW + ft * 32 + 4096), tr_read(aW + ft * 32 + 5120));
            csq[0] = ldsf4(aC); csq[1] = ldsf4(aC + 64);
            STAMPW(3, 0);
            kv_tile([]() {});
            STAMPW(4, 0);
            SCHED();
            group(acc, A, B[1], 4, [&](int i) { if (i == 2 || i == 3) { rst[i] = __builtin_amdgcn_rcpf(rst[i]); PIN(rst[i]); } });
            group(acc, A, B[2], 5, [&](int) {});
            asm volatile("s_nop 7\n\ts_nop 1");
            SCHED();
        }
        bf16x8 phq[2][4];
        v4u kg[6];
        auto kg_ld = [&](int k) { return k < 4 ? LD16(l * 16, WS_G3 + (hq * 2 + hh) * 4096 + k * 1024) : LD16(g * 16, WS_GZ + (hq * 2 + hh) * 128 + (k - 4) * 64); };
        {
            f32x4 pq[2][2][2];
#pragma unroll
            for (int mi = 0; mi < 4; mi++) {
                const f32x4 x0 = acc[mi][0] + mbt[mi] * csq[0], x1 = acc[mi][1] + mbt[mi] * csq[1];
                const bf16x8 qf = mk8(pk2(x0[0], x0[1]), pk2(x0[2], x0[3]), pk2(x1[0], x1[1]), pk2(x1[2], x1[3]));
                pq[mi >> 1][mi & 1][0] = MFMA(wfq[0], qf, zero4); pq[mi >> 1][mi & 1][1] = MFMA(wfq[1], qf, zero4);
            }
            MFMA_SETTLE2(pq[0]); MFMA_SETTLE2(pq[1]);
            if (RARE(!__all(gc[1] == tag))) { unsigned c[1]; sweep<1>(rsWS, vgc, 0, 0, tag, c); }
            ORDER_BEHIND_POLL();
            softmax2<false>(pq[0], rst[0], rst[1], g >= 2, phq[0]);
#pragma unroll
            for (int i = 0; i < 4; i++) PIN(phq[0][i]);
            SCHED();
            kv_reduce();
            SCHED();
            res_loads();
            SCHED();
            BAR();
            SCHED();
            softmax2<false>(pq[1], rst[2], rst[3], g >= 2, phq[1]);
#pragma unroll
            for (int i = 0; i < 4; i++) { PIN(phq[0][i]); PIN(phq[1][i]); }
        }
        STAMPW(5, 0);
        STAMPW(26, 0);
        v2u fv = {tag ^ 0x5bd1e995u, tag};
        unsigned sF = WS_F4 + ((rt * 4 + w) * 16 + hq) * 8, vF = l == 0 ? 0 : OOB;
        {
            unsigned vO = l * 16, sO = WS_P4 + ((rt * 8 + wm * 4) * 32 + hq * 2 + hh) * 1024;
            asm volatile("" : "+s"(sO), "+s"(sF));
            PIN(fv); PIN(vF);
            DLY(14);
            SLEEP(SL2);
            sweepz<6>(kg_ld, kg);
            DLY(15);
            STAMPW(6, 0);
            bf16x8 kvA[2][2], zA[2];
            v4u ov[4];
#pragma unroll
            for (int s = 0; s < 2; s++) {
#pragma unroll
                for (int dt = 0; dt < 2; dt++) kvA[s][dt] = mk8(kg[2 * s][2 * dt], kg[2 * s][2 * dt + 1], kg[2 * s + 1][2 * dt], kg[2 * s + 1][2 * dt + 1]);
                zA[s] = __builtin_bit_cast(bf16x8, kg[4 + s]);
            }
#define PH(mi, s) phq[(mi) >> 1][2 * ((mi) & 1) + (s)]
            f32x4 dn[4], n0[4], n1[4];
            float id[4];
#pragma unroll
            for (int mi = 0; mi < 4; mi++) dn[mi] = MFMA(zA[0], PH(mi, 0), zero4);
#pragma unroll
            for (int mi = 0; mi < 4; mi++) dn[mi] = MFMA(zA[1], PH(mi, 1), dn[mi]);
            SCHED();
#pragma unroll
            for (int mi = 0; mi <= 4; mi++) {
                if (mi < 4) {
                    n0[mi] = MFMA(kvA[0][0], PH(mi, 0), zero4); n1[mi] = MFMA(kvA[0][1], PH(mi, 0), zero4);
                    n0[mi] = MFMA(kvA[1][0], PH(mi, 1), n0[mi]); n1[mi] = MFMA(kvA[1][1], PH(mi, 1), n1[mi]);
                    id[mi] = __builtin_amdgcn_rcpf(rbf(rbf(dn[mi][0]) + 1e-6f));
                }
                if (mi > 0) {
                    const f32x4 a = n0[mi - 1] * id[mi - 1], b = n1[mi - 1] * id[mi - 1];
                    ov[mi - 1] = (v4u){pk2(a[0], a[1]), pk2(a[2], a[3]), pk2(b[0], b[1]), pk2(b[2], b[3])};
                    ST16(ov[mi - 1], vO + (mi - 1) * 32768, sO);
                }
                if (mi < 4) { SGB(0x8, 1); SGB(0x2, 6); SGB(0x8, 1); SGB(0x2, 3); SGB(0x8, 1); SGB(0x2, 3); SGB(0x8, 1); SGB(0x2, 4); }
                SCHED();
            }
#pragma unroll
            for (int mi = 0; mi < 4; mi++) PIN(ov[mi]);
        }
        BAR();
        waitv<0>();
        __builtin_amdgcn_raw_buffer_store_b64(fv, rsWS, vF, sF, AUX_SC1);
        STAMPW(7, 0);
    } else {
        const unsigned pw = lw & 1;
        const rsrc_t rsG = __builtin_amdgcn_make_buffer_rsrc((void*)WG, 0, 1024 * 3584 * 2, 0x00020000);
        const rsrc_t rsU = __builtin_amdgcn_make_buffer_rsrc((void*)WU, 0, 1024 * 3584 * 2, 0x00020000);
        unsigned vwl = kr * 57344 + (l & 7) * 16;
        unsigned sg = (kq * 256 + lw * 64) * 7168 + cg1 * 128, su = (kq * 256 + lw * 64) * 7168 + cg1u * 128;
        auto wloads = [&](int n0, int n1) {
#pragma unroll
            for (int n = n0; n < n1; n++)
                asm volatile("buffer_load_dwordx4 %0, %1, %2, %3 offen nt" : "=v"(wreg[n]) : "v"(vwl), "s"(n < 8 ? rsG : rsU), "s"((n < 8 ? sg : su) + (n & 7) * 7168));
        };
        auto repack = [&](unsigned gI) {
            const unsigned s2 = (2 * g + (q4 >> 1)) & 3;
            bf16x8 f[4];
#pragma unroll
            for (int nt = 0; nt < 4; nt++) {
                const unsigned a = ldsb + L_WO + gI * 4096 + (4 * g + q4) * 128 + ((((nt >> 1) * 2 + (p4 >> 1)) ^ s2) * 32) + (p4 & 1) * 16 + (nt & 1) * 8;
                f[nt] = cat8(tr_read(a), tr_read(a + 2048));
            }
            WAITL0();
#pragma unroll
            for (int nt = 0; nt < 4; nt++) *(LAS bf16x8*)(ldsb + L_WO + gI * 4096 + nt * 1024 + l * 16) = f[nt];
        };
        unsigned aK, aW, aS;
        auto k_pre = [&]() {
            pre_addr();
            aK = ldsb + L_KH + lw * 2048 + l * 16; aW = ldsb + L_WPHI + (lw >> 1) * 2048 + (4 * g + q4) * 64 + p4 * 8; aS = ldsb + L_SD + (pw * 64 + 32 + t16) * 2;
            PIN(aK); PIN(aW); PIN(aS);
        };
        auto kfeat = [&](auto hook) {
            bf16x8 wf[2];
            float rst[2];
#pragma unroll
            for (int ft = 0; ft < 2; ft++) {
                wf[ft] = cat8(tr_read(aW + ft * 32), tr_read(aW + ft * 32 + 1024));
                rst[ft] = __builtin_amdgcn_rcpf(lds_bf(aS + ft * 32));
            }
            sd_reads();
            PIN(wf[0]); PIN(wf[1]); PIN(rst[0]); PIN(rst[1]);
            SCHED();
            ARR_MARK(16);
            BAR();
            SCHED();
            const bf16x8 kf0 = lds128(aK), kf1 = lds128(aK + 1024);
            SCHED();
            hook(-1, rst[0]);
            hook(0, rst[0]);
            SCHED();
            f32x4 p[2][2];
            p[0][0] = MFMA(wf[0], kf0, zero4); p[0][1] = MFMA(wf[1], kf0, zero4);
            p[1][0] = MFMA(wf[0], kf1, zero4); p[1][1] = MFMA(wf[1], kf1, zero4);
            MFMA_SETTLE2(p);
            bf16x8 f[4];
            softmax2<true>(p, rst[0], rst[1], g >= 2, f, hook);
            img_write2(ia, f);
            SCHED();
            hook(5, rst[0]);
            SCHED();
            STAMPW(3, 4); STAMPW(3, 7);
            kv_tile([&]() { hook(6, rst[0]); });
            STAMPW(4, 4); STAMPW(4, 7);
        };
        auto wblk = [&](int k) {
            constexpr unsigned T[8] = {0x19181716, 0x041c1b1a, 0x08070605, 0x0c0b0a09, 0x1413120d, 0x11100015, 0x1d030201, 0x1f1e0f0e};
            return (T[k] >> (8 * lw)) & 31;
        };
        auto wo_init = [&]() {
#pragma unroll
            for (int i = 0; i < 4; i++) { vwo[i] = vWO + i * (16384 - 1024); PIN(vwo[i]); }
        };
        unsigned hs[8];
        auto wl_pre = [&]() { asm volatile("" : "+s"(sg), "+s"(su)); PIN(vwl); };
        auto wo_pre = [&]() {
            wo_init();
#pragma unroll
            for (int k = 0; k < 8; k++) { hs[k] = wo_so(wblk(k)); asm volatile("" : "+s"(hs[k])); }
        };
        auto wo_rng = [&](int k0, int k1) {
#pragma unroll
            for (int k = k0; k < k1; k++) dma16x4(ldsb + L_WO + wblk(k) * 4096, vwo, rsWO, hs[k]);
        };
        auto pf = [&](int i) {
            if (i == 0) { wloads(0, 6); SCHED(); if (lw < 2) wo_pre(); SCHED(); }
            if (i < 3 && !ffn) wo_rng(i, i + 1);
            if (i == 1 || i == 2) wloads(6 * i, i == 1 ? 12 : 16);
        };
        struct Hook {
            decltype(pf)& f; decltype(v_reads)& vr;
            DI void operator()(int k, float& x) const { PIN(x); if (k == 1) { ARR_MARK(17); BAR(); } if (k == 3) vr(); f(3 + k); PIN(x); }
            DI void operator()(int k, float& x, float& y) const { PIN(y); f(3 + k); PIN(x); }
        };
        auto tail16 = [&]() {
            kfeat(Hook{pf, v_reads});
            DLY(16);
            if (ffn) wo_rng(0, 3);
            wo_rng(3, H1);
            static_for<R1>([&](auto kc_) {
                constexpr int k = decltype(kc_)::value;
                waitv<4 * (H1 - 1 - k)>();
                repack(wblk(k));
            });
            BAR();
            res_loads();
            wo_rng(H1, 7);
            static_for<7 - R1>([&](auto kc_) {
                constexpr int k = decltype(kc_)::value + R1;
                waitv<4 * (6 - k)>();
                repack(wblk(k));
            });
            PIN(xres[0]); PIN(xres[1]); PIN(wrv[0]); PIN(wrv[1]);
            perm1();
            SCHED();
            BAR();
            wo_rng(7, 8);
        };
        auto vT23 = [&]() {
            const unsigned n = (idx * 2 + pw) * 64 + l, r = n / 12, c = n % 12;
            return idx < 24 ? ((r & 127) + (r >> 7) * 256 + 128) * 6144 + (c >> 2) * 2048 + (((xcd >> 1) * 4 + (c >> 2 ? 0 : 4) + (c & 3)) & 15) * 128 : OOB;
        };
        if (lw < 2) {
            const unsigned sA = (rt * 128 + lw * 64) * 2048, sB = lw * 32 * 6144 + hp * 128, sBq = lw * 32 * 6144 + hq * 128;
            unsigned buf = 1;
            unsigned va[4], vb[4];
#pragma unroll
            for (int i = 0; i < 4; i++) { va[i] = vA + i * (16384 - 1024); vb[i] = ((i & 1) ? vB1 : vB0) + i * (49152 - 1024); PIN(va[i]); PIN(vb[i]); }
            auto stage = [&](unsigned s, unsigned bf, int i0, int i1) {
                const unsigned kt = (s + rot) & 15;
                const unsigned lb = ldsb + bf * STAGE_B;
                const unsigned sa = sA + kt * 128, sb = kt * 393216;
                if (i0 <= 0 && 0 < i1) dma16x4(lb + lw * 8192, va, rsX, sa);
                if (i0 <= 1 && 1 < i1) dma16x4(lb + lw * 8192 + 4096, va, rsX, sa + 65536);
                if (i0 <= 2 && 2 < i1) dma16x4(lb + 16384 + 8192 + lw * 4096, vb, rsW, sb + sB + 2048);
                if (i0 <= 3 && 3 < i1) dma16x4(lb + 16384 + 16384 + lw * 4096, vb, rsW, sb + sB + 4096);
                if (i0 <= 4 && 4 < i1) dma16x4(lb + 16384 + lw * 4096, vb, rsW, sb + sBq);
            };
            stage(1, 1, 0, 4);
            SCHED();
            unsigned vt = vT23();
            PIN(vt);
            wl_pre(); k_pre();
            waitv<16>(); ARR_MARK(0); BAR();
            stage(1, 1, 4, 5);
            touch(vt, rsW, (xcd & 1) * 512 * 6144);
            waitv<1>(); ARR_MARK(1); BAR();
            stage(2, 2, 0, 5); stage(3, 0, 0, 3);
            waitv<12>(); ARR_MARK(2); BAR();
            stage(3, 0, 3, 5); stage(4, 1, 0, 4);
            waitv<16>(); ARR_MARK(3); BAR();
            stage(4, 1, 4, 5);
            buf = 2;
#pragma nounroll
            for (unsigned s = 5; s < 16; s++) {
                if (s > 5) { waitv<20>(); ARR_MARK(s - 2); BAR(); }
                stage(s, buf, 0, 5);
                buf = buf == 2 ? 0 : buf + 1;
            }
            waitv<20>(); ARR_MARK(14); BAR();
            pf(0);
            if (ffn) waitv<6>(); else waitv<10>();
            ARR_MARK(15); BAR();
            pf(1);
            tail16();
            READ_TAG();
        } else {
            SCHED();
            unsigned v3;
            const unsigned h3 = (xcd >> 1) * 4 + 3, kv3 = !(xcd & 2), wv = idx * 2 + pw;
            auto line3 = [&](unsigned per, unsigned rows, auto rowf) {
                const unsigned n = wv * (kv3 ? per : (per + 2) / 3) + l, r3 = kv3 ? n / 3 : n, c3 = kv3 ? n % 3 : 0;
                return (idx < 24 && l < (kv3 ? per : (per + 2) / 3) && r3 < rows) ? rowf(r3) * 6144 + c3 * 2048 + ((c3 ? h3 : h3 + 4) & 15) * 128 : OOB;
            };
            v3 = line3(32, 512, [](unsigned r) { return r; });
            touch(line3(16, 256, [](unsigned r) { return (r & 127) + (r >> 7) * 256; }), rsW, (xcd & 1) * 512 * 6144);
            {
                const rsrc_t rsP = __builtin_amdgcn_make_buffer_rsrc((void*)WPHI, 0, 32 * 1024 * 2, 0x00020000);
#pragma unroll
                for (int i = 0; i < 4; i++) dma16(ldsb + L_WPHI + pw * 4096 + i * 1024, l * 16, rsP, (pw ? hq : hp) * 4096 + i * 1024);
            }
            v4u sv = {SENT, SENT, SENT, SENT};
            unsigned vc4 = l < 16 ? WS_G3 + (hp * 4 + (rt >> 2)) * 2048 + pw * 1024 + (rt & 3) * 256 + l * 16
                         : l < 24 ? WS_P2 + (hp * 16 + rt) * P2_STRIDE + 8192 + pw * 128 + (l - 16) * 16
                         : (l < 32 && pw == 0 && (rt & 7) == 0) ? WS_GZ + (hp * 2 + (rt >> 3)) * 128 + (l - 24) * 16 : OOB;
            auto clears = [&](int i0, int i1) {
#pragma unroll
                for (int i = i0; i < i1; i++) {
                    if (i < 4) ST16(sv, l * 16, WS_P2 + (hp * 16 + rt) * P2_STRIDE + pw * 4096 + i * 1024);
                    if (i == 4) ST16(sv, vc4, 0);
                    if (i > 4) ST16(sv, l * 16, WS_TL + (bid * 4 + pw * 2 + i - 5) * 1024);
                }
                FRESH();
            };
            PIN(vc4); PIN(sv);
            unsigned ca = ldsb + 16384 + pw * 8192 + (8 * g + q4) * 128 + (((rt & 3) ^ (((q4 >> 1) & 1) | ((g & 1) << 1))) * 32) + p4 * 8;
            PIN(ca);
            bf16x8 cf[4];
            f32x4 cd[2] = {zero4, zero4};
            auto cs_read = [&](int st) {
#pragma unroll
                for (int i = 0; i < 4; i++) cf[i] = cat8(tr_read(ca + (st % 3) * STAGE_B + i * 4096), tr_read(ca + (st % 3) * STAGE_B + i * 4096 + 512));
                SCHED();
            };
            READ_TAG();
            waitv<5>();
            ARR_MARK(0);
            BAR();
            SCHED();
            cs_read(0);
            FRESH();
            const rsrc_t rsC = __builtin_amdgcn_make_buffer_rsrc((void*)pc0, 0, clen, 0x00020000);
            auto code_pf = [&]() { touch(l < 6 ? l * 128 : OOB, rsC, (idx * 2 + pw) * 768); };
            v4u gl[4];
            float s0 = 0.f, s1 = 0.f;
            unsigned bad;
            v2u gs;
            unsigned vgl = (l < 48 ? l : 0) * 16;
            unsigned aw0 = ldsb + L_CS + pw * 384 + (vgl >> 1), aw1 = ldsb + L_MB + (pw * 64 + l) * 4, aw2 = ldsb + L_SD + (pw * 64 + l) * 2;
            static_for<15>([&](auto tc) {
                constexpr int t = decltype(tc)::value;
                WAITL0();
                ARR_MARK(t + 1);
                BAR();
                SCHED();
                if constexpr (t < 4) {
                    __builtin_amdgcn_s_setprio(2);
#pragma unroll
                    for (int i = 0; i < 4; i++) cd[i & 1] = MFMA(ones, cf[(i & 1) * 2 + (i >> 1)], cd[i & 1]);
                    SCHED();
                    __builtin_amdgcn_s_setprio(0);
                    if constexpr (t < 3) cs_read(t + 1);
                    else __builtin_amdgcn_raw_buffer_store_b64((v2u){__float_as_uint(g ? cd[1][0] : cd[0][0]), tag}, rsWS, (g == 1 || g + pw == 0) ? g * 512 + t16 * 8 : OOB,
                                                               WS_G1 + ((hp * 4 + (rt >> 2)) * 192 + pw * 64 + (rt & 3) * 16) * 8, AUX_SC1);
                }
                if constexpr (t == 0) touch(v3, rsW, (~xcd & 1) * 512 * 6144);
                if constexpr (t == 12) code_pf();
                if constexpr (t == 8) {
                    waitv<0>();
                    __builtin_amdgcn_raw_buffer_store_b64((v2u){0u, tag}, rsWS, l == 0 ? 0 : OOB, WS_GC + ((hp * 2 + pw) * 16 + rt) * 8, AUX_SC1);
                }
                if constexpr (t == 8 || t == 9) {
#pragma unroll
                    for (int r = (t - 8) * 2; r < (t - 8) * 2 + 2; r++)
                        asm volatile("buffer_load_dwordx4 %0, %1, %2, %3 offen sc1" : "=v"(gl[r]) : "v"(vgl), "s"(rsWS), "s"(WS_G1 + ((hp * 4 + r) * 192 + pw * 96) * 8) : "memory");
                }
                if constexpr (t == 6) {
                    asm volatile("buffer_load_dwordx2 %0, %1, %2, %3 offen sc1" : "=v"(gs) : "v"(l * 8), "s"(rsWS), "s"(WS_G0 + (rt * 128 + pw * 64) * 8) : "memory");
                }
                if constexpr (t == 11) {
                    waitv<0>();
                    PIN(gs);
                    bad = gs[1] ^ tag;
#pragma unroll
                    for (int r = 0; r < 4; r++) {
                        PIN(gl[r]);
                        bad |= (gl[r][1] ^ tag) | (gl[r][3] ^ tag);
                        s0 += __uint_as_float(gl[r][0]); s1 += __uint_as_float(gl[r][2]);
                    }
                    PIN(s0); PIN(s1); PIN(bad);
                }
                if constexpr (t == 12) {
                    if (RARE(!__all(bad == 0))) {
                        s0 = 0.f; s1 = 0.f;
#pragma nounroll
                        for (unsigned r = 0; r < 4; r++) {
                            unsigned a[2];
                            sweep<2>(rsWS, vgl, WS_G1 + ((hp * 4 + r) * 192 + pw * 96) * 8, 8, tag, a);
                            s0 += __uint_as_float(a[0]); s1 += __uint_as_float(a[1]);
                        }
                        unsigned c[1];
                        sweep<1>(rsWS, l * 8, WS_G0 + (rt * 128 + pw * 64) * 8, 0, tag, c);
                        gs[0] = c[0];
                    }
                    *(LAS f32x2*)(aw0) = (f32x2){-s0, -s1};
                    stsf(aw1, bflo(gs[0]));
                    *(LAS u16*)(aw2) = (u16)(gs[0] >> 16);
                    SCHED();
                }
                if constexpr (t == 5) k_pre();
                if constexpr (t == 6) { wl_pre(); wo_pre(); }
                if constexpr (t == 7) { PIN(vgl); PIN(aw0); PIN(aw1); PIN(aw2); }
                clears(t < 7 ? t : 7, t < 7 ? t + 1 : 7);
                if constexpr (t == 13) pf(0);
                if constexpr (t == 14) pf(1);
            });
            tail16();
        }
        WAITL0();
        STAMPW(7, 4); STAMPW(7, 7);
    }
    const unsigned sFl = WS_F4 + (rt2 * 4 + (lw >> 1) + 2 * half) * 128;
    ARR_MARK(20);
    BAR();
    ARR_END();
    const unsigned wb = ldsb + L_WO + half * 65536 + l * 16;
    bf16x8 bb[2][4];
#pragma unroll
    for (int nt = 0; nt < 4; nt++) { bb[0][nt] = lds128(wb + nt * 1024); PIN(bb[0][nt]); }
    unsigned so[16], va0 = l * 16 + half * 32768, va1 = l * 16 + (half ^ 1) * 32768;
    unsigned aXw = ldsb + L_XB + w * 4096 + l * 16, aXr = ldsb + L_XB + (w ^ 4) * 4096 + l * 16, sG5 = WS_G5 + ((rt2 * 8 + lw * 2 + half) * 16 + ct) * 256;
    unsigned vG5 = g == q4 ? t16 * 16 : OOB;
#pragma unroll
    for (int st = 0; st < 16; st++) { so[st] = WS_P4 + (rt2 * 8 + lw * 2) * 32768 + ((2 * st + half + rot2) & 31) * 1024; asm volatile("" : "+s"(so[st])); }
    unsigned wt[2];
#pragma unroll
    for (int i = 0; i < 2; i++) { wt[i] = ldsb + L_WO + half * 65536 + (4 * g + q4) * 128 + (((i * 2 + (p4 >> 1)) ^ ((2 * g + (q4 >> 1)) & 3)) * 32) + (p4 & 1) * 16; PIN(wt[i]); }
    PIN(va0); PIN(va1); PIN(aXw); PIN(aXr); PIN(vG5);
    vD = kr * 16384 + (l & 7) * 16; sD = (kc * 256 + lw * 64) * 2048 + cg * 128;
    PIN(vD);
    asm volatile("" : "+s"(sG5), "+s"(sD), "+s"(rsD));
    SLEEP(SL3);
    DLY(18);
    poll16(rsWS, sFl, tag, l);
    DLY(19);
    FRESH();
    SCHED();
    STAMPW(8, 0); STAMPW(8, 4);
    unsigned swc;
    f32x4 acc2[2][4];
    bf16x8 sop[2];
    {
        auto aload = [&](int st, int m2) { return LD16(m2 ? va1 : va0, so[st]); };
        constexpr int PD = 2;
        v4u af[PD][2];
#pragma unroll
        for (int i = 0; i < PD; i++) {
#pragma unroll
            for (int m2 = 1; m2 >= 0; m2--) af[i][m2] = aload(i, m2);
            SCHED();
        }
#pragma unroll
        for (int nt = 0; nt < 4; nt++) {
            const unsigned x0 = xres[nt >> 1][2 * (nt & 1)], x1 = xres[nt >> 1][2 * (nt & 1) + 1];
            acc2[0][nt] = (f32x4){bflo(x0), bfhi(x0), bflo(x1), bfhi(x1)};
            acc2[1][nt] = zero4;
        }
        unsigned m0 = p4 == 0 ? 0x3F803F80u : 0u, m1 = p4 == 1 ? ~0u : 0u;
        PIN(m0); PIN(m1);
#pragma unroll
        for (int i = 0; i < 2; i++) {
            v4u u;
#pragma unroll
            for (int j = 0; j < 4; j++) u[j] = (wrv[i][j] & m1) | m0;
            sop[i] = __builtin_bit_cast(bf16x8, u);
            PIN(sop[i]);
        }
        {
            float sw = 0.f;
#pragma unroll
            for (int i = 0; i < 8; i++) sw = dot2(wrv[i >> 2][i & 3], 0x3F803F80u, sw);
            swc = f2s(xrow_sum(sw));
            PIN(swc);
        }
        SCHED();
#pragma unroll
        for (int st = 0; st < 16; st++) {
            if (st + 1 == 14) BAR();
            if (st + 1 < 16) {
#pragma unroll
                for (int nt = 0; nt < 4; nt++) {
                    if (st + 1 < 14) bb[(st + 1) % (2)][nt] = lds128(wb + (st + 1) * 4096 + nt * 1024);
                    else bb[(st + 1) % (2)][nt] = cat8(tr_read(wt[nt >> 1] + (st + 1) * 4096 + (nt & 1) * 8), tr_read(wt[nt >> 1] + (st + 1) * 4096 + (nt & 1) * 8 + 2048));
                }
            }
#pragma unroll
            for (int mm = 0; mm < 2; mm++) {
                const int m2 = mm ^ 1;
                const bf16x8 a = __builtin_bit_cast(bf16x8, af[st % PD][m2]);
#pragma unroll
                for (int nt = 0; nt < 4; nt++) acc2[m2][nt] = MFMA(bb[st % (2)][nt], a, acc2[m2][nt]);
            }
            if (st + PD < 16) {
#pragma unroll
                for (int m2 = 1; m2 >= 0; m2--) af[st % PD][m2] = aload(PD + st, m2);
            }
#pragma unroll
            for (int i = 0; i < (st + 1 < 14 ? 4 : 8); i++) { SGB(0x8, 1); SGB(0x100, 1); }
            SCHED();
        }
    }
    STAMPW(9, 0); STAMPW(9, 4);
#pragma unroll
    for (int nt = 0; nt < 4; nt++) stsf4(aXw + nt * 1024, acc2[1][nt]);
    SCHED();
    if (half) dloads(0, 3);
    SCHED();
    WAITL0();
    if (half) __builtin_amdgcn_s_setprio(1);
    BAR();
    SCHED();
    v4u hpk[2];
    {
        f32x4 pa[4];
#pragma unroll
        for (int nt = 0; nt < 4; nt++) pa[nt] = ldsf4(aXr + nt * 1024);
        SCHED();
#pragma unroll
        for (int i = 0; i < 8; i++) {
            const f32x4 a = acc2[0][i >> 1] + pa[i >> 1];
            hpk[i >> 2][i & 3] = pk2(a[2 * (i & 1)], a[2 * (i & 1) + 1]);
        }
        const bf16x8 h0 = __builtin_bit_cast(bf16x8, hpk[0]), h1 = __builtin_bit_cast(bf16x8, hpk[1]);
        f32x4 gq = MFMA(h0, h0, zero4), sd = MFMA(sop[0], h0, zero4);
        gq = MFMA(h1, h1, gq); sd = MFMA(sop[1], h1, sd);
        float q0 = (l & 1) ? gq[1] : gq[0], q1 = (l & 1) ? gq[3] : gq[2];
        PIN(q0); PIN(q1);
        const v4u pv = {f2s(sd[0]), f2s((l & 2) ? q1 : q0), f2s(sd[1]), swc};
        DLY(20);
        ST16(pv, vG5, sG5);
        asm volatile("s_nop 1" :: "v"(pv));
    }
    __builtin_amdgcn_s_setprio(0);
    STAMPW(10, 0); STAMPW(10, 4);
    v4u wp2[8];
    auto perm2 = [&]() {
#pragma unroll
        for (int i = 16; i < 24; i++) PIN(wreg[i]);
#pragma unroll
        for (int ip = 0; ip < 4; ip++)
#pragma unroll
            for (int c = 0; c < 4; c++) {
                const unsigned a = wreg[16 + 2 * ip][c], b = wreg[16 + 2 * ip + 1][c];
                wp2[2 * c][ip] = __builtin_amdgcn_perm(b, a, 0x05040100);
                wp2[2 * c + 1][ip] = __builtin_amdgcn_perm(b, a, 0x07060302);
            }
#pragma unroll
        for (int c = 0; c < 8; c++) PIN(wp2[c]);
    };
    const unsigned mt = rt2 * 8 + lw * 2 + half;
    {
        v4u pv[4];
        unsigned sK = WS_GK + (ct * 128 + mt) * 4, vK = l == 0 ? 0 : OOB, rk = 2047u - R2;
        asm volatile("" : "+s"(sK));
        PIN(vK); PIN(rk);
        unsigned sS = WS_G5 + mt * 4096;
        asm volatile("" : "+s"(sS));
        SLEEP(SL4);
        DLY(21);
        __builtin_amdgcn_s_setprio(1);
        float T[4];
        sweepn<4>([&](int k) { return LD16(l * 16 + k * 1024, sS); }, pv, [&]() {
            const f32x4 ps = (__builtin_bit_cast(f32x4, pv[0]) + __builtin_bit_cast(f32x4, pv[1])) + (__builtin_bit_cast(f32x4, pv[2]) + __builtin_bit_cast(f32x4, pv[3]));
            const float r = xrow_scatter<false>(ps[0], ps[1], ps[2], ps[3]);
            xrow_gather(r, T);
            return r;
        });
        STAMPW(21, 0); STAMPW(21, 4);
        const float mean = T[0] * (1.0f / 1024.0f), var = fmaxf(T[1] * (1.0f / 1024.0f) - mean * mean, 0.f);
        const unsigned sg = pk2(mean, __builtin_amdgcn_sqrtf(rbf(rbf(var) + 1e-5f)));
        const float mb2 = bflo(sg), isd2 = __builtin_amdgcn_rcpf(bfhi(sg));
        const float rsc = (T[2] - mb2 * T[3]) * isd2;
        const unsigned rb = pk2(rsc, rsc);
        const unsigned key = ((rb ^ ((unsigned)((int)rb >> 31) | 0x80000000u)) & 0xffff0000u) | rk;
        unsigned kb = umx(key, dppu<0x128>(key));
        kb = umx(kb, dppu<0x124>(kb)); kb = umx(kb, dppu<0x122>(kb)); kb = umx(kb, dppu<0x121>(kb));
        DLY(22);
        ST4(kb, vK, sK);
        SCHED();
        __builtin_amdgcn_s_setprio(0);
        if (half) { dloads(3, 8); SCHED(); }
        const bool cand = key == kb;
        v4u e[2], outv[2];
#pragma unroll
        for (int i = 0; i < 8; i++) {
            const float h0 = bflo(hpk[i >> 2][i & 3]), h1 = bfhi(hpk[i >> 2][i & 3]);
            const unsigned d = pk2(h0 - mb2, h1 - mb2);
            e[i >> 2][i & 3] = nsent(pk2(bflo(d) * isd2, bfhi(d) * isd2));
        }
        DLY(24);
        ST16(e[0], cand ? g * 16 : OOB, WS_C + (mt * 16 + ct) * 128);
        ST16(e[1], cand ? g * 16 + 64 : OOB, WS_C + (mt * 16 + ct) * 128);
        SCHED();
        STAMPW(22, 0); STAMPW(22, 4);
#pragma unroll
        for (int i = 0; i < 8; i++)
            outv[i >> 2][i & 3] = pk2(bflo(hpk[i >> 2][i & 3]) + bflo(e[i >> 2][i & 3]), bfhi(hpk[i >> 2][i & 3]) + bfhi(e[i >> 2][i & 3]));
        if (cand) {
#pragma unroll
            for (int i = 0; i < 2; i++) { *(LAS v4u*)(ldsb + L_STASH + w * 256 + i * 64 + g * 16) = hpk[i]; *(LAS v4u*)(ldsb + L_STASH + w * 256 + 128 + i * 64 + g * 16) = e[i]; }
        }
#pragma unroll
        for (int i = 0; i < 2; i++) *(LAS v4u*)(ldsb + L_OUT + (lw * 2 + half) * 2048 + t16 * 128 + (((i * 4 + g) ^ (l & 7)) * 16)) = outv[i];
    }
    const rsrc_t rsO = __builtin_amdgcn_make_buffer_rsrc((void*)OUT, 0, 2048 * 1024 * 2, 0x00020000);
    unsigned aG = ldsb + L_WO + 512;
    PIN(aG);
    unsigned vX = (2 * g + (l & 1)) * 16, aP = (2 * g + (l & 1)) * 32, aR1 = ldsb + L_RED + lw * 4352 + l * 16, vl4 = l * 4;
    unsigned aR2 = ldsb + L_RED + ((lw & 1) * 2 + ((l >> 2) & 1)) * 1088 + kr * 16 + p4 * 4;
    unsigned sX = WS_C + kq * 512 + lw * 128, sG8w = WS_G8 + ((kq * 2 + lw) * 3584 + (lw ? cg1u : cg1) * 64) * 4, sG8r = WS_G8 + (kc * 256 + lw * 64) * 4;
    unsigned sG9 = WS_G9 + (cg * 14 + kc) * 256;
    unsigned aR3 = aR1 + 17408 - lw * 2176;
    PIN(vX); PIN(aP); PIN(aR1); PIN(aR2); PIN(aR3); PIN(vl4);
    asm volatile("" : "+s"(sX), "+s"(sG8w), "+s"(sG8r), "+s"(sG9));
    SCHED();
    unsigned gkv;
    if (KD) {
        unsigned sK = WS_GK + (bid & 15) * 512;
        asm volatile("" : "+s"(sK));
        if (half) {
            perm2();
            v2u hb[1];
            __builtin_amdgcn_s_setprio(1);
            SLEEP(SL5H);
            gkv = wave_maxu(sweepz<1>([&](int) { return ldg(rsWS, l * 8, sK); }, hb));
            stsu(aG, gkv);
        } else {
            WAITL0();
            BAR();
            gkv = ldsu(aG);
        }
    } else {
    if (w == 1) {
        unsigned sK = WS_GK + (bid & 15) * 512;
        v2u hb[1];
        asm volatile("" : "+s"(sK));
        SLEEP(SL5);
        DLY(23);
        stsu(aG, wave_maxu(sweepz<1>([&](int) { return ldg(rsWS, l * 8, sK); }, hb)));
        STAMPR(23, 2);
    }
    if (half) perm2();
    STAMPW(24, 7);
    WAITL0();
    if (half) __builtin_amdgcn_s_setprio(1);
    BAR();
    STAMPW(11, 0); STAMPW(11, 4);
    gkv = ldsu(aG);
    }
    if (w < 4) {
        if (bid == 0 && tid == 0) __hip_atomic_store((gu32*)(ws + WS_EPOCH), tag, RLX_AGENT);
        v4u o2[4];
        unsigned vo[4];
#pragma unroll
        for (int i = 0; i < 4; i++) o2[i] = *(const LAS v4u*)(ldsb + L_OUT + w * 4096 + i * 1024 + kr * 128 + (((l & 7) ^ kr) * 16));
#pragma unroll
        for (int i = 0; i < 4; i++) { const unsigned R = mt * 16 + i * 8 + kr; vo[i] = 2047u - R != (gkv & 2047u) ? R * 2048 + (l & 7) * 16 : OOB; PIN(vo[i]); PIN(o2[i]); }
#pragma unroll
        for (int s = 0; s < 3; s++) {
            if (s > KD) { BAR(); SLEEP(Q1); }
            SCHED();
#pragma unroll
            for (int i = 0; i < 4; i++)
                if (((PM >> (4 * i)) & 15) == s) __builtin_amdgcn_raw_buffer_store_b128(o2[i], rsO, vo[i], ct * 128, AUX_SC1);
            SCHED();
        }
        STAMPW(12, 0);
        return;
    }

    auto gemv = [&](const v4u& x, const v4u* wp, int n, unsigned a) {
        f32x4 d[16], o[4];
#pragma unroll
        for (int c = 0; c < n + 4; c++) {
            if (c < n) d[c] = MFMA(__builtin_bit_cast(bf16x8, x), __builtin_bit_cast(bf16x8, wp[c]), zero4);
            SCHED();
            if (c >= 4) {
                const int j = c - 4;
                float t = d[j][0] + dppf<0x128>(d[j][1]);
                asm volatile("" : "+v"(t) : "v"(d[j]));
                o[j >> 2][j & 3] = t;
                if ((j & 3) == 3) stsf4(a + (j >> 2) * 1088, o[j >> 2]);
            }
            SCHED();
        }
        return o[0][0];
    };
    auto sum4 = [&](unsigned a, unsigned stride) {
        float u = ldsf(a) + ldsf(a + stride), v = ldsf(a + 2 * stride) + ldsf(a + 3 * stride);
        PIN(u); PIN(v);
        return u + v;
    };
    if (ffn) {
        v4u xq[1];
        const unsigned vx = ((~gkv & 0x7f0u) << 7) + vX;
        sweepn<1>([&](int) { return LD16(vx, sX); }, xq, [&]() { return gemv(xq[0], &wp1[0][0], 16, aR1); });
        STAMPW(13, 4);
    }
    DLY(2);
    WAITL0();
    BAR();
    if (ffn) {
        if (lw < 2) ST4(f2s(sum4(aR2, 4352)), vl4, sG8w);
        STAMPW(14, 4);
        unsigned pv[8];
        v4u ap;
        SLEEP(SL6);
        sweepn<8>([&](int k) { return LD4(vl4, sG8r + k * 3584 * 4); }, pv, [&]() {
            const float gg = rbf((__uint_as_float(pv[0]) + __uint_as_float(pv[2])) + (__uint_as_float(pv[4]) + __uint_as_float(pv[6])));
            const float uu = rbf((__uint_as_float(pv[1]) + __uint_as_float(pv[3])) + (__uint_as_float(pv[5]) + __uint_as_float(pv[7])));
            const float act = rbf(gg * __builtin_amdgcn_rcpf(1.0f + __expf(-gg))) * uu;
            const unsigned apk = pk2(act, dppf<0xB1>(act));
#pragma unroll
            for (int ip = 0; ip < 4; ip++) ap[ip] = __builtin_amdgcn_ds_bpermute(aP + ip * 8, apk);
            return act;
        });
        STAMPW(15, 4);
        gemv(ap, wp2, 8, aR3);
    }
    WAITL0();
    BAR();
    if (lw != 0) return;
    if (ffn) ST4(f2s(sum4(aR2 + 17408, 2176)), vl4, sG9);
    SCHED();
    STAMPW(16, 4);
    const unsigned gkey = rfl(gkv), srow = 2047u - (gkey & 2047u);
    if ((srow >> 7) == rt2) {
        const float rs = __uint_as_float(((gkey & 0x80000000u) ? gkey ^ 0x80000000u : ~gkey) & 0xffff0000u);
        float sig = rbf(1.0f / (1.0f + __expf(-rs)));
        const unsigned sb = ldsb + L_STASH + (((srow >> 5) & 3) + ((srow >> 4) & 1) * 4) * 256 + (t16 * 4 + g) * 2;
        float hc = lds_bf(sb), lc = lds_bf(sb + 128);
        unsigned vo = (t16 * 4 + g) * 2, so_ = srow * 2048 + ct * 128, vy = l < 32 ? l * 16 + 3072 : OOB, sY = WS_G9 + ct * 3584;
        PIN(sig); PIN(hc); PIN(lc); PIN(vo); PIN(vy);
        asm volatile("" : "+s"(so_), "+s"(sY));
        v4u yp[4];
        float y;
        if (SLN && !ffn) SLEEP(SLN);
        SLEEP(SL7);
        sweepn<4>([&](int k) { return LD16(k < 3 ? l * 16 + k * 1024 : vy, sY); }, yp, [&]() {
            const f32x4 ys = (__builtin_bit_cast(f32x4, yp[0]) + __builtin_bit_cast(f32x4, yp[1])) + (__builtin_bit_cast(f32x4, yp[2]) + __builtin_bit_cast(f32x4, yp[3]));
            y = xrow_scatter<false>(ys[0], ys[1], ys[2], ys[3]);
            return y;
        });
        DLY(1);
        __builtin_amdgcn_raw_buffer_store_b16((u16)(pk2(hc + rbf(lc + rbf(rbf(y) * sig)), 0.f) & 0xffffu), rsO, vo, so_, 0);
        STAMPW(17, 4);
    }
}
__device__ __attribute__((used, noinline)) void fk_tail() { asm volatile(".Lfk_end:"); }

extern "C" void kernel_launch(void* const* d_in, const int* in_sizes, int n_in, void* d_out, int out_size, void* d_ws,
                              size_t ws_size, hipStream_t stream) {
    (void)in_sizes; (void)n_in; (void)out_size; (void)ws_size;
    (void)hipFuncSetAttribute((const void*)fused_kernel, hipFuncAttributeMaxDynamicSharedMemorySize, LDS_BYTES);
    fused_kernel<<<256, 512, LDS_BYTES, stream>>>((const u16*)d_in[0], (const u16*)d_in[1], (const u16*)d_in[2], (const u16*)d_in[3],
                                                  (const u16*)d_in[5], (const u16*)d_in[6], (const u16*)d_in[7], (const u16*)d_in[8],
                                                  (u16*)d_out, (char*)d_ws);
}
```

```cpp
#include <hip/hip_runtime.h>
#include <stdint.h>
#include <utility>

typedef unsigned short u16;
typedef unsigned long long u64;
typedef short bf16x8 __attribute__((ext_vector_type(8)));
typedef short s16x4 __attribute__((ext_vector_type(4)));
typedef float f32x4 __attribute__((ext_vector_type(4)));
typedef float f32x2 __attribute__((ext_vector_type(2)));
typedef __bf16 bf16x2 __attribute__((ext_vector_type(2)));
typedef unsigned v4u __attribute__((ext_vector_type(4)));
typedef unsigned v2u __attribute__((ext_vector_type(2)));
#define LAS __attribute__((address_space(3)))
#define GAS __attribute__((address_space(1)))
#define CAS __attribute__((address_space(4)))
typedef GAS u64 gu64;
typedef GAS unsigned gu32;
typedef __amdgpu_buffer_rsrc_t rsrc_t;
#define DI __device__ __forceinline__
#define RLX_AGENT __ATOMIC_RELAXED, __HIP_MEMORY_SCOPE_AGENT
#define BAR() __builtin_amdgcn_s_barrier()
#define SCHED() __builtin_amdgcn_sched_barrier(0)
#define SGB(m, n) __builtin_amdgcn_sched_group_barrier((m), (n), 0)
#define WAITL0() asm volatile("s_waitcnt lgkmcnt(0)" ::: "memory")
#define WL0() __builtin_amdgcn_s_waitcnt(0xc07f)
#define MFMA(a, b, c) __builtin_amdgcn_mfma_f32_16x16x32_bf16((a), (b), (c), 0, 0, 0)
#define ORDER_BEHIND_POLL() __builtin_amdgcn_fence(__ATOMIC_ACQUIRE, "wavefront")
#define AUX_SC1 16
template <int N> DI void waitv() { asm volatile("s_waitcnt vmcnt(%0)" ::"i"(N) : "memory"); }

#define WS_EPOCH 0
#define WS_G0 0x1000
#define WS_G1 0x8000
#define WS_F4 0x78000
#define WS_GC 0x70000
#define WS_G3 0x80000
#define WS_GZ 0xC0000
#define SENT 0xFFFFFFFFu
#define WS_TL 0x100000
#define WS_G5 0x100000
#define WS_G8 0x180000
#define WS_G9 0x1A0000
#define WS_GK 0x1B4000
#define WS_C 0x1C0000
#define WS_P2 0x200000
#define P2_STRIDE 0x2180
#define WS_P4 0x700000
#define WS_USED 0xB00000
#define WS_STAMP 0x1000000

#define STAGE_B 40960
#define LDS_BYTES 163840
#define L_WO 32768
#define L_MB 161792
#define L_WPHI (L_MB - 8192)
#define L_SD (L_MB + 512)
#define L_CS (L_MB + 1024)
#define L_V L_WO
#define L_KH (L_WO + 65536)
#define L_XB 0
#define L_STASH (L_WO + 1024)
#define L_OUT (L_WO + 8192)
#define L_RED 4096

#define SL1 2
#define SL2 12
#define SL3 12
#define SL4 18
#define SL5 13
#define SL6 12
#define SL7 10
#define DL 0
#define H1 5
#define R1 3
#define PM 0x2110
#define Q1 3
#define Q2 3
#define SLN 0
#define KD 0
#define SL5H 4
#define KC 10
#define KH 3
#define AM 1
#define IW 1
#define CA 2
#define CB 3
#define CKS(i)
#define VC 3
#define VH 3
#define RH_ 1
#define ZO 1
#define PA 99
#define PB 99
#define GS 1
#define GT 12
#define NR 1
#define M2 0
#define RA 8
#define RB 4
#define RC_ 2
#define WT -1
#define DT -1
#define DLY(n) do { if (DL == (n)) { SCHED(); __builtin_amdgcn_s_sleep(16); SCHED(); } } while (0)
#define SPIN_CAP 20000u
#define W_AUX 2
#define LOG2E 1.4426950408889634f

DI unsigned rfl(unsigned x) { return __builtin_amdgcn_readfirstlane(x); }
DI float bf2f(unsigned u) { return __uint_as_float(u << 16); }
DI float bflo(unsigned u) { return __uint_as_float(u << 16); }
DI float bfhi(unsigned u) { return __uint_as_float(u & 0xffff0000u); }
DI unsigned pk2(float lo, float hi) { f32x2 v = {lo, hi}; return __builtin_bit_cast(unsigned, __builtin_convertvector(v, bf16x2)); }
DI unsigned nsent(unsigned x) { return __builtin_elementwise_min(x, SENT - 1); }
DI unsigned umx(unsigned a, unsigned b) { return __builtin_elementwise_max(a, b); }
DI float rbf(float f) { return bflo(pk2(f, f) & 0xffffu); }
DI bf16x8 mk8(unsigned a, unsigned b, unsigned c, unsigned d) { v4u v = {a, b, c, d}; return __builtin_bit_cast(bf16x8, v); }
DI void dma16(unsigned ldsaddr, unsigned voff, rsrc_t rs, unsigned soff) {
    asm volatile("s_mov_b32 m0, %0\n\ts_nop 0\n\tbuffer_load_dwordx4 %1, %2, %3 offen lds" ::"s"(ldsaddr), "v"(voff), "s"(rs), "s"(soff) : "memory");
}
DI void dma16x4(unsigned ldsaddr, const unsigned* v, rsrc_t rs, unsigned soff) {
    asm volatile("s_mov_b32 m0, %0\n\ts_nop 0\n\tbuffer_load_dwordx4 %1, %5, %6 offen lds\n\tbuffer_load_dwordx4 %2, %5, %6 offen offset:1024 lds\n\t"
                 "buffer_load_dwordx4 %3, %5, %6 offen offset:2048 lds\n\tbuffer_load_dwordx4 %4, %5, %6 offen offset:3072 lds"
                 ::"s"(ldsaddr), "v"(v[0]), "v"(v[1]), "v"(v[2]), "v"(v[3]), "s"(rs), "s"(soff) : "memory");
}
DI void touch(unsigned voff, rsrc_t rs, unsigned soff) { asm volatile("buffer_load_dword v255, %0, %1, %2 offen" ::"v"(voff), "s"(rs), "s"(soff) : "memory", "v255"); }
DI v4u ld16_asm(unsigned voff, rsrc_t rs, unsigned soff) {
    v4u r; asm volatile("buffer_load_dwordx4 %0, %1, %2, %3 offen" : "=v"(r) : "v"(voff), "s"(rs), "s"(soff) : "memory"); return r;
}
#define PIN(x) asm volatile("" : "+v"(x))
DI s16x4 tr_read(unsigned a) { return __builtin_amdgcn_ds_read_tr16_b64_v4i16((LAS s16x4*)(a)); }
DI bf16x8 cat8(s16x4 a, s16x4 b) { return __builtin_shufflevector(a, b, 0, 1, 2, 3, 4, 5, 6, 7); }
DI bf16x8 lds128(unsigned a) { return *(const LAS bf16x8*)(a); }
DI f32x4 ldsf4(unsigned a) { return *(const LAS f32x4*)(a); }
DI float ldsf(unsigned a) { return *(const LAS float*)(a); }
DI unsigned ldsu(unsigned a) { return *(const LAS unsigned*)(a); }
DI void stsf4(unsigned a, f32x4 v) { *(LAS f32x4*)(a) = v; }
DI void stsf(unsigned a, float v) { *(LAS float*)(a) = v; }
DI void stsu(unsigned a, unsigned v) { *(LAS unsigned*)(a) = v; }
template <int C> DI float dppf(float x) { return __uint_as_float(__builtin_amdgcn_update_dpp(0, __float_as_uint(x), C, 0xf, 0xf, true)); }
template <int C> DI unsigned dppu(unsigned x) { return __builtin_amdgcn_update_dpp(0, x, C, 0xf, 0xf, true); }
DI float max2f(float a, float b) { float r; asm("v_max_f32_e32 %0, %1, %2" : "=v"(r) : "v"(a), "v"(b)); return r; }
DI float max3f(float a, float b, float c) { float r; asm("v_max3_f32 %0, %1, %2, %3" : "=v"(r) : "v"(a), "v"(b), "v"(c)); return r; }
DI float row_sum(float x) { x += dppf<0x128>(x); x += dppf<0x124>(x); x += dppf<0x122>(x); x += dppf<0x121>(x); return x; }
DI float xrow_sum(float x) {
    auto a = __builtin_amdgcn_permlane16_swap(__float_as_uint(x), __float_as_uint(x), false, false);
    x = __uint_as_float(a[0]) + __uint_as_float(a[1]);
    auto b = __builtin_amdgcn_permlane32_swap(__float_as_uint(x), __float_as_uint(x), false, false);
    return __uint_as_float(b[0]) + __uint_as_float(b[1]);
}
DI float wave_sum(float x) { return xrow_sum(row_sum(x)); }
DI unsigned wave_maxu(unsigned x) {
    x = umx(x, dppu<0x128>(x)); x = umx(x, dppu<0x124>(x)); x = umx(x, dppu<0x122>(x)); x = umx(x, dppu<0x121>(x));
    auto a = __builtin_amdgcn_permlane16_swap(x, x, false, false);
    x = umx(a[0], a[1]);
    auto b = __builtin_amdgcn_permlane32_swap(x, x, false, false);
    return umx(b[0], b[1]);
}
DI float nmax3f(float a, float b, float c) { float r; asm("v_max3_f32 %0, -%1, -%2, -%3" : "=v"(r) : "v"(a), "v"(b), "v"(c)); return r; }
DI float nmax2f(float a, float b) { float r; asm("v_max_f32_e64 %0, -%1, -%2" : "=v"(r) : "v"(a), "v"(b)); return r; }
DI float mx2(float a, float b) { return __builtin_amdgcn_fmed3f(a, b, __builtin_inff()); }
template <bool MAX> DI float xrow_scatter(float a, float b, float c, float d) {
    const auto s = __builtin_amdgcn_permlane16_swap(__float_as_uint(a), __float_as_uint(b), false, false);
    const auto t = __builtin_amdgcn_permlane16_swap(__float_as_uint(c), __float_as_uint(d), false, false);
    const float u = MAX ? mx2(__uint_as_float(s[0]), __uint_as_float(s[1])) : __uint_as_float(s[0]) + __uint_as_float(s[1]);
    const float v = MAX ? mx2(__uint_as_float(t[0]), __uint_as_float(t[1])) : __uint_as_float(t[0]) + __uint_as_float(t[1]);
    if (MAX && AM) {
        float x = max2f(__uint_as_float(s[0]), __uint_as_float(s[1])), y = max2f(__uint_as_float(t[0]), __uint_as_float(t[1]));
        asm volatile("s_nop 1" : "+v"(x), "+v"(y));
        const auto q = __builtin_amdgcn_permlane32_swap(__float_as_uint(x), __float_as_uint(y), false, false);
        return max2f(__uint_as_float(q[0]), __uint_as_float(q[1]));
    }
    const auto r = __builtin_amdgcn_permlane32_swap(__float_as_uint(u), __float_as_uint(v), false, false);
    return MAX ? mx2(__uint_as_float(r[0]), __uint_as_float(r[1])) : __uint_as_float(r[0]) + __uint_as_float(r[1]);
}
DI void xrow_gather(float w, float (&o)[4]) {
    const auto r = __builtin_amdgcn_permlane32_swap(__float_as_uint(w), __float_as_uint(w), false, false);
    const auto s = __builtin_amdgcn_permlane16_swap(r[0], r[0], false, false);
    const auto t = __builtin_amdgcn_permlane16_swap(r[1], r[1], false, false);
    o[0] = __uint_as_float(s[0]); o[1] = __uint_as_float(s[1]); o[2] = __uint_as_float(t[0]); o[3] = __uint_as_float(t[1]);
}
struct NoHook { DI void operator()(int, float&) const {} DI void operator()(int, float&, float&) const {} };
struct NoPut { DI void operator()(int, const bf16x8&) const {} };
template <bool MUL, class H = NoHook, class P = NoPut> DI void softmax2(const f32x4 (&p)[2][2], float rs0, float rs1, bool hi, bf16x8 (&out)[4], H hook = H(), P put = P()) {
    float m[4];
#pragma unroll
    for (int i = 0; i < 2; i++) {
        const f32x4 &p0 = p[i][0], &p1 = p[i][1];
        m[2 * i] = max3f(max3f(p0[0], p0[1], p0[2]), max3f(p0[3], p1[0], p1[1]), max2f(p1[2], p1[3]));
        m[2 * i + 1] = max3f(nmax3f(p0[0], p0[1], p0[2]), nmax3f(p0[3], p1[0], p1[1]), nmax2f(p1[2], p1[3]));
    }
    asm volatile("s_nop 1" : "+v"(m[0]), "+v"(m[1]), "+v"(m[2]), "+v"(m[3]));
    hook(1, m[3]);
    const float rsr = hi ? rs1 : rs0;
    float a[4];
    xrow_gather(xrow_scatter<true>(m[0], m[1], m[2], m[3]) * (rsr * -LOG2E), a);
    hook(10, a[0]);
    f32x2 e[4][4];
    float s[4];
#pragma unroll
    for (int k = 0; k < 4; k++) {
        if (k == 2) hook(2, a[2], s[1]);
        const float sc = ((k >> 1) ? rs1 : rs0) * ((k & 1) ? -LOG2E : LOG2E);
#pragma unroll
        for (int j = 0; j < 4; j++) {
            const f32x4& q = p[k >> 1][j >> 1];
            const f32x2 t = __builtin_elementwise_fma((f32x2){q[2 * (j & 1)], q[2 * (j & 1) + 1]}, (f32x2){sc, sc}, (f32x2){a[k], a[k]});
            e[k][j] = (f32x2){__builtin_amdgcn_exp2f(t[0]), __builtin_amdgcn_exp2f(t[1])};
        }
        const f32x2 h = (e[k][0] + e[k][1]) + (e[k][2] + e[k][3]);
        s[k] = h[0] + h[1];
        if (k == 3) hook(3, s[k]);
    }
    float iv = __builtin_amdgcn_rcpf(xrow_scatter<false>(s[0], s[1], s[2], s[3]));
    if (MUL) iv *= rsr;
    xrow_gather(iv, a);
#pragma unroll
    for (int k = 0; k < 4; k++) {
        if (k == 2) hook(4, a[2]);
        const f32x2 c = {a[k], a[k]};
        const f32x2 x0 = e[k][0] * c, x1 = e[k][1] * c, x2 = e[k][2] * c, x3 = e[k][3] * c;
        out[k] = mk8(pk2(x0[0], x0[1]), pk2(x1[0], x1[1]), pk2(x2[0], x2[1]), pk2(x3[0], x3[1]));
        put(k, out[k]);
    }
}
DI float dot2(unsigned a, unsigned b, float c) {
    return __builtin_amdgcn_fdot2_f32_bf16(__builtin_bit_cast(bf16x2, a), __builtin_bit_cast(bf16x2, b), c, false);
}
DI void st_gran(char* ws, unsigned off, unsigned tag, unsigned val) {
    __hip_atomic_store((gu64*)(ws + off), ((u64)tag << 32) | val, RLX_AGENT);
}
DI v2u ldg(rsrc_t rs, unsigned voff, unsigned soff) { return __builtin_amdgcn_raw_buffer_load_b64(rs, voff, soff, AUX_SC1); }
#define RARE(c) __builtin_expect(!!(c), 0)
#define FRESH() asm volatile("" ::: "memory")
template <int N> DI void sweep(rsrc_t rs, unsigned voff, unsigned soff, unsigned S, unsigned tag, unsigned (&v)[N]) {
#pragma nounroll
    for (unsigned spins = 0;;) {
        unsigned bad = 0;
        FRESH();
#pragma unroll
        for (int k = 0; k < N; ++k) { const v2u x = ldg(rs, voff, soff + k * S); v[k] = x[0]; bad |= x[1] ^ tag; }
        if (__all(bad == 0)) break;
        if (++spins > SPIN_CAP) break;
        __builtin_amdgcn_s_sleep(1);
    }
}
template <int A, int B> DI unsigned budget(unsigned& spins) {
    unsigned r;
    asm volatile("s_add_i32 %0, %0, 1\n\ts_cmpk_lt_u32 %0, %2\n\ts_cselect_b32 %1, %3, %4" : "+s"(spins), "=s"(r) : "i"(SPIN_CAP), "i"(A), "i"(B) : "scc");
    SCHED();
    return r;
}
DI unsigned wmax(unsigned x) { return x; }
DI unsigned wmax(v2u x) { return umx(x[0], x[1]); }
DI unsigned wmax(v4u x) { return umx(umx(x[0], x[1]), umx(x[2], x[3])); }
template <int N, class T, class F> DI unsigned sweepz(F ld, T (&v)[N]) {
    unsigned spins = 0, lim, m;
#pragma nounroll
    do {
        FRESH();
#pragma unroll
        for (int k = 0; k < N; ++k) v[k] = ld(k);
        lim = budget<-2, -1>(spins);
        m = 0;
#pragma unroll
        for (int k = 0; k < N; ++k) m = umx(m, wmax(v[k]));
    } while (__any(m > lim));
    return m;
}
template <int N, class T, class F, class G> DI void sweepn(F ld, T (&v)[N], G use) {
    unsigned spins = 0;
    bool again;
#pragma nounroll
    do {
        FRESH();
#pragma unroll
        for (int k = 0; k < N; ++k) v[k] = ld(k);
        const int m = budget<3, 0>(spins);
        again = __any(__builtin_amdgcn_classf(use(), m));
    } while (again);
}
DI unsigned f2s(float x) { return __float_as_uint(x); }
#define OOB 0x4000000u
#define LD16(v, s) __builtin_amdgcn_raw_buffer_load_b128(rsWS, (v), (s), AUX_SC1)
#define LD4(v, s) __builtin_amdgcn_raw_buffer_load_b32(rsWS, (v), (s), AUX_SC1)
#define ST16(x, v, s) __builtin_amdgcn_raw_buffer_store_b128((x), rsWS, (v), (s), AUX_SC1)
#define ST4(x, v, s) __builtin_amdgcn_raw_buffer_store_b32((x), rsWS, (v), (s), AUX_SC1)
#define SLEEP(n) __builtin_amdgcn_s_sleep(n)
DI void poll16(rsrc_t rs, unsigned off, unsigned tag, int l) {
    unsigned spins = 0, lim, bad;
#pragma nounroll
    do {
        FRESH();
        const v2u x = ldg(rs, (l & 15) * 8, off);
        lim = budget<0, -1>(spins);
        bad = (x[1] ^ tag) | (x[0] ^ tag ^ 0x5bd1e995u);
    } while (__any(bad > lim));
    ORDER_BEHIND_POLL();
}
#define STAMPR(i, role)
#define STAMP(i)
#define STAMPW(i, ww)

#define ARR_DECL
#define KATTR
#define ARR_MARK(i)
#define ARR_END()
template <class F, int... Is> DI void static_for_impl(F&& f, std::integer_sequence<int, Is...>) { (f(std::integral_constant<int, Is>{}), ...); }
template <int N, class F> DI void static_for(F&& f) { static_for_impl(f, std::make_integer_sequence<int, N>{}); }

DI s16x4 bq(unsigned b0, unsigned b1, int j, int i) {
    const int ni = (j + 2) % 6;
    return tr_read(((ni & 1) ? b1 : b0) + (ni >> 1) * 8192 + (i >> 1) * 4096 + (i & 1) * 512);
}
template <class F> DI void group(f32x4 (&acc)[4][6], const bf16x8 (&A)[2][4], const bf16x8 (&B)[2], int j, F gap) {
    const int ni = (j + 2) % 6;
#pragma unroll
    for (int i = 0; i < 8; i++) {
        const int mi = i >> 1, ks = i & 1;
        if (ni < 4) asm volatile("v_mfma_f32_16x16x32_bf16 %0, %1, %2, %0" : "+v"(acc[mi][ni]) : "v"(B[ks]), "v"(A[ks][mi]));
        else asm volatile("v_mfma_f32_16x16x32_bf16 %0, %1, %2, %0" : "+v"(acc[mi][ni]) : "v"(A[ks][mi]), "v"(B[ks]));
        gap(i);
        SCHED();
    }
}
DI void img_write2(const unsigned (&ia)[4], const bf16x8 (&f)[4]) {
#pragma unroll
    for (int k = 0; k < 4; k++) {
        const v4u u = __builtin_bit_cast(v4u, f[k]);
        *(LAS v2u*)(ia[2 * (k & 1)] + (k >> 1) * 2048) = (v2u){u[0], u[1]};
        *(LAS v2u*)(ia[2 * (k & 1) + 1] + (k >> 1) * 2048) = (v2u){u[2], u[3]};
    }
}
struct ImgPut {
    const unsigned (&ia)[4];
    DI void operator()(int k, const bf16x8& f) const {
        if (!IW) return;
        const v4u u = __builtin_bit_cast(v4u, f);
        *(LAS v2u*)(ia[2 * (k & 1)] + (k >> 1) * 2048) = (v2u){u[0], u[1]};
        *(LAS v2u*)(ia[2 * (k & 1) + 1] + (k >> 1) * 2048) = (v2u){u[2], u[3]};
        SCHED();
    }
};
DI float lds_bf(unsigned a) { return bf2f(*(const LAS u16*)(a)); }
#define MFMA_SETTLE2(p) asm volatile("s_nop 7\n\ts_nop 1" : "+v"(p[0][0]), "+v"(p[0][1]), "+v"(p[1][0]), "+v"(p[1][1]))

__global__ KATTR __launch_bounds__(512, 2) void fused_kernel(const u16* __restrict__ X, const u16* __restrict__ WQKV, const u16* __restrict__ WPHI,
                                                       const u16* __restrict__ WO, const u16* __restrict__ WR, const u16* __restrict__ WG,
                                                       const u16* __restrict__ WU, const u16* __restrict__ WD, u16* __restrict__ OUT,
                                                       char* __restrict__ ws) {
    extern __shared__ __attribute__((aligned(16))) char lds[];
    u64 pc0; unsigned clen;
    const int tid = threadIdx.x, l = tid & 63;
    asm volatile("s_getpc_b64 %0\n.Lfk_beg:\n\ts_lshr_b32 %1, %3, 3\n\tv_and_b32 v255, 0x1e0, %2\n\tv_add_lshl_u32 v255, v255, %1, 7\n\ts_mov_b32 %1, .Lfk_end-.Lfk_beg\n\t"
                 "v_min_u32 v255, %1, v255\n\tglobal_load_dword v255, v255, %0" : "=&s"(pc0), "=&s"(clen) : "v"(tid), "s"(blockIdx.x) : "v255", "scc");
    SCHED();
    const unsigned w = rfl(tid >> 6);
    const unsigned bid = blockIdx.x, xcd = bid & 7, idx = bid >> 3;
    const unsigned rt = (xcd & 1) * 8 + (idx & 7), hp = (xcd >> 1) * 4 + (idx >> 3);
    const unsigned i16 = idx & 15, rt2 = M2 ? (xcd >> 1) * 4 + (idx & 3) : xcd * 2 + (idx >> 4), ct = M2 ? (xcd & 1) * 8 + (idx >> 2) : i16 == 4 ? 11 : i16 == 11 ? 4 : i16;
    const unsigned hq = (hp + 4) & 15;
    const unsigned rot = (rt >> 2) * 4;
    const unsigned rot2 = M2 ? (4 * ct + RA * rt2 + 2 * (xcd & 1)) & 31 : (2 * ct + RB * (rt2 >> 1) + RC_ * (rt2 & 1)) & 31;
    const unsigned ldsb = (unsigned)(size_t)lds;
    const int g = l >> 4, q4 = (l & 15) >> 2, p4 = l & 3, t16 = l & 15;
    const unsigned kr = l >> 3;
    asm volatile("" ::"s"(X), "s"(WQKV), "s"(WPHI), "s"(WO), "s"(WR), "s"(WG), "s"(WU), "s"(WD), "s"(OUT), "s"(ws));
    unsigned tag;
#define READ_TAG() do { SCHED(); tag = *(const volatile CAS unsigned*)(ws + WS_EPOCH) + 1u; asm volatile("" : "+s"(tag)); } while (0)
    const rsrc_t rsWS = __builtin_amdgcn_make_buffer_rsrc((void*)ws, 0, WS_USED, 0x00020000);
    const rsrc_t rsX = __builtin_amdgcn_make_buffer_rsrc((void*)X, 0, 2048 * 1024 * 2, 0x00020000);
    const rsrc_t rsW = __builtin_amdgcn_make_buffer_rsrc((void*)WQKV, 0, 1024 * 3072 * 2, 0x00020000);
    const unsigned vA = (kr * 1024 + (((l & 7) ^ kr) * 8)) * 2;
    const unsigned vB0 = (kr * 3072 + (((l & 7) ^ (((kr >> 1) & 1) << 1)) * 8)) * 2;
    const unsigned vB1 = (kr * 3072 + (((l & 7) ^ ((((kr >> 1) & 1) | 2) << 1)) * 8)) * 2;
    const unsigned srow1 = rt * 128 + hp * 8 + (w & 3) * 2;
    v4u xr[4];
    if (w < 4) {
#pragma unroll
        for (int i = 0; i < 4; i++) xr[i] = ld16_asm(l * 16, rsX, srow1 * 2048 + i * 1024);
    }
    {
        const unsigned vBw = (w & 1) ? vB1 : vB0;
#pragma unroll
        for (int i = 0; i < 2; i++) dma16(ldsb + (w * 2 + i) * 1024, vA, rsX, (rt * 128 + w * 16 + i * 8) * 2048 + rot * 128);
#pragma unroll
        for (int i = 0; i < 3; i++) dma16(ldsb + 16384 + i * 8192 + w * 1024, vBw, rsW, (rot * 64 + w * 8) * 6144 + (i ? hp : hq) * 128 + i * 2048);
    }
    const f32x4 zero4 = {0.f, 0.f, 0.f, 0.f};
    ARR_DECL;
    const bf16x8 ones = mk8(0x3F803F80u, 0x3F803F80u, 0x3F803F80u, 0x3F803F80u);
    const unsigned lw = w & 3;
    const bool ffn = (ct & 7) != 3;
    const unsigned fb = !ffn ? 255u : M2 ? xcd * 28 + (idx < 12 ? idx : idx - 4) : rt2 * 14 + (i16 < 3 ? i16 : i16 - 2);
    const unsigned cg = (fb + 5) & 15, kc = fb >> 4;
    const unsigned kq = fb / 56, cg1 = fb % 56, cg1u = (cg1 + 3) % 56;
    v4u wreg[24];
    STAMPW(0, 0); STAMPW(0, 4);

    const rsrc_t rsWO = __builtin_amdgcn_make_buffer_rsrc((void*)WO, 0, 1024 * 1024 * 2, 0x00020000);
    const unsigned vWO = kr * 2048 + (((l & 7) ^ (((kr >> 1) & 3) << 1)) * 16);
    unsigned vwo[4];
    auto wo_so = [&](unsigned b) { return (((2 * b & 30) + (b >> 4) + rot2) & 31) * 65536 + ct * 128; };
    const unsigned fw = (((t16 >> 1) & 3) << 2) | (((t16 >> 3) & 1) << 1) | (t16 & 1);
    unsigned ad;
    unsigned ia[4];
    bf16x8 sdv[4];
    auto pre_addr = [&]() {
#pragma unroll
        for (int k = 0; k < 4; k++) { ia[k] = ldsb + L_XB + lw * 8192 + (w >> 2) * 4096 + t16 * 128 + (((4 * k + g) ^ fw) * 8); PIN(ia[k]); }
        const unsigned rr = 4 * g + q4, fr = (((rr >> 1) & 3) << 2) | (((rr >> 3) & 1) << 1) | (rr & 1);
        ad = ldsb + L_XB + (w & 1) * 16384 + rr * 128 + ((((w >> 1) * 4 + p4) ^ fr) * 8);
        PIN(ad);
    };
    auto sd_reads = [&]() {
        unsigned sa = ldsb + L_SD + g * 8;
        PIN(sa);
#pragma unroll
        for (int s = 0; s < 4; s++) {
            const v2u a = *(const LAS v2u*)(sa + s * 64), b = *(const LAS v2u*)(sa + s * 64 + 32);
            sdv[s] = mk8(a[0], a[1], b[0], b[1]);
        }
    };
    bf16x8 v0[4], v1[4];
    auto v_reads = [&]() {
        unsigned vd = ldsb + L_V + (w & 1) * 8192 + l * 16;
        PIN(vd);
#pragma unroll
        for (int s = 0; s < 4; s++) { v0[s] = lds128(vd + s * 2048); v1[s] = lds128(vd + s * 2048 + 1024); }
    };
    auto kv_tile = [&](auto hook6) {
        const unsigned h2 = w & 1, ft = w >> 1;
        WAITL0();
        ARR_MARK(18);
        BAR();
        SCHED();
        bf16x8 pf[4];
#pragma unroll
        for (int s = 0; s < 4; s++) pf[s] = cat8(tr_read(ad + s * 4096), tr_read(ad + s * 4096 + 2048));
        SCHED();
        hook6();
        SCHED();
        f32x4 k0 = zero4, k1 = zero4, kz = zero4;
#pragma unroll
        for (int s = 0; s < 4; s++) { k0 = MFMA(pf[s], v0[s], k0); k1 = MFMA(pf[s], v1[s], k1); if (ZO) SCHED(); }
#pragma unroll
        for (int s = 0; s < 4; s++) kz = MFMA(pf[s], sdv[s], kz);
        const unsigned pbase = WS_P2 + (hp * 16 + rt) * P2_STRIDE;
        const v4u o0 = {pk2(k0[0], k0[1]), pk2(k0[2], k0[3]), pk2(k1[0], k1[1]), pk2(k1[2], k1[3])};
        DLY(11);
        __builtin_amdgcn_raw_buffer_store_b128(o0, rsWS, l * 16, pbase + h2 * 4096 + ft * 1024, AUX_SC1);
        asm volatile("s_nop 1" :: "v"(o0));
        __builtin_amdgcn_raw_buffer_store_b64((v2u){pk2(kz[0], kz[1]), pk2(kz[2], kz[3])}, rsWS, t16 == 0 ? g * 8 : OOB,
                                              pbase + 8192 + h2 * 128 + ft * 32, AUX_SC1);
        SCHED();
        ARR_MARK(19);
        BAR();
    };

    const bool zjob = lw == 0 && (rt & 7) == 0;
    v4u pp[4];
    auto pp_ld = [&](int k) {
        return LD16(kr * P2_STRIDE + (l & 7) * 16, WS_P2 + (hp * 16 + (k & 1) * 8) * P2_STRIDE +
                    (k < 2 ? (rt >> 2) * 2048 + (lw & 1) * 1024 + (rt & 3) * 256 + (lw >> 1) * 128 : 8192 + (rt >> 3) * 128));
    };
    auto kv_reduce = [&]() {
        unsigned v3 = (l & 8) ? OOB : (l & 7) * 16 + g * 4, res;
        unsigned s3 = WS_G3 + (hp * 4 + (rt >> 2)) * 2048 + (lw & 1) * 1024 + (rt & 3) * 256 + (lw >> 1) * 128;
        PIN(v3);
        asm volatile("" : "+s"(s3));
        auto red = [&](const v4u* p) {
            float lo = xrow_scatter<false>(bflo(p[0][0]) + bflo(p[1][0]), bflo(p[0][1]) + bflo(p[1][1]), bflo(p[0][2]) + bflo(p[1][2]), bflo(p[0][3]) + bflo(p[1][3]));
            float hi = xrow_scatter<false>(bfhi(p[0][0]) + bfhi(p[1][0]), bfhi(p[0][1]) + bfhi(p[1][1]), bfhi(p[0][2]) + bfhi(p[1][2]), bfhi(p[0][3]) + bfhi(p[1][3]));
            const float chk = lo;
            lo += dppf<0x128>(lo); hi += dppf<0x128>(hi);
            res = nsent(pk2(lo, hi));
            return chk;
        };
        DLY(12);
        SLEEP(SL1);
        if (RARE(zjob)) {
            unsigned rz;
            sweepn<4>(pp_ld, pp, [&]() { const float a = red(pp + 2); rz = res; return a + red(pp); });
            ST4(rz, (l & 8) ? OOB : ((l >> 2) & 1) * 64 + ((l & 1) * 2 + (g >> 1)) * 16 + ((l >> 1) & 1) * 8 + (g & 1) * 4, WS_GZ + (hp * 2 + (rt >> 3)) * 128);
        } else {
            v4u p2[2];
            sweepn<2>(pp_ld, p2, [&]() { return red(p2); });
        }
        DLY(13);
        STAMPW(25, 0);
        ST4(res, v3, s3);
    };

    const unsigned half = w >> 2;
    const unsigned R2 = rt2 * 128 + lw * 32 + half * 16 + t16;

    rsrc_t rsD = __builtin_amdgcn_make_buffer_rsrc((void*)WD, 0, 1024 * 3584 * 2, 0x00020000);
    unsigned vD, sD;
    auto dloads = [&](int i0, int i1) {
#pragma unroll
        for (int i = i0; i < i1; i++)
            wreg[16 + i] = __builtin_amdgcn_raw_buffer_load_b128(rsD, vD, sD + i * 2048, W_AUX);
        FRESH();
    };
    v4u wp1[2][8];
    auto perm1 = [&]() {
#pragma unroll
        for (int n = 0; n < 16; n++) PIN(wreg[n]);
#pragma unroll
        for (int ip = 0; ip < 4; ip++)
#pragma unroll
            for (int c = 0; c < 4; c++) {
#pragma unroll
                for (int m = 0; m < 2; m++) {
                    const unsigned a = wreg[m * 8 + 2 * ip][c], b = wreg[m * 8 + 2 * ip + 1][c];
                    wp1[m][2 * c][ip] = __builtin_amdgcn_perm(b, a, 0x05040100);
                    wp1[m][2 * c + 1][ip] = __builtin_amdgcn_perm(b, a, 0x07060302);
                }
            }
#pragma unroll
        for (int c = 0; c < 8; c++) { PIN(wp1[0][c]); PIN(wp1[1][c]); }
    };
    v4u xres[2], wrv[2];
    auto res_loads = [&]() {
#pragma unroll
        for (int i = 0; i < 2; i++) {
            xres[i] = *(const v4u*)(X + (size_t)R2 * 1024 + ct * 64 + i * 32 + 8 * g);
            wrv[i] = *(const v4u*)(WR + ct * 64 + i * 32 + 8 * g);
        }
        FRESH();
    };

    if (w < 4) {
        const unsigned wm = w & 1, hh = w >> 1;
        f32x4 acc[4][6];
        bf16x8 wf[2], wfq[2];
        float mbt[4], rst[4], csv[2];
        f32x4 mbv[4], csq[2], csk[2];
        unsigned vgc = WS_GC + (l < 32 ? hp : hq) * 256 + (l & 31) * 8;
        if (!GS) PIN(vgc);
        v2u gc;
#pragma unroll
        for (int i = 0; i < 4; i++)
#pragma unroll
            for (int j = 0; j < 6; j++) { acc[i][j] = zero4; PIN(acc[i][j]); }
        {
            const unsigned aoff0 = ldsb + (wm * 64 + t16) * 128 + (((0 + g) ^ (l & 7)) * 16);
            const unsigned aoff1 = ldsb + (wm * 64 + t16) * 128 + (((4 + g) ^ (l & 7)) * 16);
            const unsigned sk = ((q4 >> 1) & 1) | ((g & 1) << 1);
            const unsigned boffA = ldsb + 16384 + (8 * g + q4) * 128 + (((2 * hh) ^ sk) * 32) + p4 * 8;
            const unsigned boffB = ldsb + 16384 + (8 * g + q4) * 128 + (((2 * hh + 1) ^ sk) * 32) + p4 * 8;
            bf16x8 A[2][4], B[3][2];
            s16x4 lo;
            unsigned a0[3], a1[3], bA[3], bB[3];
#pragma unroll
            for (int i = 0; i < 3; i++) {
                a0[i] = aoff0 + i * STAGE_B; a1[i] = aoff1 + i * STAGE_B; bA[i] = boffA + i * STAGE_B; bB[i] = boffB + i * STAGE_B;
                PIN(a0[i]); PIN(a1[i]); PIN(bA[i]); PIN(bB[i]);
            }
            unsigned aT = ldsb + L_MB + (wm * 64 + t16) * 4, aS = ldsb + L_SD + (wm * 64 + t16) * 2, aM = ldsb + L_MB + wm * 256 + g * 16;
            unsigned aC = ldsb + L_CS + hh * 128 + g * 16, aV = ldsb + L_CS + (128 + hh * 32 + t16) * 4;
            unsigned aW = ldsb + L_WPHI + hh * 2048 + (4 * g + q4) * 64 + p4 * 8, aK = ldsb + L_KH + w * 2048 + l * 16;
            PIN(aT); PIN(aS); PIN(aM); PIN(aC); PIN(aV); PIN(aW); PIN(aK);
            pre_addr();
            READ_TAG();
            waitv<5>();
            PIN(xr[0]); PIN(xr[1]); PIN(xr[2]); PIN(xr[3]);
            {
                float s[2], q[2], x0[2];
#pragma unroll
                for (int rr = 0; rr < 2; rr++) {
                    x0[rr] = bflo(rfl(xr[2 * rr][0]));
                    s[rr] = 0.f; q[rr] = 0.f;
#pragma unroll
                    for (int i = 0; i < 4; i++) {
                        const float a = bflo(xr[2 * rr][i]) - x0[rr], b = bfhi(xr[2 * rr][i]) - x0[rr];
                        const float c = bflo(xr[2 * rr + 1][i]) - x0[rr], d = bfhi(xr[2 * rr + 1][i]) - x0[rr];
                        s[rr] += (a + b) + (c + d);
                        q[rr] += (a * a + b * b) + (c * c + d * d);
                    }
                }
                const float sq = row_sum(xrow_scatter<false>(s[0], s[1], q[0], q[1]));
#define RLF(x, n) __uint_as_float(__builtin_amdgcn_readlane(__float_as_uint(x), (n)))
                const float r0 = RLF(sq, 0), r1 = RLF(sq, 16), r2 = RLF(sq, 32), r3 = RLF(sq, 48);
                const float sm = (l ? r1 : r0) * (1.0f / 1024.0f), qm = (l ? r3 : r2) * (1.0f / 1024.0f);
                const float var = fmaxf(qm - sm * sm, 0.f);
                if (l < 2) st_gran(ws, WS_G0 + (srow1 + l) * 8, tag, pk2((l ? x0[1] : x0[0]) + sm, sqrtf(rbf(rbf(var) + 1e-5f))));
            }
            waitv<1>();
            WAITL0();
            ARR_MARK(0);
            BAR();
            SCHED();
            STAMPW(1, 0);
            auto rdB = [&](int u, int j, int i) {
                if (i & 1) B[j % 3][i >> 1] = cat8(lo, bq(bA[u], bB[u], j, i)); else lo = bq(bA[u], bB[u], j, i);
            };
            auto rdA = [&](int u, int i) { A[i & 1][i >> 1] = lds128(((i & 1) ? a1[u] : a0[u]) + (i >> 1) * 2048); };
            auto head = [&](int u) {
                group(acc, A, B[0], 0, [&](int i) { rdB(u, 1 + (i >> 2), i & 3); });
                group(acc, A, B[1], 1, [&](int i) { if (i < 4) rdB(u, 3, i); });
                group(acc, A, B[2], 2, [&](int i) { if (i < 4) rdB(u, 4, i); });
                group(acc, A, B[0], 3, [&](int i) { if (i < 4) rdB(u, 5, i); });
                WL0();
            };
#pragma unroll
            for (int i = 0; i < 4; i++) rdB(0, 0, i);
#pragma unroll
            for (int i = 0; i < 8; i++) rdA(0, i);
            SCHED();
#pragma nounroll
            for (unsigned t = 0; t < 5; t++) {
#pragma unroll
                for (int u = 0; u < 3; u++) {
                    head(u);
                    ARR_MARK(1 + t * 3 + u);
                    BAR();
                    SCHED();
                    group(acc, A, B[1], 4, [&](int i) { if (i < 4) rdB((u + 1) % 3, 0, i); });
                    group(acc, A, B[2], 5, [&](int i) { rdA((u + 1) % 3, i); });
                }
            }
            unsigned kw[4][4], rsu[4];
            f32x2 kx;
            auto kpiece = [&](int n) {
                if (n > 0) { kw[((n - 1 >> 2) + 2) & 3][(n - 1) & 3] = pk2(kx[0], kx[1]); PIN(kw[((n - 1 >> 2) + 2) & 3][(n - 1) & 3]); }
                if (n < 16) {
                    const int mi = ((n >> 2) + 2) & 3, j = n & 3;
                    const f32x4 &c = acc[mi][2 + (j >> 1)], &s_ = csk[j >> 1];
                    float x0 = __builtin_fmaf(mbt[mi], s_[2 * (j & 1)], c[2 * (j & 1)]); PIN(x0);
                    float x1 = __builtin_fmaf(mbt[mi], s_[2 * (j & 1) + 1], c[2 * (j & 1) + 1]); PIN(x1);
                    kx = (f32x2){x0, x1};
                }
                if (n == 5 || n == 9) *(LAS v4u*)(aK + (n == 9) * 1024) = (v4u){kw[2 + (n == 9)][0], kw[2 + (n == 9)][1], kw[2 + (n == 9)][2], kw[2 + (n == 9)][3]};
            };
            group(acc, A, B[0], 0, [&](int i) { rdB(0, 1 + (i >> 2), i & 3); });
            group(acc, A, B[1], 1, [&](int i) {
                if (i < 4) { rdB(0, 3, i); mbt[(i + 2) & 3] = ldsf(aT + ((i + 2) & 3) * 64); }
                else if (i < 6) csk[i - 4] = ldsf4(aC + 256 + (i - 4) * 64);
                else wf[i - 6] = cat8(tr_read(aW + (i - 6) * 32), tr_read(aW + (i - 6) * 32 + 1024));
            });
            group(acc, A, B[2], 2, [&](int i) { if (i < 4) rdB(0, 4, i); else mbv[i - 4] = ldsf4(aM + (i - 4) * 64); kpiece(i); });
            group(acc, A, B[0], 3, [&](int i) { if (i < 4) rdB(0, 5, i); if (i == 2 || i == 3) csv[i - 2] = ldsf(aV + (i - 2) * 64); if (RH_ && i >= 4) rsu[i - 4] = *(const LAS u16*)(aS + (i - 4) * 32); kpiece(8 + i); });
            DLY(10);
            WL0();
            ARR_MARK(16);
            BAR();
            SCHED();
            kpiece(16);
            f32x4 p[2][2];
#pragma unroll
            for (int i = 0; i < 4; i++) p[i >> 1][i & 1] = MFMA(wf[i & 1], mk8(kw[i >> 1][0], kw[i >> 1][1], kw[i >> 1][2], kw[i >> 1][3]), zero4);
            SCHED();
            STAMPW(2, 0);
#pragma unroll
            for (int mi = 0; mi < 4; mi++) rst[mi] = RH_ ? bf2f(rsu[mi]) : lds_bf(aS + mi * 32);
            if (!GS) gc = ldg(rsWS, vgc, 0);
#pragma unroll
            for (int a = 0; a < 2; a++)
#pragma unroll
                for (int dt = 0; dt < 2; dt++) {
                    const f32x4 x0 = acc[2 * a][4 + dt] + mbv[2 * a] * csv[dt], x1 = acc[2 * a + 1][4 + dt] + mbv[2 * a + 1] * csv[dt];
                    *(LAS v4u*)(aK + (L_V - L_KH) + w * 2048 + (a * 2 + dt) * 1024) = (v4u){pk2(x0[0], x0[1]), pk2(x0[2], x0[3]), pk2(x1[0], x1[1]), pk2(x1[2], x1[3])};
                }
            SCHED();
            rst[0] = __builtin_amdgcn_rcpf(rst[0]); rst[1] = __builtin_amdgcn_rcpf(rst[1]);
            if (KC == 0) { WAITL0(); BAR(); SCHED(); }
            bf16x8 f[4];
            struct HookC {
                decltype(v_reads)& vr; decltype(sd_reads)& sr;
                DI void operator()(int k, float& x) const { if (k == KC) { PIN(x); WAITL0(); ARR_MARK(17); BAR(); PIN(x); } if (k == VC) { PIN(x); vr(); sr(); PIN(x); } }
                DI void operator()(int k, float& x, float& y) const { if (k == KC) { PIN(y); WAITL0(); BAR(); PIN(x); } }
            };
            softmax2<true>(p, rst[0], rst[1], g >= 2, f, HookC{v_reads, sd_reads}, ImgPut{ia});
            if (!IW) img_write2(ia, f);
#pragma unroll
            for (int ft = 0; ft < 2; ft++) wfq[ft] = cat8(tr_read(aW + ft * 32 + 4096), tr_read(aW + ft * 32 + 5120));
            csq[0] = ldsf4(aC); csq[1] = ldsf4(aC + 64);
            STAMPW(3, 0);
            kv_tile([]() {});
            STAMPW(4, 0);
            SCHED();
            group(acc, A, B[1], 4, [&](int i) { if (i == 2 || i == 3) { rst[i] = __builtin_amdgcn_rcpf(rst[i]); PIN(rst[i]); } });
            group(acc, A, B[2], 5, [&](int) {});
            asm volatile("s_nop 7\n\ts_nop 1");
            SCHED();
        }
        bf16x8 phq[2][4];
        v4u kg[6];
        unsigned sG3 = WS_G3 + (hq * 2 + hh) * 4096, sGZ = WS_GZ + (hq * 2 + hh) * 128;
        {
            f32x4 pq[2][2][2];
#pragma unroll
            for (int mi = 0; mi < 4; mi++) {
                const f32x4 x0 = acc[mi][0] + mbt[mi] * csq[0], x1 = acc[mi][1] + mbt[mi] * csq[1];
                const bf16x8 qf = mk8(pk2(x0[0], x0[1]), pk2(x0[2], x0[3]), pk2(x1[0], x1[1]), pk2(x1[2], x1[3]));
                pq[mi >> 1][mi & 1][0] = MFMA(wfq[0], qf, zero4); pq[mi >> 1][mi & 1][1] = MFMA(wfq[1], qf, zero4);
            }
            MFMA_SETTLE2(pq[0]); MFMA_SETTLE2(pq[1]);
            if (!GS) if (RARE(!__all(gc[1] == tag))) { unsigned c[1]; sweep<1>(rsWS, vgc, 0, 0, tag, c); }
            ORDER_BEHIND_POLL();
            softmax2<false>(pq[0], rst[0], rst[1], g >= 2, phq[0]);
#pragma unroll
            for (int i = 0; i < 4; i++) PIN(phq[0][i]);
            SCHED();
            kv_reduce();
            SCHED();
            res_loads();
            SCHED();
            BAR();
            SCHED();
            softmax2<false>(pq[1], rst[2], rst[3], g >= 2, phq[1]);
#pragma unroll
            for (int i = 0; i < 4; i++) { PIN(phq[0][i]); PIN(phq[1][i]); }
        }
        STAMPW(5, 0);
        STAMPW(26, 0);
        v2u fv = {tag ^ 0x5bd1e995u, tag};
        unsigned sF = WS_F4 + ((rt * 4 + w) * 16 + hq) * 8, vF = l == 0 ? 0 : OOB;
        {
            unsigned vO = l * 16, sO = WS_P4 + ((rt * 8 + wm * 4) * 32 + hq * 2 + hh) * 1024;
            asm volatile("" : "+s"(sO), "+s"(sF), "+s"(sG3), "+s"(sGZ));
            PIN(fv); PIN(vF);
            DLY(14);
            SLEEP(SL2);
            v4u ov[4];
#define PH(mi, s) phq[(mi) >> 1][2 * ((mi) & 1) + (s)]
#define KVA(s, dt) __builtin_bit_cast(bf16x8, kg[2 + 2 * (s) + (dt)])
            auto pairup = [&](v4u& a, v4u& b) {
                unsigned a2 = a[2], a3 = a[3], b0 = b[0], b1 = b[1];
                asm volatile("v_swap_b32 %0, %2\n\tv_swap_b32 %1, %3" : "+v"(a2), "+v"(a3), "+v"(b0), "+v"(b1));
                a[2] = a2; a[3] = a3; b[0] = b0; b[1] = b1;
            };
            f32x4 dn[4], n0[4], n1[4];
            float id[4];
            unsigned spins = 0, lim, m;
#pragma nounroll
            do {
                FRESH();
#pragma unroll
                for (int k = 0; k < 6; k++) { kg[k] = k >= 2 ? LD16(l * 16 + (k - 2) * 1024, sG3) : LD16(g * 16 + k * 64, sGZ); SCHED(); }
                lim = budget<-2, -1>(spins);
                m = 0;
#pragma unroll
                for (int i = 0; i < 8; i++) {
                    dn[i & 3] = MFMA(__builtin_bit_cast(bf16x8, kg[i >> 2]), PH(i & 3, i >> 2), i < 4 ? zero4 : dn[i & 3]);
                    if (i >= 1 && i < 7) { m = umx(umx(m, kg[i - 1][0]), kg[i - 1][1]); PIN(m); m = umx(umx(m, kg[i - 1][2]), kg[i - 1][3]); }
                    if (i == 5) pairup(kg[2], kg[3]);
                    if (i == 7) pairup(kg[4], kg[5]);
                    SCHED();
                }
            } while (__any(m > lim));
            DLY(15);
            STAMPW(6, 0);
#pragma unroll
            for (int mi = 0; mi <= 4; mi++) {
                if (mi < 4) {
                    n0[mi] = MFMA(KVA(0, 0), PH(mi, 0), zero4); n1[mi] = MFMA(KVA(0, 1), PH(mi, 0), zero4);
                    n0[mi] = MFMA(KVA(1, 0), PH(mi, 1), n0[mi]); n1[mi] = MFMA(KVA(1, 1), PH(mi, 1), n1[mi]);
                    id[mi] = __builtin_amdgcn_rcpf(rbf(rbf(dn[mi][0]) + 1e-6f));
                    asm volatile("" :: "v"(dn[mi]));
                }
                if (mi > 0) {
                    const f32x4 a = n0[mi - 1] * id[mi - 1], b = n1[mi - 1] * id[mi - 1];
                    ov[mi - 1] = (v4u){pk2(a[0], a[1]), pk2(a[2], a[3]), pk2(b[0], b[1]), pk2(b[2], b[3])};
                    ST16(ov[mi - 1], vO + (mi - 1) * 32768, sO);
                }
                if (mi < 4) { SGB(0x8, 1); SGB(0x2, 6); SGB(0x8, 1); SGB(0x2, 3); SGB(0x8, 1); SGB(0x2, 3); SGB(0x8, 1); SGB(0x2, 4); }
                SCHED();
            }
#pragma unroll
            for (int mi = 0; mi < 4; mi++) PIN(ov[mi]);
        }
        BAR();
        waitv<0>();
        __builtin_amdgcn_raw_buffer_store_b64(fv, rsWS, vF, sF, AUX_SC1);
        STAMPW(7, 0);
    } else {
        const unsigned pw = lw & 1;
        const rsrc_t rsG = __builtin_amdgcn_make_buffer_rsrc((void*)WG, 0, 1024 * 3584 * 2, 0x00020000);
        const rsrc_t rsU = __builtin_amdgcn_make_buffer_rsrc((void*)WU, 0, 1024 * 3584 * 2, 0x00020000);
        unsigned vwl = kr * 57344 + (l & 7) * 16;
        unsigned sg = (kq * 256 + lw * 64) * 7168 + cg1 * 128, su = (kq * 256 + lw * 64) * 7168 + cg1u * 128;
        auto wloads = [&](int n0, int n1) {
#pragma unroll
            for (int n = n0; n < n1; n++)
                asm volatile("buffer_load_dwordx4 %0, %1, %2, %3 offen nt" : "=v"(wreg[n]) : "v"(vwl), "s"(n < 8 ? rsG : rsU), "s"((n < 8 ? sg : su) + (n & 7) * 7168));
        };
        auto repack = [&](unsigned gI) {
            const unsigned s2 = (2 * g + (q4 >> 1)) & 3;
            bf16x8 f[4];
#pragma unroll
            for (int nt = 0; nt < 4; nt++) {
                const unsigned a = ldsb + L_WO + gI * 4096 + (4 * g + q4) * 128 + ((((nt >> 1) * 2 + (p4 >> 1)) ^ s2) * 32) + (p4 & 1) * 16 + (nt & 1) * 8;
                f[nt] = cat8(tr_read(a), tr_read(a + 2048));
            }
            WAITL0();
#pragma unroll
            for (int nt = 0; nt < 4; nt++) *(LAS bf16x8*)(ldsb + L_WO + gI * 4096 + nt * 1024 + l * 16) = f[nt];
        };
        unsigned aK, aW, aS;
        auto k_pre = [&]() {
            pre_addr();
            aK = ldsb + L_KH + lw * 2048 + l * 16; aW = ldsb + L_WPHI + (lw >> 1) * 2048 + (4 * g + q4) * 64 + p4 * 8; aS = ldsb + L_SD + (pw * 64 + 32 + t16) * 2;
            PIN(aK); PIN(aW); PIN(aS);
        };
        auto kfeat = [&](auto hook) {
            CKS(0);
            bf16x8 wf[2];
            float rst[2];
#pragma unroll
            for (int ft = 0; ft < 2; ft++) {
                wf[ft] = cat8(tr_read(aW + ft * 32), tr_read(aW + ft * 32 + 1024));
                rst[ft] = __builtin_amdgcn_rcpf(lds_bf(aS + ft * 32));
            }
            sd_reads();
            PIN(wf[0]); PIN(wf[1]); PIN(rst[0]); PIN(rst[1]);
            SCHED();
            ARR_MARK(16);
            BAR();
            CKS(1);
            SCHED();
            const bf16x8 kf0 = lds128(aK), kf1 = lds128(aK + 1024);
            SCHED();
            hook(-1, rst[0]);
            hook(0, rst[0]);
            SCHED();
            f32x4 p[2][2];
            p[0][0] = MFMA(wf[0], kf0, zero4); p[0][1] = MFMA(wf[1], kf0, zero4);
            p[1][0] = MFMA(wf[0], kf1, zero4); p[1][1] = MFMA(wf[1], kf1, zero4);
            MFMA_SETTLE2(p);
            bf16x8 f[4];
            softmax2<true>(p, rst[0], rst[1], g >= 2, f, hook, ImgPut{ia});
            if (!IW) img_write2(ia, f);
            SCHED();
            hook(5, rst[0]);
            SCHED();
            STAMPW(3, 4); STAMPW(3, 7);
            kv_tile([&]() { hook(6, rst[0]); });
            CKS(2);
            STAMPW(4, 4); STAMPW(4, 7);
        };
        auto wblk = [&](int k) {
            constexpr unsigned T[8] = {0x19181716, 0x041c1b1a, 0x08070605, 0x0c0b0a09, 0x1413120d, 0x11100015, 0x1d030201, 0x1f1e0f0e};
            return (T[k] >> (8 * lw)) & 31;
        };
        auto wo_init = [&]() {
#pragma unroll
            for (int i = 0; i < 4; i++) { vwo[i] = vWO + i * (16384 - 1024); PIN(vwo[i]); }
        };
        unsigned hs[8];
        auto wl_pre = [&]() { asm volatile("" : "+s"(sg), "+s"(su)); PIN(vwl); };
        auto wo_pre = [&]() {
            wo_init();
#pragma unroll
            for (int k = 0; k < 8; k++) { hs[k] = wo_so(wblk(k)); asm volatile("" : "+s"(hs[k])); }
        };
        auto wo_rng = [&](int k0, int k1) {
#pragma unroll
            for (int k = k0; k < k1; k++) dma16x4(ldsb + L_WO + wblk(k) * 4096, vwo, rsWO, hs[k]);
        };
        auto pf = [&](int i) {
            if (i == 0) { wloads(0, 6); SCHED(); if (lw < 2) wo_pre(); SCHED(); }
            if (i < 3 && !ffn) wo_rng(i, i + 1);
            if (i == 1 || i == 2) wloads(6 * i, i == 1 ? 12 : 16);
        };
        struct Hook {
            decltype(pf)& f; decltype(v_reads)& vr;
            DI void operator()(int k, float& x) const { if (k == 10 && KH != 10) return; PIN(x); if (k == KH) { ARR_MARK(17); BAR(); } if (k == VH) vr(); f(3 + k); if (k == PA) __builtin_amdgcn_s_setprio(1); if (k == PB) __builtin_amdgcn_s_setprio(0); PIN(x); }
            DI void operator()(int k, float& x, float& y) const { PIN(y); if (k == KH) BAR(); f(3 + k); if (k == PA) __builtin_amdgcn_s_setprio(1); if (k == PB) __builtin_amdgcn_s_setprio(0); PIN(x); }
        };
        auto tail16 = [&]() {
            kfeat(Hook{pf, v_reads});
            DLY(16);
            if (ffn) wo_rng(0, 3);
            wo_rng(3, H1);
            static_for<R1>([&](auto kc_) {
                constexpr int k = decltype(kc_)::value;
                waitv<4 * (H1 - 1 - k)>();
                repack(wblk(k));
            });
            BAR();
            res_loads();
            wo_rng(H1, 7);
            static_for<7 - R1>([&](auto kc_) {
                constexpr int k = decltype(kc_)::value + R1;
                waitv<4 * (6 - k)>();
                repack(wblk(k));
            });
            PIN(xres[0]); PIN(xres[1]); PIN(wrv[0]); PIN(wrv[1]);
            perm1();
            SCHED();
            BAR();
            wo_rng(7, 8);
        };
        auto vT23 = [&]() {
            const unsigned n = (idx * 2 + pw) * 64 + l, r = n / 12, c = n % 12;
            return idx < 24 ? ((r & 127) + (r >> 7) * 256 + 128) * 6144 + (c >> 2) * 2048 + (((xcd >> 1) * 4 + (c >> 2 ? 0 : 4) + (c & 3)) & 15) * 128 : OOB;
        };
        if (lw < 2) {
            const unsigned sA = (rt * 128 + lw * 64) * 2048, sB = lw * 32 * 6144 + hp * 128, sBq = lw * 32 * 6144 + hq * 128;
            unsigned buf = 1;
            unsigned va[4], vb[4];
#pragma unroll
            for (int i = 0; i < 4; i++) { va[i] = vA + i * (16384 - 1024); vb[i] = ((i & 1) ? vB1 : vB0) + i * (49152 - 1024); PIN(va[i]); PIN(vb[i]); }
            auto stage = [&](unsigned s, unsigned bf, int i0, int i1) {
                const unsigned kt = (s + rot) & 15;
                const unsigned lb = ldsb + bf * STAGE_B;
                const unsigned sa = sA + kt * 128, sb = kt * 393216;
                if (i0 <= 0 && 0 < i1) dma16x4(lb + lw * 8192, va, rsX, sa);
                if (i0 <= 1 && 1 < i1) dma16x4(lb + lw * 8192 + 4096, va, rsX, sa + 65536);
                if (i0 <= 2 && 2 < i1) dma16x4(lb + 16384 + 8192 + lw * 4096, vb, rsW, sb + sB + 2048);
                if (i0 <= 3 && 3 < i1) dma16x4(lb + 16384 + 16384 + lw * 4096, vb, rsW, sb + sB + 4096);
                if (i0 <= 4 && 4 < i1) dma16x4(lb + 16384 + lw * 4096, vb, rsW, sb + sBq);
            };
            stage(1, 1, 0, 4);
            SCHED();
            unsigned vt = vT23();
            PIN(vt);
            wl_pre(); k_pre();
            waitv<16>(); ARR_MARK(0); BAR();
            stage(1, 1, 4, 5);
            touch(vt, rsW, (xcd & 1) * 512 * 6144);
            waitv<1>(); ARR_MARK(1); BAR();
            stage(2, 2, 0, 5); stage(3, 0, 0, 3);
            waitv<12>(); ARR_MARK(2); BAR();
            stage(3, 0, 3, 5); stage(4, 1, 0, 4);
            waitv<16>(); ARR_MARK(3); BAR();
            stage(4, 1, 4, 5);
            buf = 2;
#pragma nounroll
            for (unsigned s = 5; s < 16; s++) {
                if (s > 5) { waitv<20>(); ARR_MARK(s - 2); BAR(); }
                stage(s, buf, 0, 5);
                buf = buf == 2 ? 0 : buf + 1;
            }
            waitv<20>(); ARR_MARK(14); BAR();
            pf(0);
            if (ffn) waitv<6>(); else waitv<10>();
            ARR_MARK(15); BAR();
            pf(1);
            tail16();
            READ_TAG();
        } else {
            SCHED();
            unsigned v3;
            const unsigned h3 = (xcd >> 1) * 4 + 3, kv3 = !(xcd & 2), wv = idx * 2 + pw;
            auto line3 = [&](unsigned per, unsigned rows, auto rowf) {
                const unsigned n = wv * (kv3 ? per : (per + 2) / 3) + l, r3 = kv3 ? n / 3 : n, c3 = kv3 ? n % 3 : 0;
                return (idx < 24 && l < (kv3 ? per : (per + 2) / 3) && r3 < rows) ? rowf(r3) * 6144 + c3 * 2048 + ((c3 ? h3 : h3 + 4) & 15) * 128 : OOB;
            };
            v3 = line3(32, 512, [](unsigned r) { return r; });
            touch(line3(16, 256, [](unsigned r) { return (r & 127) + (r >> 7) * 256; }), rsW, (xcd & 1) * 512 * 6144);
            {
                const rsrc_t rsP = __builtin_amdgcn_make_buffer_rsrc((void*)WPHI, 0, 32 * 1024 * 2, 0x00020000);
#pragma unroll
                for (int i = 0; i < 4; i++) dma16(ldsb + L_WPHI + pw * 4096 + i * 1024, l * 16, rsP, (pw ? hq : hp) * 4096 + i * 1024);
            }
            v4u sv = {SENT, SENT, SENT, SENT};
            unsigned vc4 = l < 16 ? WS_G3 + (hp * 4 + (rt >> 2)) * 2048 + pw * 1024 + (rt & 3) * 256 + l * 16
                         : l < 24 ? WS_P2 + (hp * 16 + rt) * P2_STRIDE + 8192 + pw * 128 + (l - 16) * 16
                         : (l < 32 && pw == 0 && (rt & 7) == 0) ? WS_GZ + (hp * 2 + (rt >> 3)) * 128 + (l - 24) * 16 : OOB;
            auto clears = [&](int i0, int i1) {
#pragma unroll
                for (int i = i0; i < i1; i++) {
                    if (i < 4) ST16(sv, l * 16, WS_P2 + (hp * 16 + rt) * P2_STRIDE + pw * 4096 + i * 1024);
                    if (i == 4) ST16(sv, vc4, 0);
                    if (i > 4) ST16(sv, l * 16, WS_TL + (bid * 4 + pw * 2 + i - 5) * 1024);
                }
                FRESH();
            };
            PIN(vc4); PIN(sv);
            unsigned ca = ldsb + 16384 + pw * 8192 + (8 * g + q4) * 128 + (((rt & 3) ^ (((q4 >> 1) & 1) | ((g & 1) << 1))) * 32) + p4 * 8;
            PIN(ca);
            bf16x8 cf[4];
            f32x4 cd[2] = {zero4, zero4};
            auto cs_read = [&](int st) {
#pragma unroll
                for (int i = 0; i < 4; i++) cf[i] = cat8(tr_read(ca + (st % 3) * STAGE_B + i * 4096), tr_read(ca + (st % 3) * STAGE_B + i * 4096 + 512));
                SCHED();
            };
            READ_TAG();
            waitv<5>();
            ARR_MARK(0);
            BAR();
            SCHED();
            cs_read(0);
            FRESH();
            const rsrc_t rsC = __builtin_amdgcn_make_buffer_rsrc((void*)pc0, 0, clen, 0x00020000);
            auto code_pf = [&]() { touch(l < 6 ? l * 128 : OOB, rsC, (idx * 2 + pw) * 768); };
            v4u gl[4];
            float s0 = 0.f, s1 = 0.f;
            unsigned bad;
            v2u gs;
            unsigned vgl = (l < 48 ? l : 0) * 16;
            unsigned aw0 = ldsb + L_CS + pw * 384 + (vgl >> 1), aw1 = ldsb + L_MB + (pw * 64 + l) * 4, aw2 = ldsb + L_SD + (pw * 64 + l) * 2;
            static_for<15>([&](auto tc) {
                constexpr int t = decltype(tc)::value;
                WAITL0();
                ARR_MARK(t + 1);
                BAR();
                SCHED();
                if constexpr (t < 4) {
                    __builtin_amdgcn_s_setprio(2);
#pragma unroll
                    for (int i = 0; i < 4; i++) cd[i & 1] = MFMA(ones, cf[(i & 1) * 2 + (i >> 1)], cd[i & 1]);
                    SCHED();
                    __builtin_amdgcn_s_setprio(0);
                    if constexpr (t < 3) cs_read(t + 1);
                    else __builtin_amdgcn_raw_buffer_store_b64((v2u){__float_as_uint(g ? cd[1][0] : cd[0][0]), tag}, rsWS, (g == 1 || g + pw == 0) ? g * 512 + t16 * 8 : OOB,
                                                               WS_G1 + ((hp * 4 + (rt >> 2)) * 192 + pw * 64 + (rt & 3) * 16) * 8, AUX_SC1);
                }
                if constexpr (t == 0) touch(v3, rsW, (~xcd & 1) * 512 * 6144);
                if constexpr (t == 12) code_pf();
                if constexpr (t == 8) {
                    waitv<0>();
                    __builtin_amdgcn_raw_buffer_store_b64((v2u){0u, tag}, rsWS, l == 0 ? 0 : OOB, WS_GC + ((hp * 2 + pw) * 16 + rt) * 8, AUX_SC1);
                }
                if constexpr (t == 8 || t == 9) {
#pragma unroll
                    for (int r = (t - 8) * 2; r < (t - 8) * 2 + 2; r++)
                        asm volatile("buffer_load_dwordx4 %0, %1, %2, %3 offen sc1" : "=v"(gl[r]) : "v"(vgl), "s"(rsWS), "s"(WS_G1 + ((hp * 4 + r) * 192 + pw * 96) * 8) : "memory");
                }
                if constexpr (t == 6) {
                    asm volatile("buffer_load_dwordx2 %0, %1, %2, %3 offen sc1" : "=v"(gs) : "v"(l * 8), "s"(rsWS), "s"(WS_G0 + (rt * 128 + pw * 64) * 8) : "memory");
                }
                if constexpr (t == 11) {
                    waitv<0>();
                    PIN(gs);
                    bad = gs[1] ^ tag;
#pragma unroll
                    for (int r = 0; r < 4; r++) {
                        PIN(gl[r]);
                        bad |= (gl[r][1] ^ tag) | (gl[r][3] ^ tag);
                        s0 += __uint_as_float(gl[r][0]); s1 += __uint_as_float(gl[r][2]);
                    }
                    PIN(s0); PIN(s1); PIN(bad);
                }
                if constexpr (t == 12) {
                    if (RARE(!__all(bad == 0))) {
                        s0 = 0.f; s1 = 0.f;
#pragma nounroll
                        for (unsigned r = 0; r < 4; r++) {
                            unsigned a[2];
                            sweep<2>(rsWS, vgl, WS_G1 + ((hp * 4 + r) * 192 + pw * 96) * 8, 8, tag, a);
                            s0 += __uint_as_float(a[0]); s1 += __uint_as_float(a[1]);
                        }
                        unsigned c[1];
                        sweep<1>(rsWS, l * 8, WS_G0 + (rt * 128 + pw * 64) * 8, 0, tag, c);
                        gs[0] = c[0];
                    }
                    *(LAS f32x2*)(aw0) = (f32x2){-s0, -s1};
                    stsf(aw1, bflo(gs[0]));
                    *(LAS u16*)(aw2) = (u16)(gs[0] >> 16);
                    SCHED();
                }
                if constexpr (t == 5) k_pre();
                if constexpr (t == 6) { wl_pre(); wo_pre(); }
                if constexpr (t == 7) { PIN(vgl); PIN(aw0); PIN(aw1); PIN(aw2); }
                clears(t < 7 ? t : 7, t < 7 ? t + 1 : 7);
                if constexpr (GS && t == GT) asm volatile("buffer_load_dwordx2 %0, %1, %2, %3 offen sc1" : "=v"(gs) : "v"((l & 31) * 8), "s"(rsWS), "s"(WS_GC + (pw ? hq : hp) * 256) : "memory");
                if constexpr (GS && t == GT + 1) {
                    waitv<0>();
                    PIN(gs);
                    if (RARE(!__all(gs[1] == tag))) { unsigned c[1]; sweep<1>(rsWS, (l & 31) * 8, WS_GC + (pw ? hq : hp) * 256, 0, tag, c); }
                    SCHED();
                }
                if constexpr (t == WT) touch(l < 32 ? l * 128 : OOB, rsWO, bid * 8192 + pw * 4096);
                if constexpr (t == 13) pf(0);
                if constexpr (t == 14) pf(1);
            });
            tail16();
        }
        WAITL0();
        STAMPW(7, 4); STAMPW(7, 7);
    }
    const unsigned sFl = WS_F4 + (rt2 * 4 + (lw >> 1) + 2 * half) * 128;
    ARR_MARK(20);
    BAR();
    ARR_END();
    const unsigned wb = ldsb + L_WO + half * 65536 + l * 16;
    bf16x8 bb[2][4];
#pragma unroll
    for (int nt = 0; nt < 4; nt++) { bb[0][nt] = lds128(wb + nt * 1024); PIN(bb[0][nt]); }
    unsigned so[16], va0 = l * 16 + half * 32768, va1 = l * 16 + (half ^ 1) * 32768;
    unsigned aXw = ldsb + L_XB + w * 4096 + l * 16, aXr = ldsb + L_XB + (w ^ 4) * 4096 + l * 16, sG5 = WS_G5 + ((rt2 * 8 + lw * 2 + half) * 16 + ct) * 256;
    unsigned vG5 = g == q4 ? t16 * 16 : OOB;
#pragma unroll
    for (int st = 0; st < 16; st++) { so[st] = WS_P4 + (rt2 * 8 + lw * 2) * 32768 + ((2 * st + half + rot2) & 31) * 1024; asm volatile("" : "+s"(so[st])); }
    unsigned wt[2];
#pragma unroll
    for (int i = 0; i < 2; i++) { wt[i] = ldsb + L_WO + half * 65536 + (4 * g + q4) * 128 + (((i * 2 + (p4 >> 1)) ^ ((2 * g + (q4 >> 1)) & 3)) * 32) + (p4 & 1) * 16; PIN(wt[i]); }
    PIN(va0); PIN(va1); PIN(aXw); PIN(aXr); PIN(vG5);
    vD = kr * 16384 + (l & 7) * 16; sD = (kc * 256 + lw * 64) * 2048 + cg * 128;
    PIN(vD);
    asm volatile("" : "+s"(sG5), "+s"(sD), "+s"(rsD));
    SLEEP(SL3);
    DLY(18);
    poll16(rsWS, sFl, tag, l);
    DLY(19);
    FRESH();
    SCHED();
    STAMPW(8, 0); STAMPW(8, 4);
    unsigned swc;
    f32x4 acc2[2][4];
    bf16x8 sop[2];
    {
        auto aload = [&](int st, int m2) { return LD16(m2 ? va1 : va0, so[st]); };
        constexpr int PD = 2;
        v4u af[PD][2];
#pragma unroll
        for (int i = 0; i < PD; i++) {
#pragma unroll
            for (int m2 = 1; m2 >= 0; m2--) af[i][m2] = aload(i, m2);
            SCHED();
        }
#pragma unroll
        for (int nt = 0; nt < 4; nt++) {
            const unsigned x0 = xres[nt >> 1][2 * (nt & 1)], x1 = xres[nt >> 1][2 * (nt & 1) + 1];
            acc2[0][nt] = (f32x4){bflo(x0), bfhi(x0), bflo(x1), bfhi(x1)};
            acc2[1][nt] = zero4;
        }
        unsigned m0 = p4 == 0 ? 0x3F803F80u : 0u, m1 = p4 == 1 ? ~0u : 0u;
        PIN(m0); PIN(m1);
#pragma unroll
        for (int i = 0; i < 2; i++) {
            v4u u;
#pragma unroll
            for (int j = 0; j < 4; j++) u[j] = (wrv[i][j] & m1) | m0;
            sop[i] = __builtin_bit_cast(bf16x8, u);
            PIN(sop[i]);
        }
        {
            float sw = 0.f;
#pragma unroll
            for (int i = 0; i < 8; i++) sw = dot2(wrv[i >> 2][i & 3], 0x3F803F80u, sw);
            swc = f2s(xrow_sum(sw));
            PIN(swc);
        }
        SCHED();
#pragma unroll
        for (int st = 0; st < 16; st++) {
            if (st + 1 == 14) BAR();
            if (st + 1 < 16) {
#pragma unroll
                for (int nt = 0; nt < 4; nt++) {
                    if (st + 1 < 14) bb[(st + 1) % (2)][nt] = lds128(wb + (st + 1) * 4096 + nt * 1024);
                    else bb[(st + 1) % (2)][nt] = cat8(tr_read(wt[nt >> 1] + (st + 1) * 4096 + (nt & 1) * 8), tr_read(wt[nt >> 1] + (st + 1) * 4096 + (nt & 1) * 8 + 2048));
                }
            }
#pragma unroll
            for (int mm = 0; mm < 2; mm++) {
                const int m2 = mm ^ 1;
                const bf16x8 a = __builtin_bit_cast(bf16x8, af[st % PD][m2]);
#pragma unroll
                for (int nt = 0; nt < 4; nt++) acc2[m2][nt] = MFMA(bb[st % (2)][nt], a, acc2[m2][nt]);
            }
            if (st + PD < 16) {
#pragma unroll
                for (int m2 = 1; m2 >= 0; m2--) af[st % PD][m2] = aload(PD + st, m2);
            }
#pragma unroll
            for (int i = 0; i < (st + 1 < 14 ? 4 : 8); i++) { SGB(0x8, 1); SGB(0x100, 1); }
            SCHED();
        }
    }
    STAMPW(9, 0); STAMPW(9, 4);
#pragma unroll
    for (int nt = 0; nt < 4; nt++) stsf4(aXw + nt * 1024, acc2[1][nt]);
    SCHED();
    if (half) dloads(0, 3);
    SCHED();
    WAITL0();
    if (half) __builtin_amdgcn_s_setprio(1);
    BAR();
    SCHED();
    v4u hpk[2];
    {
        f32x4 pa[4];
#pragma unroll
        for (int nt = 0; nt < 4; nt++) pa[nt] = ldsf4(aXr + nt * 1024);
        SCHED();
#pragma unroll
        for (int i = 0; i < 8; i++) {
            const f32x4 a = acc2[0][i >> 1] + pa[i >> 1];
            hpk[i >> 2][i & 3] = pk2(a[2 * (i & 1)], a[2 * (i & 1) + 1]);
        }
        const bf16x8 h0 = __builtin_bit_cast(bf16x8, hpk[0]), h1 = __builtin_bit_cast(bf16x8, hpk[1]);
        f32x4 gq = MFMA(h0, h0, zero4), sd = MFMA(sop[0], h0, zero4);
        gq = MFMA(h1, h1, gq); sd = MFMA(sop[1], h1, sd);
        float q0 = (l & 1) ? gq[1] : gq[0], q1 = (l & 1) ? gq[3] : gq[2];
        PIN(q0); PIN(q1);
        const v4u pv = {f2s(sd[0]), f2s((l & 2) ? q1 : q0), f2s(sd[1]), swc};
        DLY(20);
        ST16(pv, vG5, sG5);
        asm volatile("s_nop 1" :: "v"(pv));
    }
    __builtin_amdgcn_s_setprio(0);
    STAMPW(10, 0); STAMPW(10, 4);
    v4u wp2[8];
    auto perm2 = [&]() {
#pragma unroll
        for (int i = 16; i < 24; i++) PIN(wreg[i]);
#pragma unroll
        for (int ip = 0; ip < 4; ip++)
#pragma unroll
            for (int c = 0; c < 4; c++) {
                const unsigned a = wreg[16 + 2 * ip][c], b = wreg[16 + 2 * ip + 1][c];
                wp2[2 * c][ip] = __builtin_amdgcn_perm(b, a, 0x05040100);
                wp2[2 * c + 1][ip] = __builtin_amdgcn_perm(b, a, 0x07060302);
            }
#pragma unroll
        for (int c = 0; c < 8; c++) PIN(wp2[c]);
    };
    const unsigned mt = rt2 * 8 + lw * 2 + half;
    {
        v4u pv[4];
        unsigned sK = WS_GK + (ct * 128 + mt) * 4, vK = l == 0 ? 0 : OOB, rk = 2047u - R2;
        asm volatile("" : "+s"(sK));
        PIN(vK); PIN(rk);
        unsigned sS = WS_G5 + mt * 4096;
        asm volatile("" : "+s"(sS));
        SLEEP(SL4);
        DLY(21);
        __builtin_amdgcn_s_setprio(1);
        float T[4];
        sweepn<4>([&](int k) { return LD16(l * 16 + k * 1024, sS); }, pv, [&]() {
            const f32x4 ps = (__builtin_bit_cast(f32x4, pv[0]) + __builtin_bit_cast(f32x4, pv[1])) + (__builtin_bit_cast(f32x4, pv[2]) + __builtin_bit_cast(f32x4, pv[3]));
            const float r = xrow_scatter<false>(ps[0], ps[1], ps[2], ps[3]);
            xrow_gather(r, T);
            return r;
        });
        STAMPW(21, 0); STAMPW(21, 4);
        const float mean = T[0] * (1.0f / 1024.0f), var = fmaxf(T[1] * (1.0f / 1024.0f) - mean * mean, 0.f);
        const unsigned sg = pk2(mean, __builtin_amdgcn_sqrtf(rbf(rbf(var) + 1e-5f)));
        const float mb2 = bflo(sg), isd2 = __builtin_amdgcn_rcpf(bfhi(sg));
        const float rsc = (T[2] - mb2 * T[3]) * isd2;
        const unsigned rb = pk2(rsc, rsc);
        const unsigned key = ((rb ^ ((unsigned)((int)rb >> 31) | 0x80000000u)) & 0xffff0000u) | rk;
        unsigned kb = umx(key, dppu<0x128>(key));
        kb = umx(kb, dppu<0x124>(kb)); kb = umx(kb, dppu<0x122>(kb)); kb = umx(kb, dppu<0x121>(kb));
        DLY(22);
        ST4(kb, vK, sK);
        SCHED();
        __builtin_amdgcn_s_setprio(0);
        if (half) { dloads(3, 8); SCHED(); }
        const bool cand = key == kb;
        v4u e[2], outv[2];
#pragma unroll
        for (int i = 0; i < 8; i++) {
            const float h0 = bflo(hpk[i >> 2][i & 3]), h1 = bfhi(hpk[i >> 2][i & 3]);
            const unsigned d = pk2(h0 - mb2, h1 - mb2);
            e[i >> 2][i & 3] = pk2(bflo(d) * isd2, bfhi(d) * isd2);
        }
        DLY(24);
        ST16(e[0], cand ? g * 16 : OOB, WS_C + (mt * 16 + ct) * 128);
        ST16(e[1], cand ? g * 16 + 64 : OOB, WS_C + (mt * 16 + ct) * 128);
        SCHED();
        STAMPW(22, 0); STAMPW(22, 4);
#pragma unroll
        for (int i = 0; i < 8; i++)
            outv[i >> 2][i & 3] = pk2(bflo(hpk[i >> 2][i & 3]) + bflo(e[i >> 2][i & 3]), bfhi(hpk[i >> 2][i & 3]) + bfhi(e[i >> 2][i & 3]));
        if (cand) {
#pragma unroll
            for (int i = 0; i < 2; i++) { *(LAS v4u*)(ldsb + L_STASH + w * 256 + i * 64 + g * 16) = hpk[i]; *(LAS v4u*)(ldsb + L_STASH + w * 256 + 128 + i * 64 + g * 16) = e[i]; }
        }
#pragma unroll
        for (int i = 0; i < 2; i++) *(LAS v4u*)(ldsb + L_OUT + (lw * 2 + half) * 2048 + t16 * 128 + (((i * 4 + g) ^ (l & 7)) * 16)) = outv[i];
    }
    const rsrc_t rsO = __builtin_amdgcn_make_buffer_rsrc((void*)OUT, 0, 2048 * 1024 * 2, 0x00020000);
    unsigned aG = ldsb + L_WO + 512;
    PIN(aG);
    unsigned vX = (2 * g + (l & 1)) * 16, aP = (2 * g + (l & 1)) * 32, aR1 = ldsb + L_RED + lw * 4352 + l * 16, vl4 = l * 4;
    unsigned aR2 = ldsb + L_RED + ((lw & 1) * 2 + ((l >> 2) & 1)) * 1088 + kr * 16 + p4 * 4;
    unsigned sX = WS_C + kq * 512 + lw * 128, sG8w = WS_G8 + ((kq * 2 + lw) * 3584 + (lw ? cg1u : cg1) * 64) * 4, sG8r = WS_G8 + (kc * 256 + lw * 64) * 4;
    unsigned sG9 = WS_G9 + (cg * 14 + kc) * 256;
    unsigned aR3 = aR1 + 17408 - lw * 2176;
    PIN(vX); PIN(aP); PIN(aR1); PIN(aR2); PIN(aR3); PIN(vl4);
    asm volatile("" : "+s"(sX), "+s"(sG8w), "+s"(sG8r), "+s"(sG9));
    SCHED();
    unsigned gkv;
    if (KD) {
        unsigned sK = WS_GK + (bid & 15) * 512;
        asm volatile("" : "+s"(sK));
        if (half) {
            perm2();
            v2u hb[1];
            __builtin_amdgcn_s_setprio(1);
            SLEEP(SL5H);
            gkv = wave_maxu(sweepz<1>([&](int) { return ldg(rsWS, l * 8, sK); }, hb));
            stsu(aG, gkv);
        } else {
            WAITL0();
            BAR();
            gkv = ldsu(aG);
        }
    } else {
    if (w == 1) {
        unsigned sK = WS_GK + (bid & 15) * 512;
        v2u hb[1];
        asm volatile("" : "+s"(sK));
        SLEEP(SL5);
        DLY(23);
        stsu(aG, wave_maxu(sweepz<1>([&](int) { return ldg(rsWS, l * 8, sK); }, hb)));
        STAMPR(23, 2);
    }
    if (half) perm2();
    STAMPW(24, 7);
    WAITL0();
    if (half) __builtin_amdgcn_s_setprio(1);
    BAR();
    STAMPW(11, 0); STAMPW(11, 4);
    gkv = ldsu(aG);
    }
    if (w < 4) {
        if (bid == 0 && tid == 0) __hip_atomic_store((gu32*)(ws + WS_EPOCH), tag, RLX_AGENT);
        v4u o2[4];
        unsigned vo[4];
#pragma unroll
        for (int i = 0; i < 4; i++) o2[i] = *(const LAS v4u*)(ldsb + L_OUT + w * 4096 + i * 1024 + kr * 128 + (((l & 7) ^ kr) * 16));
#pragma unroll
        for (int i = 0; i < 4; i++) { const unsigned R = mt * 16 + i * 8 + kr; vo[i] = 2047u - R != (gkv & 2047u) ? R * 2048 + (l & 7) * 16 : OOB; PIN(vo[i]); PIN(o2[i]); }
#pragma unroll
        for (int s = 0; s < 3; s++) {
            if (s > KD) { BAR(); SLEEP(Q1); }
            SCHED();
#pragma unroll
            for (int i = 0; i < 4; i++)
                if (((PM >> (4 * i)) & 15) == s) __builtin_amdgcn_raw_buffer_store_b128(o2[i], rsO, vo[i], ct * 128, AUX_SC1);
            SCHED();
        }
        STAMPW(12, 0);
        return;
    }

    auto gemv = [&](const v4u& x, const v4u* wp, int n, unsigned a) {
        f32x4 d[16], o[4];
#pragma unroll
        for (int c = 0; c < n + 4; c++) {
            if (c < n) d[c] = MFMA(__builtin_bit_cast(bf16x8, x), __builtin_bit_cast(bf16x8, wp[c]), zero4);
            SCHED();
            if (c >= 4) {
                const int j = c - 4;
                float t = d[j][0] + dppf<0x128>(d[j][1]);
                asm volatile("" : "+v"(t) : "v"(d[j]));
                o[j >> 2][j & 3] = t;
                if ((j & 3) == 3) stsf4(a + (j >> 2) * 1088, o[j >> 2]);
            }
            SCHED();
        }
        return o[0][0];
    };
    auto sum4 = [&](unsigned a, unsigned stride) {
        float u = ldsf(a) + ldsf(a + stride), v = ldsf(a + 2 * stride) + ldsf(a + 3 * stride);
        PIN(u); PIN(v);
        return u + v;
    };
    if (ffn) {
        v4u xq[1];
        const unsigned vx = ((~gkv & 0x7f0u) << 7) + vX;
        sweepn<1>([&](int) { return LD16(vx, sX); }, xq, [&]() { return gemv(xq[0], &wp1[0][0], 16, aR1); });
        STAMPW(13, 4);
    }
    DLY(2);
    WAITL0();
    BAR();
    if (ffn) {
        if (lw < 2) ST4(f2s(sum4(aR2, 4352)), vl4, sG8w);
        STAMPW(14, 4);
        unsigned pv[8];
        v4u ap;
        SLEEP(SL6);
        sweepn<8>([&](int k) { return LD4(vl4, sG8r + k * 3584 * 4); }, pv, [&]() {
            const float gg = rbf((__uint_as_float(pv[0]) + __uint_as_float(pv[2])) + (__uint_as_float(pv[4]) + __uint_as_float(pv[6])));
            const float uu = rbf((__uint_as_float(pv[1]) + __uint_as_float(pv[3])) + (__uint_as_float(pv[5]) + __uint_as_float(pv[7])));
            const float act = rbf(gg * __builtin_amdgcn_rcpf(1.0f + __expf(-gg))) * uu;
            const unsigned apk = pk2(act, dppf<0xB1>(act));
#pragma unroll
            for (int ip = 0; ip < 4; ip++) ap[ip] = __builtin_amdgcn_ds_bpermute(aP + ip * 8, apk);
            return act;
        });
        STAMPW(15, 4);
        gemv(ap, wp2, 8, aR3);
    }
    WAITL0();
    BAR();
    if (lw != 0) return;
    if (ffn) ST4(f2s(sum4(aR2 + 17408, 2176)), vl4, sG9);
    SCHED();
    STAMPW(16, 4);
    const unsigned gkey = rfl(gkv), srow = 2047u - (gkey & 2047u);
    if ((srow >> 7) == rt2) {
        const float rs = __uint_as_float(((gkey & 0x80000000u) ? gkey ^ 0x80000000u : ~gkey) & 0xffff0000u);
        float sig = rbf(1.0f / (1.0f + __expf(-rs)));
        const unsigned sb = ldsb + L_STASH + (((srow >> 5) & 3) + ((srow >> 4) & 1) * 4) * 256 + (t16 * 4 + g) * 2;
        float hc = lds_bf(sb), lc = lds_bf(sb + 128);
        unsigned vo = (t16 * 4 + g) * 2, so_ = srow * 2048 + ct * 128, vy = l < 32 ? l * 16 + 3072 : OOB, sY = WS_G9 + ct * 3584;
        PIN(sig); PIN(hc); PIN(lc); PIN(vo); PIN(vy);
        asm volatile("" : "+s"(so_), "+s"(sY));
        v4u yp[4];
        float y;
        if (SLN && !ffn) SLEEP(SLN);
        SLEEP(SL7);
        sweepn<4>([&](int k) { return LD16(k < 3 ? l * 16 + k * 1024 : vy, sY); }, yp, [&]() {
            const f32x4 ys = (__builtin_bit_cast(f32x4, yp[0]) + __builtin_bit_cast(f32x4, yp[1])) + (__builtin_bit_cast(f32x4, yp[2]) + __builtin_bit_cast(f32x4, yp[3]));
            y = xrow_scatter<false>(ys[0], ys[1], ys[2], ys[3]);
            return y;
        });
        DLY(1);
        __builtin_amdgcn_raw_buffer_store_b16((u16)(pk2(hc + rbf(lc + rbf(rbf(y) * sig)), 0.f) & 0xffffu), rsO, vo, so_, 0);
        STAMPW(17, 4);
    }
}
__device__ __attribute__((used, noinline)) void fk_tail() { asm volatile(".Lfk_end:"); }

extern "C" void kernel_launch(void* const* d_in, const int* in_sizes, int n_in, void* d_out, int out_size, void* d_ws,
                              size_t ws_size, hipStream_t stream) {
    (void)in_sizes; (void)n_in; (void)out_size; (void)ws_size;
    (void)hipFuncSetAttribute((const void*)fused_kernel, hipFuncAttributeMaxDynamicSharedMemorySize, LDS_BYTES);
    fused_kernel<<<256, 512, LDS_BYTES, stream>>>((const u16*)d_in[0], (const u16*)d_in[1], (const u16*)d_in[2], (const u16*)d_in[3],
                                                  (const u16*)d_in[5], (const u16*)d_in[6], (const u16*)d_in[7], (const u16*)d_in[8],
                                                  (u16*)d_out, (char*)d_ws);
}
```

```cpp
#include <hip/hip_runtime.h>
#include <stdint.h>
#include <utility>

typedef unsigned short u16;
typedef unsigned long long u64;
typedef short bf16x8 __attribute__((ext_vector_type(8)));
typedef short s16x4 __attribute__((ext_vector_type(4)));
typedef float f32x4 __attribute__((ext_vector_type(4)));
typedef float f32x2 __attribute__((ext_vector_type(2)));
typedef __bf16 bf16x2 __attribute__((ext_vector_type(2)));
typedef unsigned v4u __attribute__((ext_vector_type(4)));
typedef unsigned v2u __attribute__((ext_vector_type(2)));
#define LAS __attribute__((address_space(3)))
#define GAS __attribute__((address_space(1)))
#define CAS __attribute__((address_space(4)))
typedef GAS u64 gu64;
typedef GAS unsigned gu32;
typedef __amdgpu_buffer_rsrc_t rsrc_t;
#define DI __device__ __forceinline__
#define RLX_AGENT __ATOMIC_RELAXED, __HIP_MEMORY_SCOPE_AGENT
#define BAR() __builtin_amdgcn_s_barrier()
#define SCHED() __builtin_amdgcn_sched_barrier(0)
#define SGB(m, n) __builtin_amdgcn_sched_group_barrier((m), (n), 0)
#define WAITL0() asm volatile("s_waitcnt lgkmcnt(0)" ::: "memory")
#define WL0() __builtin_amdgcn_s_waitcnt(0xc07f)
#define MFMA(a, b, c) __builtin_amdgcn_mfma_f32_16x16x32_bf16((a), (b), (c), 0, 0, 0)
#define ORDER_BEHIND_POLL() __builtin_amdgcn_fence(__ATOMIC_ACQUIRE, "wavefront")
#define AUX_SC1 16
template <int N> DI void waitv() { asm volatile("s_waitcnt vmcnt(%0)" ::"i"(N) : "memory"); }

#define WS_EPOCH 0
#define WS_G0 0x1000
#define WS_G1 0x8000
#define WS_F4 0x78000
#define WS_GC 0x70000
#define WS_G3 0x80000
#define WS_GZ 0xC0000
#define SENT 0xFFFFFFFFu
#define WS_TL 0x100000
#define WS_G5 0x100000
#define WS_G8 0x180000
#define WS_G9 0x1A0000
#define WS_GK 0x1B4000
#define WS_C 0x1C0000
#define WS_P2 0x200000
#define P2_STRIDE 0x2180
#define WS_P4 0x700000
#define WS_USED 0xB00000
#define WS_STAMP 0x1000000

#define STAGE_B 40960
#define LDS_BYTES 163840
#define L_WO 32768
#define L_MB 161792
#define L_WPHI (L_MB - 8192)
#define L_SD (L_MB + 512)
#define L_CS (L_MB + 1024)
#define L_V L_WO
#define L_KH (L_WO + 65536)
#define L_XB 0
#define L_STASH (L_WO + 1024)
#define L_OUT (L_WO + 8192)
#define L_RED 4096

#define SPIN_CAP 20000u
#define W_AUX 2
#define LOG2E 1.4426950408889634f

DI unsigned rfl(unsigned x) { return __builtin_amdgcn_readfirstlane(x); }
DI float bf2f(unsigned u) { return __uint_as_float(u << 16); }
DI float bflo(unsigned u) { return __uint_as_float(u << 16); }
DI float bfhi(unsigned u) { return __uint_as_float(u & 0xffff0000u); }
DI unsigned pk2(float lo, float hi) { f32x2 v = {lo, hi}; return __builtin_bit_cast(unsigned, __builtin_convertvector(v, bf16x2)); }
DI unsigned nsent(unsigned x) { return __builtin_elementwise_min(x, SENT - 1); }
DI unsigned umx(unsigned a, unsigned b) { return __builtin_elementwise_max(a, b); }
DI float rbf(float f) { return bflo(pk2(f, f) & 0xffffu); }
DI bf16x8 mk8(unsigned a, unsigned b, unsigned c, unsigned d) { v4u v = {a, b, c, d}; return __builtin_bit_cast(bf16x8, v); }
DI void dma16(unsigned ldsaddr, unsigned voff, rsrc_t rs, unsigned soff) {
    asm volatile("s_mov_b32 m0, %0\n\ts_nop 0\n\tbuffer_load_dwordx4 %1, %2, %3 offen lds" ::"s"(ldsaddr), "v"(voff), "s"(rs), "s"(soff) : "memory");
}
DI void dma16x4(unsigned ldsaddr, const unsigned* v, rsrc_t rs, unsigned soff) {
    asm volatile("s_mov_b32 m0, %0\n\ts_nop 0\n\tbuffer_load_dwordx4 %1, %5, %6 offen lds\n\tbuffer_load_dwordx4 %2, %5, %6 offen offset:1024 lds\n\t"
                 "buffer_load_dwordx4 %3, %5, %6 offen offset:2048 lds\n\tbuffer_load_dwordx4 %4, %5, %6 offen offset:3072 lds"
                 ::"s"(ldsaddr), "v"(v[0]), "v"(v[1]), "v"(v[2]), "v"(v[3]), "s"(rs), "s"(soff) : "memory");
}
DI void touch(unsigned voff, rsrc_t rs, unsigned soff) { asm volatile("buffer_load_dword v255, %0, %1, %2 offen" ::"v"(voff), "s"(rs), "s"(soff) : "memory", "v255"); }
DI v4u ld16_asm(unsigned voff, rsrc_t rs, unsigned soff) {
    v4u r; asm volatile("buffer_load_dwordx4 %0, %1, %2, %3 offen" : "=v"(r) : "v"(voff), "s"(rs), "s"(soff) : "memory"); return r;
}
#define PIN(x) asm volatile("" : "+v"(x))
DI s16x4 tr_read(unsigned a) { return __builtin_amdgcn_ds_read_tr16_b64_v4i16((LAS s16x4*)(a)); }
DI bf16x8 cat8(s16x4 a, s16x4 b) { return __builtin_shufflevector(a, b, 0, 1, 2, 3, 4, 5, 6, 7); }
DI bf16x8 lds128(unsigned a) { return *(const LAS bf16x8*)(a); }
DI f32x4 ldsf4(unsigned a) { return *(const LAS f32x4*)(a); }
DI float ldsf(unsigned a) { return *(const LAS float*)(a); }
DI unsigned ldsu(unsigned a) { return *(const LAS unsigned*)(a); }
DI void stsf4(unsigned a, f32x4 v) { *(LAS f32x4*)(a) = v; }
DI void stsf(unsigned a, float v) { *(LAS float*)(a) = v; }
DI void stsu(unsigned a, unsigned v) { *(LAS unsigned*)(a) = v; }
template <int C> DI float dppf(float x) { return __uint_as_float(__builtin_amdgcn_update_dpp(0, __float_as_uint(x), C, 0xf, 0xf, true)); }
template <int C> DI unsigned dppu(unsigned x) { return __builtin_amdgcn_update_dpp(0, x, C, 0xf, 0xf, true); }
DI float max2f(float a, float b) { float r; asm("v_max_f32_e32 %0, %1, %2" : "=v"(r) : "v"(a), "v"(b)); return r; }
DI float max3f(float a, float b, float c) { float r; asm("v_max3_f32 %0, %1, %2, %3" : "=v"(r) : "v"(a), "v"(b), "v"(c)); return r; }
DI float row_sum(float x) { x += dppf<0x128>(x); x += dppf<0x124>(x); x += dppf<0x122>(x); x += dppf<0x121>(x); return x; }
DI float xrow_sum(float x) {
    auto a = __builtin_amdgcn_permlane16_swap(__float_as_uint(x), __float_as_uint(x), false, false);
    x = __uint_as_float(a[0]) + __uint_as_float(a[1]);
    auto b = __builtin_amdgcn_permlane32_swap(__float_as_uint(x), __float_as_uint(x), false, false);
    return __uint_as_float(b[0]) + __uint_as_float(b[1]);
}
DI unsigned wave_maxu(unsigned x) {
    x = umx(x, dppu<0x128>(x)); x = umx(x, dppu<0x124>(x)); x = umx(x, dppu<0x122>(x)); x = umx(x, dppu<0x121>(x));
    auto a = __builtin_amdgcn_permlane16_swap(x, x, false, false);
    x = umx(a[0], a[1]);
    auto b = __builtin_amdgcn_permlane32_swap(x, x, false, false);
    return umx(b[0], b[1]);
}
DI float nmax3f(float a, float b, float c) { float r; asm("v_max3_f32 %0, -%1, -%2, -%3" : "=v"(r) : "v"(a), "v"(b), "v"(c)); return r; }
DI float nmax2f(float a, float b) { float r; asm("v_max_f32_e64 %0, -%1, -%2" : "=v"(r) : "v"(a), "v"(b)); return r; }
template <bool MAX> DI float xrow_scatter(float a, float b, float c, float d) {
    const auto s = __builtin_amdgcn_permlane16_swap(__float_as_uint(a), __float_as_uint(b), false, false);
    const auto t = __builtin_amdgcn_permlane16_swap(__float_as_uint(c), __float_as_uint(d), false, false);
    float u = MAX ? max2f(__uint_as_float(s[0]), __uint_as_float(s[1])) : __uint_as_float(s[0]) + __uint_as_float(s[1]);
    float v = MAX ? max2f(__uint_as_float(t[0]), __uint_as_float(t[1])) : __uint_as_float(t[0]) + __uint_as_float(t[1]);
    if (MAX) asm volatile("s_nop 1" : "+v"(u), "+v"(v));
    const auto r = __builtin_amdgcn_permlane32_swap(__float_as_uint(u), __float_as_uint(v), false, false);
    return MAX ? max2f(__uint_as_float(r[0]), __uint_as_float(r[1])) : __uint_as_float(r[0]) + __uint_as_float(r[1]);
}
DI void xrow_gather(float w, float (&o)[4]) {
    const auto r = __builtin_amdgcn_permlane32_swap(__float_as_uint(w), __float_as_uint(w), false, false);
    const auto s = __builtin_amdgcn_permlane16_swap(r[0], r[0], false, false);
    const auto t = __builtin_amdgcn_permlane16_swap(r[1], r[1], false, false);
    o[0] = __uint_as_float(s[0]); o[1] = __uint_as_float(s[1]); o[2] = __uint_as_float(t[0]); o[3] = __uint_as_float(t[1]);
}
struct NoHook { DI void operator()(int, float&) const {} DI void operator()(int, float&, float&) const {} };
struct NoPut { DI void operator()(int, const bf16x8&) const {} };
template <bool MUL, class H = NoHook, class P = NoPut> DI void softmax2(const f32x4 (&p)[2][2], float rs0, float rs1, bool hi, bf16x8 (&out)[4], H hook = H(), P put = P()) {
    float m[4];
#pragma unroll
    for (int i = 0; i < 2; i++) {
        const f32x4 &p0 = p[i][0], &p1 = p[i][1];
        m[2 * i] = max3f(max3f(p0[0], p0[1], p0[2]), max3f(p0[3], p1[0], p1[1]), max2f(p1[2], p1[3]));
        m[2 * i + 1] = max3f(nmax3f(p0[0], p0[1], p0[2]), nmax3f(p0[3], p1[0], p1[1]), nmax2f(p1[2], p1[3]));
    }
    asm volatile("s_nop 1" : "+v"(m[0]), "+v"(m[1]), "+v"(m[2]), "+v"(m[3]));
    hook(1, m[3]);
    const float rsr = hi ? rs1 : rs0;
    float a[4];
    xrow_gather(xrow_scatter<true>(m[0], m[1], m[2], m[3]) * (rsr * -LOG2E), a);
    hook(10, a[0]);
    f32x2 e[4][4];
    float s[4];
#pragma unroll
    for (int k = 0; k < 4; k++) {
        if (k == 2) hook(2, a[2], s[1]);
        const float sc = ((k >> 1) ? rs1 : rs0) * ((k & 1) ? -LOG2E : LOG2E);
#pragma unroll
        for (int j = 0; j < 4; j++) {
            const f32x4& q = p[k >> 1][j >> 1];
            const f32x2 t = __builtin_elementwise_fma((f32x2){q[2 * (j & 1)], q[2 * (j & 1) + 1]}, (f32x2){sc, sc}, (f32x2){a[k], a[k]});
            e[k][j] = (f32x2){__builtin_amdgcn_exp2f(t[0]), __builtin_amdgcn_exp2f(t[1])};
        }
        const f32x2 h = (e[k][0] + e[k][1]) + (e[k][2] + e[k][3]);
        s[k] = h[0] + h[1];
        if (k == 3) hook(3, s[k]);
    }
    float iv = __builtin_amdgcn_rcpf(xrow_scatter<false>(s[0], s[1], s[2], s[3]));
    if (MUL) iv *= rsr;
    xrow_gather(iv, a);
#pragma unroll
    for (int k = 0; k < 4; k++) {
        if (k == 2) hook(4, a[2]);
        const f32x2 c = {a[k], a[k]};
        const f32x2 x0 = e[k][0] * c, x1 = e[k][1] * c, x2 = e[k][2] * c, x3 = e[k][3] * c;
        out[k] = mk8(pk2(x0[0], x0[1]), pk2(x1[0], x1[1]), pk2(x2[0], x2[1]), pk2(x3[0], x3[1]));
        put(k, out[k]);
    }
}
DI float dot2(unsigned a, unsigned b, float c) {
    return __builtin_amdgcn_fdot2_f32_bf16(__builtin_bit_cast(bf16x2, a), __builtin_bit_cast(bf16x2, b), c, false);
}
DI void st_gran(char* ws, unsigned off, unsigned tag, unsigned val) {
    __hip_atomic_store((gu64*)(ws + off), ((u64)tag << 32) | val, RLX_AGENT);
}
DI v2u ldg(rsrc_t rs, unsigned voff, unsigned soff) { return __builtin_amdgcn_raw_buffer_load_b64(rs, voff, soff, AUX_SC1); }
#define RARE(c) __builtin_expect(!!(c), 0)
#define FRESH() asm volatile("" ::: "memory")
template <int N> DI void sweep(rsrc_t rs, unsigned voff, unsigned soff, unsigned S, unsigned tag, unsigned (&v)[N]) {
#pragma nounroll
    for (unsigned spins = 0;;) {
        unsigned bad = 0;
        FRESH();
#pragma unroll
        for (int k = 0; k < N; ++k) { const v2u x = ldg(rs, voff, soff + k * S); v[k] = x[0]; bad |= x[1] ^ tag; }
        if (__all(bad == 0)) break;
        if (++spins > SPIN_CAP) break;
        __builtin_amdgcn_s_sleep(1);
    }
}
template <int A, int B> DI unsigned budget(unsigned& spins) {
    unsigned r;
    asm volatile("s_add_i32 %0, %0, 1\n\ts_cmpk_lt_u32 %0, %2\n\ts_cselect_b32 %1, %3, %4" : "+s"(spins), "=s"(r) : "i"(SPIN_CAP), "i"(A), "i"(B) : "scc");
    SCHED();
    return r;
}
DI unsigned wmax(unsigned x) { return x; }
DI unsigned wmax(v2u x) { return umx(x[0], x[1]); }
DI unsigned wmax(v4u x) { return umx(umx(x[0], x[1]), umx(x[2], x[3])); }
template <int N, class T, class F> DI unsigned sweepz(F ld, T (&v)[N]) {
    unsigned spins = 0, lim, m;
#pragma nounroll
    do {
        FRESH();
#pragma unroll
        for (int k = 0; k < N; ++k) v[k] = ld(k);
        lim = budget<-2, -1>(spins);
        m = 0;
#pragma unroll
        for (int k = 0; k < N; ++k) m = umx(m, wmax(v[k]));
    } while (__any(m > lim));
    return m;
}
template <int N, class T, class F, class G> DI void sweepn(F ld, T (&v)[N], G use) {
    unsigned spins = 0;
    bool again;
#pragma nounroll
    do {
        FRESH();
#pragma unroll
        for (int k = 0; k < N; ++k) v[k] = ld(k);
        const int m = budget<3, 0>(spins);
        again = __any(__builtin_amdgcn_classf(use(), m));
    } while (again);
}
#define OOB 0x4000000u
#define LD16(v, s) __builtin_amdgcn_raw_buffer_load_b128(rsWS, (v), (s), AUX_SC1)
#define LD4(v, s) __builtin_amdgcn_raw_buffer_load_b32(rsWS, (v), (s), AUX_SC1)
#define ST16(x, v, s) __builtin_amdgcn_raw_buffer_store_b128((x), rsWS, (v), (s), AUX_SC1)
#define ST4(x, v, s) __builtin_amdgcn_raw_buffer_store_b32((x), rsWS, (v), (s), AUX_SC1)
#define SLEEP(n) __builtin_amdgcn_s_sleep(n)
DI void poll16(rsrc_t rs, unsigned off, unsigned tag, int l) {
    unsigned spins = 0, lim, bad;
#pragma nounroll
    do {
        FRESH();
        const v2u x = ldg(rs, (l & 15) * 8, off);
        lim = budget<0, -1>(spins);
        bad = (x[1] ^ tag) | (x[0] ^ tag ^ 0x5bd1e995u);
    } while (__any(bad > lim));
    ORDER_BEHIND_POLL();
}
#define STAMPR(i, role)
#define STAMP(i)
#define STAMPW(i, ww)

#define ARR_DECL
#define KATTR
#define ARR_MARK(i)
#define ARR_END()
template <class F, int... Is> DI void static_for_impl(F&& f, std::integer_sequence<int, Is...>) { (f(std::integral_constant<int, Is>{}), ...); }
template <int N, class F> DI void static_for(F&& f) { static_for_impl(f, std::make_integer_sequence<int, N>{}); }

DI s16x4 bq(unsigned b0, unsigned b1, int j, int i) {
    const int ni = (j + 2) % 6;
    return tr_read(((ni & 1) ? b1 : b0) + (ni >> 1) * 8192 + (i >> 1) * 4096 + (i & 1) * 512);
}
template <class F> DI void group(f32x4 (&acc)[4][6], const bf16x8 (&A)[2][4], const bf16x8 (&B)[2], int j, F gap) {
    const int ni = (j + 2) % 6;
#pragma unroll
    for (int i = 0; i < 8; i++) {
        const int mi = i >> 1, ks = i & 1;
        if (ni < 4) asm volatile("v_mfma_f32_16x16x32_bf16 %0, %1, %2, %0" : "+v"(acc[mi][ni]) : "v"(B[ks]), "v"(A[ks][mi]));
        else asm volatile("v_mfma_f32_16x16x32_bf16 %0, %1, %2, %0" : "+v"(acc[mi][ni]) : "v"(A[ks][mi]), "v"(B[ks]));
        gap(i);
        SCHED();
    }
}
struct ImgPut {
    const unsigned (&ia)[4];
    DI void operator()(int k, const bf16x8& f) const {
        const v4u u = __builtin_bit_cast(v4u, f);
        *(LAS v2u*)(ia[2 * (k & 1)] + (k >> 1) * 2048) = (v2u){u[0], u[1]};
        *(LAS v2u*)(ia[2 * (k & 1) + 1] + (k >> 1) * 2048) = (v2u){u[2], u[3]};
        SCHED();
    }
};
DI float lds_bf(unsigned a) { return bf2f(*(const LAS u16*)(a)); }
#define MFMA_SETTLE2(p) asm volatile("s_nop 7\n\ts_nop 1" : "+v"(p[0][0]), "+v"(p[0][1]), "+v"(p[1][0]), "+v"(p[1][1]))

__global__ KATTR __launch_bounds__(512, 2) void fused_kernel(const u16* __restrict__ X, const u16* __restrict__ WQKV, const u16* __restrict__ WPHI,
                                                       const u16* __restrict__ WO, const u16* __restrict__ WR, const u16* __restrict__ WG,
                                                       const u16* __restrict__ WU, const u16* __restrict__ WD, u16* __restrict__ OUT,
                                                       char* __restrict__ ws) {
    extern __shared__ __attribute__((aligned(16))) char lds[];
    u64 pc0; unsigned clen;
    const int tid = threadIdx.x, l = tid & 63;
    asm volatile("s_getpc_b64 %0\n.Lfk_beg:\n\ts_lshr_b32 %1, %3, 3\n\tv_and_b32 v255, 0x1e0, %2\n\tv_add_lshl_u32 v255, v255, %1, 7\n\ts_mov_b32 %1, .Lfk_end-.Lfk_beg\n\t"
                 "v_min_u32 v255, %1, v255\n\tglobal_load_dword v255, v255, %0" : "=&s"(pc0), "=&s"(clen) : "v"(tid), "s"(blockIdx.x) : "v255", "scc");
    SCHED();
    const unsigned w = rfl(tid >> 6);
    const unsigned bid = blockIdx.x, xcd = bid & 7, idx = bid >> 3;
    const unsigned rt = (xcd & 1) * 8 + (idx & 7), hp = (xcd >> 1) * 4 + (idx >> 3);
    const unsigned i16 = idx & 15, rt2 = xcd * 2 + (idx >> 4), ct = i16 == 4 ? 11 : i16 == 11 ? 4 : i16;
    const unsigned hq = (hp + 4) & 15;
    const unsigned rot = (rt >> 2) * 4;
    const unsigned rot2 = (2 * (rt2 + ct)) & 31;
    const unsigned ldsb = (unsigned)(size_t)lds;
    const int g = l >> 4, q4 = (l & 15) >> 2, p4 = l & 3, t16 = l & 15;
    const unsigned kr = l >> 3;
    asm volatile("" ::"s"(X), "s"(WQKV), "s"(WPHI), "s"(WO), "s"(WR), "s"(WG), "s"(WU), "s"(WD), "s"(OUT), "s"(ws));
    unsigned tag;
#define READ_TAG() do { SCHED(); tag = *(const volatile CAS unsigned*)(ws + WS_EPOCH) + 1u; asm volatile("" : "+s"(tag)); } while (0)
    const rsrc_t rsWS = __builtin_amdgcn_make_buffer_rsrc((void*)ws, 0, WS_USED, 0x00020000);
    const rsrc_t rsX = __builtin_amdgcn_make_buffer_rsrc((void*)X, 0, 2048 * 1024 * 2, 0x00020000);
    const rsrc_t rsW = __builtin_amdgcn_make_buffer_rsrc((void*)WQKV, 0, 1024 * 3072 * 2, 0x00020000);
    const unsigned vA = (kr * 1024 + (((l & 7) ^ kr) * 8)) * 2;
    const unsigned vB0 = (kr * 3072 + (((l & 7) ^ (((kr >> 1) & 1) << 1)) * 8)) * 2;
    const unsigned vB1 = (kr * 3072 + (((l & 7) ^ ((((kr >> 1) & 1) | 2) << 1)) * 8)) * 2;
    const unsigned srow1 = rt * 128 + hp * 8 + (w & 3) * 2;
    v4u xr[4];
    if (w < 4) {
#pragma unroll
        for (int i = 0; i < 4; i++) xr[i] = ld16_asm(l * 16, rsX, srow1 * 2048 + i * 1024);
    }
    {
        const unsigned vBw = (w & 1) ? vB1 : vB0;
#pragma unroll
        for (int i = 0; i < 2; i++) dma16(ldsb + (w * 2 + i) * 1024, vA, rsX, (rt * 128 + w * 16 + i * 8) * 2048 + rot * 128);
#pragma unroll
        for (int i = 0; i < 3; i++) dma16(ldsb + 16384 + i * 8192 + w * 1024, vBw, rsW, (rot * 64 + w * 8) * 6144 + (i ? hp : hq) * 128 + i * 2048);
    }
    const f32x4 zero4 = {0.f, 0.f, 0.f, 0.f};
    ARR_DECL;
    const bf16x8 ones = mk8(0x3F803F80u, 0x3F803F80u, 0x3F803F80u, 0x3F803F80u);
    const unsigned lw = w & 3;
    const bool ffn = (ct & 7) != 3;
    const unsigned fb = !ffn ? 255u : rt2 * 14 + (i16 < 3 ? i16 : i16 - 2);
    const unsigned cg = (fb + 5) & 15, kc = fb >> 4;
    const unsigned kq = fb / 56, cg1 = fb % 56, cg1u = (cg1 + 3) % 56;
    v4u wreg[24];
    STAMPW(0, 0); STAMPW(0, 4);

    const rsrc_t rsWO = __builtin_amdgcn_make_buffer_rsrc((void*)WO, 0, 1024 * 1024 * 2, 0x00020000);
    const unsigned vWO = kr * 2048 + (((l & 7) ^ (((kr >> 1) & 3) << 1)) * 16);
    unsigned vwo[4];
    auto wo_so = [&](unsigned b) { return (((2 * b & 30) + (b >> 4) + rot2) & 31) * 65536 + ct * 128; };
    const unsigned fw = (((t16 >> 1) & 3) << 2) | (((t16 >> 3) & 1) << 1) | (t16 & 1);
    unsigned ad;
    unsigned ia[4];
    bf16x8 sdv[4];
    auto pre_addr = [&]() {
#pragma unroll
        for (int k = 0; k < 4; k++) { ia[k] = ldsb + L_XB + lw * 8192 + (w >> 2) * 4096 + t16 * 128 + (((4 * k + g) ^ fw) * 8); PIN(ia[k]); }
        const unsigned rr = 4 * g + q4, fr = (((rr >> 1) & 3) << 2) | (((rr >> 3) & 1) << 1) | (rr & 1);
        ad = ldsb + L_XB + (w & 1) * 16384 + rr * 128 + ((((w >> 1) * 4 + p4) ^ fr) * 8);
        PIN(ad);
    };
    auto sd_reads = [&]() {
        unsigned sa = ldsb + L_SD + g * 8;
        PIN(sa);
#pragma unroll
        for (int s = 0; s < 4; s++) {
            const v2u a = *(const LAS v2u*)(sa + s * 64), b = *(const LAS v2u*)(sa + s * 64 + 32);
            sdv[s] = mk8(a[0], a[1], b[0], b[1]);
        }
    };
    bf16x8 v0[4], v1[4];
    auto v_reads = [&]() {
        unsigned vd = ldsb + L_V + (w & 1) * 8192 + l * 16;
        PIN(vd);
#pragma unroll
        for (int s = 0; s < 4; s++) { v0[s] = lds128(vd + s * 2048); v1[s] = lds128(vd + s * 2048 + 1024); }
    };
    auto kv_tile = [&](auto hook6) {
        const unsigned h2 = w & 1, ft = w >> 1;
        WAITL0();
        ARR_MARK(18);
        BAR();
        SCHED();
        bf16x8 pf[4];
#pragma unroll
        for (int s = 0; s < 4; s++) pf[s] = cat8(tr_read(ad + s * 4096), tr_read(ad + s * 4096 + 2048));
        SCHED();
        hook6();
        SCHED();
        f32x4 k0 = zero4, k1 = zero4, kz = zero4;
#pragma unroll
        for (int s = 0; s < 4; s++) { k0 = MFMA(pf[s], v0[s], k0); k1 = MFMA(pf[s], v1[s], k1); SCHED(); }
#pragma unroll
        for (int s = 0; s < 4; s++) kz = MFMA(pf[s], sdv[s], kz);
        const unsigned pbase = WS_P2 + (hp * 16 + rt) * P2_STRIDE;
        const v4u o0 = {pk2(k0[0], k0[1]), pk2(k0[2], k0[3]), pk2(k1[0], k1[1]), pk2(k1[2], k1[3])};
        __builtin_amdgcn_raw_buffer_store_b128(o0, rsWS, l * 16, pbase + h2 * 4096 + ft * 1024, AUX_SC1);
        asm volatile("s_nop 1" :: "v"(o0));
        __builtin_amdgcn_raw_buffer_store_b64((v2u){pk2(kz[0], kz[1]), pk2(kz[2], kz[3])}, rsWS, t16 == 0 ? g * 8 : OOB,
                                              pbase + 8192 + h2 * 128 + ft * 32, AUX_SC1);
        SCHED();
        ARR_MARK(19);
        BAR();
    };

    const bool zjob = lw == 0 && (rt & 7) == 0;
    v4u pp[4];
    auto pp_ld = [&](int k) {
        return LD16(kr * P2_STRIDE + (l & 7) * 16, WS_P2 + (hp * 16 + (k & 1) * 8) * P2_STRIDE +
                    (k < 2 ? (rt >> 2) * 2048 + (lw & 1) * 1024 + (rt & 3) * 256 + (lw >> 1) * 128 : 8192 + (rt >> 3) * 128));
    };
    auto kv_reduce = [&]() {
        unsigned v3 = (l & 8) ? OOB : (l & 7) * 16 + g * 4, res;
        unsigned s3 = WS_G3 + (hp * 4 + (rt >> 2)) * 2048 + (lw & 1) * 1024 + (rt & 3) * 256 + (lw >> 1) * 128;
        PIN(v3);
        asm volatile("" : "+s"(s3));
        auto red = [&](const v4u* p) {
            float lo = xrow_scatter<false>(bflo(p[0][0]) + bflo(p[1][0]), bflo(p[0][1]) + bflo(p[1][1]), bflo(p[0][2]) + bflo(p[1][2]), bflo(p[0][3]) + bflo(p[1][3]));
            float hi = xrow_scatter<false>(bfhi(p[0][0]) + bfhi(p[1][0]), bfhi(p[0][1]) + bfhi(p[1][1]), bfhi(p[0][2]) + bfhi(p[1][2]), bfhi(p[0][3]) + bfhi(p[1][3]));
            const float chk = lo;
            lo += dppf<0x128>(lo); hi += dppf<0x128>(hi);
            res = nsent(pk2(lo, hi));
            return chk;
        };
        SLEEP(2);
        if (RARE(zjob)) {
            unsigned rz;
            sweepn<4>(pp_ld, pp, [&]() { const float a = red(pp + 2); rz = res; return a + red(pp); });
            ST4(rz, (l & 8) ? OOB : ((l >> 2) & 1) * 64 + ((l & 1) * 2 + (g >> 1)) * 16 + ((l >> 1) & 1) * 8 + (g & 1) * 4, WS_GZ + (hp * 2 + (rt >> 3)) * 128);
        } else {
            v4u p2[2];
            sweepn<2>(pp_ld, p2, [&]() { return red(p2); });
        }
        STAMPW(25, 0);
        ST4(res, v3, s3);
    };

    const unsigned half = w >> 2;
    const unsigned R2 = rt2 * 128 + lw * 32 + half * 16 + t16;

    rsrc_t rsD = __builtin_amdgcn_make_buffer_rsrc((void*)WD, 0, 1024 * 3584 * 2, 0x00020000);
    unsigned vD, sD;
    auto dloads = [&](int i0, int i1) {
#pragma unroll
        for (int i = i0; i < i1; i++)
            wreg[16 + i] = __builtin_amdgcn_raw_buffer_load_b128(rsD, vD, sD + i * 2048, W_AUX);
        FRESH();
    };
    v4u wp1[2][8];
    auto perm1 = [&]() {
#pragma unroll
        for (int n = 0; n < 16; n++) PIN(wreg[n]);
#pragma unroll
        for (int ip = 0; ip < 4; ip++)
#pragma unroll
            for (int c = 0; c < 4; c++) {
#pragma unroll
                for (int m = 0; m < 2; m++) {
                    const unsigned a = wreg[m * 8 + 2 * ip][c], b = wreg[m * 8 + 2 * ip + 1][c];
                    wp1[m][2 * c][ip] = __builtin_amdgcn_perm(b, a, 0x05040100);
                    wp1[m][2 * c + 1][ip] = __builtin_amdgcn_perm(b, a, 0x07060302);
                }
            }
#pragma unroll
        for (int c = 0; c < 8; c++) { PIN(wp1[0][c]); PIN(wp1[1][c]); }
    };
    v4u xres[2], wrv[2];
    auto res_loads = [&]() {
#pragma unroll
        for (int i = 0; i < 2; i++) {
            xres[i] = *(const v4u*)(X + (size_t)R2 * 1024 + ct * 64 + i * 32 + 8 * g);
            wrv[i] = *(const v4u*)(WR + ct * 64 + i * 32 + 8 * g);
        }
        FRESH();
    };

    if (w < 4) {
        const unsigned wm = w & 1, hh = w >> 1;
        f32x4 acc[4][6];
        bf16x8 wf[2], wfq[2];
        float mbt[4], rst[4], csv[2];
        f32x4 mbv[4], csq[2], csk[2];
#pragma unroll
        for (int i = 0; i < 4; i++)
#pragma unroll
            for (int j = 0; j < 6; j++) { acc[i][j] = zero4; PIN(acc[i][j]); }
        {
            const unsigned aoff0 = ldsb + (wm * 64 + t16) * 128 + (((0 + g) ^ (l & 7)) * 16);
            const unsigned aoff1 = ldsb + (wm * 64 + t16) * 128 + (((4 + g) ^ (l & 7)) * 16);
            const unsigned sk = ((q4 >> 1) & 1) | ((g & 1) << 1);
            const unsigned boffA = ldsb + 16384 + (8 * g + q4) * 128 + (((2 * hh) ^ sk) * 32) + p4 * 8;
            const unsigned boffB = ldsb + 16384 + (8 * g + q4) * 128 + (((2 * hh + 1) ^ sk) * 32) + p4 * 8;
            bf16x8 A[2][4], B[3][2];
            s16x4 lo;
            unsigned a0[3], a1[3], bA[3], bB[3];
#pragma unroll
            for (int i = 0; i < 3; i++) {
                a0[i] = aoff0 + i * STAGE_B; a1[i] = aoff1 + i * STAGE_B; bA[i] = boffA + i * STAGE_B; bB[i] = boffB + i * STAGE_B;
                PIN(a0[i]); PIN(a1[i]); PIN(bA[i]); PIN(bB[i]);
            }
            unsigned aT = ldsb + L_MB + (wm * 64 + t16) * 4, aS = ldsb + L_SD + (wm * 64 + t16) * 2, aM = ldsb + L_MB + wm * 256 + g * 16;
            unsigned aC = ldsb + L_CS + hh * 128 + g * 16, aV = ldsb + L_CS + (128 + hh * 32 + t16) * 4;
            unsigned aW = ldsb + L_WPHI + hh * 2048 + (4 * g + q4) * 64 + p4 * 8, aK = ldsb + L_KH + w * 2048 + l * 16;
            PIN(aT); PIN(aS); PIN(aM); PIN(aC); PIN(aV); PIN(aW); PIN(aK);
            pre_addr();
            READ_TAG();
            waitv<5>();
            PIN(xr[0]); PIN(xr[1]); PIN(xr[2]); PIN(xr[3]);
            {
                float s[2], q[2], x0[2];
#pragma unroll
                for (int rr = 0; rr < 2; rr++) {
                    x0[rr] = bflo(rfl(xr[2 * rr][0]));
                    s[rr] = 0.f; q[rr] = 0.f;
#pragma unroll
                    for (int i = 0; i < 4; i++) {
                        const float a = bflo(xr[2 * rr][i]) - x0[rr], b = bfhi(xr[2 * rr][i]) - x0[rr];
                        const float c = bflo(xr[2 * rr + 1][i]) - x0[rr], d = bfhi(xr[2 * rr + 1][i]) - x0[rr];
                        s[rr] += (a + b) + (c + d);
                        q[rr] += (a * a + b * b) + (c * c + d * d);
                    }
                }
                const float sq = row_sum(xrow_scatter<false>(s[0], s[1], q[0], q[1]));
#define RLF(x, n) __uint_as_float(__builtin_amdgcn_readlane(__float_as_uint(x), (n)))
                const float r0 = RLF(sq, 0), r1 = RLF(sq, 16), r2 = RLF(sq, 32), r3 = RLF(sq, 48);
                const float sm = (l ? r1 : r0) * (1.0f / 1024.0f), qm = (l ? r3 : r2) * (1.0f / 1024.0f);
                const float var = fmaxf(qm - sm * sm, 0.f);
                if (l < 2) st_gran(ws, WS_G0 + (srow1 + l) * 8, tag, pk2((l ? x0[1] : x0[0]) + sm, sqrtf(rbf(rbf(var) + 1e-5f))));
            }
            waitv<1>();
            WAITL0();
            ARR_MARK(0);
            BAR();
            SCHED();
            STAMPW(1, 0);
            auto rdB = [&](int u, int j, int i) {
                if (i & 1) B[j % 3][i >> 1] = cat8(lo, bq(bA[u], bB[u], j, i)); else lo = bq(bA[u], bB[u], j, i);
            };
            auto rdA = [&](int u, int i) { A[i & 1][i >> 1] = lds128(((i & 1) ? a1[u] : a0[u]) + (i >> 1) * 2048); };
            auto head = [&](int u) {
                group(acc, A, B[0], 0, [&](int i) { rdB(u, 1 + (i >> 2), i & 3); });
                group(acc, A, B[1], 1, [&](int i) { if (i < 4) rdB(u, 3, i); });
                group(acc, A, B[2], 2, [&](int i) { if (i < 4) rdB(u, 4, i); });
                group(acc, A, B[0], 3, [&](int i) { if (i < 4) rdB(u, 5, i); });
                WL0();
            };
#pragma unroll
            for (int i = 0; i < 4; i++) rdB(0, 0, i);
#pragma unroll
            for (int i = 0; i < 8; i++) rdA(0, i);
            SCHED();
#pragma nounroll
            for (unsigned t = 0; t < 5; t++) {
#pragma unroll
                for (int u = 0; u < 3; u++) {
                    head(u);
                    ARR_MARK(1 + t * 3 + u);
                    BAR();
                    SCHED();
                    group(acc, A, B[1], 4, [&](int i) { if (i < 4) rdB((u + 1) % 3, 0, i); });
                    group(acc, A, B[2], 5, [&](int i) { rdA((u + 1) % 3, i); });
                }
            }
            unsigned kw[4][4], rsu[4];
            f32x2 kx;
            auto kpiece = [&](int n) {
                if (n > 0) { kw[((n - 1 >> 2) + 2) & 3][(n - 1) & 3] = pk2(kx[0], kx[1]); PIN(kw[((n - 1 >> 2) + 2) & 3][(n - 1) & 3]); }
                if (n < 16) {
                    const int mi = ((n >> 2) + 2) & 3, j = n & 3;
                    const f32x4 &c = acc[mi][2 + (j >> 1)], &s_ = csk[j >> 1];
                    float x0 = __builtin_fmaf(mbt[mi], s_[2 * (j & 1)], c[2 * (j & 1)]); PIN(x0);
                    float x1 = __builtin_fmaf(mbt[mi], s_[2 * (j & 1) + 1], c[2 * (j & 1) + 1]); PIN(x1);
                    kx = (f32x2){x0, x1};
                }
                if (n == 5 || n == 9) *(LAS v4u*)(aK + (n == 9) * 1024) = (v4u){kw[2 + (n == 9)][0], kw[2 + (n == 9)][1], kw[2 + (n == 9)][2], kw[2 + (n == 9)][3]};
            };
            group(acc, A, B[0], 0, [&](int i) { rdB(0, 1 + (i >> 2), i & 3); });
            group(acc, A, B[1], 1, [&](int i) {
                if (i < 4) { rdB(0, 3, i); mbt[(i + 2) & 3] = ldsf(aT + ((i + 2) & 3) * 64); }
                else if (i < 6) csk[i - 4] = ldsf4(aC + 256 + (i - 4) * 64);
                else wf[i - 6] = cat8(tr_read(aW + (i - 6) * 32), tr_read(aW + (i - 6) * 32 + 1024));
            });
            group(acc, A, B[2], 2, [&](int i) { if (i < 4) rdB(0, 4, i); else mbv[i - 4] = ldsf4(aM + (i - 4) * 64); kpiece(i); });
            group(acc, A, B[0], 3, [&](int i) {
                if (i < 4) rdB(0, 5, i); else rsu[i - 4] = *(const LAS u16*)(aS + (i - 4) * 32);
                if (i == 2 || i == 3) csv[i - 2] = ldsf(aV + (i - 2) * 64);
                kpiece(8 + i);
            });
            WL0();
            ARR_MARK(16);
            BAR();
            SCHED();
            kpiece(16);
            f32x4 p[2][2];
#pragma unroll
            for (int i = 0; i < 4; i++) p[i >> 1][i & 1] = MFMA(wf[i & 1], mk8(kw[i >> 1][0], kw[i >> 1][1], kw[i >> 1][2], kw[i >> 1][3]), zero4);
            SCHED();
            STAMPW(2, 0);
#pragma unroll
            for (int mi = 0; mi < 4; mi++) rst[mi] = bf2f(rsu[mi]);
#pragma unroll
            for (int a = 0; a < 2; a++)
#pragma unroll
                for (int dt = 0; dt < 2; dt++) {
                    const f32x4 x0 = acc[2 * a][4 + dt] + mbv[2 * a] * csv[dt], x1 = acc[2 * a + 1][4 + dt] + mbv[2 * a + 1] * csv[dt];
                    *(LAS v4u*)(aK + (L_V - L_KH) + w * 2048 + (a * 2 + dt) * 1024) = (v4u){pk2(x0[0], x0[1]), pk2(x0[2], x0[3]), pk2(x1[0], x1[1]), pk2(x1[2], x1[3])};
                }
            SCHED();
            rst[0] = __builtin_amdgcn_rcpf(rst[0]); rst[1] = __builtin_amdgcn_rcpf(rst[1]);
            bf16x8 f[4];
            struct HookC {
                decltype(v_reads)& vr; decltype(sd_reads)& sr;
                DI void operator()(int k, float& x) const { if (k == 10) { PIN(x); WAITL0(); ARR_MARK(17); BAR(); PIN(x); } if (k == 3) { PIN(x); vr(); sr(); PIN(x); } }
                DI void operator()(int, float&, float&) const {}
            };
            softmax2<true>(p, rst[0], rst[1], g >= 2, f, HookC{v_reads, sd_reads}, ImgPut{ia});
#pragma unroll
            for (int ft = 0; ft < 2; ft++) wfq[ft] = cat8(tr_read(aW + ft * 32 + 4096), tr_read(aW + ft * 32 + 5120));
            csq[0] = ldsf4(aC); csq[1] = ldsf4(aC + 64);
            STAMPW(3, 0);
            kv_tile([]() {});
            STAMPW(4, 0);
            SCHED();
            group(acc, A, B[1], 4, [&](int i) { if (i == 2 || i == 3) { rst[i] = __builtin_amdgcn_rcpf(rst[i]); PIN(rst[i]); } });
            group(acc, A, B[2], 5, [&](int) {});
            asm volatile("s_nop 7\n\ts_nop 1");
            SCHED();
        }
        bf16x8 phq[2][4];
        v4u kg[6];
        unsigned sG3 = WS_G3 + (hq * 2 + hh) * 4096, sGZ = WS_GZ + (hq * 2 + hh) * 128;
        {
            f32x4 pq[2][2][2];
#pragma unroll
            for (int mi = 0; mi < 4; mi++) {
                const f32x4 x0 = acc[mi][0] + mbt[mi] * csq[0], x1 = acc[mi][1] + mbt[mi] * csq[1];
                const bf16x8 qf = mk8(pk2(x0[0], x0[1]), pk2(x0[2], x0[3]), pk2(x1[0], x1[1]), pk2(x1[2], x1[3]));
                pq[mi >> 1][mi & 1][0] = MFMA(wfq[0], qf, zero4); pq[mi >> 1][mi & 1][1] = MFMA(wfq[1], qf, zero4);
            }
            MFMA_SETTLE2(pq[0]); MFMA_SETTLE2(pq[1]);
            softmax2<false>(pq[0], rst[0], rst[1], g >= 2, phq[0]);
#pragma unroll
            for (int i = 0; i < 4; i++) PIN(phq[0][i]);
            SCHED();
            kv_reduce();
            SCHED();
            res_loads();
            SCHED();
            BAR();
            SCHED();
            softmax2<false>(pq[1], rst[2], rst[3], g >= 2, phq[1]);
#pragma unroll
            for (int i = 0; i < 4; i++) { PIN(phq[0][i]); PIN(phq[1][i]); }
        }
        STAMPW(5, 0);
        STAMPW(26, 0);
        v2u fv = {tag ^ 0x5bd1e995u, tag};
        unsigned sF = WS_F4 + ((rt * 4 + w) * 16 + hq) * 8, vF = l == 0 ? 0 : OOB;
        {
            unsigned vO = l * 16, sO = WS_P4 + ((rt * 8 + wm * 4) * 32 + hq * 2 + hh) * 1024;
            asm volatile("" : "+s"(sO), "+s"(sF), "+s"(sG3), "+s"(sGZ));
            PIN(fv); PIN(vF);
            SLEEP(10);
            v4u ov[4];
#define PH(mi, s) phq[(mi) >> 1][2 * ((mi) & 1) + (s)]
#define KVA(s, dt) __builtin_bit_cast(bf16x8, kg[2 + 2 * (s) + (dt)])
            auto pairup = [&](v4u& a, v4u& b) {
                unsigned a2 = a[2], a3 = a[3], b0 = b[0], b1 = b[1];
                asm volatile("v_swap_b32 %0, %2\n\tv_swap_b32 %1, %3" : "+v"(a2), "+v"(a3), "+v"(b0), "+v"(b1));
                a[2] = a2; a[3] = a3; b[0] = b0; b[1] = b1;
            };
            f32x4 dn[4], n0[4], n1[4];
            float id[4];
            unsigned spins = 0, lim, m;
#pragma nounroll
            do {
                FRESH();
#pragma unroll
                for (int k = 0; k < 6; k++) { kg[k] = k >= 2 ? LD16(l * 16 + (k - 2) * 1024, sG3) : LD16(g * 16 + k * 64, sGZ); SCHED(); }
                lim = budget<-2, -1>(spins);
                m = 0;
#pragma unroll
                for (int i = 0; i < 8; i++) {
                    dn[i & 3] = MFMA(__builtin_bit_cast(bf16x8, kg[i >> 2]), PH(i & 3, i >> 2), i < 4 ? zero4 : dn[i & 3]);
                    if (i >= 1 && i < 7) { m = umx(umx(m, kg[i - 1][0]), kg[i - 1][1]); PIN(m); m = umx(umx(m, kg[i - 1][2]), kg[i - 1][3]); }
                    if (i == 5) pairup(kg[2], kg[3]);
                    if (i == 7) pairup(kg[4], kg[5]);
                    SCHED();
                }
            } while (__any(m > lim));
            STAMPW(6, 0);
#pragma unroll
            for (int mi = 0; mi <= 4; mi++) {
                if (mi < 4) {
                    n0[mi] = MFMA(KVA(0, 0), PH(mi, 0), zero4); n1[mi] = MFMA(KVA(0, 1), PH(mi, 0), zero4);
                    n0[mi] = MFMA(KVA(1, 0), PH(mi, 1), n0[mi]); n1[mi] = MFMA(KVA(1, 1), PH(mi, 1), n1[mi]);
                    id[mi] = __builtin_amdgcn_rcpf(rbf(rbf(dn[mi][0]) + 1e-6f));
                    asm volatile("" :: "v"(dn[mi]));
                }
                if (mi > 0) {
                    const f32x4 a = n0[mi - 1] * id[mi - 1], b = n1[mi - 1] * id[mi - 1];
                    ov[mi - 1] = (v4u){pk2(a[0], a[1]), pk2(a[2], a[3]), pk2(b[0], b[1]), pk2(b[2], b[3])};
                    ST16(ov[mi - 1], vO + (mi - 1) * 32768, sO);
                }
                if (mi < 4) { SGB(0x8, 1); SGB(0x2, 6); SGB(0x8, 1); SGB(0x2, 3); SGB(0x8, 1); SGB(0x2, 3); SGB(0x8, 1); SGB(0x2, 4); }
                SCHED();
            }
#pragma unroll
            for (int mi = 0; mi < 4; mi++) PIN(ov[mi]);
        }
        BAR();
        waitv<0>();
        __builtin_amdgcn_raw_buffer_store_b64(fv, rsWS, vF, sF, AUX_SC1);
        STAMPW(7, 0);
    } else {
        const unsigned pw = lw & 1;
        const rsrc_t rsG = __builtin_amdgcn_make_buffer_rsrc((void*)WG, 0, 1024 * 3584 * 2, 0x00020000);
        const rsrc_t rsU = __builtin_amdgcn_make_buffer_rsrc((void*)WU, 0, 1024 * 3584 * 2, 0x00020000);
        unsigned vwl = kr * 57344 + (l & 7) * 16;
        unsigned sg = (kq * 256 + lw * 64) * 7168 + cg1 * 128, su = (kq * 256 + lw * 64) * 7168 + cg1u * 128;
        auto wloads = [&](int n0, int n1) {
#pragma unroll
            for (int n = n0; n < n1; n++)
                asm volatile("buffer_load_dwordx4 %0, %1, %2, %3 offen nt" : "=v"(wreg[n]) : "v"(vwl), "s"(n < 8 ? rsG : rsU), "s"((n < 8 ? sg : su) + (n & 7) * 7168));
        };
        auto repack = [&](unsigned gI) {
            const unsigned s2 = (2 * g + (q4 >> 1)) & 3;
            bf16x8 f[4];
#pragma unroll
            for (int nt = 0; nt < 4; nt++) {
                const unsigned a = ldsb + L_WO + gI * 4096 + (4 * g + q4) * 128 + ((((nt >> 1) * 2 + (p4 >> 1)) ^ s2) * 32) + (p4 & 1) * 16 + (nt & 1) * 8;
                f[nt] = cat8(tr_read(a), tr_read(a + 2048));
            }
            WAITL0();
#pragma unroll
            for (int nt = 0; nt < 4; nt++) *(LAS bf16x8*)(ldsb + L_WO + gI * 4096 + nt * 1024 + l * 16) = f[nt];
        };
        unsigned aK, aW, aS;
        auto k_pre = [&]() {
            pre_addr();
            aK = ldsb + L_KH + lw * 2048 + l * 16; aW = ldsb + L_WPHI + (lw >> 1) * 2048 + (4 * g + q4) * 64 + p4 * 8; aS = ldsb + L_SD + (pw * 64 + 32 + t16) * 2;
            PIN(aK); PIN(aW); PIN(aS);
        };
        auto kfeat = [&](auto hook) {
            bf16x8 wf[2];
            float rst[2];
#pragma unroll
            for (int ft = 0; ft < 2; ft++) {
                wf[ft] = cat8(tr_read(aW + ft * 32), tr_read(aW + ft * 32 + 1024));
                rst[ft] = __builtin_amdgcn_rcpf(lds_bf(aS + ft * 32));
            }
            sd_reads();
            PIN(wf[0]); PIN(wf[1]); PIN(rst[0]); PIN(rst[1]);
            SCHED();
            ARR_MARK(16);
            BAR();
            SCHED();
            const bf16x8 kf0 = lds128(aK), kf1 = lds128(aK + 1024);
            SCHED();
            hook(-1, rst[0]);
            hook(0, rst[0]);
            SCHED();
            f32x4 p[2][2];
            p[0][0] = MFMA(wf[0], kf0, zero4); p[0][1] = MFMA(wf[1], kf0, zero4);
            p[1][0] = MFMA(wf[0], kf1, zero4); p[1][1] = MFMA(wf[1], kf1, zero4);
            MFMA_SETTLE2(p);
            bf16x8 f[4];
            softmax2<true>(p, rst[0], rst[1], g >= 2, f, hook, ImgPut{ia});
            SCHED();
            hook(5, rst[0]);
            SCHED();
            STAMPW(3, 4); STAMPW(3, 7);
            kv_tile([&]() { hook(6, rst[0]); });
            STAMPW(4, 4); STAMPW(4, 7);
        };
        auto wblk = [&](int k) {
            constexpr unsigned T[8] = {0x19181716, 0x041c1b1a, 0x08070605, 0x0c0b0a09, 0x1413120d, 0x11100015, 0x1d030201, 0x1f1e0f0e};
            return (T[k] >> (8 * lw)) & 31;
        };
        auto wo_init = [&]() {
#pragma unroll
            for (int i = 0; i < 4; i++) { vwo[i] = vWO + i * (16384 - 1024); PIN(vwo[i]); }
        };
        unsigned hs[8];
        auto wl_pre = [&]() { asm volatile("" : "+s"(sg), "+s"(su)); PIN(vwl); };
        auto wo_pre = [&]() {
            wo_init();
#pragma unroll
            for (int k = 0; k < 8; k++) { hs[k] = wo_so(wblk(k)); asm volatile("" : "+s"(hs[k])); }
        };
        auto wo_rng = [&](int k0, int k1) {
#pragma unroll
            for (int k = k0; k < k1; k++) dma16x4(ldsb + L_WO + wblk(k) * 4096, vwo, rsWO, hs[k]);
        };
        auto pf = [&](int i) {
            if (i == 0) { wloads(0, 6); SCHED(); if (lw < 2) wo_pre(); SCHED(); }
            if (i < 3 && !ffn) wo_rng(i, i + 1);
            if (i == 1 || i == 2) wloads(6 * i, i == 1 ? 12 : 16);
        };
        struct Hook {
            decltype(pf)& f; decltype(v_reads)& vr;
            DI void operator()(int k, float& x) const { if (k == 10) return; PIN(x); if (k == 3) { ARR_MARK(17); BAR(); vr(); } f(3 + k); PIN(x); }
            DI void operator()(int k, float& x, float& y) const { PIN(y); f(3 + k); PIN(x); }
        };
        auto tail16 = [&]() {
            kfeat(Hook{pf, v_reads});
            if (ffn) wo_rng(0, 3);
            wo_rng(3, 5);
            static_for<3>([&](auto kc_) {
                constexpr int k = decltype(kc_)::value;
                waitv<4 * (4 - k)>();
                repack(wblk(k));
            });
            BAR();
            res_loads();
            wo_rng(5, 7);
            static_for<4>([&](auto kc_) {
                constexpr int k = decltype(kc_)::value + 3;
                waitv<4 * (6 - k)>();
                repack(wblk(k));
            });
            PIN(xres[0]); PIN(xres[1]); PIN(wrv[0]); PIN(wrv[1]);
            perm1();
            SCHED();
            BAR();
            wo_rng(7, 8);
        };
        auto vT23 = [&]() {
            const unsigned n = (idx * 2 + pw) * 64 + l, r = n / 12, c = n % 12;
            return idx < 24 ? ((r & 127) + (r >> 7) * 256 + 128) * 6144 + (c >> 2) * 2048 + (((xcd >> 1) * 4 + (c >> 2 ? 0 : 4) + (c & 3)) & 15) * 128 : OOB;
        };
        if (lw < 2) {
            const unsigned sA = (rt * 128 + lw * 64) * 2048, sB = lw * 32 * 6144 + hp * 128, sBq = lw * 32 * 6144 + hq * 128;
            unsigned buf = 1;
            unsigned va[4], vb[4];
#pragma unroll
            for (int i = 0; i < 4; i++) { va[i] = vA + i * (16384 - 1024); vb[i] = ((i & 1) ? vB1 : vB0) + i * (49152 - 1024); PIN(va[i]); PIN(vb[i]); }
            auto stage = [&](unsigned s, unsigned bf, int i0, int i1) {
                const unsigned kt = (s + rot) & 15;
                const unsigned lb = ldsb + bf * STAGE_B;
                const unsigned sa = sA + kt * 128, sb = kt * 393216;
                if (i0 <= 0 && 0 < i1) dma16x4(lb + lw * 8192, va, rsX, sa);
                if (i0 <= 1 && 1 < i1) dma16x4(lb + lw * 8192 + 4096, va, rsX, sa + 65536);
                if (i0 <= 2 && 2 < i1) dma16x4(lb + 16384 + 8192 + lw * 4096, vb, rsW, sb + sB + 2048);
                if (i0 <= 3 && 3 < i1) dma16x4(lb + 16384 + 16384 + lw * 4096, vb, rsW, sb + sB + 4096);
                if (i0 <= 4 && 4 < i1) dma16x4(lb + 16384 + lw * 4096, vb, rsW, sb + sBq);
            };
            stage(1, 1, 0, 4);
            SCHED();
            unsigned vt = vT23();
            PIN(vt);
            wl_pre(); k_pre();
            waitv<16>(); ARR_MARK(0); BAR();
            stage(1, 1, 4, 5);
            touch(vt, rsW, (xcd & 1) * 512 * 6144);
            waitv<1>(); ARR_MARK(1); BAR();
            stage(2, 2, 0, 5); stage(3, 0, 0, 3);
            waitv<12>(); ARR_MARK(2); BAR();
            stage(3, 0, 3, 5); stage(4, 1, 0, 4);
            waitv<16>(); ARR_MARK(3); BAR();
            stage(4, 1, 4, 5);
            buf = 2;
#pragma nounroll
            for (unsigned s = 5; s < 16; s++) {
                if (s > 5) { waitv<20>(); ARR_MARK(s - 2); BAR(); }
                stage(s, buf, 0, 5);
                buf = buf == 2 ? 0 : buf + 1;
            }
            waitv<20>(); ARR_MARK(14); BAR();
            pf(0);
            if (ffn) waitv<6>(); else waitv<10>();
            ARR_MARK(15); BAR();
            pf(1);
            tail16();
            READ_TAG();
        } else {
            SCHED();
            unsigned v3;
            const unsigned h3 = (xcd >> 1) * 4 + 3, kv3 = !(xcd & 2), wv = idx * 2 + pw;
            auto line3 = [&](unsigned per, unsigned rows, auto rowf) {
                const unsigned n = wv * (kv3 ? per : (per + 2) / 3) + l, r3 = kv3 ? n / 3 : n, c3 = kv3 ? n % 3 : 0;
                return (idx < 24 && l < (kv3 ? per : (per + 2) / 3) && r3 < rows) ? rowf(r3) * 6144 + c3 * 2048 + ((c3 ? h3 : h3 + 4) & 15) * 128 : OOB;
            };
            v3 = line3(32, 512, [](unsigned r) { return r; });
            touch(line3(16, 256, [](unsigned r) { return (r & 127) + (r >> 7) * 256; }), rsW, (xcd & 1) * 512 * 6144);
            {
                const rsrc_t rsP = __builtin_amdgcn_make_buffer_rsrc((void*)WPHI, 0, 32 * 1024 * 2, 0x00020000);
#pragma unroll
                for (int i = 0; i < 4; i++) dma16(ldsb + L_WPHI + pw * 4096 + i * 1024, l * 16, rsP, (pw ? hq : hp) * 4096 + i * 1024);
            }
            v4u sv = {SENT, SENT, SENT, SENT};
            unsigned vc4 = l < 16 ? WS_G3 + (hp * 4 + (rt >> 2)) * 2048 + pw * 1024 + (rt & 3) * 256 + l * 16
                         : l < 24 ? WS_P2 + (hp * 16 + rt) * P2_STRIDE + 8192 + pw * 128 + (l - 16) * 16
                         : (l < 32 && pw == 0 && (rt & 7) == 0) ? WS_GZ + (hp * 2 + (rt >> 3)) * 128 + (l - 24) * 16 : OOB;
            auto clears = [&](int i0, int i1) {
#pragma unroll
                for (int i = i0; i < i1; i++) {
                    if (i < 4) ST16(sv, l * 16, WS_P2 + (hp * 16 + rt) * P2_STRIDE + pw * 4096 + i * 1024);
                    if (i == 4) ST16(sv, vc4, 0);
                    if (i > 4) ST16(sv, l * 16, WS_TL + (bid * 4 + pw * 2 + i - 5) * 1024);
                }
                FRESH();
            };
            PIN(vc4); PIN(sv);
            unsigned ca = ldsb + 16384 + pw * 8192 + (8 * g + q4) * 128 + (((rt & 3) ^ (((q4 >> 1) & 1) | ((g & 1) << 1))) * 32) + p4 * 8;
            PIN(ca);
            bf16x8 cf[4];
            f32x4 cd[2] = {zero4, zero4};
            auto cs_read = [&](int st) {
#pragma unroll
                for (int i = 0; i < 4; i++) cf[i] = cat8(tr_read(ca + (st % 3) * STAGE_B + i * 4096), tr_read(ca + (st % 3) * STAGE_B + i * 4096 + 512));
                SCHED();
            };
            READ_TAG();
            waitv<5>();
            ARR_MARK(0);
            BAR();
            SCHED();
            cs_read(0);
            FRESH();
            const rsrc_t rsC = __builtin_amdgcn_make_buffer_rsrc((void*)pc0, 0, clen, 0x00020000);
            auto code_pf = [&]() { touch(l < 6 ? l * 128 : OOB, rsC, (idx * 2 + pw) * 768); };
            v4u gl[4];
            float s0 = 0.f, s1 = 0.f;
            unsigned bad;
            v2u gs;
            unsigned vgl = (l < 48 ? l : 0) * 16;
            unsigned aw0 = ldsb + L_CS + pw * 384 + (vgl >> 1), aw1 = ldsb + L_MB + (pw * 64 + l) * 4, aw2 = ldsb + L_SD + (pw * 64 + l) * 2;
            static_for<15>([&](auto tc) {
                constexpr int t = decltype(tc)::value;
                WAITL0();
                ARR_MARK(t + 1);
                BAR();
                SCHED();
                if constexpr (t < 4) {
                    __builtin_amdgcn_s_setprio(2);
#pragma unroll
                    for (int i = 0; i < 4; i++) cd[i & 1] = MFMA(ones, cf[(i & 1) * 2 + (i >> 1)], cd[i & 1]);
                    SCHED();
                    __builtin_amdgcn_s_setprio(0);
                    if constexpr (t < 3) cs_read(t + 1);
                    else __builtin_amdgcn_raw_buffer_store_b64((v2u){__float_as_uint(g ? cd[1][0] : cd[0][0]), tag}, rsWS, (g == 1 || g + pw == 0) ? g * 512 + t16 * 8 : OOB,
                                                               WS_G1 + ((hp * 4 + (rt >> 2)) * 192 + pw * 64 + (rt & 3) * 16) * 8, AUX_SC1);
                }
                if constexpr (t == 0) touch(v3, rsW, (~xcd & 1) * 512 * 6144);
                if constexpr (t == 12) code_pf();
                if constexpr (t == 8) {
                    waitv<0>();
                    __builtin_amdgcn_raw_buffer_store_b64((v2u){0u, tag}, rsWS, l == 0 ? 0 : OOB, WS_GC + ((hp * 2 + pw) * 16 + rt) * 8, AUX_SC1);
                }
                if constexpr (t == 8 || t == 9) {
#pragma unroll
                    for (int r = (t - 8) * 2; r < (t - 8) * 2 + 2; r++)
                        asm volatile("buffer_load_dwordx4 %0, %1, %2, %3 offen sc1" : "=v"(gl[r]) : "v"(vgl), "s"(rsWS), "s"(WS_G1 + ((hp * 4 + r) * 192 + pw * 96) * 8) : "memory");
                }
                if constexpr (t == 6) {
                    asm volatile("buffer_load_dwordx2 %0, %1, %2, %3 offen sc1" : "=v"(gs) : "v"(l * 8), "s"(rsWS), "s"(WS_G0 + (rt * 128 + pw * 64) * 8) : "memory");
                }
                if constexpr (t == 11) {
                    waitv<0>();
                    PIN(gs);
                    bad = gs[1] ^ tag;
#pragma unroll
                    for (int r = 0; r < 4; r++) {
                        PIN(gl[r]);
                        bad |= (gl[r][1] ^ tag) | (gl[r][3] ^ tag);
                        s0 += __uint_as_float(gl[r][0]); s1 += __uint_as_float(gl[r][2]);
                    }
                    PIN(s0); PIN(s1); PIN(bad);
                }
                if constexpr (t == 12) {
                    if (RARE(!__all(bad == 0))) {
                        s0 = 0.f; s1 = 0.f;
#pragma nounroll
                        for (unsigned r = 0; r < 4; r++) {
                            unsigned a[2];
                            sweep<2>(rsWS, vgl, WS_G1 + ((hp * 4 + r) * 192 + pw * 96) * 8, 8, tag, a);
                            s0 += __uint_as_float(a[0]); s1 += __uint_as_float(a[1]);
                        }
                        unsigned c[1];
                        sweep<1>(rsWS, l * 8, WS_G0 + (rt * 128 + pw * 64) * 8, 0, tag, c);
                        gs[0] = c[0];
                    }
                    *(LAS f32x2*)(aw0) = (f32x2){-s0, -s1};
                    stsf(aw1, bflo(gs[0]));
                    *(LAS u16*)(aw2) = (u16)(gs[0] >> 16);
                    SCHED();
                }
                if constexpr (t == 5) k_pre();
                if constexpr (t == 6) { wl_pre(); wo_pre(); }
                if constexpr (t == 7) { PIN(vgl); PIN(aw0); PIN(aw1); PIN(aw2); }
                clears(t < 7 ? t : 7, t < 7 ? t + 1 : 7);
                if constexpr (t == 12) asm volatile("buffer_load_dwordx2 %0, %1, %2, %3 offen sc1" : "=v"(gs) : "v"((l & 31) * 8), "s"(rsWS), "s"(WS_GC + (pw ? hq : hp) * 256) : "memory");
                if constexpr (t == 13) {
                    waitv<0>();
                    PIN(gs);
                    if (RARE(!__all(gs[1] == tag))) { unsigned c[1]; sweep<1>(rsWS, (l & 31) * 8, WS_GC + (pw ? hq : hp) * 256, 0, tag, c); }
                    SCHED();
                    pf(0);
                }
                if constexpr (t == 14) pf(1);
            });
            tail16();
        }
        WAITL0();
        STAMPW(7, 4); STAMPW(7, 7);
    }
    const unsigned sFl = WS_F4 + (rt2 * 4 + (lw >> 1) + 2 * half) * 128;
    ARR_MARK(20);
    BAR();
    ARR_END();
    const unsigned wb = ldsb + L_WO + half * 65536 + l * 16;
    bf16x8 bb[2][4];
#pragma unroll
    for (int nt = 0; nt < 4; nt++) { bb[0][nt] = lds128(wb + nt * 1024); PIN(bb[0][nt]); }
    unsigned so[16], va0 = l * 16 + half * 32768, va1 = l * 16 + (half ^ 1) * 32768;
    unsigned aXw = ldsb + L_XB + w * 4096 + l * 16, aXr = ldsb + L_XB + (w ^ 4) * 4096 + l * 16, sG5 = WS_G5 + ((rt2 * 8 + lw * 2 + half) * 16 + ct) * 256;
    unsigned vG5 = g == q4 ? t16 * 16 : OOB;
#pragma unroll
    for (int st = 0; st < 16; st++) { so[st] = WS_P4 + (rt2 * 8 + lw * 2) * 32768 + ((2 * st + half + rot2) & 31) * 1024; asm volatile("" : "+s"(so[st])); }
    unsigned wt[2];
#pragma unroll
    for (int i = 0; i < 2; i++) { wt[i] = ldsb + L_WO + half * 65536 + (4 * g + q4) * 128 + (((i * 2 + (p4 >> 1)) ^ ((2 * g + (q4 >> 1)) & 3)) * 32) + (p4 & 1) * 16; PIN(wt[i]); }
    PIN(va0); PIN(va1); PIN(aXw); PIN(aXr); PIN(vG5);
    vD = kr * 16384 + (l & 7) * 16; sD = (kc * 256 + lw * 64) * 2048 + cg * 128;
    PIN(vD);
    asm volatile("" : "+s"(sG5), "+s"(sD), "+s"(rsD));
    SLEEP(12);
    poll16(rsWS, sFl, tag, l);
    FRESH();
    SCHED();
    STAMPW(8, 0); STAMPW(8, 4);
    unsigned swc;
    f32x4 acc2[2][4];
    bf16x8 sop[2];
    {
        auto aload = [&](int st, int m2) { return LD16(m2 ? va1 : va0, so[st]); };
        constexpr int PD = 2;
        v4u af[PD][2];
#pragma unroll
        for (int i = 0; i < PD; i++) {
#pragma unroll
            for (int m2 = 1; m2 >= 0; m2--) af[i][m2] = aload(i, m2);
            SCHED();
        }
#pragma unroll
        for (int nt = 0; nt < 4; nt++) {
            const unsigned x0 = xres[nt >> 1][2 * (nt & 1)], x1 = xres[nt >> 1][2 * (nt & 1) + 1];
            acc2[0][nt] = (f32x4){bflo(x0), bfhi(x0), bflo(x1), bfhi(x1)};
            acc2[1][nt] = zero4;
        }
        unsigned m0 = p4 == 0 ? 0x3F803F80u : 0u, m1 = p4 == 1 ? ~0u : 0u;
        PIN(m0); PIN(m1);
#pragma unroll
        for (int i = 0; i < 2; i++) {
            v4u u;
#pragma unroll
            for (int j = 0; j < 4; j++) u[j] = (wrv[i][j] & m1) | m0;
            sop[i] = __builtin_bit_cast(bf16x8, u);
            PIN(sop[i]);
        }
        {
            float sw = 0.f;
#pragma unroll
            for (int i = 0; i < 8; i++) sw = dot2(wrv[i >> 2][i & 3], 0x3F803F80u, sw);
            swc = __float_as_uint(xrow_sum(sw));
            PIN(swc);
        }
        SCHED();
#pragma unroll
        for (int st = 0; st < 16; st++) {
            if (st + 1 == 14) BAR();
            if (st + 1 < 16) {
#pragma unroll
                for (int nt = 0; nt < 4; nt++) {
                    if (st + 1 < 14) bb[(st + 1) % (2)][nt] = lds128(wb + (st + 1) * 4096 + nt * 1024);
                    else bb[(st + 1) % (2)][nt] = cat8(tr_read(wt[nt >> 1] + (st + 1) * 4096 + (nt & 1) * 8), tr_read(wt[nt >> 1] + (st + 1) * 4096 + (nt & 1) * 8 + 2048));
                }
            }
#pragma unroll
            for (int mm = 0; mm < 2; mm++) {
                const int m2 = mm ^ 1;
                const bf16x8 a = __builtin_bit_cast(bf16x8, af[st % PD][m2]);
#pragma unroll
                for (int nt = 0; nt < 4; nt++) acc2[m2][nt] = MFMA(bb[st % (2)][nt], a, acc2[m2][nt]);
            }
            if (st + PD < 16) {
#pragma unroll
                for (int m2 = 1; m2 >= 0; m2--) af[st % PD][m2] = aload(PD + st, m2);
            }
#pragma unroll
            for (int i = 0; i < (st + 1 < 14 ? 4 : 8); i++) { SGB(0x8, 1); SGB(0x100, 1); }
            SCHED();
        }
    }
    STAMPW(9, 0); STAMPW(9, 4);
#pragma unroll
    for (int nt = 0; nt < 4; nt++) stsf4(aXw + nt * 1024, acc2[1][nt]);
    SCHED();
    if (half) dloads(0, 3);
    SCHED();
    WAITL0();
    if (half) __builtin_amdgcn_s_setprio(1);
    BAR();
    SCHED();
    v4u hpk[2];
    {
        f32x4 pa[4];
#pragma unroll
        for (int nt = 0; nt < 4; nt++) pa[nt] = ldsf4(aXr + nt * 1024);
        SCHED();
#pragma unroll
        for (int i = 0; i < 8; i++) {
            const f32x4 a = acc2[0][i >> 1] + pa[i >> 1];
            hpk[i >> 2][i & 3] = pk2(a[2 * (i & 1)], a[2 * (i & 1) + 1]);
        }
        const bf16x8 h0 = __builtin_bit_cast(bf16x8, hpk[0]), h1 = __builtin_bit_cast(bf16x8, hpk[1]);
        f32x4 gq = MFMA(h0, h0, zero4), sd = MFMA(sop[0], h0, zero4);
        gq = MFMA(h1, h1, gq); sd = MFMA(sop[1], h1, sd);
        float q0 = (l & 1) ? gq[1] : gq[0], q1 = (l & 1) ? gq[3] : gq[2];
        PIN(q0); PIN(q1);
        const v4u pv = {__float_as_uint(sd[0]), __float_as_uint((l & 2) ? q1 : q0), __float_as_uint(sd[1]), swc};
        ST16(pv, vG5, sG5);
        asm volatile("s_nop 1" :: "v"(pv));
    }
    __builtin_amdgcn_s_setprio(0);
    STAMPW(10, 0); STAMPW(10, 4);
    v4u wp2[8];
    auto perm2 = [&]() {
#pragma unroll
        for (int i = 16; i < 24; i++) PIN(wreg[i]);
#pragma unroll
        for (int ip = 0; ip < 4; ip++)
#pragma unroll
            for (int c = 0; c < 4; c++) {
                const unsigned a = wreg[16 + 2 * ip][c], b = wreg[16 + 2 * ip + 1][c];
                wp2[2 * c][ip] = __builtin_amdgcn_perm(b, a, 0x05040100);
                wp2[2 * c + 1][ip] = __builtin_amdgcn_perm(b, a, 0x07060302);
            }
#pragma unroll
        for (int c = 0; c < 8; c++) PIN(wp2[c]);
    };
    const unsigned mt = rt2 * 8 + lw * 2 + half;
    {
        v4u pv[4];
        unsigned sK = WS_GK + (ct * 128 + mt) * 4, vK = l == 0 ? 0 : OOB, rk = 2047u - R2;
        asm volatile("" : "+s"(sK));
        PIN(vK); PIN(rk);
        unsigned sS = WS_G5 + mt * 4096;
        asm volatile("" : "+s"(sS));
        SLEEP(18);
        __builtin_amdgcn_s_setprio(1);
        float T[4];
        sweepn<4>([&](int k) { return LD16(l * 16 + k * 1024, sS); }, pv, [&]() {
            const f32x4 ps = (__builtin_bit_cast(f32x4, pv[0]) + __builtin_bit_cast(f32x4, pv[1])) + (__builtin_bit_cast(f32x4, pv[2]) + __builtin_bit_cast(f32x4, pv[3]));
            const float r = xrow_scatter<false>(ps[0], ps[1], ps[2], ps[3]);
            xrow_gather(r, T);
            return r;
        });
        STAMPW(21, 0); STAMPW(21, 4);
        const float mean = T[0] * (1.0f / 1024.0f), var = fmaxf(T[1] * (1.0f / 1024.0f) - mean * mean, 0.f);
        const unsigned sg = pk2(mean, __builtin_amdgcn_sqrtf(rbf(rbf(var) + 1e-5f)));
        const float mb2 = bflo(sg), isd2 = __builtin_amdgcn_rcpf(bfhi(sg));
        const float rsc = (T[2] - mb2 * T[3]) * isd2;
        const unsigned rb = pk2(rsc, rsc);
        const unsigned key = ((rb ^ ((unsigned)((int)rb >> 31) | 0x80000000u)) & 0xffff0000u) | rk;
        unsigned kb = umx(key, dppu<0x128>(key));
        kb = umx(kb, dppu<0x124>(kb)); kb = umx(kb, dppu<0x122>(kb)); kb = umx(kb, dppu<0x121>(kb));
        ST4(kb, vK, sK);
        SCHED();
        __builtin_amdgcn_s_setprio(0);
        if (half) { dloads(3, 8); SCHED(); }
        const bool cand = key == kb;
        v4u e[2], outv[2];
#pragma unroll
        for (int i = 0; i < 8; i++) {
            const float h0 = bflo(hpk[i >> 2][i & 3]), h1 = bfhi(hpk[i >> 2][i & 3]);
            const unsigned d = pk2(h0 - mb2, h1 - mb2);
            e[i >> 2][i & 3] = pk2(bflo(d) * isd2, bfhi(d) * isd2);
        }
        ST16(e[0], cand ? g * 16 : OOB, WS_C + (mt * 16 + ct) * 128);
        ST16(e[1], cand ? g * 16 + 64 : OOB, WS_C + (mt * 16 + ct) * 128);
        SCHED();
        STAMPW(22, 0); STAMPW(22, 4);
#pragma unroll
        for (int i = 0; i < 8; i++)
            outv[i >> 2][i & 3] = pk2(bflo(hpk[i >> 2][i & 3]) + bflo(e[i >> 2][i & 3]), bfhi(hpk[i >> 2][i & 3]) + bfhi(e[i >> 2][i & 3]));
        if (cand) {
#pragma unroll
            for (int i = 0; i < 2; i++) { *(LAS v4u*)(ldsb + L_STASH + w * 256 + i * 64 + g * 16) = hpk[i]; *(LAS v4u*)(ldsb + L_STASH + w * 256 + 128 + i * 64 + g * 16) = e[i]; }
        }
#pragma unroll
        for (int i = 0; i < 2; i++) *(LAS v4u*)(ldsb + L_OUT + (lw * 2 + half) * 2048 + t16 * 128 + (((i * 4 + g) ^ (l & 7)) * 16)) = outv[i];
    }
    const rsrc_t rsO = __builtin_amdgcn_make_buffer_rsrc((void*)OUT, 0, 2048 * 1024 * 2, 0x00020000);
    unsigned aG = ldsb + L_WO + 512;
    PIN(aG);
    unsigned vX = (2 * g + (l & 1)) * 16, aP = (2 * g + (l & 1)) * 32, aR1 = ldsb + L_RED + lw * 4352 + l * 16, vl4 = l * 4;
    unsigned aR2 = ldsb + L_RED + ((lw & 1) * 2 + ((l >> 2) & 1)) * 1088 + kr * 16 + p4 * 4;
    unsigned sX = WS_C + kq * 512 + lw * 128, sG8w = WS_G8 + ((kq * 2 + lw) * 3584 + (lw ? cg1u : cg1) * 64) * 4, sG8r = WS_G8 + (kc * 256 + lw * 64) * 4;
    unsigned sG9 = WS_G9 + (cg * 14 + kc) * 256;
    unsigned aR3 = aR1 + 17408 - lw * 2176;
    PIN(vX); PIN(aP); PIN(aR1); PIN(aR2); PIN(aR3); PIN(vl4);
    asm volatile("" : "+s"(sX), "+s"(sG8w), "+s"(sG8r), "+s"(sG9));
    SCHED();
    if (w == 1) {
        unsigned sK = WS_GK + (bid & 15) * 512;
        v2u hb[1];
        asm volatile("" : "+s"(sK));
        SLEEP(13);
        stsu(aG, wave_maxu(sweepz<1>([&](int) { return ldg(rsWS, l * 8, sK); }, hb)));
        STAMPR(23, 2);
    }
    if (half) perm2();
    STAMPW(24, 7);
    WAITL0();
    if (half) __builtin_amdgcn_s_setprio(1);
    BAR();
    STAMPW(11, 0); STAMPW(11, 4);
    const unsigned gkv = ldsu(aG);
    if (w < 4) {
        if (bid == 0 && tid == 0) __hip_atomic_store((gu32*)(ws + WS_EPOCH), tag, RLX_AGENT);
        v4u o2[4];
        unsigned vo[4];
#pragma unroll
        for (int i = 0; i < 4; i++) o2[i] = *(const LAS v4u*)(ldsb + L_OUT + w * 4096 + i * 1024 + kr * 128 + (((l & 7) ^ kr) * 16));
#pragma unroll
        for (int i = 0; i < 4; i++) { const unsigned R = mt * 16 + i * 8 + kr; vo[i] = 2047u - R != (gkv & 2047u) ? R * 2048 + (l & 7) * 16 : OOB; PIN(vo[i]); PIN(o2[i]); }
#pragma unroll
        for (int s = 0; s < 3; s++) {
            if (s) { BAR(); SLEEP(3); }
            SCHED();
#pragma unroll
            for (int i = 0; i < 4; i++)
                if ((i + 1) / 2 == s) __builtin_amdgcn_raw_buffer_store_b128(o2[i], rsO, vo[i], ct * 128, AUX_SC1);
            SCHED();
        }
        STAMPW(12, 0);
        return;
    }

    auto gemv = [&](const v4u& x, const v4u* wp, int n, unsigned a) {
        f32x4 d[16], o[4];
#pragma unroll
        for (int c = 0; c < n + 4; c++) {
            if (c < n) d[c] = MFMA(__builtin_bit_cast(bf16x8, x), __builtin_bit_cast(bf16x8, wp[c]), zero4);
            SCHED();
            if (c >= 4) {
                const int j = c - 4;
                float t = d[j][0] + dppf<0x128>(d[j][1]);
                asm volatile("" : "+v"(t) : "v"(d[j]));
                o[j >> 2][j & 3] = t;
                if ((j & 3) == 3) stsf4(a + (j >> 2) * 1088, o[j >> 2]);
            }
            SCHED();
        }
        return o[0][0];
    };
    auto sum4 = [&](unsigned a, unsigned stride) {
        float u = ldsf(a) + ldsf(a + stride), v = ldsf(a + 2 * stride) + ldsf(a + 3 * stride);
        PIN(u); PIN(v);
        return u + v;
    };
    if (ffn) {
        v4u xq[1];
        const unsigned vx = ((~gkv & 0x7f0u) << 7) + vX;
        sweepn<1>([&](int) { return LD16(vx, sX); }, xq, [&]() { return gemv(xq[0], &wp1[0][0], 16, aR1); });
        STAMPW(13, 4);
    }
    WAITL0();
    BAR();
    if (ffn) {
        if (lw < 2) ST4(__float_as_uint(sum4(aR2, 4352)), vl4, sG8w);
        STAMPW(14, 4);
        unsigned pv[8];
        v4u ap;
        SLEEP(12);
        sweepn<8>([&](int k) { return LD4(vl4, sG8r + k * 3584 * 4); }, pv, [&]() {
            const float gg = rbf((__uint_as_float(pv[0]) + __uint_as_float(pv[2])) + (__uint_as_float(pv[4]) + __uint_as_float(pv[6])));
            const float uu = rbf((__uint_as_float(pv[1]) + __uint_as_float(pv[3])) + (__uint_as_float(pv[5]) + __uint_as_float(pv[7])));
            const float act = rbf(gg * __builtin_amdgcn_rcpf(1.0f + __expf(-gg))) * uu;
            const unsigned apk = pk2(act, dppf<0xB1>(act));
#pragma unroll
            for (int ip = 0; ip < 4; ip++) ap[ip] = __builtin_amdgcn_ds_bpermute(aP + ip * 8, apk);
            return act;
        });
        STAMPW(15, 4);
        gemv(ap, wp2, 8, aR3);
    }
    WAITL0();
    BAR();
    if (lw != 0) return;
    if (ffn) ST4(__float_as_uint(sum4(aR2 + 17408, 2176)), vl4, sG9);
    SCHED();
    STAMPW(16, 4);
    const unsigned gkey = rfl(gkv), srow = 2047u - (gkey & 2047u);
    if ((srow >> 7) == rt2) {
        const float rs = __uint_as_float(((gkey & 0x80000000u) ? gkey ^ 0x80000000u : ~gkey) & 0xffff0000u);
        float sig = rbf(1.0f / (1.0f + __expf(-rs)));
        const unsigned sb = ldsb + L_STASH + (((srow >> 5) & 3) + ((srow >> 4) & 1) * 4) * 256 + (t16 * 4 + g) * 2;
        float hc = lds_bf(sb), lc = lds_bf(sb + 128);
        unsigned vo = (t16 * 4 + g) * 2, so_ = srow * 2048 + ct * 128, vy = l < 32 ? l * 16 + 3072 : OOB, sY = WS_G9 + ct * 3584;
        PIN(sig); PIN(hc); PIN(lc); PIN(vo); PIN(vy);
        asm volatile("" : "+s"(so_), "+s"(sY));
        v4u yp[4];
        float y;
        SLEEP(7);
        sweepn<4>([&](int k) { return LD16(k < 3 ? l * 16 + k * 1024 : vy, sY); }, yp, [&]() {
            const f32x4 ys = (__builtin_bit_cast(f32x4, yp[0]) + __builtin_bit_cast(f32x4, yp[1])) + (__builtin_bit_cast(f32x4, yp[2]) + __builtin_bit_cast(f32x4, yp[3]));
            y = xrow_scatter<false>(ys[0], ys[1], ys[2], ys[3]);
            return y;
        });
        __builtin_amdgcn_raw_buffer_store_b16((u16)(pk2(hc + rbf(lc + rbf(rbf(y) * sig)), 0.f) & 0xffffu), rsO, vo, so_, 0);
        STAMPW(17, 4);
    }
}
__device__ __attribute__((used, noinline)) void fk_tail() { asm volatile(".Lfk_end:"); }

extern "C" void kernel_launch(void* const* d_in, const int* in_sizes, int n_in, void* d_out, int out_size, void* d_ws,
                              size_t ws_size, hipStream_t stream) {
    (void)in_sizes; (void)n_in; (void)out_size; (void)ws_size;
    (void)hipFuncSetAttribute((const void*)fused_kernel, hipFuncAttributeMaxDynamicSharedMemorySize, LDS_BYTES);
    fused_kernel<<<256, 512, LDS_BYTES, stream>>>((const u16*)d_in[0], (const u16*)d_in[1], (const u16*)d_in[2], (const u16*)d_in[3],
                                                  (const u16*)d_in[5], (const u16*)d_in[6], (const u16*)d_in[7], (const u16*)d_in[8],
                                                  (u16*)d_out, (char*)d_ws);
}
```

```cpp
#include <hip/hip_runtime.h>
#include <stdint.h>
#include <utility>

typedef unsigned short u16;
typedef unsigned long long u64;
typedef short bf16x8 __attribute__((ext_vector_type(8)));
typedef short s16x4 __attribute__((ext_vector_type(4)));
typedef float f32x4 __attribute__((ext_vector_type(4)));
typedef float f32x2 __attribute__((ext_vector_type(2)));
typedef __bf16 bf16x2 __attribute__((ext_vector_type(2)));
typedef unsigned v4u __attribute__((ext_vector_type(4)));
typedef unsigned v2u __attribute__((ext_vector_type(2)));
#define LAS __attribute__((address_space(3)))
#define GAS __attribute__((address_space(1)))
#define CAS __attribute__((address_space(4)))
typedef GAS u64 gu64;
typedef GAS unsigned gu32;
typedef __amdgpu_buffer_rsrc_t rsrc_t;
#define DI __device__ __forceinline__
#define RLX_AGENT __ATOMIC_RELAXED, __HIP_MEMORY_SCOPE_AGENT
#define BAR() __builtin_amdgcn_s_barrier()
#define SCHED() __builtin_amdgcn_sched_barrier(0)
#define SGB(m, n) __builtin_amdgcn_sched_group_barrier((m), (n), 0)
#define WAITL0() asm volatile("s_waitcnt lgkmcnt(0)" ::: "memory")
#define WL0() __builtin_amdgcn_s_waitcnt(0xc07f)
#define MFMA(a, b, c) __builtin_amdgcn_mfma_f32_16x16x32_bf16((a), (b), (c), 0, 0, 0)
#define ORDER_BEHIND_POLL() __builtin_amdgcn_fence(__ATOMIC_ACQUIRE, "wavefront")
#define AUX_SC1 16
template <int N> DI void waitv() { asm volatile("s_waitcnt vmcnt(%0)" ::"i"(N) : "memory"); }

#define WS_EPOCH 0
#define WS_G0 0x1000
#define WS_G1 0x8000
#define WS_F4 0x78000
#define WS_GC 0x70000
#define WS_G3 0x80000
#define WS_GZ 0xC0000
#define SENT 0xFFFFFFFFu
#define WS_TL 0x100000
#define WS_G5 0x100000
#define WS_G8 0x180000
#define WS_G9 0x1A0000
#define WS_GK 0x1B4000
#define WS_C 0x1C0000
#define WS_P2 0x200000
#define P2_STRIDE 0x2180
#define WS_P4 0x700000
#define WS_USED 0xB00000
#define WS_STAMP 0x1000000

#define STAGE_B 40960
#define LDS_BYTES 163840
#define L_WO 32768
#define L_MB 161792
#define L_WPHI (L_MB - 8192)
#define L_SD (L_MB + 512)
#define L_CS (L_MB + 1024)
#define L_V L_WO
#define L_KH (L_WO + 65536)
#define L_XB 0
#define L_STASH (L_WO + 1024)
#define L_OUT (L_WO + 8192)
#define L_RED 4096

#define SPIN_CAP 20000u
#define W_AUX 2
#define LOG2E 1.4426950408889634f

DI unsigned rfl(unsigned x) { return __builtin_amdgcn_readfirstlane(x); }
DI float bf2f(unsigned u) { return __uint_as_float(u << 16); }
DI float bflo(unsigned u) { return __uint_as_float(u << 16); }
DI float bfhi(unsigned u) { return __uint_as_float(u & 0xffff0000u); }
DI unsigned pk2(float lo, float hi) { f32x2 v = {lo, hi}; return __builtin_bit_cast(unsigned, __builtin_convertvector(v, bf16x2)); }
DI unsigned nsent(unsigned x) { return __builtin_elementwise_min(x, SENT - 1); }
DI unsigned umx(unsigned a, unsigned b) { return __builtin_elementwise_max(a, b); }
DI float rbf(float f) { return bflo(pk2(f, f) & 0xffffu); }
DI bf16x8 mk8(unsigned a, unsigned b, unsigned c, unsigned d) { v4u v = {a, b, c, d}; return __builtin_bit_cast(bf16x8, v); }
DI void dma16(unsigned ldsaddr, unsigned voff, rsrc_t rs, unsigned soff) {
    asm volatile("s_mov_b32 m0, %0\n\ts_nop 0\n\tbuffer_load_dwordx4 %1, %2, %3 offen lds" ::"s"(ldsaddr), "v"(voff), "s"(rs), "s"(soff) : "memory");
}
DI void dma16x4(unsigned ldsaddr, const unsigned* v, rsrc_t rs, unsigned soff) {
    asm volatile("s_mov_b32 m0, %0\n\ts_nop 0\n\tbuffer_load_dwordx4 %1, %5, %6 offen lds\n\tbuffer_load_dwordx4 %2, %5, %6 offen offset:1024 lds\n\t"
                 "buffer_load_dwordx4 %3, %5, %6 offen offset:2048 lds\n\tbuffer_load_dwordx4 %4, %5, %6 offen offset:3072 lds"
                 ::"s"(ldsaddr), "v"(v[0]), "v"(v[1]), "v"(v[2]), "v"(v[3]), "s"(rs), "s"(soff) : "memory");
}
DI void touch(unsigned voff, rsrc_t rs, unsigned soff) { asm volatile("buffer_load_dword v255, %0, %1, %2 offen" ::"v"(voff), "s"(rs), "s"(soff) : "memory", "v255"); }
DI v4u ld16_asm(unsigned voff, rsrc_t rs, unsigned soff) {
    v4u r; asm volatile("buffer_load_dwordx4 %0, %1, %2, %3 offen" : "=v"(r) : "v"(voff), "s"(rs), "s"(soff) : "memory"); return r;
}
#define PIN(x) asm volatile("" : "+v"(x))
DI s16x4 tr_read(unsigned a) { return __builtin_amdgcn_ds_read_tr16_b64_v4i16((LAS s16x4*)(a)); }
DI bf16x8 cat8(s16x4 a, s16x4 b) { return __builtin_shufflevector(a, b, 0, 1, 2, 3, 4, 5, 6, 7); }
DI bf16x8 lds128(unsigned a) { return *(const LAS bf16x8*)(a); }
DI f32x4 ldsf4(unsigned a) { return *(const LAS f32x4*)(a); }
DI float ldsf(unsigned a) { return *(const LAS float*)(a); }
DI unsigned ldsu(unsigned a) { return *(const LAS unsigned*)(a); }
DI void stsf4(unsigned a, f32x4 v) { *(LAS f32x4*)(a) = v; }
DI void stsf(unsigned a, float v) { *(LAS float*)(a) = v; }
DI void stsu(unsigned a, unsigned v) { *(LAS unsigned*)(a) = v; }
template <int C> DI float dppf(float x) { return __uint_as_float(__builtin_amdgcn_update_dpp(0, __float_as_uint(x), C, 0xf, 0xf, true)); }
template <int C> DI unsigned dppu(unsigned x) { return __builtin_amdgcn_update_dpp(0, x, C, 0xf, 0xf, true); }
DI float max2f(float a, float b) { float r; asm("v_max_f32_e32 %0, %1, %2" : "=v"(r) : "v"(a), "v"(b)); return r; }
DI float max3f(float a, float b, float c) { float r; asm("v_max3_f32 %0, %1, %2, %3" : "=v"(r) : "v"(a), "v"(b), "v"(c)); return r; }
DI float row_sum(float x) { x += dppf<0x128>(x); x += dppf<0x124>(x); x += dppf<0x122>(x); x += dppf<0x121>(x); return x; }
DI float xrow_sum(float x) {
    auto a = __builtin_amdgcn_permlane16_swap(__float_as_uint(x), __float_as_uint(x), false, false);
    x = __uint_as_float(a[0]) + __uint_as_float(a[1]);
    auto b = __builtin_amdgcn_permlane32_swap(__float_as_uint(x), __float_as_uint(x), false, false);
    return __uint_as_float(b[0]) + __uint_as_float(b[1]);
}
DI unsigned wave_maxu(unsigned x) {
    x = umx(x, dppu<0x128>(x)); x = umx(x, dppu<0x124>(x)); x = umx(x, dppu<0x122>(x)); x = umx(x, dppu<0x121>(x));
    auto a = __builtin_amdgcn_permlane16_swap(x, x, false, false);
    x = umx(a[0], a[1]);
    auto b = __builtin_amdgcn_permlane32_swap(x, x, false, false);
    return umx(b[0], b[1]);
}
DI float nmax3f(float a, float b, float c) { float r; asm("v_max3_f32 %0, -%1, -%2, -%3" : "=v"(r) : "v"(a), "v"(b), "v"(c)); return r; }
DI float nmax2f(float a, float b) { float r; asm("v_max_f32_e64 %0, -%1, -%2" : "=v"(r) : "v"(a), "v"(b)); return r; }
template <bool MAX> DI float xrow_scatter(float a, float b, float c, float d) {
    const auto s = __builtin_amdgcn_permlane16_swap(__float_as_uint(a), __float_as_uint(b), false, false);
    const auto t = __builtin_amdgcn_permlane16_swap(__float_as_uint(c), __float_as_uint(d), false, false);
    float u = MAX ? max2f(__uint_as_float(s[0]), __uint_as_float(s[1])) : __uint_as_float(s[0]) + __uint_as_float(s[1]);
    float v = MAX ? max2f(__uint_as_float(t[0]), __uint_as_float(t[1])) : __uint_as_float(t[0]) + __uint_as_float(t[1]);
    if (MAX) asm volatile("s_nop 1" : "+v"(u), "+v"(v));
    const auto r = __builtin_amdgcn_permlane32_swap(__float_as_uint(u), __float_as_uint(v), false, false);
    return MAX ? max2f(__uint_as_float(r[0]), __uint_as_float(r[1])) : __uint_as_float(r[0]) + __uint_as_float(r[1]);
}
DI void xrow_gather(float w, float (&o)[4]) {
    const auto r = __builtin_amdgcn_permlane32_swap(__float_as_uint(w), __float_as_uint(w), false, false);
    const auto s = __builtin_amdgcn_permlane16_swap(r[0], r[0], false, false);
    const auto t = __builtin_amdgcn_permlane16_swap(r[1], r[1], false, false);
    o[0] = __uint_as_float(s[0]); o[1] = __uint_as_float(s[1]); o[2] = __uint_as_float(t[0]); o[3] = __uint_as_float(t[1]);
}
struct NoHook { DI void operator()(int, float&) const {} DI void operator()(int, float&, float&) const {} };
struct NoPut { DI void operator()(int, const bf16x8&) const {} };
template <bool MUL, class H = NoHook, class P = NoPut> DI void softmax2(const f32x4 (&p)[2][2], float rs0, float rs1, bool hi, bf16x8 (&out)[4], H hook = H(), P put = P()) {
    float m[4];
#pragma unroll
    for (int i = 0; i < 2; i++) {
        const f32x4 &p0 = p[i][0], &p1 = p[i][1];
        m[2 * i] = max3f(max3f(p0[0], p0[1], p0[2]), max3f(p0[3], p1[0], p1[1]), max2f(p1[2], p1[3]));
        m[2 * i + 1] = max3f(nmax3f(p0[0], p0[1], p0[2]), nmax3f(p0[3], p1[0], p1[1]), nmax2f(p1[2], p1[3]));
    }
    asm volatile("s_nop 1" : "+v"(m[0]), "+v"(m[1]), "+v"(m[2]), "+v"(m[3]));
    hook(1, m[3]);
    const float rsr = hi ? rs1 : rs0;
    float a[4];
    xrow_gather(xrow_scatter<true>(m[0], m[1], m[2], m[3]) * (rsr * -LOG2E), a);
    hook(10, a[0]);
    f32x2 e[4][4];
    float s[4];
#pragma unroll
    for (int k = 0; k < 4; k++) {
        if (k == 2) hook(2, a[2], s[1]);
        const float sc = ((k >> 1) ? rs1 : rs0) * ((k & 1) ? -LOG2E : LOG2E);
#pragma unroll
        for (int j = 0; j < 4; j++) {
            const f32x4& q = p[k >> 1][j >> 1];
            const f32x2 t = __builtin_elementwise_fma((f32x2){q[2 * (j & 1)], q[2 * (j & 1) + 1]}, (f32x2){sc, sc}, (f32x2){a[k], a[k]});
            e[k][j] = (f32x2){__builtin_amdgcn_exp2f(t[0]), __builtin_amdgcn_exp2f(t[1])};
        }
        const f32x2 h = (e[k][0] + e[k][1]) + (e[k][2] + e[k][3]);
        s[k] = h[0] + h[1];
        if (k == 3) hook(3, s[k]);
    }
    float iv = __builtin_amdgcn_rcpf(xrow_scatter<false>(s[0], s[1], s[2], s[3]));
    if (MUL) iv *= rsr;
    xrow_gather(iv, a);
#pragma unroll
    for (int k = 0; k < 4; k++) {
        if (k == 2) hook(4, a[2]);
        const f32x2 c = {a[k], a[k]};
        const f32x2 x0 = e[k][0] * c, x1 = e[k][1] * c, x2 = e[k][2] * c, x3 = e[k][3] * c;
        out[k] = mk8(pk2(x0[0], x0[1]), pk2(x1[0], x1[1]), pk2(x2[0], x2[1]), pk2(x3[0], x3[1]));
        put(k, out[k]);
    }
}
DI float dot2(unsigned a, unsigned b, float c) {
    return __builtin_amdgcn_fdot2_f32_bf16(__builtin_bit_cast(bf16x2, a), __builtin_bit_cast(bf16x2, b), c, false);
}
DI void st_gran(char* ws, unsigned off, unsigned tag, unsigned val) {
    __hip_atomic_store((gu64*)(ws + off), ((u64)tag << 32) | val, RLX_AGENT);
}
DI v2u ldg(rsrc_t rs, unsigned voff, unsigned soff) { return __builtin_amdgcn_raw_buffer_load_b64(rs, voff, soff, AUX_SC1); }
#define RARE(c) __builtin_expect(!!(c), 0)
#define FRESH() asm volatile("" ::: "memory")
template <int N> DI void sweep(rsrc_t rs, unsigned voff, unsigned soff, unsigned S, unsigned tag, unsigned (&v)[N]) {
#pragma nounroll
    for (unsigned spins = 0;;) {
        unsigned bad = 0;
        FRESH();
#pragma unroll
        for (int k = 0; k < N; ++k) { const v2u x = ldg(rs, voff, soff + k * S); v[k] = x[0]; bad |= x[1] ^ tag; }
        if (__all(bad == 0)) break;
        if (++spins > SPIN_CAP) break;
        __builtin_amdgcn_s_sleep(1);
    }
}
template <int A, int B> DI unsigned budget(unsigned& spins) {
    unsigned r;
    asm volatile("s_add_i32 %0, %0, 1\n\ts_cmpk_lt_u32 %0, %2\n\ts_cselect_b32 %1, %3, %4" : "+s"(spins), "=s"(r) : "i"(SPIN_CAP), "i"(A), "i"(B) : "scc");
    SCHED();
    return r;
}
DI unsigned wmax(unsigned x) { return x; }
DI unsigned wmax(v2u x) { return umx(x[0], x[1]); }
DI unsigned wmax(v4u x) { return umx(umx(x[0], x[1]), umx(x[2], x[3])); }
template <int N, class T, class F> DI unsigned sweepz(F ld, T (&v)[N]) {
    unsigned spins = 0, lim, m;
#pragma nounroll
    do {
        FRESH();
#pragma unroll
        for (int k = 0; k < N; ++k) v[k] = ld(k);
        lim = budget<-2, -1>(spins);
        m = 0;
#pragma unroll
        for (int k = 0; k < N; ++k) m = umx(m, wmax(v[k]));
    } while (__any(m > lim));
    return m;
}
template <int N, class T, class F, class G> DI void sweepn(F ld, T (&v)[N], G use) {
    unsigned spins = 0;
    bool again;
#pragma nounroll
    do {
        FRESH();
#pragma unroll
        for (int k = 0; k < N; ++k) v[k] = ld(k);
        const int m = budget<3, 0>(spins);
        again = __any(__builtin_amdgcn_classf(use(), m));
    } while (again);
}
#define OOB 0x4000000u
#define LD16(v, s) __builtin_amdgcn_raw_buffer_load_b128(rsWS, (v), (s), AUX_SC1)
#define LD4(v, s) __builtin_amdgcn_raw_buffer_load_b32(rsWS, (v), (s), AUX_SC1)
#define ST16(x, v, s) __builtin_amdgcn_raw_buffer_store_b128((x), rsWS, (v), (s), AUX_SC1)
#define ST4(x, v, s) __builtin_amdgcn_raw_buffer_store_b32((x), rsWS, (v), (s), AUX_SC1)
#define SLEEP(n) __builtin_amdgcn_s_sleep(n)
DI void poll16(rsrc_t rs, unsigned off, unsigned tag, int l) {
    unsigned spins = 0, lim, bad;
#pragma nounroll
    do {
        FRESH();
        const v2u x = ldg(rs, (l & 15) * 8, off);
        lim = budget<0, -1>(spins);
        bad = (x[1] ^ tag) | (x[0] ^ tag ^ 0x5bd1e995u);
    } while (__any(bad > lim));
    ORDER_BEHIND_POLL();
}
#define STAMPR(i, role)
#define STAMP(i)
#define STAMPW(i, ww)

#define ARR_DECL
#define KATTR
#define ARR_MARK(i)
#define ARR_END()
template <class F, int... Is> DI void static_for_impl(F&& f, std::integer_sequence<int, Is...>) { (f(std::integral_constant<int, Is>{}), ...); }
template <int N, class F> DI void static_for(F&& f) { static_for_impl(f, std::make_integer_sequence<int, N>{}); }

DI s16x4 bq(unsigned b0, unsigned b1, int j, int i) {
    const int ni = (j + 2) % 6;
    return tr_read(((ni & 1) ? b1 : b0) + (ni >> 1) * 8192 + (i >> 1) * 4096 + (i & 1) * 512);
}
template <class F> DI void group(f32x4 (&acc)[4][6], const bf16x8 (&A)[2][4], const bf16x8 (&B)[2], int j, F gap) {
    const int ni = (j + 2) % 6;
#pragma unroll
    for (int i = 0; i < 8; i++) {
        const int mi = i >> 1, ks = i & 1;
        if (ni < 4) asm volatile("v_mfma_f32_16x16x32_bf16 %0, %1, %2, %0" : "+v"(acc[mi][ni]) : "v"(B[ks]), "v"(A[ks][mi]));
        else asm volatile("v_mfma_f32_16x16x32_bf16 %0, %1, %2, %0" : "+v"(acc[mi][ni]) : "v"(A[ks][mi]), "v"(B[ks]));
        gap(i);
        SCHED();
    }
}
struct ImgPut {
    const unsigned (&ia)[4];
    DI void operator()(int k, const bf16x8& f) const {
        const v4u u = __builtin_bit_cast(v4u, f);
        *(LAS v2u*)(ia[2 * (k & 1)] + (k >> 1) * 2048) = (v2u){u[0], u[1]};
        *(LAS v2u*)(ia[2 * (k & 1) + 1] + (k >> 1) * 2048) = (v2u){u[2], u[3]};
        SCHED();
    }
};
DI float lds_bf(unsigned a) { return bf2f(*(const LAS u16*)(a)); }
#define MFMA_SETTLE2(p) asm volatile("s_nop 7\n\ts_nop 1" : "+v"(p[0][0]), "+v"(p[0][1]), "+v"(p[1][0]), "+v"(p[1][1]))

__global__ KATTR __launch_bounds__(512, 2) void fused_kernel(const u16* __restrict__ X, const u16* __restrict__ WQKV, const u16* __restrict__ WPHI,
                                                       const u16* __restrict__ WO, const u16* __restrict__ WR, const u16* __restrict__ WG,
                                                       const u16* __restrict__ WU, const u16* __restrict__ WD, u16* __restrict__ OUT,
                                                       char* __restrict__ ws) {
    extern __shared__ __attribute__((aligned(16))) char lds[];
    u64 pc0; unsigned clen;
    const int tid = threadIdx.x, l = tid & 63;
    asm volatile("s_getpc_b64 %0\n.Lfk_beg:\n\ts_lshr_b32 %1, %3, 3\n\tv_and_b32 v255, 0x1e0, %2\n\tv_add_lshl_u32 v255, v255, %1, 7\n\ts_mov_b32 %1, .Lfk_end-.Lfk_beg\n\t"
                 "v_min_u32 v255, %1, v255\n\tglobal_load_dword v255, v255, %0" : "=&s"(pc0), "=&s"(clen) : "v"(tid), "s"(blockIdx.x) : "v255", "scc");
    SCHED();
    const unsigned w = rfl(tid >> 6);
    const unsigned bid = blockIdx.x, xcd = bid & 7, idx = bid >> 3;
    const unsigned rt = (xcd & 1) * 8 + (idx & 7), hp = (xcd >> 1) * 4 + (idx >> 3);
    const unsigned i16 = idx & 15, rt2 = xcd * 2 + (idx >> 4), ct = i16 == 4 ? 11 : i16 == 11 ? 4 : i16;
    const unsigned hq = (hp + 4) & 15;
    const unsigned rot = (rt >> 2) * 4;
    const unsigned rot2 = (2 * (rt2 + ct)) & 31;
    const unsigned ldsb = (unsigned)(size_t)lds;
    const int g = l >> 4, q4 = (l & 15) >> 2, p4 = l & 3, t16 = l & 15;
    const unsigned kr = l >> 3;
    asm volatile("" ::"s"(X), "s"(WQKV), "s"(WPHI), "s"(WO), "s"(WR), "s"(WG), "s"(WU), "s"(WD), "s"(OUT), "s"(ws));
    unsigned tag;
#define READ_TAG() do { SCHED(); tag = *(const volatile CAS unsigned*)(ws + WS_EPOCH) + 1u; asm volatile("" : "+s"(tag)); } while (0)
    const rsrc_t rsWS = __builtin_amdgcn_make_buffer_rsrc((void*)ws, 0, WS_USED, 0x00020000);
    const rsrc_t rsX = __builtin_amdgcn_make_buffer_rsrc((void*)X, 0, 2048 * 1024 * 2, 0x00020000);
    const rsrc_t rsW = __builtin_amdgcn_make_buffer_rsrc((void*)WQKV, 0, 1024 * 3072 * 2, 0x00020000);
    const unsigned vA = (kr * 1024 + (((l & 7) ^ kr) * 8)) * 2;
    const unsigned vB0 = (kr * 3072 + (((l & 7) ^ (((kr >> 1) & 1) << 1)) * 8)) * 2;
    const unsigned vB1 = (kr * 3072 + (((l & 7) ^ ((((kr >> 1) & 1) | 2) << 1)) * 8)) * 2;
    const unsigned srow1 = rt * 128 + hp * 8 + (w & 3) * 2;
    v4u xr[4];
    if (w < 4) {
#pragma unroll
        for (int i = 0; i < 4; i++) xr[i] = ld16_asm(l * 16, rsX, srow1 * 2048 + i * 1024);
    }
    {
        const unsigned vBw = (w & 1) ? vB1 : vB0;
#pragma unroll
        for (int i = 0; i < 2; i++) dma16(ldsb + (w * 2 + i) * 1024, vA, rsX, (rt * 128 + w * 16 + i * 8) * 2048 + rot * 128);
#pragma unroll
        for (int i = 0; i < 3; i++) dma16(ldsb + 16384 + i * 8192 + w * 1024, vBw, rsW, (rot * 64 + w * 8) * 6144 + (i ? hp : hq) * 128 + i * 2048);
    }
    const f32x4 zero4 = {0.f, 0.f, 0.f, 0.f};
    ARR_DECL;
    const bf16x8 ones = mk8(0x3F803F80u, 0x3F803F80u, 0x3F803F80u, 0x3F803F80u);
    const unsigned lw = w & 3;
    const bool ffn = (ct & 7) != 3;
    const unsigned fb = !ffn ? 255u : rt2 * 14 + (i16 < 3 ? i16 : i16 - 2);
    const unsigned cg = (fb + 5) & 15, kc = fb >> 4;
    const unsigned kq = fb / 56, cg1 = fb % 56, cg1u = (cg1 + 3) % 56;
    v4u wreg[24];
    STAMPW(0, 0); STAMPW(0, 4);

    const rsrc_t rsWO = __builtin_amdgcn_make_buffer_rsrc((void*)WO, 0, 1024 * 1024 * 2, 0x00020000);
    const unsigned vWO = kr * 2048 + (((l & 7) ^ (((kr >> 1) & 3) << 1)) * 16);
    unsigned vwo[4];
    auto wo_so = [&](unsigned b) { return (((2 * b & 30) + (b >> 4) + rot2) & 31) * 65536 + ct * 128; };
    const unsigned fw = (((t16 >> 1) & 3) << 2) | (((t16 >> 3) & 1) << 1) | (t16 & 1);
    unsigned ad;
    unsigned ia[4];
    bf16x8 sdv[4];
    auto pre_addr = [&]() {
#pragma unroll
        for (int k = 0; k < 4; k++) { ia[k] = ldsb + L_XB + lw * 8192 + (w >> 2) * 4096 + t16 * 128 + (((4 * k + g) ^ fw) * 8); PIN(ia[k]); }
        const unsigned rr = 4 * g + q4, fr = (((rr >> 1) & 3) << 2) | (((rr >> 3) & 1) << 1) | (rr & 1);
        ad = ldsb + L_XB + (w & 1) * 16384 + rr * 128 + ((((w >> 1) * 4 + p4) ^ fr) * 8);
        PIN(ad);
    };
    auto sd_reads = [&]() {
        unsigned sa = ldsb + L_SD + g * 8;
        PIN(sa);
#pragma unroll
        for (int s = 0; s < 4; s++) {
            const v2u a = *(const LAS v2u*)(sa + s * 64), b = *(const LAS v2u*)(sa + s * 64 + 32);
            sdv[s] = mk8(a[0], a[1], b[0], b[1]);
        }
    };
    bf16x8 v0[4], v1[4];
    auto v_reads = [&]() {
        unsigned vd = ldsb + L_V + (w & 1) * 8192 + l * 16;
        PIN(vd);
#pragma unroll
        for (int s = 0; s < 4; s++) { v0[s] = lds128(vd + s * 2048); v1[s] = lds128(vd + s * 2048 + 1024); }
    };
    auto kv_tile = [&](auto hook6) {
        const unsigned h2 = w & 1, ft = w >> 1;
        WAITL0();
        ARR_MARK(18);
        BAR();
        SCHED();
        bf16x8 pf[4];
#pragma unroll
        for (int s = 0; s < 4; s++) pf[s] = cat8(tr_read(ad + s * 4096), tr_read(ad + s * 4096 + 2048));
        SCHED();
        hook6();
        SCHED();
        f32x4 k0 = zero4, k1 = zero4, kz = zero4;
#pragma unroll
        for (int s = 0; s < 4; s++) { k0 = MFMA(pf[s], v0[s], k0); k1 = MFMA(pf[s], v1[s], k1); SCHED(); }
#pragma unroll
        for (int s = 0; s < 4; s++) kz = MFMA(pf[s], sdv[s], kz);
        const unsigned pbase = WS_P2 + (hp * 16 + rt) * P2_STRIDE;
        const v4u o0 = {pk2(k0[0], k0[1]), pk2(k0[2], k0[3]), pk2(k1[0], k1[1]), pk2(k1[2], k1[3])};
        __builtin_amdgcn_raw_buffer_store_b128(o0, rsWS, l * 16, pbase + h2 * 4096 + ft * 1024, AUX_SC1);
        asm volatile("s_nop 1" :: "v"(o0));
        __builtin_amdgcn_raw_buffer_store_b64((v2u){pk2(kz[0], kz[1]), pk2(kz[2], kz[3])}, rsWS, t16 == 0 ? g * 8 : OOB,
                                              pbase + 8192 + h2 * 128 + ft * 32, AUX_SC1);
        SCHED();
        ARR_MARK(19);
        BAR();
    };

    const bool zjob = lw == 0 && (rt & 7) == 0;
    v4u pp[4];
    auto pp_ld = [&](int k) {
        return LD16(kr * P2_STRIDE + (l & 7) * 16, WS_P2 + (hp * 16 + (k & 1) * 8) * P2_STRIDE +
                    (k < 2 ? (rt >> 2) * 2048 + (lw & 1) * 1024 + (rt & 3) * 256 + (lw >> 1) * 128 : 8192 + (rt >> 3) * 128));
    };
    auto kv_reduce = [&]() {
        unsigned v3 = (l & 8) ? OOB : (l & 7) * 16 + g * 4, res;
        unsigned s3 = WS_G3 + (hp * 4 + (rt >> 2)) * 2048 + (lw & 1) * 1024 + (rt & 3) * 256 + (lw >> 1) * 128;
        PIN(v3);
        asm volatile("" : "+s"(s3));
        auto red = [&](const v4u* p) {
            float lo = xrow_scatter<false>(bflo(p[0][0]) + bflo(p[1][0]), bflo(p[0][1]) + bflo(p[1][1]), bflo(p[0][2]) + bflo(p[1][2]), bflo(p[0][3]) + bflo(p[1][3]));
            float hi = xrow_scatter<false>(bfhi(p[0][0]) + bfhi(p[1][0]), bfhi(p[0][1]) + bfhi(p[1][1]), bfhi(p[0][2]) + bfhi(p[1][2]), bfhi(p[0][3]) + bfhi(p[1][3]));
            const float chk = lo;
            lo += dppf<0x128>(lo); hi += dppf<0x128>(hi);
            res = nsent(pk2(lo, hi));
            return chk;
        };
        SLEEP(2);
        if (RARE(zjob)) {
            unsigned rz;
            sweepn<4>(pp_ld, pp, [&]() { const float a = red(pp + 2); rz = res; return a + red(pp); });
            ST4(rz, (l & 8) ? OOB : ((l >> 2) & 1) * 64 + ((l & 1) * 2 + (g >> 1)) * 16 + ((l >> 1) & 1) * 8 + (g & 1) * 4, WS_GZ + (hp * 2 + (rt >> 3)) * 128);
        } else {
            v4u p2[2];
            sweepn<2>(pp_ld, p2, [&]() { return red(p2); });
        }
        STAMPW(25, 0);
        ST4(res, v3, s3);
    };

    const unsigned half = w >> 2;
    const unsigned R2 = rt2 * 128 + lw * 32 + half * 16 + t16;

    rsrc_t rsD = __builtin_amdgcn_make_buffer_rsrc((void*)WD, 0, 1024 * 3584 * 2, 0x00020000);
    unsigned vD, sD;
    auto dloads = [&](int i0, int i1) {
#pragma unroll
        for (int i = i0; i < i1; i++)
            wreg[16 + i] = __builtin_amdgcn_raw_buffer_load_b128(rsD, vD, sD + i * 2048, W_AUX);
        FRESH();
    };
    v4u wp1[2][8];
    auto perm1 = [&]() {
#pragma unroll
        for (int n = 0; n < 16; n++) PIN(wreg[n]);
#pragma unroll
        for (int ip = 0; ip < 4; ip++)
#pragma unroll
            for (int c = 0; c < 4; c++) {
#pragma unroll
                for (int m = 0; m < 2; m++) {
                    const unsigned a = wreg[m * 8 + 2 * ip][c], b = wreg[m * 8 + 2 * ip + 1][c];
                    wp1[m][2 * c][ip] = __builtin_amdgcn_perm(b, a, 0x05040100);
                    wp1[m][2 * c + 1][ip] = __builtin_amdgcn_perm(b, a, 0x07060302);
                }
            }
#pragma unroll
        for (int c = 0; c < 8; c++) { PIN(wp1[0][c]); PIN(wp1[1][c]); }
    };
    v4u xres[2], wrv[2];
    auto res_loads = [&]() {
#pragma unroll
        for (int i = 0; i < 2; i++) {
            xres[i] = *(const v4u*)(X + (size_t)R2 * 1024 + ct * 64 + i * 32 + 8 * g);
            wrv[i] = *(const v4u*)(WR + ct * 64 + i * 32 + 8 * g);
        }
        FRESH();
    };

    if (w < 4) {
        const unsigned wm = w & 1, hh = w >> 1;
        f32x4 acc[4][6];
        bf16x8 wf[2], wfq[2];
        float mbt[4], rst[4], csv[2];
        f32x4 mbv[4], csq[2], csk[2];
#pragma unroll
        for (int i = 0; i < 4; i++)
#pragma unroll
            for (int j = 0; j < 6; j++) { acc[i][j] = zero4; PIN(acc[i][j]); }
        {
            const unsigned aoff0 = ldsb + (wm * 64 + t16) * 128 + (((0 + g) ^ (l & 7)) * 16);
            const unsigned aoff1 = ldsb + (wm * 64 + t16) * 128 + (((4 + g) ^ (l & 7)) * 16);
            const unsigned sk = ((q4 >> 1) & 1) | ((g & 1) << 1);
            const unsigned boffA = ldsb + 16384 + (8 * g + q4) * 128 + (((2 * hh) ^ sk) * 32) + p4 * 8;
            const unsigned boffB = ldsb + 16384 + (8 * g + q4) * 128 + (((2 * hh + 1) ^ sk) * 32) + p4 * 8;
            bf16x8 A[2][4], B[3][2];
            s16x4 lo;
            unsigned a0[3], a1[3], bA[3], bB[3];
#pragma unroll
            for (int i = 0; i < 3; i++) {
                a0[i] = aoff0 + i * STAGE_B; a1[i] = aoff1 + i * STAGE_B; bA[i] = boffA + i * STAGE_B; bB[i] = boffB + i * STAGE_B;
                PIN(a0[i]); PIN(a1[i]); PIN(bA[i]); PIN(bB[i]);
            }
            unsigned aT = ldsb + L_MB + (wm * 64 + t16) * 4, aS = ldsb + L_SD + (wm * 64 + t16) * 2, aM = ldsb + L_MB + wm * 256 + g * 16;
            unsigned aC = ldsb + L_CS + hh * 128 + g * 16, aV = ldsb + L_CS + (128 + hh * 32 + t16) * 4;
            unsigned aW = ldsb + L_WPHI + hh * 2048 + (4 * g + q4) * 64 + p4 * 8, aK = ldsb + L_KH + w * 2048 + l * 16;
            PIN(aT); PIN(aS); PIN(aM); PIN(aC); PIN(aV); PIN(aW); PIN(aK);
            pre_addr();
            READ_TAG();
            waitv<5>();
            PIN(xr[0]); PIN(xr[1]); PIN(xr[2]); PIN(xr[3]);
            {
                float s[2], q[2], x0[2];
#pragma unroll
                for (int rr = 0; rr < 2; rr++) {
                    x0[rr] = bflo(rfl(xr[2 * rr][0]));
                    s[rr] = 0.f; q[rr] = 0.f;
#pragma unroll
                    for (int i = 0; i < 4; i++) {
                        const float a = bflo(xr[2 * rr][i]) - x0[rr], b = bfhi(xr[2 * rr][i]) - x0[rr];
                        const float c = bflo(xr[2 * rr + 1][i]) - x0[rr], d = bfhi(xr[2 * rr + 1][i]) - x0[rr];
                        s[rr] += (a + b) + (c + d);
                        q[rr] += (a * a + b * b) + (c * c + d * d);
                    }
                }
                const float sq = row_sum(xrow_scatter<false>(s[0], s[1], q[0], q[1]));
#define RLF(x, n) __uint_as_float(__builtin_amdgcn_readlane(__float_as_uint(x), (n)))
                const float r0 = RLF(sq, 0), r1 = RLF(sq, 16), r2 = RLF(sq, 32), r3 = RLF(sq, 48);
                const float sm = (l ? r1 : r0) * (1.0f / 1024.0f), qm = (l ? r3 : r2) * (1.0f / 1024.0f);
                const float var = fmaxf(qm - sm * sm, 0.f);
                if (l < 2) st_gran(ws, WS_G0 + (srow1 + l) * 8, tag, pk2((l ? x0[1] : x0[0]) + sm, sqrtf(rbf(rbf(var) + 1e-5f))));
            }
            waitv<1>();
            WAITL0();
            ARR_MARK(0);
            BAR();
            SCHED();
            STAMPW(1, 0);
            auto rdB = [&](int u, int j, int i) {
                if (i & 1) B[j % 3][i >> 1] = cat8(lo, bq(bA[u], bB[u], j, i)); else lo = bq(bA[u], bB[u], j, i);
            };
            auto rdA = [&](int u, int i) { A[i & 1][i >> 1] = lds128(((i & 1) ? a1[u] : a0[u]) + (i >> 1) * 2048); };
            auto head = [&](int u) {
                group(acc, A, B[0], 0, [&](int i) { rdB(u, 1 + (i >> 2), i & 3); });
                group(acc, A, B[1], 1, [&](int i) { if (i < 4) rdB(u, 3, i); });
                group(acc, A, B[2], 2, [&](int i) { if (i < 4) rdB(u, 4, i); });
                group(acc, A, B[0], 3, [&](int i) { if (i < 4) rdB(u, 5, i); });
                WL0();
            };
#pragma unroll
            for (int i = 0; i < 4; i++) rdB(0, 0, i);
#pragma unroll
            for (int i = 0; i < 8; i++) rdA(0, i);
            SCHED();
#pragma nounroll
            for (unsigned t = 0; t < 5; t++) {
#pragma unroll
                for (int u = 0; u < 3; u++) {
                    head(u);
                    ARR_MARK(1 + t * 3 + u);
                    BAR();
                    SCHED();
                    group(acc, A, B[1], 4, [&](int i) { if (i < 4) rdB((u + 1) % 3, 0, i); });
                    group(acc, A, B[2], 5, [&](int i) { rdA((u + 1) % 3, i); });
                }
            }
            unsigned kw[4][4], rsu[4];
            f32x2 kx;
            auto kpiece = [&](int n) {
                if (n > 0) { kw[((n - 1 >> 2) + 2) & 3][(n - 1) & 3] = pk2(kx[0], kx[1]); PIN(kw[((n - 1 >> 2) + 2) & 3][(n - 1) & 3]); }
                if (n < 16) {
                    const int mi = ((n >> 2) + 2) & 3, j = n & 3;
                    const f32x4 &c = acc[mi][2 + (j >> 1)], &s_ = csk[j >> 1];
                    float x0 = __builtin_fmaf(mbt[mi], s_[2 * (j & 1)], c[2 * (j & 1)]); PIN(x0);
                    float x1 = __builtin_fmaf(mbt[mi], s_[2 * (j & 1) + 1], c[2 * (j & 1) + 1]); PIN(x1);
                    kx = (f32x2){x0, x1};
                }
                if (n == 5 || n == 9) *(LAS v4u*)(aK + (n == 9) * 1024) = (v4u){kw[2 + (n == 9)][0], kw[2 + (n == 9)][1], kw[2 + (n == 9)][2], kw[2 + (n == 9)][3]};
            };
            group(acc, A, B[0], 0, [&](int i) { rdB(0, 1 + (i >> 2), i & 3); });
            group(acc, A, B[1], 1, [&](int i) {
                if (i < 4) { rdB(0, 3, i); mbt[(i + 2) & 3] = ldsf(aT + ((i + 2) & 3) * 64); }
                else if (i < 6) csk[i - 4] = ldsf4(aC + 256 + (i - 4) * 64);
                else wf[i - 6] = cat8(tr_read(aW + (i - 6) * 32), tr_read(aW + (i - 6) * 32 + 1024));
            });
            group(acc, A, B[2], 2, [&](int i) { if (i < 4) rdB(0, 4, i); else mbv[i - 4] = ldsf4(aM + (i - 4) * 64); kpiece(i); });
            group(acc, A, B[0], 3, [&](int i) {
                if (i < 4) rdB(0, 5, i); else rsu[i - 4] = *(const LAS u16*)(aS + (i - 4) * 32);
                if (i == 2 || i == 3) csv[i - 2] = ldsf(aV + (i - 2) * 64);
                kpiece(8 + i);
            });
            WL0();
            ARR_MARK(16);
            BAR();
            SCHED();
            kpiece(16);
            f32x4 p[2][2];
#pragma unroll
            for (int i = 0; i < 4; i++) p[i >> 1][i & 1] = MFMA(wf[i & 1], mk8(kw[i >> 1][0], kw[i >> 1][1], kw[i >> 1][2], kw[i >> 1][3]), zero4);
            SCHED();
            STAMPW(2, 0);
#pragma unroll
            for (int mi = 0; mi < 4; mi++) rst[mi] = bf2f(rsu[mi]);
#pragma unroll
            for (int a = 0; a < 2; a++)
#pragma unroll
                for (int dt = 0; dt < 2; dt++) {
                    const f32x4 x0 = acc[2 * a][4 + dt] + mbv[2 * a] * csv[dt], x1 = acc[2 * a + 1][4 + dt] + mbv[2 * a + 1] * csv[dt];
                    *(LAS v4u*)(aK + (L_V - L_KH) + w * 2048 + (a * 2 + dt) * 1024) = (v4u){pk2(x0[0], x0[1]), pk2(x0[2], x0[3]), pk2(x1[0], x1[1]), pk2(x1[2], x1[3])};
                }
            SCHED();
            rst[0] = __builtin_amdgcn_rcpf(rst[0]); rst[1] = __builtin_amdgcn_rcpf(rst[1]);
            bf16x8 f[4];
            struct HookC {
                decltype(v_reads)& vr; decltype(sd_reads)& sr;
                DI void operator()(int k, float& x) const { if (k == 10) { PIN(x); WAITL0(); ARR_MARK(17); BAR(); PIN(x); } if (k == 3) { PIN(x); vr(); sr(); PIN(x); } }
                DI void operator()(int, float&, float&) const {}
            };
            softmax2<true>(p, rst[0], rst[1], g >= 2, f, HookC{v_reads, sd_reads}, ImgPut{ia});
#pragma unroll
            for (int ft = 0; ft < 2; ft++) wfq[ft] = cat8(tr_read(aW + ft * 32 + 4096), tr_read(aW + ft * 32 + 5120));
            csq[0] = ldsf4(aC); csq[1] = ldsf4(aC + 64);
            STAMPW(3, 0);
            kv_tile([]() {});
            STAMPW(4, 0);
            SCHED();
            group(acc, A, B[1], 4, [&](int i) { if (i == 2 || i == 3) { rst[i] = __builtin_amdgcn_rcpf(rst[i]); PIN(rst[i]); } });
            group(acc, A, B[2], 5, [&](int) {});
            asm volatile("s_nop 7\n\ts_nop 1");
            SCHED();
        }
        bf16x8 phq[2][4];
        v4u kg[6];
        unsigned sG3 = WS_G3 + (hq * 2 + hh) * 4096, sGZ = WS_GZ + (hq * 2 + hh) * 128;
        {
            f32x4 pq[2][2][2];
#pragma unroll
            for (int mi = 0; mi < 4; mi++) {
                const f32x4 x0 = acc[mi][0] + mbt[mi] * csq[0], x1 = acc[mi][1] + mbt[mi] * csq[1];
                const bf16x8 qf = mk8(pk2(x0[0], x0[1]), pk2(x0[2], x0[3]), pk2(x1[0], x1[1]), pk2(x1[2], x1[3]));
                pq[mi >> 1][mi & 1][0] = MFMA(wfq[0], qf, zero4); pq[mi >> 1][mi & 1][1] = MFMA(wfq[1], qf, zero4);
            }
            MFMA_SETTLE2(pq[0]); MFMA_SETTLE2(pq[1]);
            softmax2<false>(pq[0], rst[0], rst[1], g >= 2, phq[0]);
#pragma unroll
            for (int i = 0; i < 4; i++) PIN(phq[0][i]);
            SCHED();
            kv_reduce();
            SCHED();
            res_loads();
            SCHED();
            BAR();
            SCHED();
            softmax2<false>(pq[1], rst[2], rst[3], g >= 2, phq[1]);
#pragma unroll
            for (int i = 0; i < 4; i++) { PIN(phq[0][i]); PIN(phq[1][i]); }
        }
        STAMPW(5, 0);
        STAMPW(26, 0);
        v2u fv = {tag ^ 0x5bd1e995u, tag};
        unsigned sF = WS_F4 + ((rt * 4 + w) * 16 + hq) * 8, vF = l == 0 ? 0 : OOB;
        {
            unsigned vO = l * 16 + hh * 1024, sO = WS_P4 + ((rt * 8 + wm * 4) * 32 + hq * 2) * 1024;
            asm volatile("" : "+s"(sO), "+s"(sF), "+s"(sG3), "+s"(sGZ));
            PIN(fv); PIN(vF);
            SLEEP(10);
            v4u ov[4];
#define PH(mi, s) phq[(mi) >> 1][2 * ((mi) & 1) + (s)]
#define KVA(s, dt) __builtin_bit_cast(bf16x8, kg[2 + 2 * (s) + (dt)])
            auto pairup = [&](v4u& a, v4u& b) {
                unsigned a2 = a[2], a3 = a[3], b0 = b[0], b1 = b[1];
                asm volatile("v_swap_b32 %0, %2\n\tv_swap_b32 %1, %3" : "+v"(a2), "+v"(a3), "+v"(b0), "+v"(b1));
                a[2] = a2; a[3] = a3; b[0] = b0; b[1] = b1;
            };
            f32x4 dn[4], n0[4], n1[4];
            float id[4];
            unsigned spins = 0, lim, m;
#pragma nounroll
            do {
                FRESH();
#pragma unroll
                for (int k = 0; k < 6; k++) { kg[k] = k >= 2 ? LD16(l * 16 + (k - 2) * 1024, sG3) : LD16(g * 16 + k * 64, sGZ); SCHED(); }
                lim = budget<-2, -1>(spins);
                m = 0;
#pragma unroll
                for (int i = 0; i < 8; i++) {
                    dn[i & 3] = MFMA(__builtin_bit_cast(bf16x8, kg[i >> 2]), PH(i & 3, i >> 2), i < 4 ? zero4 : dn[i & 3]);
                    if (i >= 1 && i < 7) { m = umx(umx(m, kg[i - 1][0]), kg[i - 1][1]); PIN(m); m = umx(umx(m, kg[i - 1][2]), kg[i - 1][3]); }
                    if (i == 5) pairup(kg[2], kg[3]);
                    if (i == 7) pairup(kg[4], kg[5]);
                    SCHED();
                }
            } while (__any(m > lim));
            STAMPW(6, 0);
#pragma unroll
            for (int mi = 0; mi <= 4; mi++) {
                if (mi < 4) {
                    n0[mi] = MFMA(KVA(0, 0), PH(mi, 0), zero4); n1[mi] = MFMA(KVA(0, 1), PH(mi, 0), zero4);
                    n0[mi] = MFMA(KVA(1, 0), PH(mi, 1), n0[mi]); n1[mi] = MFMA(KVA(1, 1), PH(mi, 1), n1[mi]);
                    id[mi] = __builtin_amdgcn_rcpf(rbf(rbf(dn[mi][0]) + 1e-6f));
                    asm volatile("" :: "v"(dn[mi]));
                }
                if (mi > 0) {
                    const f32x4 a = n0[mi - 1] * id[mi - 1], b = n1[mi - 1] * id[mi - 1];
                    ov[mi - 1] = (v4u){pk2(a[0], a[1]), pk2(a[2], a[3]), pk2(b[0], b[1]), pk2(b[2], b[3])};
                    ST16(ov[mi - 1], vO ^ ((mi - 1) * 32768 + ((mi - 1) & 1) * 1024), sO);
                }
                if (mi < 4) { SGB(0x8, 1); SGB(0x2, 6); SGB(0x8, 1); SGB(0x2, 3); SGB(0x8, 1); SGB(0x2, 3); SGB(0x8, 1); SGB(0x2, 4); }
                SCHED();
            }
#pragma unroll
            for (int mi = 0; mi < 4; mi++) PIN(ov[mi]);
        }
        BAR();
        waitv<0>();
        __builtin_amdgcn_raw_buffer_store_b64(fv, rsWS, vF, sF, AUX_SC1);
        STAMPW(7, 0);
    } else {
        const unsigned pw = lw & 1;
        const rsrc_t rsG = __builtin_amdgcn_make_buffer_rsrc((void*)WG, 0, 1024 * 3584 * 2, 0x00020000);
        const rsrc_t rsU = __builtin_amdgcn_make_buffer_rsrc((void*)WU, 0, 1024 * 3584 * 2, 0x00020000);
        unsigned vwl = kr * 57344 + (l & 7) * 16;
        unsigned sg = (kq * 256 + lw * 64) * 7168 + cg1 * 128, su = (kq * 256 + lw * 64) * 7168 + cg1u * 128;
        auto wloads = [&](int n0, int n1) {
#pragma unroll
            for (int n = n0; n < n1; n++)
                asm volatile("buffer_load_dwordx4 %0, %1, %2, %3 offen nt" : "=v"(wreg[n]) : "v"(vwl), "s"(n < 8 ? rsG : rsU), "s"((n < 8 ? sg : su) + (n & 7) * 7168));
        };
        auto repack = [&](unsigned gI) {
            const unsigned s2 = (2 * g + (q4 >> 1)) & 3;
            bf16x8 f[4];
#pragma unroll
            for (int nt = 0; nt < 4; nt++) {
                const unsigned a = ldsb + L_WO + gI * 4096 + (4 * g + q4) * 128 + ((((nt >> 1) * 2 + (p4 >> 1)) ^ s2) * 32) + (p4 & 1) * 16 + (nt & 1) * 8;
                f[nt] = cat8(tr_read(a), tr_read(a + 2048));
            }
            WAITL0();
#pragma unroll
            for (int nt = 0; nt < 4; nt++) *(LAS bf16x8*)(ldsb + L_WO + gI * 4096 + nt * 1024 + l * 16) = f[nt];
        };
        unsigned aK, aW, aS;
        auto k_pre = [&]() {
            pre_addr();
            aK = ldsb + L_KH + lw * 2048 + l * 16; aW = ldsb + L_WPHI + (lw >> 1) * 2048 + (4 * g + q4) * 64 + p4 * 8; aS = ldsb + L_SD + (pw * 64 + 32 + t16) * 2;
            PIN(aK); PIN(aW); PIN(aS);
        };
        auto kfeat = [&](auto hook) {
            bf16x8 wf[2];
            float rst[2];
#pragma unroll
            for (int ft = 0; ft < 2; ft++) {
                wf[ft] = cat8(tr_read(aW + ft * 32), tr_read(aW + ft * 32 + 1024));
                rst[ft] = __builtin_amdgcn_rcpf(lds_bf(aS + ft * 32));
            }
            sd_reads();
            PIN(wf[0]); PIN(wf[1]); PIN(rst[0]); PIN(rst[1]);
            SCHED();
            ARR_MARK(16);
            BAR();
            SCHED();
            const bf16x8 kf0 = lds128(aK), kf1 = lds128(aK + 1024);
            SCHED();
            hook(-1, rst[0]);
            hook(0, rst[0]);
            SCHED();
            f32x4 p[2][2];
            p[0][0] = MFMA(wf[0], kf0, zero4); p[0][1] = MFMA(wf[1], kf0, zero4);
            p[1][0] = MFMA(wf[0], kf1, zero4); p[1][1] = MFMA(wf[1], kf1, zero4);
            MFMA_SETTLE2(p);
            bf16x8 f[4];
            softmax2<true>(p, rst[0], rst[1], g >= 2, f, hook, ImgPut{ia});
            SCHED();
            hook(5, rst[0]);
            SCHED();
            STAMPW(3, 4); STAMPW(3, 7);
            kv_tile([&]() { hook(6, rst[0]); });
            STAMPW(4, 4); STAMPW(4, 7);
        };
        auto wblk = [&](int k) {
            constexpr unsigned T[8] = {0x19181716, 0x041c1b1a, 0x08070605, 0x0c0b0a09, 0x1413120d, 0x11100015, 0x1d030201, 0x1f1e0f0e};
            return (T[k] >> (8 * lw)) & 31;
        };
        auto wo_init = [&]() {
#pragma unroll
            for (int i = 0; i < 4; i++) { vwo[i] = vWO + i * (16384 - 1024); PIN(vwo[i]); }
        };
        unsigned hs[8];
        auto wl_pre = [&]() { asm volatile("" : "+s"(sg), "+s"(su)); PIN(vwl); };
        auto wo_pre = [&]() {
            wo_init();
#pragma unroll
            for (int k = 0; k < 8; k++) { hs[k] = wo_so(wblk(k)); asm volatile("" : "+s"(hs[k])); }
        };
        auto wo_rng = [&](int k0, int k1) {
#pragma unroll
            for (int k = k0; k < k1; k++) dma16x4(ldsb + L_WO + wblk(k) * 4096, vwo, rsWO, hs[k]);
        };
        auto pf = [&](int i) {
            if (i == 0) { wloads(0, 6); SCHED(); if (lw < 2) wo_pre(); SCHED(); }
            if (i < 3 && !ffn) wo_rng(i, i + 1);
            if (i == 1 || i == 2) wloads(6 * i, i == 1 ? 12 : 16);
        };
        struct Hook {
            decltype(pf)& f; decltype(v_reads)& vr;
            DI void operator()(int k, float& x) const { if (k == 10) return; PIN(x); if (k == 3) { ARR_MARK(17); BAR(); vr(); } f(3 + k); PIN(x); }
            DI void operator()(int k, float& x, float& y) const { PIN(y); f(3 + k); PIN(x); }
        };
        auto tail16 = [&]() {
            kfeat(Hook{pf, v_reads});
            if (ffn) wo_rng(0, 3);
            wo_rng(3, 5);
            static_for<3>([&](auto kc_) {
                constexpr int k = decltype(kc_)::value;
                waitv<4 * (4 - k)>();
                repack(wblk(k));
            });
            BAR();
            res_loads();
            wo_rng(5, 7);
            static_for<4>([&](auto kc_) {
                constexpr int k = decltype(kc_)::value + 3;
                waitv<4 * (6 - k)>();
                repack(wblk(k));
            });
            PIN(xres[0]); PIN(xres[1]); PIN(wrv[0]); PIN(wrv[1]);
            perm1();
            SCHED();
            BAR();
            wo_rng(7, 8);
        };
        auto vT23 = [&]() {
            const unsigned n = (idx * 2 + pw) * 64 + l, r = n / 12, c = n % 12;
            return idx < 24 ? ((r & 127) + (r >> 7) * 256 + 128) * 6144 + (c >> 2) * 2048 + (((xcd >> 1) * 4 + (c >> 2 ? 0 : 4) + (c & 3)) & 15) * 128 : OOB;
        };
        if (lw < 2) {
            const unsigned sA = (rt * 128 + lw * 64) * 2048, sB = lw * 32 * 6144 + hp * 128, sBq = lw * 32 * 6144 + hq * 128;
            unsigned buf = 1;
            unsigned va[4], vb[4];
#pragma unroll
            for (int i = 0; i < 4; i++) { va[i] = vA + i * (16384 - 1024); vb[i] = ((i & 1) ? vB1 : vB0) + i * (49152 - 1024); PIN(va[i]); PIN(vb[i]); }
            auto stage = [&](unsigned s, unsigned bf, int i0, int i1) {
                const unsigned kt = (s + rot) & 15;
                const unsigned lb = ldsb + bf * STAGE_B;
                const unsigned sa = sA + kt * 128, sb = kt * 393216;
                if (i0 <= 0 && 0 < i1) dma16x4(lb + lw * 8192, va, rsX, sa);
                if (i0 <= 1 && 1 < i1) dma16x4(lb + lw * 8192 + 4096, va, rsX, sa + 65536);
                if (i0 <= 2 && 2 < i1) dma16x4(lb + 16384 + 8192 + lw * 4096, vb, rsW, sb + sB + 2048);
                if (i0 <= 3 && 3 < i1) dma16x4(lb + 16384 + 16384 + lw * 4096, vb, rsW, sb + sB + 4096);
                if (i0 <= 4 && 4 < i1) dma16x4(lb + 16384 + lw * 4096, vb, rsW, sb + sBq);
            };
            stage(1, 1, 0, 4);
            SCHED();
            unsigned vt = vT23();
            PIN(vt);
            wl_pre(); k_pre();
            waitv<16>(); ARR_MARK(0); BAR();
            stage(1, 1, 4, 5);
            touch(vt, rsW, (xcd & 1) * 512 * 6144);
            waitv<1>(); ARR_MARK(1); BAR();
            stage(2, 2, 0, 5); stage(3, 0, 0, 3);
            waitv<12>(); ARR_MARK(2); BAR();
            stage(3, 0, 3, 5); stage(4, 1, 0, 4);
            waitv<16>(); ARR_MARK(3); BAR();
            stage(4, 1, 4, 5);
            buf = 2;
#pragma nounroll
            for (unsigned s = 5; s < 16; s++) {
                if (s > 5) { waitv<20>(); ARR_MARK(s - 2); BAR(); }
                stage(s, buf, 0, 5);
                buf = buf == 2 ? 0 : buf + 1;
            }
            waitv<20>(); ARR_MARK(14); BAR();
            pf(0);
            if (ffn) waitv<6>(); else waitv<10>();
            ARR_MARK(15); BAR();
            pf(1);
            tail16();
            READ_TAG();
        } else {
            SCHED();
            unsigned v3;
            const unsigned h3 = (xcd >> 1) * 4 + 3, kv3 = !(xcd & 2), wv = idx * 2 + pw;
            auto line3 = [&](unsigned per, unsigned rows, auto rowf) {
                const unsigned n = wv * (kv3 ? per : (per + 2) / 3) + l, r3 = kv3 ? n / 3 : n, c3 = kv3 ? n % 3 : 0;
                return (idx < 24 && l < (kv3 ? per : (per + 2) / 3) && r3 < rows) ? rowf(r3) * 6144 + c3 * 2048 + ((c3 ? h3 : h3 + 4) & 15) * 128 : OOB;
            };
            v3 = line3(32, 512, [](unsigned r) { return r; });
            touch(line3(16, 256, [](unsigned r) { return (r & 127) + (r >> 7) * 256; }), rsW, (xcd & 1) * 512 * 6144);
            {
                const rsrc_t rsP = __builtin_amdgcn_make_buffer_rsrc((void*)WPHI, 0, 32 * 1024 * 2, 0x00020000);
#pragma unroll
                for (int i = 0; i < 4; i++) dma16(ldsb + L_WPHI + pw * 4096 + i * 1024, l * 16, rsP, (pw ? hq : hp) * 4096 + i * 1024);
            }
            v4u sv = {SENT, SENT, SENT, SENT};
            unsigned vc4 = l < 16 ? WS_G3 + (hp * 4 + (rt >> 2)) * 2048 + pw * 1024 + (rt & 3) * 256 + l * 16
                         : l < 24 ? WS_P2 + (hp * 16 + rt) * P2_STRIDE + 8192 + pw * 128 + (l - 16) * 16
                         : (l < 32 && pw == 0 && (rt & 7) == 0) ? WS_GZ + (hp * 2 + (rt >> 3)) * 128 + (l - 24) * 16 : OOB;
            auto clears = [&](int i0, int i1) {
#pragma unroll
                for (int i = i0; i < i1; i++) {
                    if (i < 4) ST16(sv, l * 16, WS_P2 + (hp * 16 + rt) * P2_STRIDE + pw * 4096 + i * 1024);
                    if (i == 4) ST16(sv, vc4, 0);
                    if (i > 4) ST16(sv, l * 16, WS_TL + (bid * 4 + pw * 2 + i - 5) * 1024);
                }
                FRESH();
            };
            PIN(vc4); PIN(sv);
            unsigned ca = ldsb + 16384 + pw * 8192 + (8 * g + q4) * 128 + (((rt & 3) ^ (((q4 >> 1) & 1) | ((g & 1) << 1))) * 32) + p4 * 8;
            PIN(ca);
            bf16x8 cf[4];
            f32x4 cd[2] = {zero4, zero4};
            auto cs_read = [&](int st) {
#pragma unroll
                for (int i = 0; i < 4; i++) cf[i] = cat8(tr_read(ca + (st % 3) * STAGE_B + i * 4096), tr_read(ca + (st % 3) * STAGE_B + i * 4096 + 512));
                SCHED();
            };
            READ_TAG();
            waitv<5>();
            ARR_MARK(0);
            BAR();
            SCHED();
            cs_read(0);
            FRESH();
            const rsrc_t rsC = __builtin_amdgcn_make_buffer_rsrc((void*)pc0, 0, clen, 0x00020000);
            auto code_pf = [&]() { touch(l < 6 ? l * 128 : OOB, rsC, (idx * 2 + pw) * 768); };
            v4u gl[4];
            float s0 = 0.f, s1 = 0.f;
            unsigned bad;
            v2u gs;
            unsigned vgl = (l < 48 ? l : 0) * 16;
            unsigned aw0 = ldsb + L_CS + pw * 384 + (vgl >> 1), aw1 = ldsb + L_MB + (pw * 64 + l) * 4, aw2 = ldsb + L_SD + (pw * 64 + l) * 2;
            static_for<15>([&](auto tc) {
                constexpr int t = decltype(tc)::value;
                WAITL0();
                ARR_MARK(t + 1);
                BAR();
                SCHED();
                if constexpr (t < 4) {
                    __builtin_amdgcn_s_setprio(2);
#pragma unroll
                    for (int i = 0; i < 4; i++) cd[i & 1] = MFMA(ones, cf[(i & 1) * 2 + (i >> 1)], cd[i & 1]);
                    SCHED();
                    __builtin_amdgcn_s_setprio(0);
                    if constexpr (t < 3) cs_read(t + 1);
                    else __builtin_amdgcn_raw_buffer_store_b64((v2u){__float_as_uint(g ? cd[1][0] : cd[0][0]), tag}, rsWS, (g == 1 || g + pw == 0) ? g * 512 + t16 * 8 : OOB,
                                                               WS_G1 + ((hp * 4 + (rt >> 2)) * 192 + pw * 64 + (rt & 3) * 16) * 8, AUX_SC1);
                }
                if constexpr (t == 0) touch(v3, rsW, (~xcd & 1) * 512 * 6144);
                if constexpr (t == 12) code_pf();
                if constexpr (t == 8) {
                    waitv<0>();
                    __builtin_amdgcn_raw_buffer_store_b64((v2u){0u, tag}, rsWS, l == 0 ? 0 : OOB, WS_GC + ((hp * 2 + pw) * 16 + rt) * 8, AUX_SC1);
                }
                if constexpr (t == 8 || t == 9) {
#pragma unroll
                    for (int r = (t - 8) * 2; r < (t - 8) * 2 + 2; r++)
                        asm volatile("buffer_load_dwordx4 %0, %1, %2, %3 offen sc1" : "=v"(gl[r]) : "v"(vgl), "s"(rsWS), "s"(WS_G1 + ((hp * 4 + r) * 192 + pw * 96) * 8) : "memory");
                }
                if constexpr (t == 6) {
                    asm volatile("buffer_load_dwordx2 %0, %1, %2, %3 offen sc1" : "=v"(gs) : "v"(l * 8), "s"(rsWS), "s"(WS_G0 + (rt * 128 + pw * 64) * 8) : "memory");
                }
                if constexpr (t == 11) {
                    waitv<0>();
                    PIN(gs);
                    bad = gs[1] ^ tag;
#pragma unroll
                    for (int r = 0; r < 4; r++) {
                        PIN(gl[r]);
                        bad |= (gl[r][1] ^ tag) | (gl[r][3] ^ tag);
                        s0 += __uint_as_float(gl[r][0]); s1 += __uint_as_float(gl[r][2]);
                    }
                    PIN(s0); PIN(s1); PIN(bad);
                }
                if constexpr (t == 12) {
                    if (RARE(!__all(bad == 0))) {
                        s0 = 0.f; s1 = 0.f;
#pragma nounroll
                        for (unsigned r = 0; r < 4; r++) {
                            unsigned a[2];
                            sweep<2>(rsWS, vgl, WS_G1 + ((hp * 4 + r) * 192 + pw * 96) * 8, 8, tag, a);
                            s0 += __uint_as_float(a[0]); s1 += __uint_as_float(a[1]);
                        }
                        unsigned c[1];
                        sweep<1>(rsWS, l * 8, WS_G0 + (rt * 128 + pw * 64) * 8, 0, tag, c);
                        gs[0] = c[0];
                    }
                    *(LAS f32x2*)(aw0) = (f32x2){-s0, -s1};
                    stsf(aw1, bflo(gs[0]));
                    *(LAS u16*)(aw2) = (u16)(gs[0] >> 16);
                    SCHED();
                }
                if constexpr (t == 5) k_pre();
                if constexpr (t == 6) { wl_pre(); wo_pre(); }
                if constexpr (t == 7) { PIN(vgl); PIN(aw0); PIN(aw1); PIN(aw2); }
                clears(t < 7 ? t : 7, t < 7 ? t + 1 : 7);
                if constexpr (t == 12) asm volatile("buffer_load_dwordx2 %0, %1, %2, %3 offen sc1" : "=v"(gs) : "v"((l & 31) * 8), "s"(rsWS), "s"(WS_GC + (pw ? hq : hp) * 256) : "memory");
                if constexpr (t == 13) {
                    waitv<0>();
                    PIN(gs);
                    if (RARE(!__all(gs[1] == tag))) { unsigned c[1]; sweep<1>(rsWS, (l & 31) * 8, WS_GC + (pw ? hq : hp) * 256, 0, tag, c); }
                    SCHED();
                    pf(0);
                }
                if constexpr (t == 14) pf(1);
            });
            tail16();
        }
        WAITL0();
        STAMPW(7, 4); STAMPW(7, 7);
    }
    const unsigned sFl = WS_F4 + (rt2 * 4 + (lw >> 1) + 2 * half) * 128;
    vD = kr * 16384 + (l & 7) * 16; sD = (kc * 256 + lw * 64) * 2048 + cg * 128;
    PIN(vD);
    asm volatile("" : "+s"(sD), "+s"(rsD));
    if (half) dloads(0, 1);
    SCHED();
    ARR_MARK(20);
    BAR();
    ARR_END();
    const unsigned wb = ldsb + L_WO + half * 65536 + l * 16;
    bf16x8 bb[2][4];
#pragma unroll
    for (int nt = 0; nt < 4; nt++) { bb[0][nt] = lds128(wb + nt * 1024); PIN(bb[0][nt]); }
    unsigned so[16], va0 = l * 16 + half * 32768, va1 = l * 16 + (half ^ 1) * 32768 + 1024;
    unsigned aXw = ldsb + L_XB + w * 4096 + l * 16, aXr = ldsb + L_XB + (w ^ 4) * 4096 + l * 16, sG5 = WS_G5 + ((rt2 * 8 + lw * 2 + half) * 16 + ct) * 256;
    unsigned vG5 = g == q4 ? t16 * 16 : OOB;
#pragma unroll
    for (int st = 0; st < 16; st++) { so[st] = WS_P4 + (rt2 * 8 + lw * 2) * 32768 + ((2 * st + rot2) & 31) * 1024; asm volatile("" : "+s"(so[st])); }
    unsigned wt[2];
#pragma unroll
    for (int i = 0; i < 2; i++) { wt[i] = ldsb + L_WO + half * 65536 + (4 * g + q4) * 128 + (((i * 2 + (p4 >> 1)) ^ ((2 * g + (q4 >> 1)) & 3)) * 32) + (p4 & 1) * 16; PIN(wt[i]); }
    PIN(va0); PIN(va1); PIN(aXw); PIN(aXr); PIN(vG5);
    asm volatile("" : "+s"(sG5));
    SLEEP(12);
    poll16(rsWS, sFl, tag, l);
    FRESH();
    SCHED();
    STAMPW(8, 0); STAMPW(8, 4);
    unsigned swc;
    f32x4 acc2[2][4];
    bf16x8 sop[2];
    {
        auto aload = [&](int st, int m2) { return LD16(m2 ? va1 : va0, so[st]); };
        constexpr int PD = 2;
        v4u af[PD][2];
#pragma unroll
        for (int i = 0; i < PD; i++) {
#pragma unroll
            for (int m2 = 1; m2 >= 0; m2--) af[i][m2] = aload(i, m2);
            SCHED();
        }
#pragma unroll
        for (int nt = 0; nt < 4; nt++) {
            const unsigned x0 = xres[nt >> 1][2 * (nt & 1)], x1 = xres[nt >> 1][2 * (nt & 1) + 1];
            acc2[0][nt] = (f32x4){bflo(x0), bfhi(x0), bflo(x1), bfhi(x1)};
            acc2[1][nt] = zero4;
        }
        unsigned m0 = p4 == 0 ? 0x3F803F80u : 0u, m1 = p4 == 1 ? ~0u : 0u;
        PIN(m0); PIN(m1);
#pragma unroll
        for (int i = 0; i < 2; i++) {
            v4u u;
#pragma unroll
            for (int j = 0; j < 4; j++) u[j] = (wrv[i][j] & m1) | m0;
            sop[i] = __builtin_bit_cast(bf16x8, u);
            PIN(sop[i]);
        }
        {
            float sw = 0.f;
#pragma unroll
            for (int i = 0; i < 8; i++) sw = dot2(wrv[i >> 2][i & 3], 0x3F803F80u, sw);
            swc = __float_as_uint(xrow_sum(sw));
            PIN(swc);
        }
        SCHED();
#pragma unroll
        for (int st = 0; st < 16; st++) {
            if (st == 2) BAR();
            if (st + 1 < 16) {
#pragma unroll
                for (int nt = 0; nt < 4; nt++) {
                    if (st + 1 < 14) bb[(st + 1) % (2)][nt] = lds128(wb + (st + 1) * 4096 + nt * 1024);
                    else bb[(st + 1) % (2)][nt] = cat8(tr_read(wt[nt >> 1] + (st + 1) * 4096 + (nt & 1) * 8), tr_read(wt[nt >> 1] + (st + 1) * 4096 + (nt & 1) * 8 + 2048));
                }
            }
#pragma unroll
            for (int mm = 0; mm < 2; mm++) {
                const int m2 = mm ^ 1;
                const bf16x8 a = __builtin_bit_cast(bf16x8, af[st % PD][m2]);
#pragma unroll
                for (int nt = 0; nt < 4; nt++) acc2[m2][nt] = MFMA(bb[st % (2)][nt], a, acc2[m2][nt]);
            }
            if (st + PD < 16) {
#pragma unroll
                for (int m2 = 1; m2 >= 0; m2--) af[st % PD][m2] = aload(PD + st, m2);
            }
#pragma unroll
            for (int i = 0; i < (st + 1 < 14 ? 4 : 8); i++) { SGB(0x8, 1); SGB(0x100, 1); }
            SCHED();
        }
    }
    STAMPW(9, 0); STAMPW(9, 4);
#pragma unroll
    for (int nt = 0; nt < 4; nt++) stsf4(aXw + nt * 1024, acc2[1][nt]);
    SCHED();
    if (half) dloads(1, 4);
    SCHED();
    WAITL0();
    if (half) __builtin_amdgcn_s_setprio(1);
    BAR();
    SCHED();
    v4u hpk[2];
    {
        f32x4 pa[4];
#pragma unroll
        for (int nt = 0; nt < 4; nt++) pa[nt] = ldsf4(aXr + nt * 1024);
        SCHED();
#pragma unroll
        for (int i = 0; i < 8; i++) {
            const f32x4 a = acc2[0][i >> 1] + pa[i >> 1];
            hpk[i >> 2][i & 3] = pk2(a[2 * (i & 1)], a[2 * (i & 1) + 1]);
        }
        const bf16x8 h0 = __builtin_bit_cast(bf16x8, hpk[0]), h1 = __builtin_bit_cast(bf16x8, hpk[1]);
        f32x4 gq = MFMA(h0, h0, zero4), sd = MFMA(sop[0], h0, zero4);
        gq = MFMA(h1, h1, gq); sd = MFMA(sop[1], h1, sd);
        float q0 = (l & 1) ? gq[1] : gq[0], q1 = (l & 1) ? gq[3] : gq[2];
        PIN(q0); PIN(q1);
        const v4u pv = {__float_as_uint(sd[0]), __float_as_uint((l & 2) ? q1 : q0), __float_as_uint(sd[1]), swc};
        ST16(pv, vG5, sG5);
        asm volatile("s_nop 1" :: "v"(pv));
    }
    __builtin_amdgcn_s_setprio(0);
    STAMPW(10, 0); STAMPW(10, 4);
    v4u wp2[8];
    auto perm2 = [&]() {
#pragma unroll
        for (int i = 16; i < 24; i++) PIN(wreg[i]);
#pragma unroll
        for (int ip = 0; ip < 4; ip++)
#pragma unroll
            for (int c = 0; c < 4; c++) {
                const unsigned a = wreg[16 + 2 * ip][c], b = wreg[16 + 2 * ip + 1][c];
                wp2[2 * c][ip] = __builtin_amdgcn_perm(b, a, 0x05040100);
                wp2[2 * c + 1][ip] = __builtin_amdgcn_perm(b, a, 0x07060302);
            }
#pragma unroll
        for (int c = 0; c < 8; c++) PIN(wp2[c]);
    };
    const unsigned mt = rt2 * 8 + lw * 2 + half;
    {
        v4u pv[4];
        unsigned sK = WS_GK + (ct * 128 + mt) * 4, vK = l == 0 ? 0 : OOB, rk = 2047u - R2;
        asm volatile("" : "+s"(sK));
        PIN(vK); PIN(rk);
        unsigned sS = WS_G5 + mt * 4096;
        asm volatile("" : "+s"(sS));
        SLEEP(18);
        __builtin_amdgcn_s_setprio(1);
        float T[4];
        sweepn<4>([&](int k) { return LD16(l * 16 + k * 1024, sS); }, pv, [&]() {
            const f32x4 ps = (__builtin_bit_cast(f32x4, pv[0]) + __builtin_bit_cast(f32x4, pv[1])) + (__builtin_bit_cast(f32x4, pv[2]) + __builtin_bit_cast(f32x4, pv[3]));
            const float r = xrow_scatter<false>(ps[0], ps[1], ps[2], ps[3]);
            xrow_gather(r, T);
            return r;
        });
        STAMPW(21, 0); STAMPW(21, 4);
        const float mean = T[0] * (1.0f / 1024.0f), var = fmaxf(T[1] * (1.0f / 1024.0f) - mean * mean, 0.f);
        const unsigned sg = pk2(mean, __builtin_amdgcn_sqrtf(rbf(rbf(var) + 1e-5f)));
        const float mb2 = bflo(sg), isd2 = __builtin_amdgcn_rcpf(bfhi(sg));
        const float rsc = (T[2] - mb2 * T[3]) * isd2;
        const unsigned rb = pk2(rsc, rsc);
        const unsigned key = ((rb ^ ((unsigned)((int)rb >> 31) | 0x80000000u)) & 0xffff0000u) | rk;
        unsigned kb = umx(key, dppu<0x128>(key));
        kb = umx(kb, dppu<0x124>(kb)); kb = umx(kb, dppu<0x122>(kb)); kb = umx(kb, dppu<0x121>(kb));
        ST4(kb, vK, sK);
        SCHED();
        __builtin_amdgcn_s_setprio(0);
        if (half) { dloads(4, 8); SCHED(); }
        const bool cand = key == kb;
        v4u e[2], outv[2];
#pragma unroll
        for (int i = 0; i < 8; i++) {
            const float h0 = bflo(hpk[i >> 2][i & 3]), h1 = bfhi(hpk[i >> 2][i & 3]);
            const unsigned d = pk2(h0 - mb2, h1 - mb2);
            e[i >> 2][i & 3] = pk2(bflo(d) * isd2, bfhi(d) * isd2);
        }
        ST16(e[0], cand ? g * 16 : OOB, WS_C + (mt * 16 + ct) * 128);
        ST16(e[1], cand ? g * 16 + 64 : OOB, WS_C + (mt * 16 + ct) * 128);
        SCHED();
        STAMPW(22, 0); STAMPW(22, 4);
#pragma unroll
        for (int i = 0; i < 8; i++)
            outv[i >> 2][i & 3] = pk2(bflo(hpk[i >> 2][i & 3]) + bflo(e[i >> 2][i & 3]), bfhi(hpk[i >> 2][i & 3]) + bfhi(e[i >> 2][i & 3]));
        if (cand) {
#pragma unroll
            for (int i = 0; i < 2; i++) { *(LAS v4u*)(ldsb + L_STASH + w * 256 + i * 64 + g * 16) = hpk[i]; *(LAS v4u*)(ldsb + L_STASH + w * 256 + 128 + i * 64 + g * 16) = e[i]; }
        }
#pragma unroll
        for (int i = 0; i < 2; i++) *(LAS v4u*)(ldsb + L_OUT + (lw * 2 + half) * 2048 + t16 * 128 + (((i * 4 + g) ^ (l & 7)) * 16)) = outv[i];
    }
    const rsrc_t rsO = __builtin_amdgcn_make_buffer_rsrc((void*)OUT, 0, 2048 * 1024 * 2, 0x00020000);
    unsigned aG = ldsb + L_WO + 512;
    PIN(aG);
    unsigned vX = (2 * g + (l & 1)) * 16, aP = (2 * g + (l & 1)) * 32, aR1 = ldsb + L_RED + lw * 4352 + l * 16, vl4 = l * 4;
    unsigned aR2 = ldsb + L_RED + ((lw & 1) * 2 + ((l >> 2) & 1)) * 1088 + kr * 16 + p4 * 4;
    unsigned sX = WS_C + kq * 512 + lw * 128, sG8w = WS_G8 + ((kq * 2 + lw) * 3584 + (lw ? cg1u : cg1) * 64) * 4, sG8r = WS_G8 + (kc * 256 + lw * 64) * 4;
    unsigned sG9 = WS_G9 + (cg * 14 + kc) * 256;
    unsigned aR3 = aR1 + 17408 - lw * 2176;
    PIN(vX); PIN(aP); PIN(aR1); PIN(aR2); PIN(aR3); PIN(vl4);
    asm volatile("" : "+s"(sX), "+s"(sG8w), "+s"(sG8r), "+s"(sG9));
    SCHED();
    if (w == 1) {
        unsigned sK = WS_GK + (bid & 15) * 512;
        v2u hb[1];
        asm volatile("" : "+s"(sK));
        SLEEP(13);
        stsu(aG, wave_maxu(sweepz<1>([&](int) { return ldg(rsWS, l * 8, sK); }, hb)));
        STAMPR(23, 2);
    }
    if (half) perm2();
    STAMPW(24, 7);
    WAITL0();
    if (half) __builtin_amdgcn_s_setprio(1);
    BAR();
    STAMPW(11, 0); STAMPW(11, 4);
    const unsigned gkv = ldsu(aG);
    if (w < 4) {
        if (bid == 0 && tid == 0) __hip_atomic_store((gu32*)(ws + WS_EPOCH), tag, RLX_AGENT);
        v4u o2[4];
        unsigned vo[4];
#pragma unroll
        for (int i = 0; i < 4; i++) o2[i] = *(const LAS v4u*)(ldsb + L_OUT + w * 4096 + i * 1024 + kr * 128 + (((l & 7) ^ kr) * 16));
#pragma unroll
        for (int i = 0; i < 4; i++) { const unsigned R = mt * 16 + i * 8 + kr; vo[i] = 2047u - R != (gkv & 2047u) ? R * 2048 + (l & 7) * 16 : OOB; PIN(vo[i]); PIN(o2[i]); }
#pragma unroll
        for (int s = 0; s < 3; s++) {
            if (s) { BAR(); SLEEP(3); }
            SCHED();
#pragma unroll
            for (int i = 0; i < 4; i++)
                if ((i + 1) / 2 == s) __builtin_amdgcn_raw_buffer_store_b128(o2[i], rsO, vo[i], ct * 128, AUX_SC1);
            SCHED();
        }
        STAMPW(12, 0);
        return;
    }

    auto gemv = [&](const v4u& x, const v4u* wp, int n, unsigned a) {
        f32x4 d[16], o[4];
#pragma unroll
        for (int c = 0; c < n + 4; c++) {
            if (c < n) d[c] = MFMA(__builtin_bit_cast(bf16x8, x), __builtin_bit_cast(bf16x8, wp[c]), zero4);
            SCHED();
            if (c >= 4) {
                const int j = c - 4;
                float t = d[j][0] + dppf<0x128>(d[j][1]);
                asm volatile("" : "+v"(t) : "v"(d[j]));
                o[j >> 2][j & 3] = t;
                if ((j & 3) == 3) stsf4(a + (j >> 2) * 1088, o[j >> 2]);
            }
            SCHED();
        }
        return o[0][0];
    };
    auto sum4 = [&](unsigned a, unsigned stride) {
        float u = ldsf(a) + ldsf(a + stride), v = ldsf(a + 2 * stride) + ldsf(a + 3 * stride);
        PIN(u); PIN(v);
        return u + v;
    };
    if (ffn) {
        v4u xq[1];
        const unsigned vx = ((~gkv & 0x7f0u) << 7) + vX;
        sweepn<1>([&](int) { return LD16(vx, sX); }, xq, [&]() { return gemv(xq[0], &wp1[0][0], 16, aR1); });
        STAMPW(13, 4);
    }
    WAITL0();
    BAR();
    if (ffn) {
        if (lw < 2) ST4(__float_as_uint(sum4(aR2, 4352)), vl4, sG8w);
        STAMPW(14, 4);
        unsigned pv[8];
        v4u ap;
        SLEEP(12);
        sweepn<8>([&](int k) { return LD4(vl4, sG8r + k * 3584 * 4); }, pv, [&]() {
            const float gg = rbf((__uint_as_float(pv[0]) + __uint_as_float(pv[2])) + (__uint_as_float(pv[4]) + __uint_as_float(pv[6])));
            const float uu = rbf((__uint_as_float(pv[1]) + __uint_as_float(pv[3])) + (__uint_as_float(pv[5]) + __uint_as_float(pv[7])));
            const float act = rbf(gg * __builtin_amdgcn_rcpf(1.0f + __expf(-gg))) * uu;
            const unsigned apk = pk2(act, dppf<0xB1>(act));
#pragma unroll
            for (int ip = 0; ip < 4; ip++) ap[ip] = __builtin_amdgcn_ds_bpermute(aP + ip * 8, apk);
            return act;
        });
        STAMPW(15, 4);
        gemv(ap, wp2, 8, aR3);
    }
    WAITL0();
    BAR();
    if (lw != 0) return;
    if (ffn) ST4(__float_as_uint(sum4(aR2 + 17408, 2176)), vl4, sG9);
    SCHED();
    STAMPW(16, 4);
    const unsigned gkey = rfl(gkv), srow = 2047u - (gkey & 2047u);
    if ((srow >> 7) == rt2) {
        const float rs = __uint_as_float(((gkey & 0x80000000u) ? gkey ^ 0x80000000u : ~gkey) & 0xffff0000u);
        float sig = rbf(1.0f / (1.0f + __expf(-rs)));
        const unsigned sb = ldsb + L_STASH + (((srow >> 5) & 3) + ((srow >> 4) & 1) * 4) * 256 + (t16 * 4 + g) * 2;
        float hc = lds_bf(sb), lc = lds_bf(sb + 128);
        unsigned vo = (t16 * 4 + g) * 2, so_ = srow * 2048 + ct * 128, vy = l < 32 ? l * 16 + 3072 : OOB, sY = WS_G9 + ct * 3584;
        PIN(sig); PIN(hc); PIN(lc); PIN(vo); PIN(vy);
        asm volatile("" : "+s"(so_), "+s"(sY));
        v4u yp[4];
        float y;
        SLEEP(7);
        sweepn<4>([&](int k) { return LD16(k < 3 ? l * 16 + k * 1024 : vy, sY); }, yp, [&]() {
            const f32x4 ys = (__builtin_bit_cast(f32x4, yp[0]) + __builtin_bit_cast(f32x4, yp[1])) + (__builtin_bit_cast(f32x4, yp[2]) + __builtin_bit_cast(f32x4, yp[3]));
            y = xrow_scatter<false>(ys[0], ys[1], ys[2], ys[3]);
            return y;
        });
        __builtin_amdgcn_raw_buffer_store_b16((u16)(pk2(hc + rbf(lc + rbf(rbf(y) * sig)), 0.f) & 0xffffu), rsO, vo, so_, 0);
        STAMPW(17, 4);
    }
}
__device__ __attribute__((used, noinline)) void fk_tail() { asm volatile(".Lfk_end:"); }

extern "C" void kernel_launch(void* const* d_in, const int* in_sizes, int n_in, void* d_out, int out_size, void* d_ws,
                              size_t ws_size, hipStream_t stream) {
    (void)in_sizes; (void)n_in; (void)out_size; (void)ws_size;
    (void)hipFuncSetAttribute((const void*)fused_kernel, hipFuncAttributeMaxDynamicSharedMemorySize, LDS_BYTES);
    fused_kernel<<<256, 512, LDS_BYTES, stream>>>((const u16*)d_in[0], (const u16*)d_in[1], (const u16*)d_in[2], (const u16*)d_in[3],
                                                  (const u16*)d_in[5], (const u16*)d_in[6], (const u16*)d_in[7], (const u16*)d_in[8],
                                                  (u16*)d_out, (char*)d_ws);
}
```

```cpp
#include <hip/hip_runtime.h>
#include <stdint.h>
#include <utility>

typedef unsigned short u16;
typedef unsigned long long u64;
typedef short bf16x8 __attribute__((ext_vector_type(8)));
typedef short s16x4 __attribute__((ext_vector_type(4)));
typedef float f32x4 __attribute__((ext_vector_type(4)));
typedef float f32x2 __attribute__((ext_vector_type(2)));
typedef __bf16 bf16x2 __attribute__((ext_vector_type(2)));
typedef unsigned v4u __attribute__((ext_vector_type(4)));
typedef unsigned v2u __attribute__((ext_vector_type(2)));
#define LAS __attribute__((address_space(3)))
#define GAS __attribute__((address_space(1)))
#define CAS __attribute__((address_space(4)))
typedef GAS u64 gu64;
typedef GAS unsigned gu32;
typedef __amdgpu_buffer_rsrc_t rsrc_t;
#define DI __device__ __forceinline__
#define RLX_AGENT __ATOMIC_RELAXED, __HIP_MEMORY_SCOPE_AGENT
#define BAR() __builtin_amdgcn_s_barrier()
#define SCHED() __builtin_amdgcn_sched_barrier(0)
#define SGB(m, n) __builtin_amdgcn_sched_group_barrier((m), (n), 0)
#define WAITL0() asm volatile("s_waitcnt lgkmcnt(0)" ::: "memory")
#define WL0() __builtin_amdgcn_s_waitcnt(0xc07f)
#define MFMA(a, b, c) __builtin_amdgcn_mfma_f32_16x16x32_bf16((a), (b), (c), 0, 0, 0)
#define ORDER_BEHIND_POLL() __builtin_amdgcn_fence(__ATOMIC_ACQUIRE, "wavefront")
#define AUX_SC1 16
template <int N> DI void waitv() { asm volatile("s_waitcnt vmcnt(%0)" ::"i"(N) : "memory"); }

#define WS_EPOCH 0
#define WS_G0 0x1000
#define WS_G1 0x8000
#define WS_F4 0x78000
#define WS_GC 0x70000
#define WS_G3 0x80000
#define WS_GZ 0xC0000
#define SENT 0xFFFFFFFFu
#define WS_TL 0x100000
#define WS_G5 0x100000
#define WS_G8 0x180000
#define WS_G9 0x1A0000
#define WS_GK 0x1B4000
#define WS_C 0x1C0000
#define WS_P2 0x200000
#define P2_STRIDE 0x2180
#define WS_P4 0x700000
#define WS_USED 0xB00000
#define WS_STAMP 0x1000000

#define STAGE_B 40960
#define LDS_BYTES 163840
#define L_WO 32768
#define L_MB 161792
#define L_WPHI (L_MB - 8192)
#define L_SD (L_MB + 512)
#define L_CS (L_MB + 1024)
#define L_V L_WO
#define L_KH (L_WO + 65536)
#define L_XB 0
#define L_STASH (L_WO + 1024)
#define L_OUT (L_WO + 8192)
#define L_RED 4096

#define SPIN_CAP 20000u
#define W_AUX 2
#define LOG2E 1.4426950408889634f

DI unsigned rfl(unsigned x) { return __builtin_amdgcn_readfirstlane(x); }
DI float bf2f(unsigned u) { return __uint_as_float(u << 16); }
DI float bflo(unsigned u) { return __uint_as_float(u << 16); }
DI float bfhi(unsigned u) { return __uint_as_float(u & 0xffff0000u); }
DI unsigned pk2(float lo, float hi) { f32x2 v = {lo, hi}; return __builtin_bit_cast(unsigned, __builtin_convertvector(v, bf16x2)); }
DI unsigned nsent(unsigned x) { return __builtin_elementwise_min(x, SENT - 1); }
DI unsigned umx(unsigned a, unsigned b) { return __builtin_elementwise_max(a, b); }
DI float rbf(float f) { return bflo(pk2(f, f) & 0xffffu); }
DI bf16x8 mk8(unsigned a, unsigned b, unsigned c, unsigned d) { v4u v = {a, b, c, d}; return __builtin_bit_cast(bf16x8, v); }
DI void dma16(unsigned ldsaddr, unsigned voff, rsrc_t rs, unsigned soff) {
    asm volatile("s_mov_b32 m0, %0\n\ts_nop 0\n\tbuffer_load_dwordx4 %1, %2, %3 offen lds" ::"s"(ldsaddr), "v"(voff), "s"(rs), "s"(soff) : "memory");
}
DI void dma16x4(unsigned ldsaddr, const unsigned* v, rsrc_t rs, unsigned soff) {
    asm volatile("s_mov_b32 m0, %0\n\ts_nop 0\n\tbuffer_load_dwordx4 %1, %5, %6 offen lds\n\tbuffer_load_dwordx4 %2, %5, %6 offen offset:1024 lds\n\t"
                 "buffer_load_dwordx4 %3, %5, %6 offen offset:2048 lds\n\tbuffer_load_dwordx4 %4, %5, %6 offen offset:3072 lds"
                 ::"s"(ldsaddr), "v"(v[0]), "v"(v[1]), "v"(v[2]), "v"(v[3]), "s"(rs), "s"(soff) : "memory");
}
DI void touch(unsigned voff, rsrc_t rs, unsigned soff) { asm volatile("buffer_load_dword v255, %0, %1, %2 offen" ::"v"(voff), "s"(rs), "s"(soff) : "memory", "v255"); }
DI v4u ld16_asm(unsigned voff, rsrc_t rs, unsigned soff) {
    v4u r; asm volatile("buffer_load_dwordx4 %0, %1, %2, %3 offen" : "=v"(r) : "v"(voff), "s"(rs), "s"(soff) : "memory"); return r;
}
#define PIN(x) asm volatile("" : "+v"(x))
DI s16x4 tr_read(unsigned a) { return __builtin_amdgcn_ds_read_tr16_b64_v4i16((LAS s16x4*)(a)); }
DI bf16x8 cat8(s16x4 a, s16x4 b) { return __builtin_shufflevector(a, b, 0, 1, 2, 3, 4, 5, 6, 7); }
DI bf16x8 lds128(unsigned a) { return *(const LAS bf16x8*)(a); }
DI f32x4 ldsf4(unsigned a) { return *(const LAS f32x4*)(a); }
DI float ldsf(unsigned a) { return *(const LAS float*)(a); }
DI unsigned ldsu(unsigned a) { return *(const LAS unsigned*)(a); }
DI void stsf4(unsigned a, f32x4 v) { *(LAS f32x4*)(a) = v; }
DI void stsf(unsigned a, float v) { *(LAS float*)(a) = v; }
DI void stsu(unsigned a, unsigned v) { *(LAS unsigned*)(a) = v; }
template <int C> DI float dppf(float x) { return __uint_as_float(__builtin_amdgcn_update_dpp(0, __float_as_uint(x), C, 0xf, 0xf, true)); }
template <int C> DI unsigned dppu(unsigned x) { return __builtin_amdgcn_update_dpp(0, x, C, 0xf, 0xf, true); }
DI float max2f(float a, float b) { float r; asm("v_max_f32_e32 %0, %1, %2" : "=v"(r) : "v"(a), "v"(b)); return r; }
DI float max3f(float a, float b, float c) { float r; asm("v_max3_f32 %0, %1, %2, %3" : "=v"(r) : "v"(a), "v"(b), "v"(c)); return r; }
DI float row_sum(float x) { x += dppf<0x128>(x); x += dppf<0x124>(x); x += dppf<0x122>(x); x += dppf<0x121>(x); return x; }
DI float xrow_sum(float x) {
    auto a = __builtin_amdgcn_permlane16_swap(__float_as_uint(x), __float_as_uint(x), false, false);
    x = __uint_as_float(a[0]) + __uint_as_float(a[1]);
    auto b = __builtin_amdgcn_permlane32_swap(__float_as_uint(x), __float_as_uint(x), false, false);
    return __uint_as_float(b[0]) + __uint_as_float(b[1]);
}
DI unsigned wave_maxu(unsigned x) {
    x = umx(x, dppu<0x128>(x)); x = umx(x, dppu<0x124>(x)); x = umx(x, dppu<0x122>(x)); x = umx(x, dppu<0x121>(x));
    auto a = __builtin_amdgcn_permlane16_swap(x, x, false, false);
    x = umx(a[0], a[1]);
    auto b = __builtin_amdgcn_permlane32_swap(x, x, false, false);
    return umx(b[0], b[1]);
}
DI float nmax3f(float a, float b, float c) { float r; asm("v_max3_f32 %0, -%1, -%2, -%3" : "=v"(r) : "v"(a), "v"(b), "v"(c)); return r; }
DI float nmax2f(float a, float b) { float r; asm("v_max_f32_e64 %0, -%1, -%2" : "=v"(r) : "v"(a), "v"(b)); return r; }
template <bool MAX> DI float xrow_scatter(float a, float b, float c, float d) {
    const auto s = __builtin_amdgcn_permlane16_swap(__float_as_uint(a), __float_as_uint(b), false, false);
    const auto t = __builtin_amdgcn_permlane16_swap(__float_as_uint(c), __float_as_uint(d), false, false);
    float u = MAX ? max2f(__uint_as_float(s[0]), __uint_as_float(s[1])) : __uint_as_float(s[0]) + __uint_as_float(s[1]);
    float v = MAX ? max2f(__uint_as_float(t[0]), __uint_as_float(t[1])) : __uint_as_float(t[0]) + __uint_as_float(t[1]);
    if (MAX) asm volatile("s_nop 1" : "+v"(u), "+v"(v));
    const auto r = __builtin_amdgcn_permlane32_swap(__float_as_uint(u), __float_as_uint(v), false, false);
    return MAX ? max2f(__uint_as_float(r[0]), __uint_as_float(r[1])) : __uint_as_float(r[0]) + __uint_as_float(r[1]);
}
DI void xrow_gather(float w, float (&o)[4]) {
    const auto r = __builtin_amdgcn_permlane32_swap(__float_as_uint(w), __float_as_uint(w), false, false);
    const auto s = __builtin_amdgcn_permlane16_swap(r[0], r[0], false, false);
    const auto t = __builtin_amdgcn_permlane16_swap(r[1], r[1], false, false);
    o[0] = __uint_as_float(s[0]); o[1] = __uint_as_float(s[1]); o[2] = __uint_as_float(t[0]); o[3] = __uint_as_float(t[1]);
}
struct NoHook { DI void operator()(int, float&) const {} DI void operator()(int, float&, float&) const {} };
struct NoPut { DI void operator()(int, const bf16x8&) const {} };
template <bool MUL, class H = NoHook, class P = NoPut> DI void softmax2(const f32x4 (&p)[2][2], float rs0, float rs1, bool hi, bf16x8 (&out)[4], H hook = H(), P put = P()) {
    float m[4];
#pragma unroll
    for (int i = 0; i < 2; i++) {
        const f32x4 &p0 = p[i][0], &p1 = p[i][1];
        m[2 * i] = max3f(max3f(p0[0], p0[1], p0[2]), max3f(p0[3], p1[0], p1[1]), max2f(p1[2], p1[3]));
        m[2 * i + 1] = max3f(nmax3f(p0[0], p0[1], p0[2]), nmax3f(p0[3], p1[0], p1[1]), nmax2f(p1[2], p1[3]));
    }
    asm volatile("s_nop 1" : "+v"(m[0]), "+v"(m[1]), "+v"(m[2]), "+v"(m[3]));
    hook(1, m[3]);
    const float rsr = hi ? rs1 : rs0;
    float a[4];
    xrow_gather(xrow_scatter<true>(m[0], m[1], m[2], m[3]) * (rsr * -LOG2E), a);
    hook(10, a[0]);
    f32x2 e[4][4];
    float s[4];
#pragma unroll
    for (int k = 0; k < 4; k++) {
        if (k == 2) hook(2, a[2], s[1]);
        const float sc = ((k >> 1) ? rs1 : rs0) * ((k & 1) ? -LOG2E : LOG2E);
#pragma unroll
        for (int j = 0; j < 4; j++) {
            const f32x4& q = p[k >> 1][j >> 1];
            const f32x2 t = __builtin_elementwise_fma((f32x2){q[2 * (j & 1)], q[2 * (j & 1) + 1]}, (f32x2){sc, sc}, (f32x2){a[k], a[k]});
            e[k][j] = (f32x2){__builtin_amdgcn_exp2f(t[0]), __builtin_amdgcn_exp2f(t[1])};
        }
        const f32x2 h = (e[k][0] + e[k][1]) + (e[k][2] + e[k][3]);
        s[k] = h[0] + h[1];
        if (k == 3) hook(3, s[k]);
    }
    float iv = __builtin_amdgcn_rcpf(xrow_scatter<false>(s[0], s[1], s[2], s[3]));
    if (MUL) iv *= rsr;
    xrow_gather(iv, a);
#pragma unroll
    for (int k = 0; k < 4; k++) {
        if (k == 2) hook(4, a[2]);
        const f32x2 c = {a[k], a[k]};
        const f32x2 x0 = e[k][0] * c, x1 = e[k][1] * c, x2 = e[k][2] * c, x3 = e[k][3] * c;
        out[k] = mk8(pk2(x0[0], x0[1]), pk2(x1[0], x1[1]), pk2(x2[0], x2[1]), pk2(x3[0], x3[1]));
        put(k, out[k]);
    }
}
DI float dot2(unsigned a, unsigned b, float c) {
    return __builtin_amdgcn_fdot2_f32_bf16(__builtin_bit_cast(bf16x2, a), __builtin_bit_cast(bf16x2, b), c, false);
}
DI void st_gran(char* ws, unsigned off, unsigned tag, unsigned val) {
    __hip_atomic_store((gu64*)(ws + off), ((u64)tag << 32) | val, RLX_AGENT);
}
DI v2u ldg(rsrc_t rs, unsigned voff, unsigned soff) { return __builtin_amdgcn_raw_buffer_load_b64(rs, voff, soff, AUX_SC1); }
#define RARE(c) __builtin_expect(!!(c), 0)
#define FRESH() asm volatile("" ::: "memory")
template <int N> DI void sweep(rsrc_t rs, unsigned voff, unsigned soff, unsigned S, unsigned tag, unsigned (&v)[N]) {
#pragma nounroll
    for (unsigned spins = 0;;) {
        unsigned bad = 0;
        FRESH();
#pragma unroll
        for (int k = 0; k < N; ++k) { const v2u x = ldg(rs, voff, soff + k * S); v[k] = x[0]; bad |= x[1] ^ tag; }
        if (__all(bad == 0)) break;
        if (++spins > SPIN_CAP) break;
        __builtin_amdgcn_s_sleep(1);
    }
}
template <int A, int B> DI unsigned budget(unsigned& spins) {
    unsigned r;
    asm volatile("s_add_i32 %0, %0, 1\n\ts_cmpk_lt_u32 %0, %2\n\ts_cselect_b32 %1, %3, %4" : "+s"(spins), "=s"(r) : "i"(SPIN_CAP), "i"(A), "i"(B) : "scc");
    SCHED();
    return r;
}
DI unsigned wmax(unsigned x) { return x; }
DI unsigned wmax(v2u x) { return umx(x[0], x[1]); }
DI unsigned wmax(v4u x) { return umx(umx(x[0], x[1]), umx(x[2], x[3])); }
template <int N, class T, class F> DI unsigned sweepz(F ld, T (&v)[N]) {
    unsigned spins = 0, lim, m;
#pragma nounroll
    do {
        FRESH();
#pragma unroll
        for (int k = 0; k < N; ++k) v[k] = ld(k);
        lim = budget<-2, -1>(spins);
        m = 0;
#pragma unroll
        for (int k = 0; k < N; ++k) m = umx(m, wmax(v[k]));
    } while (__any(m > lim));
    return m;
}
template <int N, class T, class F, class G> DI void sweepn(F ld, T (&v)[N], G use) {
    unsigned spins = 0;
    bool again;
#pragma nounroll
    do {
        FRESH();
#pragma unroll
        for (int k = 0; k < N; ++k) v[k] = ld(k);
        const int m = budget<3, 0>(spins);
        again = __any(__builtin_amdgcn_classf(use(), m));
    } while (again);
}
#define OOB 0x4000000u
#define LD16(v, s) __builtin_amdgcn_raw_buffer_load_b128(rsWS, (v), (s), AUX_SC1)
#define LD4(v, s) __builtin_amdgcn_raw_buffer_load_b32(rsWS, (v), (s), AUX_SC1)
#define ST16(x, v, s) __builtin_amdgcn_raw_buffer_store_b128((x), rsWS, (v), (s), AUX_SC1)
#define ST4(x, v, s) __builtin_amdgcn_raw_buffer_store_b32((x), rsWS, (v), (s), AUX_SC1)
#define SLEEP(n) __builtin_amdgcn_s_sleep(n)
DI void poll16(rsrc_t rs, unsigned off, unsigned tag, int l) {
    unsigned spins = 0, lim, bad;
#pragma nounroll
    do {
        FRESH();
        const v2u x = ldg(rs, (l & 15) * 8, off);
        lim = budget<0, -1>(spins);
        bad = (x[1] ^ tag) | (x[0] ^ tag ^ 0x5bd1e995u);
    } while (__any(bad > lim));
    ORDER_BEHIND_POLL();
}
#define STAMPR(i, role)
#define STAMP(i)
#define STAMPW(i, ww)

#define ARR_DECL
#define KATTR
#define ARR_MARK(i)
#define ARR_END()
template <class F, int... Is> DI void static_for_impl(F&& f, std::integer_sequence<int, Is...>) { (f(std::integral_constant<int, Is>{}), ...); }
template <int N, class F> DI void static_for(F&& f) { static_for_impl(f, std::make_integer_sequence<int, N>{}); }

DI s16x4 bq(unsigned b0, unsigned b1, int j, int i) {
    const int ni = (j + 2) % 6;
    return tr_read(((ni & 1) ? b1 : b0) + (ni >> 1) * 8192 + (i >> 1) * 4096 + (i & 1) * 512);
}
template <class F> DI void group(f32x4 (&acc)[4][6], const bf16x8 (&A)[2][4], const bf16x8 (&B)[2], int j, F gap) {
    const int ni = (j + 2) % 6;
#pragma unroll
    for (int i = 0; i < 8; i++) {
        const int mi = i >> 1, ks = i & 1;
        if (ni < 4) asm volatile("v_mfma_f32_16x16x32_bf16 %0, %1, %2, %0" : "+v"(acc[mi][ni]) : "v"(B[ks]), "v"(A[ks][mi]));
        else asm volatile("v_mfma_f32_16x16x32_bf16 %0, %1, %2, %0" : "+v"(acc[mi][ni]) : "v"(A[ks][mi]), "v"(B[ks]));
        gap(i);
        SCHED();
    }
}
struct ImgPut {
    const unsigned (&ia)[4];
    DI void operator()(int k, const bf16x8& f) const {
        const v4u u = __builtin_bit_cast(v4u, f);
        *(LAS v2u*)(ia[2 * (k & 1)] + (k >> 1) * 2048) = (v2u){u[0], u[1]};
        *(LAS v2u*)(ia[2 * (k & 1) + 1] + (k >> 1) * 2048) = (v2u){u[2], u[3]};
        SCHED();
    }
};
DI float lds_bf(unsigned a) { return bf2f(*(const LAS u16*)(a)); }
#define MFMA_SETTLE2(p) asm volatile("s_nop 7\n\ts_nop 1" : "+v"(p[0][0]), "+v"(p[0][1]), "+v"(p[1][0]), "+v"(p[1][1]))

__global__ KATTR __launch_bounds__(512, 2) void fused_kernel(const u16* __restrict__ X, const u16* __restrict__ WQKV, const u16* __restrict__ WPHI,
                                                       const u16* __restrict__ WO, const u16* __restrict__ WR, const u16* __restrict__ WG,
                                                       const u16* __restrict__ WU, const u16* __restrict__ WD, u16* __restrict__ OUT,
                                                       char* __restrict__ ws) {
    extern __shared__ __attribute__((aligned(16))) char lds[];
    u64 pc0; unsigned clen;
    const int tid = threadIdx.x, l = tid & 63;
    asm volatile("s_getpc_b64 %0\n.Lfk_beg:\n\ts_lshr_b32 %1, %3, 3\n\tv_and_b32 v255, 0x1e0, %2\n\tv_add_lshl_u32 v255, v255, %1, 7\n\ts_mov_b32 %1, .Lfk_end-.Lfk_beg\n\t"
                 "v_min_u32 v255, %1, v255\n\tglobal_load_dword v255, v255, %0" : "=&s"(pc0), "=&s"(clen) : "v"(tid), "s"(blockIdx.x) : "v255", "scc");
    SCHED();
    const unsigned w = rfl(tid >> 6);
    const unsigned bid = blockIdx.x, xcd = bid & 7, idx = bid >> 3;
    const unsigned rt = (xcd & 1) * 8 + (idx & 7), hp = (xcd >> 1) * 4 + (idx >> 3);
    const unsigned i16 = idx & 15, rt2 = xcd * 2 + (idx >> 4), ct = i16 == 4 ? 11 : i16 == 11 ? 4 : i16;
    const unsigned hq = (hp + 4) & 15;
    const unsigned rot = (rt >> 2) * 4;
    const unsigned rot2 = (2 * (rt2 + ct)) & 31;
    const unsigned ldsb = (unsigned)(size_t)lds;
    const int g = l >> 4, q4 = (l & 15) >> 2, p4 = l & 3, t16 = l & 15;
    const unsigned kr = l >> 3;
    asm volatile("" ::"s"(X), "s"(WQKV), "s"(WPHI), "s"(WO), "s"(WR), "s"(WG), "s"(WU), "s"(WD), "s"(OUT), "s"(ws));
    unsigned tag;
#define READ_TAG() do { SCHED(); tag = *(const volatile CAS unsigned*)(ws + WS_EPOCH) + 1u; asm volatile("" : "+s"(tag)); } while (0)
    const rsrc_t rsWS = __builtin_amdgcn_make_buffer_rsrc((void*)ws, 0, WS_USED, 0x00020000);
    const rsrc_t rsX = __builtin_amdgcn_make_buffer_rsrc((void*)X, 0, 2048 * 1024 * 2, 0x00020000);
    const rsrc_t rsW = __builtin_amdgcn_make_buffer_rsrc((void*)WQKV, 0, 1024 * 3072 * 2, 0x00020000);
    const unsigned vA = (kr * 1024 + (((l & 7) ^ kr) * 8)) * 2;
    const unsigned vB0 = (kr * 3072 + (((l & 7) ^ (((kr >> 1) & 1) << 1)) * 8)) * 2;
    const unsigned vB1 = (kr * 3072 + (((l & 7) ^ ((((kr >> 1) & 1) | 2) << 1)) * 8)) * 2;
    const unsigned srow1 = rt * 128 + hp * 8 + (w & 3) * 2;
    v4u xr[4];
    if (w < 4) {
#pragma unroll
        for (int i = 0; i < 4; i++) xr[i] = ld16_asm(l * 16, rsX, srow1 * 2048 + i * 1024);
    }
    {
        const unsigned vBw = (w & 1) ? vB1 : vB0;
#pragma unroll
        for (int i = 0; i < 2; i++) dma16(ldsb + (w * 2 + i) * 1024, vA, rsX, (rt * 128 + w * 16 + i * 8) * 2048 + rot * 128);
#pragma unroll
        for (int i = 0; i < 3; i++) dma16(ldsb + 16384 + i * 8192 + w * 1024, vBw, rsW, (rot * 64 + w * 8) * 6144 + (i ? hp : hq) * 128 + i * 2048);
    }
    const f32x4 zero4 = {0.f, 0.f, 0.f, 0.f};
    ARR_DECL;
    const bf16x8 ones = mk8(0x3F803F80u, 0x3F803F80u, 0x3F803F80u, 0x3F803F80u);
    const unsigned lw = w & 3;
    const bool ffn = (ct & 7) != 3;
    const unsigned fb = !ffn ? 255u : rt2 * 14 + (i16 < 3 ? i16 : i16 - 2);
    const unsigned cg = (fb + 5) & 15, kc = fb >> 4;
    const unsigned kq = fb / 56, mat = (fb % 56) / 28, cg1 = (fb % 28) * 2;
    v4u wreg[24];
    STAMPW(0, 0); STAMPW(0, 4);

    const rsrc_t rsWO = __builtin_amdgcn_make_buffer_rsrc((void*)WO, 0, 1024 * 1024 * 2, 0x00020000);
    const unsigned vWO = kr * 2048 + (((l & 7) ^ (((kr >> 1) & 3) << 1)) * 16);
    unsigned vwo[4];
    auto wo_so = [&](unsigned b) { return (((2 * b & 30) + (b >> 4) + rot2) & 31) * 65536 + ct * 128; };
    const unsigned fw = (((t16 >> 1) & 3) << 2) | (((t16 >> 3) & 1) << 1) | (t16 & 1);
    unsigned ad;
    unsigned ia[4];
    bf16x8 sdv[4];
    auto pre_addr = [&]() {
#pragma unroll
        for (int k = 0; k < 4; k++) { ia[k] = ldsb + L_XB + lw * 8192 + (w >> 2) * 4096 + t16 * 128 + (((4 * k + g) ^ fw) * 8); PIN(ia[k]); }
        const unsigned rr = 4 * g + q4, fr = (((rr >> 1) & 3) << 2) | (((rr >> 3) & 1) << 1) | (rr & 1);
        ad = ldsb + L_XB + (w & 1) * 16384 + rr * 128 + ((((w >> 1) * 4 + p4) ^ fr) * 8);
        PIN(ad);
    };
    auto sd_reads = [&]() {
        unsigned sa = ldsb + L_SD + g * 8;
        PIN(sa);
#pragma unroll
        for (int s = 0; s < 4; s++) {
            const v2u a = *(const LAS v2u*)(sa + s * 64), b = *(const LAS v2u*)(sa + s * 64 + 32);
            sdv[s] = mk8(a[0], a[1], b[0], b[1]);
        }
    };
    bf16x8 v0[4], v1[4];
    auto v_reads = [&]() {
        unsigned vd = ldsb + L_V + (w & 1) * 8192 + l * 16;
        PIN(vd);
#pragma unroll
        for (int s = 0; s < 4; s++) { v0[s] = lds128(vd + s * 2048); v1[s] = lds128(vd + s * 2048 + 1024); }
    };
    auto kv_tile = [&](auto hook6) {
        const unsigned h2 = w & 1, ft = w >> 1;
        WAITL0();
        ARR_MARK(18);
        BAR();
        SCHED();
        bf16x8 pf[4];
#pragma unroll
        for (int s = 0; s < 4; s++) pf[s] = cat8(tr_read(ad + s * 4096), tr_read(ad + s * 4096 + 2048));
        SCHED();
        hook6();
        SCHED();
        f32x4 k0 = zero4, k1 = zero4, kz = zero4;
#pragma unroll
        for (int s = 0; s < 4; s++) { k0 = MFMA(pf[s], v0[s], k0); k1 = MFMA(pf[s], v1[s], k1); SCHED(); }
#pragma unroll
        for (int s = 0; s < 4; s++) kz = MFMA(pf[s], sdv[s], kz);
        const unsigned pbase = WS_P2 + (hp * 16 + rt) * P2_STRIDE;
        const v4u o0 = {pk2(k0[0], k0[1]), pk2(k0[2], k0[3]), pk2(k1[0], k1[1]), pk2(k1[2], k1[3])};
        __builtin_amdgcn_raw_buffer_store_b128(o0, rsWS, l * 16, pbase + h2 * 4096 + ft * 1024, AUX_SC1);
        asm volatile("s_nop 1" :: "v"(o0));
        __builtin_amdgcn_raw_buffer_store_b64((v2u){pk2(kz[0], kz[1]), pk2(kz[2], kz[3])}, rsWS, t16 == 0 ? g * 8 : OOB,
                                              pbase + 8192 + h2 * 128 + ft * 32, AUX_SC1);
        SCHED();
        ARR_MARK(19);
        BAR();
    };

    const bool zjob = lw == 0 && (rt & 7) == 0;
    v4u pp[4];
    auto pp_ld = [&](int k) {
        return LD16(kr * P2_STRIDE + (l & 7) * 16, WS_P2 + (hp * 16 + (k & 1) * 8) * P2_STRIDE +
                    (k < 2 ? (rt >> 2) * 2048 + (lw & 1) * 1024 + (rt & 3) * 256 + (lw >> 1) * 128 : 8192 + (rt >> 3) * 128));
    };
    auto kv_reduce = [&]() {
        unsigned v3 = (l & 8) ? OOB : (l & 7) * 16 + g * 4, res;
        unsigned s3 = WS_G3 + (hp * 4 + (rt >> 2)) * 2048 + (lw & 1) * 1024 + (rt & 3) * 256 + (lw >> 1) * 128;
        PIN(v3);
        asm volatile("" : "+s"(s3));
        auto red = [&](const v4u* p) {
            float lo = xrow_scatter<false>(bflo(p[0][0]) + bflo(p[1][0]), bflo(p[0][1]) + bflo(p[1][1]), bflo(p[0][2]) + bflo(p[1][2]), bflo(p[0][3]) + bflo(p[1][3]));
            float hi = xrow_scatter<false>(bfhi(p[0][0]) + bfhi(p[1][0]), bfhi(p[0][1]) + bfhi(p[1][1]), bfhi(p[0][2]) + bfhi(p[1][2]), bfhi(p[0][3]) + bfhi(p[1][3]));
            const float chk = lo;
            lo += dppf<0x128>(lo); hi += dppf<0x128>(hi);
            res = nsent(pk2(lo, hi));
            return chk;
        };
        SLEEP(2);
        if (RARE(zjob)) {
            unsigned rz;
            sweepn<4>(pp_ld, pp, [&]() { const float a = red(pp + 2); rz = res; return a + red(pp); });
            ST4(rz, (l & 8) ? OOB : ((l >> 2) & 1) * 64 + ((l & 1) * 2 + (g >> 1)) * 16 + ((l >> 1) & 1) * 8 + (g & 1) * 4, WS_GZ + (hp * 2 + (rt >> 3)) * 128);
        } else {
            v4u p2[2];
            sweepn<2>(pp_ld, p2, [&]() { return red(p2); });
        }
        STAMPW(25, 0);
        ST4(res, v3, s3);
    };

    const unsigned half = w >> 2;
    const unsigned R2 = rt2 * 128 + lw * 32 + half * 16 + t16;

    rsrc_t rsD = __builtin_amdgcn_make_buffer_rsrc((void*)WD, 0, 1024 * 3584 * 2, 0x00020000);
    unsigned vD, sD;
    auto dloads = [&](int i0, int i1) {
#pragma unroll
        for (int i = i0; i < i1; i++)
            wreg[16 + i] = __builtin_amdgcn_raw_buffer_load_b128(rsD, vD, sD + i * 2048, W_AUX);
        FRESH();
    };
    v4u wp1[2][8];
    auto perm1 = [&]() {
#pragma unroll
        for (int n = 0; n < 16; n++) PIN(wreg[n]);
#pragma unroll
        for (int ip = 0; ip < 4; ip++)
#pragma unroll
            for (int c = 0; c < 4; c++) {
#pragma unroll
                for (int m = 0; m < 2; m++) {
                    const unsigned a = wreg[m * 8 + 2 * ip][c], b = wreg[m * 8 + 2 * ip + 1][c];
                    wp1[m][2 * c][ip] = __builtin_amdgcn_perm(b, a, 0x05040100);
                    wp1[m][2 * c + 1][ip] = __builtin_amdgcn_perm(b, a, 0x07060302);
                }
            }
#pragma unroll
        for (int c = 0; c < 8; c++) { PIN(wp1[0][c]); PIN(wp1[1][c]); }
    };
    v4u xres[2], wrv[2];
    auto res_loads = [&]() {
#pragma unroll
        for (int i = 0; i < 2; i++) {
            xres[i] = *(const v4u*)(X + (size_t)R2 * 1024 + ct * 64 + i * 32 + 8 * g);
            wrv[i] = *(const v4u*)(WR + ct * 64 + i * 32 + 8 * g);
        }
        FRESH();
    };

    if (w < 4) {
        const unsigned wm = w & 1, hh = w >> 1;
        f32x4 acc[4][6];
        bf16x8 wf[2], wfq[2];
        float mbt[4], rst[4], csv[2];
        f32x4 mbv[4], csq[2], csk[2];
#pragma unroll
        for (int i = 0; i < 4; i++)
#pragma unroll
            for (int j = 0; j < 6; j++) { acc[i][j] = zero4; PIN(acc[i][j]); }
        {
            const unsigned aoff0 = ldsb + (wm * 64 + t16) * 128 + (((0 + g) ^ (l & 7)) * 16);
            const unsigned aoff1 = ldsb + (wm * 64 + t16) * 128 + (((4 + g) ^ (l & 7)) * 16);
            const unsigned sk = ((q4 >> 1) & 1) | ((g & 1) << 1);
            const unsigned boffA = ldsb + 16384 + (8 * g + q4) * 128 + (((2 * hh) ^ sk) * 32) + p4 * 8;
            const unsigned boffB = ldsb + 16384 + (8 * g + q4) * 128 + (((2 * hh + 1) ^ sk) * 32) + p4 * 8;
            bf16x8 A[2][4], B[3][2];
            s16x4 lo;
            unsigned a0[3], a1[3], bA[3], bB[3];
#pragma unroll
            for (int i = 0; i < 3; i++) {
                a0[i] = aoff0 + i * STAGE_B; a1[i] = aoff1 + i * STAGE_B; bA[i] = boffA + i * STAGE_B; bB[i] = boffB + i * STAGE_B;
                PIN(a0[i]); PIN(a1[i]); PIN(bA[i]); PIN(bB[i]);
            }
            unsigned aT = ldsb + L_MB + (wm * 64 + t16) * 4, aS = ldsb + L_SD + (wm * 64 + t16) * 2, aM = ldsb + L_MB + wm * 256 + g * 16;
            unsigned aC = ldsb + L_CS + hh * 128 + g * 16, aV = ldsb + L_CS + (128 + hh * 32 + t16) * 4;
            unsigned aW = ldsb + L_WPHI + hh * 2048 + (4 * g + q4) * 64 + p4 * 8, aK = ldsb + L_KH + w * 2048 + l * 16;
            PIN(aT); PIN(aS); PIN(aM); PIN(aC); PIN(aV); PIN(aW); PIN(aK);
            pre_addr();
            READ_TAG();
            waitv<5>();
            PIN(xr[0]); PIN(xr[1]); PIN(xr[2]); PIN(xr[3]);
            {
                float s[2], q[2], x0[2];
#pragma unroll
                for (int rr = 0; rr < 2; rr++) {
                    x0[rr] = bflo(rfl(xr[2 * rr][0]));
                    s[rr] = 0.f; q[rr] = 0.f;
#pragma unroll
                    for (int i = 0; i < 4; i++) {
                        const float a = bflo(xr[2 * rr][i]) - x0[rr], b = bfhi(xr[2 * rr][i]) - x0[rr];
                        const float c = bflo(xr[2 * rr + 1][i]) - x0[rr], d = bfhi(xr[2 * rr + 1][i]) - x0[rr];
                        s[rr] += (a + b) + (c + d);
                        q[rr] += (a * a + b * b) + (c * c + d * d);
                    }
                }
                const float sq = row_sum(xrow_scatter<false>(s[0], s[1], q[0], q[1]));
#define RLF(x, n) __uint_as_float(__builtin_amdgcn_readlane(__float_as_uint(x), (n)))
                const float r0 = RLF(sq, 0), r1 = RLF(sq, 16), r2 = RLF(sq, 32), r3 = RLF(sq, 48);
                const float sm = (l ? r1 : r0) * (1.0f / 1024.0f), qm = (l ? r3 : r2) * (1.0f / 1024.0f);
                const float var = fmaxf(qm - sm * sm, 0.f);
                if (l < 2) st_gran(ws, WS_G0 + (srow1 + l) * 8, tag, pk2((l ? x0[1] : x0[0]) + sm, sqrtf(rbf(rbf(var) + 1e-5f))));
            }
            waitv<1>();
            WAITL0();
            ARR_MARK(0);
            BAR();
            SCHED();
            STAMPW(1, 0);
            auto rdB = [&](int u, int j, int i) {
                if (i & 1) B[j % 3][i >> 1] = cat8(lo, bq(bA[u], bB[u], j, i)); else lo = bq(bA[u], bB[u], j, i);
            };
            auto rdA = [&](int u, int i) { A[i & 1][i >> 1] = lds128(((i & 1) ? a1[u] : a0[u]) + (i >> 1) * 2048); };
            auto head = [&](int u) {
                group(acc, A, B[0], 0, [&](int i) { rdB(u, 1 + (i >> 2), i & 3); });
                group(acc, A, B[1], 1, [&](int i) { if (i < 4) rdB(u, 3, i); });
                group(acc, A, B[2], 2, [&](int i) { if (i < 4) rdB(u, 4, i); });
                group(acc, A, B[0], 3, [&](int i) { if (i < 4) rdB(u, 5, i); });
                WL0();
            };
#pragma unroll
            for (int i = 0; i < 4; i++) rdB(0, 0, i);
#pragma unroll
            for (int i = 0; i < 8; i++) rdA(0, i);
            SCHED();
#pragma nounroll
            for (unsigned t = 0; t < 5; t++) {
#pragma unroll
                for (int u = 0; u < 3; u++) {
                    head(u);
                    ARR_MARK(1 + t * 3 + u);
                    BAR();
                    SCHED();
                    group(acc, A, B[1], 4, [&](int i) { if (i < 4) rdB((u + 1) % 3, 0, i); });
                    group(acc, A, B[2], 5, [&](int i) { rdA((u + 1) % 3, i); });
                }
            }
            unsigned kw[4][4], rsu[4];
            f32x2 kx;
            auto kpiece = [&](int n) {
                if (n > 0) { kw[((n - 1 >> 2) + 2) & 3][(n - 1) & 3] = pk2(kx[0], kx[1]); PIN(kw[((n - 1 >> 2) + 2) & 3][(n - 1) & 3]); }
                if (n < 16) {
                    const int mi = ((n >> 2) + 2) & 3, j = n & 3;
                    const f32x4 &c = acc[mi][2 + (j >> 1)], &s_ = csk[j >> 1];
                    float x0 = __builtin_fmaf(mbt[mi], s_[2 * (j & 1)], c[2 * (j & 1)]); PIN(x0);
                    float x1 = __builtin_fmaf(mbt[mi], s_[2 * (j & 1) + 1], c[2 * (j & 1) + 1]); PIN(x1);
                    kx = (f32x2){x0, x1};
                }
                if (n == 5 || n == 9) *(LAS v4u*)(aK + (n == 9) * 1024) = (v4u){kw[2 + (n == 9)][0], kw[2 + (n == 9)][1], kw[2 + (n == 9)][2], kw[2 + (n == 9)][3]};
            };
            group(acc, A, B[0], 0, [&](int i) { rdB(0, 1 + (i >> 2), i & 3); });
            group(acc, A, B[1], 1, [&](int i) {
                if (i < 4) { rdB(0, 3, i); mbt[(i + 2) & 3] = ldsf(aT + ((i + 2) & 3) * 64); }
                else if (i < 6) csk[i - 4] = ldsf4(aC + 256 + (i - 4) * 64);
                else wf[i - 6] = cat8(tr_read(aW + (i - 6) * 32), tr_read(aW + (i - 6) * 32 + 1024));
            });
            group(acc, A, B[2], 2, [&](int i) { if (i < 4) rdB(0, 4, i); else mbv[i - 4] = ldsf4(aM + (i - 4) * 64); kpiece(i); });
            group(acc, A, B[0], 3, [&](int i) {
                if (i < 4) rdB(0, 5, i); else rsu[i - 4] = *(const LAS u16*)(aS + (i - 4) * 32);
                if (i == 2 || i == 3) csv[i - 2] = ldsf(aV + (i - 2) * 64);
                kpiece(8 + i);
            });
            WL0();
            ARR_MARK(16);
            BAR();
            SCHED();
            kpiece(16);
            f32x4 p[2][2];
#pragma unroll
            for (int i = 0; i < 4; i++) p[i >> 1][i & 1] = MFMA(wf[i & 1], mk8(kw[i >> 1][0], kw[i >> 1][1], kw[i >> 1][2], kw[i >> 1][3]), zero4);
            SCHED();
            STAMPW(2, 0);
#pragma unroll
            for (int mi = 0; mi < 4; mi++) rst[mi] = bf2f(rsu[mi]);
#pragma unroll
            for (int a = 0; a < 2; a++)
#pragma unroll
                for (int dt = 0; dt < 2; dt++) {
                    const f32x4 x0 = acc[2 * a][4 + dt] + mbv[2 * a] * csv[dt], x1 = acc[2 * a + 1][4 + dt] + mbv[2 * a + 1] * csv[dt];
                    *(LAS v4u*)(aK + (L_V - L_KH) + w * 2048 + (a * 2 + dt) * 1024) = (v4u){pk2(x0[0], x0[1]), pk2(x0[2], x0[3]), pk2(x1[0], x1[1]), pk2(x1[2], x1[3])};
                }
            SCHED();
            rst[0] = __builtin_amdgcn_rcpf(rst[0]); rst[1] = __builtin_amdgcn_rcpf(rst[1]);
            bf16x8 f[4];
            struct HookC {
                decltype(v_reads)& vr; decltype(sd_reads)& sr;
                DI void operator()(int k, float& x) const { if (k == 10) { PIN(x); WAITL0(); ARR_MARK(17); BAR(); PIN(x); } if (k == 3) { PIN(x); vr(); sr(); PIN(x); } }
                DI void operator()(int, float&, float&) const {}
            };
            softmax2<true>(p, rst[0], rst[1], g >= 2, f, HookC{v_reads, sd_reads}, ImgPut{ia});
#pragma unroll
            for (int ft = 0; ft < 2; ft++) wfq[ft] = cat8(tr_read(aW + ft * 32 + 4096), tr_read(aW + ft * 32 + 5120));
            csq[0] = ldsf4(aC); csq[1] = ldsf4(aC + 64);
            STAMPW(3, 0);
            kv_tile([]() {});
            STAMPW(4, 0);
            SCHED();
            group(acc, A, B[1], 4, [&](int i) { if (i == 2 || i == 3) { rst[i] = __builtin_amdgcn_rcpf(rst[i]); PIN(rst[i]); } });
            group(acc, A, B[2], 5, [&](int) {});
            asm volatile("s_nop 7\n\ts_nop 1");
            SCHED();
        }
        bf16x8 phq[2][4];
        v4u kg[6];
        unsigned sG3 = WS_G3 + (hq * 2 + hh) * 4096, sGZ = WS_GZ + (hq * 2 + hh) * 128;
        {
            f32x4 pq[2][2][2];
#pragma unroll
            for (int mi = 0; mi < 4; mi++) {
                const f32x4 x0 = acc[mi][0] + mbt[mi] * csq[0], x1 = acc[mi][1] + mbt[mi] * csq[1];
                const bf16x8 qf = mk8(pk2(x0[0], x0[1]), pk2(x0[2], x0[3]), pk2(x1[0], x1[1]), pk2(x1[2], x1[3]));
                pq[mi >> 1][mi & 1][0] = MFMA(wfq[0], qf, zero4); pq[mi >> 1][mi & 1][1] = MFMA(wfq[1], qf, zero4);
            }
            MFMA_SETTLE2(pq[0]); MFMA_SETTLE2(pq[1]);
            softmax2<false>(pq[0], rst[0], rst[1], g >= 2, phq[0]);
#pragma unroll
            for (int i = 0; i < 4; i++) PIN(phq[0][i]);
            SCHED();
            kv_reduce();
            SCHED();
            res_loads();
            SCHED();
            BAR();
            SCHED();
            softmax2<false>(pq[1], rst[2], rst[3], g >= 2, phq[1]);
#pragma unroll
            for (int i = 0; i < 4; i++) { PIN(phq[0][i]); PIN(phq[1][i]); }
        }
        STAMPW(5, 0);
        STAMPW(26, 0);
        v2u fv = {tag ^ 0x5bd1e995u, tag};
        unsigned sF = WS_F4 + ((rt * 4 + w) * 16 + hq) * 8, vF = l == 0 ? 0 : OOB;
        {
            unsigned vO = l * 16 + hh * 1024, sO = WS_P4 + ((rt * 8 + wm * 4) * 32 + hq * 2) * 1024;
            asm volatile("" : "+s"(sO), "+s"(sF), "+s"(sG3), "+s"(sGZ));
            PIN(fv); PIN(vF);
            SLEEP(10);
            v4u ov[4];
#define PH(mi, s) phq[(mi) >> 1][2 * ((mi) & 1) + (s)]
#define KVA(s, dt) __builtin_bit_cast(bf16x8, kg[2 + 2 * (s) + (dt)])
            auto pairup = [&](v4u& a, v4u& b) {
                unsigned a2 = a[2], a3 = a[3], b0 = b[0], b1 = b[1];
                asm volatile("v_swap_b32 %0, %2\n\tv_swap_b32 %1, %3" : "+v"(a2), "+v"(a3), "+v"(b0), "+v"(b1));
                a[2] = a2; a[3] = a3; b[0] = b0; b[1] = b1;
            };
            f32x4 dn[4], n0[4], n1[4];
            float id[4];
            unsigned spins = 0, lim, m;
#pragma nounroll
            do {
                FRESH();
#pragma unroll
                for (int k = 0; k < 6; k++) { kg[k] = k >= 2 ? LD16(l * 16 + (k - 2) * 1024, sG3) : LD16(g * 16 + k * 64, sGZ); SCHED(); }
                lim = budget<-2, -1>(spins);
                m = 0;
#pragma unroll
                for (int i = 0; i < 8; i++) {
                    dn[i & 3] = MFMA(__builtin_bit_cast(bf16x8, kg[i >> 2]), PH(i & 3, i >> 2), i < 4 ? zero4 : dn[i & 3]);
                    if (i >= 1 && i < 7) { m = umx(umx(m, kg[i - 1][0]), kg[i - 1][1]); PIN(m); m = umx(umx(m, kg[i - 1][2]), kg[i - 1][3]); }
                    if (i == 5) pairup(kg[2], kg[3]);
                    if (i == 7) pairup(kg[4], kg[5]);
                    SCHED();
                }
            } while (__any(m > lim));
            STAMPW(6, 0);
#pragma unroll
            for (int mi = 0; mi <= 4; mi++) {
                if (mi < 4) {
                    n0[mi] = MFMA(KVA(0, 0), PH(mi, 0), zero4); n1[mi] = MFMA(KVA(0, 1), PH(mi, 0), zero4);
                    n0[mi] = MFMA(KVA(1, 0), PH(mi, 1), n0[mi]); n1[mi] = MFMA(KVA(1, 1), PH(mi, 1), n1[mi]);
                    id[mi] = __builtin_amdgcn_rcpf(rbf(rbf(dn[mi][0]) + 1e-6f));
                    asm volatile("" :: "v"(dn[mi]));
                }
                if (mi > 0) {
                    const f32x4 a = n0[mi - 1] * id[mi - 1], b = n1[mi - 1] * id[mi - 1];
                    ov[mi - 1] = (v4u){pk2(a[0], a[1]), pk2(a[2], a[3]), pk2(b[0], b[1]), pk2(b[2], b[3])};
                    ST16(ov[mi - 1], vO ^ ((mi - 1) * 32768 + ((mi - 1) & 1) * 1024), sO);
                }
                if (mi < 4) { SGB(0x8, 1); SGB(0x2, 6); SGB(0x8, 1); SGB(0x2, 3); SGB(0x8, 1); SGB(0x2, 3); SGB(0x8, 1); SGB(0x2, 4); }
                SCHED();
            }
#pragma unroll
            for (int mi = 0; mi < 4; mi++) PIN(ov[mi]);
        }
        BAR();
        waitv<0>();
        __builtin_amdgcn_raw_buffer_store_b64(fv, rsWS, vF, sF, AUX_SC1);
        STAMPW(7, 0);
    } else {
        const unsigned pw = lw & 1;
        const rsrc_t rsG = __builtin_amdgcn_make_buffer_rsrc((void*)(mat ? WU : WG), 0, 1024 * 3584 * 2, 0x00020000);
        unsigned vwl = kr * 57344 + (l & 7) * 16;
        unsigned sg = (kq * 256 + lw * 64) * 7168 + cg1 * 128;
        auto wloads = [&](int n0, int n1) {
#pragma unroll
            for (int j = n0; j < n1; j++) {
                const int n = (j & 1) * 8 + (j >> 1);
                asm volatile("buffer_load_dwordx4 %0, %1, %2, %3 offen nt" : "=v"(wreg[n]) : "v"(vwl), "s"(rsG), "s"(sg + (n >> 3) * 128 + (n & 7) * 7168));
            }
        };
        auto repack = [&](unsigned gI) {
            const unsigned s2 = (2 * g + (q4 >> 1)) & 3;
            bf16x8 f[4];
#pragma unroll
            for (int nt = 0; nt < 4; nt++) {
                const unsigned a = ldsb + L_WO + gI * 4096 + (4 * g + q4) * 128 + ((((nt >> 1) * 2 + (p4 >> 1)) ^ s2) * 32) + (p4 & 1) * 16 + (nt & 1) * 8;
                f[nt] = cat8(tr_read(a), tr_read(a + 2048));
            }
            WAITL0();
#pragma unroll
            for (int nt = 0; nt < 4; nt++) *(LAS bf16x8*)(ldsb + L_WO + gI * 4096 + nt * 1024 + l * 16) = f[nt];
        };
        unsigned aK, aW, aS;
        auto k_pre = [&]() {
            pre_addr();
            aK = ldsb + L_KH + lw * 2048 + l * 16; aW = ldsb + L_WPHI + (lw >> 1) * 2048 + (4 * g + q4) * 64 + p4 * 8; aS = ldsb + L_SD + (pw * 64 + 32 + t16) * 2;
            PIN(aK); PIN(aW); PIN(aS);
        };
        auto kfeat = [&](auto hook) {
            bf16x8 wf[2];
            float rst[2];
#pragma unroll
            for (int ft = 0; ft < 2; ft++) {
                wf[ft] = cat8(tr_read(aW + ft * 32), tr_read(aW + ft * 32 + 1024));
                rst[ft] = __builtin_amdgcn_rcpf(lds_bf(aS + ft * 32));
            }
            sd_reads();
            PIN(wf[0]); PIN(wf[1]); PIN(rst[0]); PIN(rst[1]);
            SCHED();
            ARR_MARK(16);
            BAR();
            SCHED();
            const bf16x8 kf0 = lds128(aK), kf1 = lds128(aK + 1024);
            SCHED();
            hook(-1, rst[0]);
            hook(0, rst[0]);
            SCHED();
            f32x4 p[2][2];
            p[0][0] = MFMA(wf[0], kf0, zero4); p[0][1] = MFMA(wf[1], kf0, zero4);
            p[1][0] = MFMA(wf[0], kf1, zero4); p[1][1] = MFMA(wf[1], kf1, zero4);
            MFMA_SETTLE2(p);
            bf16x8 f[4];
            softmax2<true>(p, rst[0], rst[1], g >= 2, f, hook, ImgPut{ia});
            SCHED();
            hook(5, rst[0]);
            SCHED();
            STAMPW(3, 4); STAMPW(3, 7);
            kv_tile([&]() { hook(6, rst[0]); });
            STAMPW(4, 4); STAMPW(4, 7);
        };
        auto wblk = [&](int k) {
            constexpr unsigned T[8] = {0x19181716, 0x041c1b1a, 0x08070605, 0x0c0b0a09, 0x1413120d, 0x11100015, 0x1d030201, 0x1f1e0f0e};
            return (T[k] >> (8 * lw)) & 31;
        };
        auto wo_init = [&]() {
#pragma unroll
            for (int i = 0; i < 4; i++) { vwo[i] = vWO + i * (16384 - 1024); PIN(vwo[i]); }
        };
        unsigned hs[8];
        auto wl_pre = [&]() { asm volatile("" : "+s"(sg)); PIN(vwl); };
        auto wo_pre = [&]() {
            wo_init();
#pragma unroll
            for (int k = 0; k < 8; k++) { hs[k] = wo_so(wblk(k)); asm volatile("" : "+s"(hs[k])); }
        };
        auto wo_rng = [&](int k0, int k1) {
#pragma unroll
            for (int k = k0; k < k1; k++) dma16x4(ldsb + L_WO + wblk(k) * 4096, vwo, rsWO, hs[k]);
        };
        auto pf = [&](int i) {
            if (i == 0) { wloads(0, 6); SCHED(); if (lw < 2) wo_pre(); SCHED(); }
            if (i < 3 && !ffn) wo_rng(i, i + 1);
            if (i == 1 || i == 2) wloads(6 * i, i == 1 ? 12 : 16);
        };
        struct Hook {
            decltype(pf)& f; decltype(v_reads)& vr;
            DI void operator()(int k, float& x) const { if (k == 10) return; PIN(x); if (k == 3) { ARR_MARK(17); BAR(); vr(); } f(3 + k); PIN(x); }
            DI void operator()(int k, float& x, float& y) const { PIN(y); f(3 + k); PIN(x); }
        };
        auto tail16 = [&]() {
            kfeat(Hook{pf, v_reads});
            if (ffn) wo_rng(0, 3);
            wo_rng(3, 5);
            static_for<3>([&](auto kc_) {
                constexpr int k = decltype(kc_)::value;
                waitv<4 * (4 - k)>();
                repack(wblk(k));
            });
            BAR();
            res_loads();
            wo_rng(5, 7);
            static_for<4>([&](auto kc_) {
                constexpr int k = decltype(kc_)::value + 3;
                waitv<4 * (6 - k)>();
                repack(wblk(k));
            });
            PIN(xres[0]); PIN(xres[1]); PIN(wrv[0]); PIN(wrv[1]);
            perm1();
            SCHED();
            BAR();
            wo_rng(7, 8);
        };
        auto vT23 = [&]() {
            const unsigned n = (idx * 2 + pw) * 64 + l, r = n / 12, c = n % 12;
            return idx < 24 ? ((r & 127) + (r >> 7) * 256 + 128) * 6144 + (c >> 2) * 2048 + (((xcd >> 1) * 4 + (c >> 2 ? 0 : 4) + (c & 3)) & 15) * 128 : OOB;
        };
        if (lw < 2) {
            const unsigned sA = (rt * 128 + lw * 64) * 2048, sB = lw * 32 * 6144 + hp * 128, sBq = lw * 32 * 6144 + hq * 128;
            unsigned buf = 1;
            unsigned va[4], vb[4];
#pragma unroll
            for (int i = 0; i < 4; i++) { va[i] = vA + i * (16384 - 1024); vb[i] = ((i & 1) ? vB1 : vB0) + i * (49152 - 1024); PIN(va[i]); PIN(vb[i]); }
            auto stage = [&](unsigned s, unsigned bf, int i0, int i1) {
                const unsigned kt = (s + rot) & 15;
                const unsigned lb = ldsb + bf * STAGE_B;
                const unsigned sa = sA + kt * 128, sb = kt * 393216;
                if (i0 <= 0 && 0 < i1) dma16x4(lb + lw * 8192, va, rsX, sa);
                if (i0 <= 1 && 1 < i1) dma16x4(lb + lw * 8192 + 4096, va, rsX, sa + 65536);
                if (i0 <= 2 && 2 < i1) dma16x4(lb + 16384 + 8192 + lw * 4096, vb, rsW, sb + sB + 2048);
                if (i0 <= 3 && 3 < i1) dma16x4(lb + 16384 + 16384 + lw * 4096, vb, rsW, sb + sB + 4096);
                if (i0 <= 4 && 4 < i1) dma16x4(lb + 16384 + lw * 4096, vb, rsW, sb + sBq);
            };
            stage(1, 1, 0, 4);
            SCHED();
            unsigned vt = vT23();
            PIN(vt);
            wl_pre(); k_pre();
            waitv<16>(); ARR_MARK(0); BAR();
            stage(1, 1, 4, 5);
            touch(vt, rsW, (xcd & 1) * 512 * 6144);
            waitv<1>(); ARR_MARK(1); BAR();
            stage(2, 2, 0, 5); stage(3, 0, 0, 3);
            waitv<12>(); ARR_MARK(2); BAR();
            stage(3, 0, 3, 5); stage(4, 1, 0, 4);
            waitv<16>(); ARR_MARK(3); BAR();
            stage(4, 1, 4, 5);
            buf = 2;
#pragma nounroll
            for (unsigned s = 5; s < 16; s++) {
                if (s > 5) { waitv<20>(); ARR_MARK(s - 2); BAR(); }
                stage(s, buf, 0, 5);
                buf = buf == 2 ? 0 : buf + 1;
            }
            waitv<20>(); ARR_MARK(14); BAR();
            pf(0);
            if (ffn) waitv<6>(); else waitv<10>();
            ARR_MARK(15); BAR();
            pf(1);
            tail16();
            READ_TAG();
        } else {
            SCHED();
            unsigned v3;
            const unsigned h3 = (xcd >> 1) * 4 + 3, kv3 = !(xcd & 2), wv = idx * 2 + pw;
            auto line3 = [&](unsigned per, unsigned rows, auto rowf) {
                const unsigned n = wv * (kv3 ? per : (per + 2) / 3) + l, r3 = kv3 ? n / 3 : n, c3 = kv3 ? n % 3 : 0;
                return (idx < 24 && l < (kv3 ? per : (per + 2) / 3) && r3 < rows) ? rowf(r3) * 6144 + c3 * 2048 + ((c3 ? h3 : h3 + 4) & 15) * 128 : OOB;
            };
            v3 = line3(32, 512, [](unsigned r) { return r; });
            touch(line3(16, 256, [](unsigned r) { return (r & 127) + (r >> 7) * 256; }), rsW, (xcd & 1) * 512 * 6144);
            {
                const rsrc_t rsP = __builtin_amdgcn_make_buffer_rsrc((void*)WPHI, 0, 32 * 1024 * 2, 0x00020000);
#pragma unroll
                for (int i = 0; i < 4; i++) dma16(ldsb + L_WPHI + pw * 4096 + i * 1024, l * 16, rsP, (pw ? hq : hp) * 4096 + i * 1024);
            }
            v4u sv = {SENT, SENT, SENT, SENT};
            unsigned vc4 = l < 16 ? WS_G3 + (hp * 4 + (rt >> 2)) * 2048 + pw * 1024 + (rt & 3) * 256 + l * 16
                         : l < 24 ? WS_P2 + (hp * 16 + rt) * P2_STRIDE + 8192 + pw * 128 + (l - 16) * 16
                         : (l < 32 && pw == 0 && (rt & 7) == 0) ? WS_GZ + (hp * 2 + (rt >> 3)) * 128 + (l - 24) * 16 : OOB;
            auto clears = [&](int i0, int i1) {
#pragma unroll
                for (int i = i0; i < i1; i++) {
                    if (i < 4) ST16(sv, l * 16, WS_P2 + (hp * 16 + rt) * P2_STRIDE + pw * 4096 + i * 1024);
                    if (i == 4) ST16(sv, vc4, 0);
                    if (i > 4) ST16(sv, l * 16, WS_TL + (bid * 4 + pw * 2 + i - 5) * 1024);
                }
                FRESH();
            };
            PIN(vc4); PIN(sv);
            unsigned ca = ldsb + 16384 + pw * 8192 + (8 * g + q4) * 128 + (((rt & 3) ^ (((q4 >> 1) & 1) | ((g & 1) << 1))) * 32) + p4 * 8;
            PIN(ca);
            bf16x8 cf[4];
            f32x4 cd[2] = {zero4, zero4};
            auto cs_read = [&](int st) {
#pragma unroll
                for (int i = 0; i < 4; i++) cf[i] = cat8(tr_read(ca + (st % 3) * STAGE_B + i * 4096), tr_read(ca + (st % 3) * STAGE_B + i * 4096 + 512));
                SCHED();
            };
            READ_TAG();
            waitv<5>();
            ARR_MARK(0);
            BAR();
            SCHED();
            cs_read(0);
            FRESH();
            const rsrc_t rsC = __builtin_amdgcn_make_buffer_rsrc((void*)pc0, 0, clen, 0x00020000);
            auto code_pf = [&]() { touch(l < 6 ? l * 128 : OOB, rsC, (idx * 2 + pw) * 768); };
            v4u gl[4];
            float s0 = 0.f, s1 = 0.f;
            unsigned bad;
            v2u gs;
            unsigned vgl = (l < 48 ? l : 0) * 16;
            unsigned aw0 = ldsb + L_CS + pw * 384 + (vgl >> 1), aw1 = ldsb + L_MB + (pw * 64 + l) * 4, aw2 = ldsb + L_SD + (pw * 64 + l) * 2;
            static_for<15>([&](auto tc) {
                constexpr int t = decltype(tc)::value;
                WAITL0();
                ARR_MARK(t + 1);
                BAR();
                SCHED();
                if constexpr (t < 4) {
                    __builtin_amdgcn_s_setprio(2);
#pragma unroll
                    for (int i = 0; i < 4; i++) cd[i & 1] = MFMA(ones, cf[(i & 1) * 2 + (i >> 1)], cd[i & 1]);
                    SCHED();
                    __builtin_amdgcn_s_setprio(0);
                    if constexpr (t < 3) cs_read(t + 1);
                    else __builtin_amdgcn_raw_buffer_store_b64((v2u){__float_as_uint(g ? cd[1][0] : cd[0][0]), tag}, rsWS, (g == 1 || g + pw == 0) ? g * 512 + t16 * 8 : OOB,
                                                               WS_G1 + ((hp * 4 + (rt >> 2)) * 192 + pw * 64 + (rt & 3) * 16) * 8, AUX_SC1);
                }
                if constexpr (t == 0) touch(v3, rsW, (~xcd & 1) * 512 * 6144);
                if constexpr (t == 12) code_pf();
                if constexpr (t == 8) {
                    waitv<0>();
                    __builtin_amdgcn_raw_buffer_store_b64((v2u){0u, tag}, rsWS, l == 0 ? 0 : OOB, WS_GC + ((hp * 2 + pw) * 16 + rt) * 8, AUX_SC1);
                }
                if constexpr (t == 8 || t == 9) {
#pragma unroll
                    for (int r = (t - 8) * 2; r < (t - 8) * 2 + 2; r++)
                        asm volatile("buffer_load_dwordx4 %0, %1, %2, %3 offen sc1" : "=v"(gl[r]) : "v"(vgl), "s"(rsWS), "s"(WS_G1 + ((hp * 4 + r) * 192 + pw * 96) * 8) : "memory");
                }
                if constexpr (t == 6) {
                    asm volatile("buffer_load_dwordx2 %0, %1, %2, %3 offen sc1" : "=v"(gs) : "v"(l * 8), "s"(rsWS), "s"(WS_G0 + (rt * 128 + pw * 64) * 8) : "memory");
                }
                if constexpr (t == 11) {
                    waitv<0>();
                    PIN(gs);
                    bad = gs[1] ^ tag;
#pragma unroll
                    for (int r = 0; r < 4; r++) {
                        PIN(gl[r]);
                        bad |= (gl[r][1] ^ tag) | (gl[r][3] ^ tag);
                        s0 += __uint_as_float(gl[r][0]); s1 += __uint_as_float(gl[r][2]);
                    }
                    PIN(s0); PIN(s1); PIN(bad);
                }
                if constexpr (t == 12) {
                    if (RARE(!__all(bad == 0))) {
                        s0 = 0.f; s1 = 0.f;
#pragma nounroll
                        for (unsigned r = 0; r < 4; r++) {
                            unsigned a[2];
                            sweep<2>(rsWS, vgl, WS_G1 + ((hp * 4 + r) * 192 + pw * 96) * 8, 8, tag, a);
                            s0 += __uint_as_float(a[0]); s1 += __uint_as_float(a[1]);
                        }
                        unsigned c[1];
                        sweep<1>(rsWS, l * 8, WS_G0 + (rt * 128 + pw * 64) * 8, 0, tag, c);
                        gs[0] = c[0];
                    }
                    *(LAS f32x2*)(aw0) = (f32x2){-s0, -s1};
                    stsf(aw1, bflo(gs[0]));
                    *(LAS u16*)(aw2) = (u16)(gs[0] >> 16);
                    SCHED();
                }
                if constexpr (t == 5) k_pre();
                if constexpr (t == 6) { wl_pre(); wo_pre(); }
                if constexpr (t == 7) { PIN(vgl); PIN(aw0); PIN(aw1); PIN(aw2); }
                clears(t < 7 ? t : 7, t < 7 ? t + 1 : 7);
                if constexpr (t == 12) asm volatile("buffer_load_dwordx2 %0, %1, %2, %3 offen sc1" : "=v"(gs) : "v"((l & 31) * 8), "s"(rsWS), "s"(WS_GC + (pw ? hq : hp) * 256) : "memory");
                if constexpr (t == 13) {
                    waitv<0>();
                    PIN(gs);
                    if (RARE(!__all(gs[1] == tag))) { unsigned c[1]; sweep<1>(rsWS, (l & 31) * 8, WS_GC + (pw ? hq : hp) * 256, 0, tag, c); }
                    SCHED();
                    pf(0);
                }
                if constexpr (t == 14) pf(1);
            });
            tail16();
        }
        WAITL0();
        STAMPW(7, 4); STAMPW(7, 7);
    }
    const unsigned sFl = WS_F4 + (rt2 * 4 + (lw >> 1) + 2 * half) * 128;
    vD = kr * 16384 + (l & 7) * 16; sD = (kc * 256 + lw * 64) * 2048 + cg * 128;
    PIN(vD);
    asm volatile("" : "+s"(sD), "+s"(rsD));
    if (half) dloads(0, 1);
    SCHED();
    ARR_MARK(20);
    BAR();
    ARR_END();
    const unsigned wb = ldsb + L_WO + half * 65536 + l * 16;
    bf16x8 bb[2][4];
#pragma unroll
    for (int nt = 0; nt < 4; nt++) { bb[0][nt] = lds128(wb + nt * 1024); PIN(bb[0][nt]); }
    unsigned so[16], va0 = l * 16 + half * 32768, va1 = l * 16 + (half ^ 1) * 32768 + 1024;
    unsigned aXw = ldsb + L_XB + w * 4096 + l * 16, aXr = ldsb + L_XB + (w ^ 4) * 4096 + l * 16, sG5 = WS_G5 + ((rt2 * 8 + lw * 2 + half) * 16 + ct) * 256;
    unsigned vG5 = g == q4 ? t16 * 16 : OOB;
#pragma unroll
    for (int st = 0; st < 16; st++) { so[st] = WS_P4 + (rt2 * 8 + lw * 2) * 32768 + ((2 * st + rot2) & 31) * 1024; asm volatile("" : "+s"(so[st])); }
    unsigned wt[2];
#pragma unroll
    for (int i = 0; i < 2; i++) { wt[i] = ldsb + L_WO + half * 65536 + (4 * g + q4) * 128 + (((i * 2 + (p4 >> 1)) ^ ((2 * g + (q4 >> 1)) & 3)) * 32) + (p4 & 1) * 16; PIN(wt[i]); }
    PIN(va0); PIN(va1); PIN(aXw); PIN(aXr); PIN(vG5);
    asm volatile("" : "+s"(sG5));
    SLEEP(13);
    poll16(rsWS, sFl, tag, l);
    FRESH();
    SCHED();
    STAMPW(8, 0); STAMPW(8, 4);
    unsigned swc;
    f32x4 acc2[2][4];
    bf16x8 sop[2];
    {
        auto aload = [&](int st, int m2) { return LD16(m2 ? va1 : va0, so[st]); };
        constexpr int PD = 2;
        v4u af[PD][2];
#pragma unroll
        for (int i = 0; i < PD; i++) {
#pragma unroll
            for (int m2 = 1; m2 >= 0; m2--) af[i][m2] = aload(i, m2);
            SCHED();
        }
#pragma unroll
        for (int nt = 0; nt < 4; nt++) {
            const unsigned x0 = xres[nt >> 1][2 * (nt & 1)], x1 = xres[nt >> 1][2 * (nt & 1) + 1];
            acc2[0][nt] = (f32x4){bflo(x0), bfhi(x0), bflo(x1), bfhi(x1)};
            acc2[1][nt] = zero4;
        }
        unsigned m0 = p4 == 0 ? 0x3F803F80u : 0u, m1 = p4 == 1 ? ~0u : 0u;
        PIN(m0); PIN(m1);
#pragma unroll
        for (int i = 0; i < 2; i++) {
            v4u u;
#pragma unroll
            for (int j = 0; j < 4; j++) u[j] = (wrv[i][j] & m1) | m0;
            sop[i] = __builtin_bit_cast(bf16x8, u);
            PIN(sop[i]);
        }
        {
            float sw = 0.f;
#pragma unroll
            for (int i = 0; i < 8; i++) sw = dot2(wrv[i >> 2][i & 3], 0x3F803F80u, sw);
            swc = __float_as_uint(xrow_sum(sw));
            PIN(swc);
        }
        SCHED();
#pragma unroll
        for (int st = 0; st < 16; st++) {
            if (st == 2) BAR();
            if (st + 1 < 16) {
#pragma unroll
                for (int nt = 0; nt < 4; nt++) {
                    if (st + 1 < 14) bb[(st + 1) % (2)][nt] = lds128(wb + (st + 1) * 4096 + nt * 1024);
                    else bb[(st + 1) % (2)][nt] = cat8(tr_read(wt[nt >> 1] + (st + 1) * 4096 + (nt & 1) * 8), tr_read(wt[nt >> 1] + (st + 1) * 4096 + (nt & 1) * 8 + 2048));
                }
            }
#pragma unroll
            for (int mm = 0; mm < 2; mm++) {
                const int m2 = mm ^ 1;
                const bf16x8 a = __builtin_bit_cast(bf16x8, af[st % PD][m2]);
#pragma unroll
                for (int nt = 0; nt < 4; nt++) acc2[m2][nt] = MFMA(bb[st % (2)][nt], a, acc2[m2][nt]);
            }
            if (st + PD < 16) {
#pragma unroll
                for (int m2 = 1; m2 >= 0; m2--) af[st % PD][m2] = aload(PD + st, m2);
            }
#pragma unroll
            for (int i = 0; i < (st + 1 < 14 ? 4 : 8); i++) { SGB(0x8, 1); SGB(0x100, 1); }
            SCHED();
        }
    }
    STAMPW(9, 0); STAMPW(9, 4);
#pragma unroll
    for (int nt = 0; nt < 4; nt++) stsf4(aXw + nt * 1024, acc2[1][nt]);
    SCHED();
    if (half) dloads(1, 4);
    SCHED();
    WAITL0();
    if (half) __builtin_amdgcn_s_setprio(1);
    BAR();
    SCHED();
    v4u hpk[2];
    {
        f32x4 pa[4];
#pragma unroll
        for (int nt = 0; nt < 4; nt++) pa[nt] = ldsf4(aXr + nt * 1024);
        SCHED();
#pragma unroll
        for (int i = 0; i < 8; i++) {
            const f32x4 a = acc2[0][i >> 1] + pa[i >> 1];
            hpk[i >> 2][i & 3] = pk2(a[2 * (i & 1)], a[2 * (i & 1) + 1]);
        }
        const bf16x8 h0 = __builtin_bit_cast(bf16x8, hpk[0]), h1 = __builtin_bit_cast(bf16x8, hpk[1]);
        f32x4 gq = MFMA(h0, h0, zero4), sd = MFMA(sop[0], h0, zero4);
        gq = MFMA(h1, h1, gq); sd = MFMA(sop[1], h1, sd);
        float q0 = (l & 1) ? gq[1] : gq[0], q1 = (l & 1) ? gq[3] : gq[2];
        PIN(q0); PIN(q1);
        const v4u pv = {__float_as_uint(sd[0]), __float_as_uint((l & 2) ? q1 : q0), __float_as_uint(sd[1]), swc};
        ST16(pv, vG5, sG5);
        asm volatile("s_nop 1" :: "v"(pv));
    }
    __builtin_amdgcn_s_setprio(0);
    STAMPW(10, 0); STAMPW(10, 4);
    v4u wp2[8];
    auto perm2 = [&]() {
#pragma unroll
        for (int i = 16; i < 24; i++) PIN(wreg[i]);
#pragma unroll
        for (int ip = 0; ip < 4; ip++)
#pragma unroll
            for (int c = 0; c < 4; c++) {
                const unsigned a = wreg[16 + 2 * ip][c], b = wreg[16 + 2 * ip + 1][c];
                wp2[2 * c][ip] = __builtin_amdgcn_perm(b, a, 0x05040100);
                wp2[2 * c + 1][ip] = __builtin_amdgcn_perm(b, a, 0x07060302);
            }
#pragma unroll
        for (int c = 0; c < 8; c++) PIN(wp2[c]);
    };
    const unsigned mt = rt2 * 8 + lw * 2 + half;
    {
        v4u pv[4];
        unsigned sK = WS_GK + (ct * 128 + mt) * 4, vK = l == 0 ? 0 : OOB, rk = 2047u - R2;
        asm volatile("" : "+s"(sK));
        PIN(vK); PIN(rk);
        unsigned sS = WS_G5 + mt * 4096;
        asm volatile("" : "+s"(sS));
        SLEEP(18);
        __builtin_amdgcn_s_setprio(1);
        float T[4];
        sweepn<4>([&](int k) { return LD16(l * 16 + k * 1024, sS); }, pv, [&]() {
            const f32x4 ps = (__builtin_bit_cast(f32x4, pv[0]) + __builtin_bit_cast(f32x4, pv[1])) + (__builtin_bit_cast(f32x4, pv[2]) + __builtin_bit_cast(f32x4, pv[3]));
            const float r = xrow_scatter<false>(ps[0], ps[1], ps[2], ps[3]);
            xrow_gather(r, T);
            return r;
        });
        STAMPW(21, 0); STAMPW(21, 4);
        const float mean = T[0] * (1.0f / 1024.0f), var = fmaxf(T[1] * (1.0f / 1024.0f) - mean * mean, 0.f);
        const unsigned sg = pk2(mean, __builtin_amdgcn_sqrtf(rbf(rbf(var) + 1e-5f)));
        const float mb2 = bflo(sg), isd2 = __builtin_amdgcn_rcpf(bfhi(sg));
        const float rsc = (T[2] - mb2 * T[3]) * isd2;
        const unsigned rb = pk2(rsc, rsc);
        const unsigned key = ((rb ^ ((unsigned)((int)rb >> 31) | 0x80000000u)) & 0xffff0000u) | rk;
        unsigned kb = umx(key, dppu<0x128>(key));
        kb = umx(kb, dppu<0x124>(kb)); kb = umx(kb, dppu<0x122>(kb)); kb = umx(kb, dppu<0x121>(kb));
        ST4(kb, vK, sK);
        SCHED();
        __builtin_amdgcn_s_setprio(0);
        if (half) { dloads(4, 8); SCHED(); }
        const bool cand = key == kb;
        v4u e[2], outv[2];
#pragma unroll
        for (int i = 0; i < 8; i++) {
            const float h0 = bflo(hpk[i >> 2][i & 3]), h1 = bfhi(hpk[i >> 2][i & 3]);
            const unsigned d = pk2(h0 - mb2, h1 - mb2);
            e[i >> 2][i & 3] = pk2(bflo(d) * isd2, bfhi(d) * isd2);
        }
        ST16(e[0], cand ? g * 16 : OOB, WS_C + (mt * 16 + ct) * 128);
        ST16(e[1], cand ? g * 16 + 64 : OOB, WS_C + (mt * 16 + ct) * 128);
        SCHED();
        STAMPW(22, 0); STAMPW(22, 4);
#pragma unroll
        for (int i = 0; i < 8; i++)
            outv[i >> 2][i & 3] = pk2(bflo(hpk[i >> 2][i & 3]) + bflo(e[i >> 2][i & 3]), bfhi(hpk[i >> 2][i & 3]) + bfhi(e[i >> 2][i & 3]));
        if (cand) {
#pragma unroll
            for (int i = 0; i < 2; i++) { *(LAS v4u*)(ldsb + L_STASH + w * 256 + i * 64 + g * 16) = hpk[i]; *(LAS v4u*)(ldsb + L_STASH + w * 256 + 128 + i * 64 + g * 16) = e[i]; }
        }
#pragma unroll
        for (int i = 0; i < 2; i++) *(LAS v4u*)(ldsb + L_OUT + (lw * 2 + half) * 2048 + t16 * 128 + (((i * 4 + g) ^ (l & 7)) * 16)) = outv[i];
    }
    const rsrc_t rsO = __builtin_amdgcn_make_buffer_rsrc((void*)OUT, 0, 2048 * 1024 * 2, 0x00020000);
    unsigned aG = ldsb + L_WO + 512;
    PIN(aG);
    unsigned vX = (2 * g + (l & 1)) * 16, aP = (2 * g + (l & 1)) * 32, aR1 = ldsb + L_RED + lw * 4352 + l * 16, vl4 = l * 4;
    unsigned aR2 = ldsb + L_RED + ((lw & 1) * 2 + ((l >> 2) & 1)) * 1088 + kr * 16 + p4 * 4;
    unsigned sX = WS_C + kq * 512 + lw * 128, sG8w = WS_G8 + ((kq * 2 + mat) * 3584 + (cg1 + lw) * 64) * 4, sG8r = WS_G8 + (kc * 256 + lw * 64) * 4;
    unsigned sG9 = WS_G9 + (cg * 14 + kc) * 256;
    unsigned aR3 = aR1 + 17408 - lw * 2176;
    PIN(vX); PIN(aP); PIN(aR1); PIN(aR2); PIN(aR3); PIN(vl4);
    asm volatile("" : "+s"(sX), "+s"(sG8w), "+s"(sG8r), "+s"(sG9));
    SCHED();
    if (w == 1) {
        unsigned sK = WS_GK + (bid & 15) * 512;
        v2u hb[1];
        asm volatile("" : "+s"(sK));
        SLEEP(13);
        stsu(aG, wave_maxu(sweepz<1>([&](int) { return ldg(rsWS, l * 8, sK); }, hb)));
        STAMPR(23, 2);
    }
    if (half) perm2();
    STAMPW(24, 7);
    WAITL0();
    if (half) __builtin_amdgcn_s_setprio(1);
    BAR();
    STAMPW(11, 0); STAMPW(11, 4);
    const unsigned gkv = ldsu(aG);
    if (w < 4) {
        if (bid == 0 && tid == 0) __hip_atomic_store((gu32*)(ws + WS_EPOCH), tag, RLX_AGENT);
        v4u o2[4];
        unsigned vo[4];
#pragma unroll
        for (int i = 0; i < 4; i++) o2[i] = *(const LAS v4u*)(ldsb + L_OUT + w * 4096 + i * 1024 + kr * 128 + (((l & 7) ^ kr) * 16));
#pragma unroll
        for (int i = 0; i < 4; i++) { const unsigned R = mt * 16 + i * 8 + kr; vo[i] = 2047u - R != (gkv & 2047u) ? R * 2048 + (l & 7) * 16 : OOB; PIN(vo[i]); PIN(o2[i]); }
#pragma unroll
        for (int s = 0; s < 3; s++) {
            if (s) { BAR(); SLEEP(3); }
            SCHED();
#pragma unroll
            for (int i = 0; i < 4; i++)
                if ((i + 1) / 2 == s) __builtin_amdgcn_raw_buffer_store_b128(o2[i], rsO, vo[i], ct * 128, AUX_SC1);
            SCHED();
        }
        STAMPW(12, 0);
        return;
    }

    auto gemv = [&](const v4u& x, const v4u* wp, int n, unsigned a) {
        f32x4 d[16], o[4];
#pragma unroll
        for (int c = 0; c < n + 4; c++) {
            if (c < n) d[c] = MFMA(__builtin_bit_cast(bf16x8, x), __builtin_bit_cast(bf16x8, wp[c]), zero4);
            SCHED();
            if (c >= 4) {
                const int j = c - 4;
                float t = d[j][0] + dppf<0x128>(d[j][1]);
                asm volatile("" : "+v"(t) : "v"(d[j]));
                o[j >> 2][j & 3] = t;
                if ((j & 3) == 3) stsf4(a + (j >> 2) * 1088, o[j >> 2]);
            }
            SCHED();
        }
        return o[0][0];
    };
    auto sum4 = [&](unsigned a, unsigned stride) {
        float u = ldsf(a) + ldsf(a + stride), v = ldsf(a + 2 * stride) + ldsf(a + 3 * stride);
        PIN(u); PIN(v);
        return u + v;
    };
    if (ffn) {
        v4u xq[1];
        const unsigned vx = ((~gkv & 0x7f0u) << 7) + vX;
        sweepn<1>([&](int) { return LD16(vx, sX); }, xq, [&]() { return gemv(xq[0], &wp1[0][0], 16, aR1); });
        STAMPW(13, 4);
    }
    WAITL0();
    BAR();
    if (ffn) {
        if (lw < 2) ST4(__float_as_uint(sum4(aR2, 4352)), vl4, sG8w);
        STAMPW(14, 4);
        unsigned pv[8];
        v4u ap;
        SLEEP(12);
        sweepn<8>([&](int k) { return LD4(vl4, sG8r + k * 3584 * 4); }, pv, [&]() {
            const float gg = rbf((__uint_as_float(pv[0]) + __uint_as_float(pv[2])) + (__uint_as_float(pv[4]) + __uint_as_float(pv[6])));
            const float uu = rbf((__uint_as_float(pv[1]) + __uint_as_float(pv[3])) + (__uint_as_float(pv[5]) + __uint_as_float(pv[7])));
            const float act = rbf(gg * __builtin_amdgcn_rcpf(1.0f + __expf(-gg))) * uu;
            const unsigned apk = pk2(act, dppf<0xB1>(act));
#pragma unroll
            for (int ip = 0; ip < 4; ip++) ap[ip] = __builtin_amdgcn_ds_bpermute(aP + ip * 8, apk);
            return act;
        });
        STAMPW(15, 4);
        gemv(ap, wp2, 8, aR3);
    }
    WAITL0();
    BAR();
    if (lw != 0) return;
    if (ffn) ST4(__float_as_uint(sum4(aR2 + 17408, 2176)), vl4, sG9);
    SCHED();
    STAMPW(16, 4);
    const unsigned gkey = rfl(gkv), srow = 2047u - (gkey & 2047u);
    if ((srow >> 7) == rt2) {
        const float rs = __uint_as_float(((gkey & 0x80000000u) ? gkey ^ 0x80000000u : ~gkey) & 0xffff0000u);
        float sig = rbf(1.0f / (1.0f + __expf(-rs)));
        const unsigned sb = ldsb + L_STASH + (((srow >> 5) & 3) + ((srow >> 4) & 1) * 4) * 256 + (t16 * 4 + g) * 2;
        float hc = lds_bf(sb), lc = lds_bf(sb + 128);
        unsigned vo = (t16 * 4 + g) * 2, so_ = srow * 2048 + ct * 128, vy = l < 32 ? l * 16 + 3072 : OOB, sY = WS_G9 + ct * 3584;
        PIN(sig); PIN(hc); PIN(lc); PIN(vo); PIN(vy);
        asm volatile("" : "+s"(so_), "+s"(sY));
        v4u yp[4];
        float y;
        SLEEP(7);
        sweepn<4>([&](int k) { return LD16(k < 3 ? l * 16 + k * 1024 : vy, sY); }, yp, [&]() {
            const f32x4 ys = (__builtin_bit_cast(f32x4, yp[0]) + __builtin_bit_cast(f32x4, yp[1])) + (__builtin_bit_cast(f32x4, yp[2]) + __builtin_bit_cast(f32x4, yp[3]));
            y = xrow_scatter<false>(ys[0], ys[1], ys[2], ys[3]);
            return y;
        });
        __builtin_amdgcn_raw_buffer_store_b16((u16)(pk2(hc + rbf(lc + rbf(rbf(y) * sig)), 0.f) & 0xffffu), rsO, vo, so_, 0);
        STAMPW(17, 4);
    }
}
__device__ __attribute__((used, noinline)) void fk_tail() { asm volatile(".Lfk_end:"); }

extern "C" void kernel_launch(void* const* d_in, const int* in_sizes, int n_in, void* d_out, int out_size, void* d_ws,
                              size_t ws_size, hipStream_t stream) {
    (void)in_sizes; (void)n_in; (void)out_size; (void)ws_size;
    (void)hipFuncSetAttribute((const void*)fused_kernel, hipFuncAttributeMaxDynamicSharedMemorySize, LDS_BYTES);
    fused_kernel<<<256, 512, LDS_BYTES, stream>>>((const u16*)d_in[0], (const u16*)d_in[1], (const u16*)d_in[2], (const u16*)d_in[3],
                                                  (const u16*)d_in[5], (const u16*)d_in[6], (const u16*)d_in[7], (const u16*)d_in[8],
                                                  (u16*)d_out, (char*)d_ws);
}
```

```cpp
#include <hip/hip_runtime.h>
#include <stdint.h>
#include <utility>

typedef unsigned short u16;
typedef unsigned long long u64;
typedef short bf16x8 __attribute__((ext_vector_type(8)));
typedef short s16x4 __attribute__((ext_vector_type(4)));
typedef float f32x4 __attribute__((ext_vector_type(4)));
typedef float f32x2 __attribute__((ext_vector_type(2)));
typedef __bf16 bf16x2 __attribute__((ext_vector_type(2)));
typedef unsigned v4u __attribute__((ext_vector_type(4)));
typedef unsigned v2u __attribute__((ext_vector_type(2)));
#define LAS __attribute__((address_space(3)))
#define GAS __attribute__((address_space(1)))
#define CAS __attribute__((address_space(4)))
typedef GAS u64 gu64;
typedef GAS unsigned gu32;
typedef __amdgpu_buffer_rsrc_t rsrc_t;
#define DI __device__ __forceinline__
#define RLX_AGENT __ATOMIC_RELAXED, __HIP_MEMORY_SCOPE_AGENT
#define BAR() __builtin_amdgcn_s_barrier()
#define SCHED() __builtin_amdgcn_sched_barrier(0)
#define SGB(m, n) __builtin_amdgcn_sched_group_barrier((m), (n), 0)
#define WAITL0() asm volatile("s_waitcnt lgkmcnt(0)" ::: "memory")
#define WL0() __builtin_amdgcn_s_waitcnt(0xc07f)
#define MFMA(a, b, c) __builtin_amdgcn_mfma_f32_16x16x32_bf16((a), (b), (c), 0, 0, 0)
#define ORDER_BEHIND_POLL() __builtin_amdgcn_fence(__ATOMIC_ACQUIRE, "wavefront")
#define AUX_SC1 16
template <int N> DI void waitv() { asm volatile("s_waitcnt vmcnt(%0)" ::"i"(N) : "memory"); }

#define WS_EPOCH 0
#define WS_G0 0x1000
#define WS_G1 0x8000
#define WS_F4 0x78000
#define WS_GC 0x70000
#define WS_G3 0x80000
#define WS_GZ 0xC0000
#define SENT 0xFFFFFFFFu
#define WS_TL 0x100000
#define WS_G5 0x100000
#define WS_G8 0x180000
#define WS_G9 0x1A0000
#define WS_GK 0x1B4000
#define WS_C 0x1C0000
#define WS_P2 0x200000
#define P2_STRIDE 0x2180
#define WS_P4 0x700000
#define WS_USED 0xB00000
#define WS_STAMP 0x1000000

#define STAGE_B 40960
#define LDS_BYTES 163840
#define L_WO 32768
#define L_MB 161792
#define L_WPHI (L_MB - 8192)
#define L_SD (L_MB + 512)
#define L_CS (L_MB + 1024)
#define L_V L_WO
#define L_KH (L_WO + 65536)
#define L_XB 0
#define L_STASH (L_WO + 1024)
#define L_OUT (L_WO + 8192)
#define L_RED 4096

#define SPIN_CAP 20000u
#define W_AUX 2
#define LOG2E 1.4426950408889634f

DI unsigned rfl(unsigned x) { return __builtin_amdgcn_readfirstlane(x); }
DI float bf2f(unsigned u) { return __uint_as_float(u << 16); }
DI float bflo(unsigned u) { return __uint_as_float(u << 16); }
DI float bfhi(unsigned u) { return __uint_as_float(u & 0xffff0000u); }
DI unsigned pk2(float lo, float hi) { f32x2 v = {lo, hi}; return __builtin_bit_cast(unsigned, __builtin_convertvector(v, bf16x2)); }
DI unsigned nsent(unsigned x) { return __builtin_elementwise_min(x, SENT - 1); }
DI unsigned umx(unsigned a, unsigned b) { return __builtin_elementwise_max(a, b); }
DI float rbf(float f) { return bflo(pk2(f, f) & 0xffffu); }
DI bf16x8 mk8(unsigned a, unsigned b, unsigned c, unsigned d) { v4u v = {a, b, c, d}; return __builtin_bit_cast(bf16x8, v); }
DI void dma16(unsigned ldsaddr, unsigned voff, rsrc_t rs, unsigned soff) {
    asm volatile("s_mov_b32 m0, %0\n\ts_nop 0\n\tbuffer_load_dwordx4 %1, %2, %3 offen lds" ::"s"(ldsaddr), "v"(voff), "s"(rs), "s"(soff) : "memory");
}
DI void dma16x4(unsigned ldsaddr, const unsigned* v, rsrc_t rs, unsigned soff) {
    asm volatile("s_mov_b32 m0, %0\n\ts_nop 0\n\tbuffer_load_dwordx4 %1, %5, %6 offen lds\n\tbuffer_load_dwordx4 %2, %5, %6 offen offset:1024 lds\n\t"
                 "buffer_load_dwordx4 %3, %5, %6 offen offset:2048 lds\n\tbuffer_load_dwordx4 %4, %5, %6 offen offset:3072 lds"
                 ::"s"(ldsaddr), "v"(v[0]), "v"(v[1]), "v"(v[2]), "v"(v[3]), "s"(rs), "s"(soff) : "memory");
}
DI void touch(unsigned voff, rsrc_t rs, unsigned soff) { asm volatile("buffer_load_dword v255, %0, %1, %2 offen" ::"v"(voff), "s"(rs), "s"(soff) : "memory", "v255"); }
DI v4u ld16_asm(unsigned voff, rsrc_t rs, unsigned soff) {
    v4u r; asm volatile("buffer_load_dwordx4 %0, %1, %2, %3 offen" : "=v"(r) : "v"(voff), "s"(rs), "s"(soff) : "memory"); return r;
}
#define PIN(x) asm volatile("" : "+v"(x))
DI s16x4 tr_read(unsigned a) { return __builtin_amdgcn_ds_read_tr16_b64_v4i16((LAS s16x4*)(a)); }
DI bf16x8 cat8(s16x4 a, s16x4 b) { return __builtin_shufflevector(a, b, 0, 1, 2, 3, 4, 5, 6, 7); }
DI bf16x8 lds128(unsigned a) { return *(const LAS bf16x8*)(a); }
DI f32x4 ldsf4(unsigned a) { return *(const LAS f32x4*)(a); }
DI float ldsf(unsigned a) { return *(const LAS float*)(a); }
DI unsigned ldsu(unsigned a) { return *(const LAS unsigned*)(a); }
DI void stsf4(unsigned a, f32x4 v) { *(LAS f32x4*)(a) = v; }
DI void stsf(unsigned a, float v) { *(LAS float*)(a) = v; }
DI void stsu(unsigned a, unsigned v) { *(LAS unsigned*)(a) = v; }
template <int C> DI float dppf(float x) { return __uint_as_float(__builtin_amdgcn_update_dpp(0, __float_as_uint(x), C, 0xf, 0xf, true)); }
template <int C> DI unsigned dppu(unsigned x) { return __builtin_amdgcn_update_dpp(0, x, C, 0xf, 0xf, true); }
DI float max2f(float a, float b) { float r; asm("v_max_f32_e32 %0, %1, %2" : "=v"(r) : "v"(a), "v"(b)); return r; }
DI float max3f(float a, float b, float c) { float r; asm("v_max3_f32 %0, %1, %2, %3" : "=v"(r) : "v"(a), "v"(b), "v"(c)); return r; }
DI float row_sum(float x) { x += dppf<0x128>(x); x += dppf<0x124>(x); x += dppf<0x122>(x); x += dppf<0x121>(x); return x; }
DI float xrow_sum(float x) {
    auto a = __builtin_amdgcn_permlane16_swap(__float_as_uint(x), __float_as_uint(x), false, false);
    x = __uint_as_float(a[0]) + __uint_as_float(a[1]);
    auto b = __builtin_amdgcn_permlane32_swap(__float_as_uint(x), __float_as_uint(x), false, false);
    return __uint_as_float(b[0]) + __uint_as_float(b[1]);
}
DI unsigned wave_maxu(unsigned x) {
    x = umx(x, dppu<0x128>(x)); x = umx(x, dppu<0x124>(x)); x = umx(x, dppu<0x122>(x)); x = umx(x, dppu<0x121>(x));
    auto a = __builtin_amdgcn_permlane16_swap(x, x, false, false);
    x = umx(a[0], a[1]);
    auto b = __builtin_amdgcn_permlane32_swap(x, x, false, false);
    return umx(b[0], b[1]);
}
DI float nmax3f(float a, float b, float c) { float r; asm("v_max3_f32 %0, -%1, -%2, -%3" : "=v"(r) : "v"(a), "v"(b), "v"(c)); return r; }
DI float nmax2f(float a, float b) { float r; asm("v_max_f32_e64 %0, -%1, -%2" : "=v"(r) : "v"(a), "v"(b)); return r; }
template <bool MAX> DI float xrow_scatter(float a, float b, float c, float d) {
    const auto s = __builtin_amdgcn_permlane16_swap(__float_as_uint(a), __float_as_uint(b), false, false);
    const auto t = __builtin_amdgcn_permlane16_swap(__float_as_uint(c), __float_as_uint(d), false, false);
    float u = MAX ? max2f(__uint_as_float(s[0]), __uint_as_float(s[1])) : __uint_as_float(s[0]) + __uint_as_float(s[1]);
    float v = MAX ? max2f(__uint_as_float(t[0]), __uint_as_float(t[1])) : __uint_as_float(t[0]) + __uint_as_float(t[1]);
    if (MAX) asm volatile("s_nop 1" : "+v"(u), "+v"(v));
    const auto r = __builtin_amdgcn_permlane32_swap(__float_as_uint(u), __float_as_uint(v), false, false);
    return MAX ? max2f(__uint_as_float(r[0]), __uint_as_float(r[1])) : __uint_as_float(r[0]) + __uint_as_float(r[1]);
}
DI void xrow_gather(float w, float (&o)[4]) {
    const auto r = __builtin_amdgcn_permlane32_swap(__float_as_uint(w), __float_as_uint(w), false, false);
    const auto s = __builtin_amdgcn_permlane16_swap(r[0], r[0], false, false);
    const auto t = __builtin_amdgcn_permlane16_swap(r[1], r[1], false, false);
    o[0] = __uint_as_float(s[0]); o[1] = __uint_as_float(s[1]); o[2] = __uint_as_float(t[0]); o[3] = __uint_as_float(t[1]);
}
struct NoHook { DI void operator()(int, float&) const {} DI void operator()(int, float&, float&) const {} };
struct NoPut { DI void operator()(int, const bf16x8&) const {} };
template <bool MUL, class H = NoHook, class P = NoPut> DI void softmax2(const f32x4 (&p)[2][2], float rs0, float rs1, bool hi, bf16x8 (&out)[4], H hook = H(), P put = P()) {
    float m[4];
#pragma unroll
    for (int i = 0; i < 2; i++) {
        const f32x4 &p0 = p[i][0], &p1 = p[i][1];
        m[2 * i] = max3f(max3f(p0[0], p0[1], p0[2]), max3f(p0[3], p1[0], p1[1]), max2f(p1[2], p1[3]));
        m[2 * i + 1] = max3f(nmax3f(p0[0], p0[1], p0[2]), nmax3f(p0[3], p1[0], p1[1]), nmax2f(p1[2], p1[3]));
    }
    asm volatile("s_nop 1" : "+v"(m[0]), "+v"(m[1]), "+v"(m[2]), "+v"(m[3]));
    hook(1, m[3]);
    const float rsr = hi ? rs1 : rs0;
    float a[4];
    xrow_gather(xrow_scatter<true>(m[0], m[1], m[2], m[3]) * (rsr * -LOG2E), a);
    hook(10, a[0]);
    f32x2 e[4][4];
    float s[4];
#pragma unroll
    for (int k = 0; k < 4; k++) {
        if (k == 2) hook(2, a[2], s[1]);
        const float sc = ((k >> 1) ? rs1 : rs0) * ((k & 1) ? -LOG2E : LOG2E);
#pragma unroll
        for (int j = 0; j < 4; j++) {
            const f32x4& q = p[k >> 1][j >> 1];
            const f32x2 t = __builtin_elementwise_fma((f32x2){q[2 * (j & 1)], q[2 * (j & 1) + 1]}, (f32x2){sc, sc}, (f32x2){a[k], a[k]});
            e[k][j] = (f32x2){__builtin_amdgcn_exp2f(t[0]), __builtin_amdgcn_exp2f(t[1])};
        }
        const f32x2 h = (e[k][0] + e[k][1]) + (e[k][2] + e[k][3]);
        s[k] = h[0] + h[1];
        if (k == 3) hook(3, s[k]);
    }
    float iv = __builtin_amdgcn_rcpf(xrow_scatter<false>(s[0], s[1], s[2], s[3]));
    if (MUL) iv *= rsr;
    xrow_gather(iv, a);
#pragma unroll
    for (int k = 0; k < 4; k++) {
        if (k == 2) hook(4, a[2]);
        const f32x2 c = {a[k], a[k]};
        const f32x2 x0 = e[k][0] * c, x1 = e[k][1] * c, x2 = e[k][2] * c, x3 = e[k][3] * c;
        out[k] = mk8(pk2(x0[0], x0[1]), pk2(x1[0], x1[1]), pk2(x2[0], x2[1]), pk2(x3[0], x3[1]));
        put(k, out[k]);
    }
}
DI float dot2(unsigned a, unsigned b, float c) {
    return __builtin_amdgcn_fdot2_f32_bf16(__builtin_bit_cast(bf16x2, a), __builtin_bit_cast(bf16x2, b), c, false);
}
DI void st_gran(char* ws, unsigned off, unsigned tag, unsigned val) {
    __hip_atomic_store((gu64*)(ws + off), ((u64)tag << 32) | val, RLX_AGENT);
}
DI v2u ldg(rsrc_t rs, unsigned voff, unsigned soff) { return __builtin_amdgcn_raw_buffer_load_b64(rs, voff, soff, AUX_SC1); }
#define RARE(c) __builtin_expect(!!(c), 0)
#define FRESH() asm volatile("" ::: "memory")
template <int N> DI void sweep(rsrc_t rs, unsigned voff, unsigned soff, unsigned S, unsigned tag, unsigned (&v)[N]) {
#pragma nounroll
    for (unsigned spins = 0;;) {
        unsigned bad = 0;
        FRESH();
#pragma unroll
        for (int k = 0; k < N; ++k) { const v2u x = ldg(rs, voff, soff + k * S); v[k] = x[0]; bad |= x[1] ^ tag; }
        if (__all(bad == 0)) break;
        if (++spins > SPIN_CAP) break;
        __builtin_amdgcn_s_sleep(1);
    }
}
template <int A, int B> DI unsigned budget(unsigned& spins) {
    unsigned r;
    asm volatile("s_add_i32 %0, %0, 1\n\ts_cmpk_lt_u32 %0, %2\n\ts_cselect_b32 %1, %3, %4" : "+s"(spins), "=s"(r) : "i"(SPIN_CAP), "i"(A), "i"(B) : "scc");
    SCHED();
    return r;
}
DI unsigned wmax(unsigned x) { return x; }
DI unsigned wmax(v2u x) { return umx(x[0], x[1]); }
DI unsigned wmax(v4u x) { return umx(umx(x[0], x[1]), umx(x[2], x[3])); }
template <int N, class T, class F> DI unsigned sweepz(F ld, T (&v)[N]) {
    unsigned spins = 0, lim, m;
#pragma nounroll
    do {
        FRESH();
#pragma unroll
        for (int k = 0; k < N; ++k) v[k] = ld(k);
        lim = budget<-2, -1>(spins);
        m = 0;
#pragma unroll
        for (int k = 0; k < N; ++k) m = umx(m, wmax(v[k]));
    } while (__any(m > lim));
    return m;
}
template <int N, class T, class F, class G> DI void sweepn(F ld, T (&v)[N], G use) {
    unsigned spins = 0;
    bool again;
#pragma nounroll
    do {
        FRESH();
#pragma unroll
        for (int k = 0; k < N; ++k) v[k] = ld(k);
        const int m = budget<3, 0>(spins);
        again = __any(__builtin_amdgcn_classf(use(), m));
    } while (again);
}
#define OOB 0x4000000u
#define LD16(v, s) __builtin_amdgcn_raw_buffer_load_b128(rsWS, (v), (s), AUX_SC1)
#define LD4(v, s) __builtin_amdgcn_raw_buffer_load_b32(rsWS, (v), (s), AUX_SC1)
#define ST16(x, v, s) __builtin_amdgcn_raw_buffer_store_b128((x), rsWS, (v), (s), AUX_SC1)
#define ST4(x, v, s) __builtin_amdgcn_raw_buffer_store_b32((x), rsWS, (v), (s), AUX_SC1)
#define SLEEP(n) __builtin_amdgcn_s_sleep(n)
DI void poll16(rsrc_t rs, unsigned off, unsigned tag, int l) {
    unsigned spins = 0, lim, bad;
#pragma nounroll
    do {
        FRESH();
        const v2u x = ldg(rs, (l & 15) * 8, off);
        lim = budget<0, -1>(spins);
        bad = (x[1] ^ tag) | (x[0] ^ tag ^ 0x5bd1e995u);
    } while (__any(bad > lim));
    ORDER_BEHIND_POLL();
}
#define STAMPR(i, role)
#define STAMP(i)
#define STAMPW(i, ww)

#define ARR_DECL
#define KATTR
#define ARR_MARK(i)
#define ARR_END()
template <class F, int... Is> DI void static_for_impl(F&& f, std::integer_sequence<int, Is...>) { (f(std::integral_constant<int, Is>{}), ...); }
template <int N, class F> DI void static_for(F&& f) { static_for_impl(f, std::make_integer_sequence<int, N>{}); }

DI s16x4 bq(unsigned b0, unsigned b1, int j, int i) {
    const int ni = (j + 2) % 6;
    return tr_read(((ni & 1) ? b1 : b0) + (ni >> 1) * 8192 + (i >> 1) * 4096 + (i & 1) * 512);
}
template <class F> DI void group(f32x4 (&acc)[4][6], const bf16x8 (&A)[2][4], const bf16x8 (&B)[2], int j, F gap) {
    const int ni = (j + 2) % 6;
#pragma unroll
    for (int i = 0; i < 8; i++) {
        const int mi = i >> 1, ks = i & 1;
        if (ni < 4) asm volatile("v_mfma_f32_16x16x32_bf16 %0, %1, %2, %0" : "+v"(acc[mi][ni]) : "v"(B[ks]), "v"(A[ks][mi]));
        else asm volatile("v_mfma_f32_16x16x32_bf16 %0, %1, %2, %0" : "+v"(acc[mi][ni]) : "v"(A[ks][mi]), "v"(B[ks]));
        gap(i);
        SCHED();
    }
}
struct ImgPut {
    const unsigned (&ia)[4];
    DI void operator()(int k, const bf16x8& f) const {
        const v4u u = __builtin_bit_cast(v4u, f);
        *(LAS v2u*)(ia[2 * (k & 1)] + (k >> 1) * 2048) = (v2u){u[0], u[1]};
        *(LAS v2u*)(ia[2 * (k & 1) + 1] + (k >> 1) * 2048) = (v2u){u[2], u[3]};
        SCHED();
    }
};
DI float lds_bf(unsigned a) { return bf2f(*(const LAS u16*)(a)); }
#define MFMA_SETTLE2(p) asm volatile("s_nop 7\n\ts_nop 1" : "+v"(p[0][0]), "+v"(p[0][1]), "+v"(p[1][0]), "+v"(p[1][1]))

__global__ KATTR __launch_bounds__(512, 2) void fused_kernel(const u16* __restrict__ X, const u16* __restrict__ WQKV, const u16* __restrict__ WPHI,
                                                       const u16* __restrict__ WO, const u16* __restrict__ WR, const u16* __restrict__ WG,
                                                       const u16* __restrict__ WU, const u16* __restrict__ WD, u16* __restrict__ OUT,
                                                       char* __restrict__ ws) {
    extern __shared__ __attribute__((aligned(16))) char lds[];
    u64 pc0; unsigned clen;
    const int tid = threadIdx.x, l = tid & 63;
    asm volatile("s_getpc_b64 %0\n.Lfk_beg:\n\ts_lshr_b32 %1, %3, 3\n\tv_and_b32 v255, 0x1e0, %2\n\tv_add_lshl_u32 v255, v255, %1, 7\n\ts_mov_b32 %1, .Lfk_end-.Lfk_beg\n\t"
                 "v_min_u32 v255, %1, v255\n\tglobal_load_dword v255, v255, %0" : "=&s"(pc0), "=&s"(clen) : "v"(tid), "s"(blockIdx.x) : "v255", "scc");
    SCHED();
    const unsigned w = rfl(tid >> 6);
    const unsigned bid = blockIdx.x, xcd = bid & 7, idx = bid >> 3;
    const unsigned rt = (xcd & 1) * 8 + (idx & 7), hp = (xcd >> 1) * 4 + (idx >> 3);
    const unsigned i16 = idx & 15, rt2 = xcd * 2 + (idx >> 4), ct = i16 == 4 ? 11 : i16 == 11 ? 4 : i16;
    const unsigned hq = (hp + 4) & 15;
    const unsigned rot = (rt >> 2) * 4;
    const unsigned rot2 = (2 * (rt2 + ct)) & 31;
    const unsigned ldsb = (unsigned)(size_t)lds;
    const int g = l >> 4, q4 = (l & 15) >> 2, p4 = l & 3, t16 = l & 15;
    const unsigned kr = l >> 3;
    asm volatile("" ::"s"(X), "s"(WQKV), "s"(WPHI), "s"(WO), "s"(WR), "s"(WG), "s"(WU), "s"(WD), "s"(OUT), "s"(ws));
    unsigned tag;
#define READ_TAG() do { SCHED(); tag = *(const volatile CAS unsigned*)(ws + WS_EPOCH) + 1u; asm volatile("" : "+s"(tag)); } while (0)
    const rsrc_t rsWS = __builtin_amdgcn_make_buffer_rsrc((void*)ws, 0, WS_USED, 0x00020000);
    const rsrc_t rsX = __builtin_amdgcn_make_buffer_rsrc((void*)X, 0, 2048 * 1024 * 2, 0x00020000);
    const rsrc_t rsW = __builtin_amdgcn_make_buffer_rsrc((void*)WQKV, 0, 1024 * 3072 * 2, 0x00020000);
    const unsigned vA = (kr * 1024 + (((l & 7) ^ kr) * 8)) * 2;
    const unsigned vB0 = (kr * 3072 + (((l & 7) ^ (((kr >> 1) & 1) << 1)) * 8)) * 2;
    const unsigned vB1 = (kr * 3072 + (((l & 7) ^ ((((kr >> 1) & 1) | 2) << 1)) * 8)) * 2;
    const unsigned srow1 = rt * 128 + hp * 8 + (w & 3) * 2;
    v4u xr[4];
    if (w < 4) {
#pragma unroll
        for (int i = 0; i < 4; i++) xr[i] = ld16_asm(l * 16, rsX, srow1 * 2048 + i * 1024);
    }
    {
        const unsigned vBw = (w & 1) ? vB1 : vB0;
#pragma unroll
        for (int i = 0; i < 2; i++) dma16(ldsb + (w * 2 + i) * 1024, vA, rsX, (rt * 128 + w * 16 + i * 8) * 2048 + rot * 128);
#pragma unroll
        for (int i = 0; i < 3; i++) dma16(ldsb + 16384 + i * 8192 + w * 1024, vBw, rsW, (rot * 64 + w * 8) * 6144 + (i ? hp : hq) * 128 + i * 2048);
    }
    const f32x4 zero4 = {0.f, 0.f, 0.f, 0.f};
    ARR_DECL;
    const bf16x8 ones = mk8(0x3F803F80u, 0x3F803F80u, 0x3F803F80u, 0x3F803F80u);
    const unsigned lw = w & 3;
    const bool ffn = (ct & 7) != 3;
    const unsigned fb = !ffn ? 255u : rt2 * 14 + (i16 < 3 ? i16 : i16 - 2);
    const unsigned cg = (fb + 5) & 15, kc = fb >> 4;
    const unsigned kq = fb / 56, mat = (fb % 56) / 28, cg1 = (fb % 28) * 2;
    v4u wreg[24];
    STAMPW(0, 0); STAMPW(0, 4);

    const rsrc_t rsWO = __builtin_amdgcn_make_buffer_rsrc((void*)WO, 0, 1024 * 1024 * 2, 0x00020000);
    const unsigned vWO = kr * 2048 + (((l & 7) ^ (((kr >> 1) & 3) << 1)) * 16);
    unsigned vwo[4];
    auto wo_so = [&](unsigned b) { return (((2 * b & 30) + (b >> 4) + rot2) & 31) * 65536 + ct * 128; };
    const unsigned fw = (((t16 >> 1) & 3) << 2) | (((t16 >> 3) & 1) << 1) | (t16 & 1);
    unsigned ad;
    unsigned ia[4];
    bf16x8 sdv[4];
    auto pre_addr = [&]() {
#pragma unroll
        for (int k = 0; k < 4; k++) { ia[k] = ldsb + L_XB + lw * 8192 + (w >> 2) * 4096 + t16 * 128 + (((4 * k + g) ^ fw) * 8); PIN(ia[k]); }
        const unsigned rr = 4 * g + q4, fr = (((rr >> 1) & 3) << 2) | (((rr >> 3) & 1) << 1) | (rr & 1);
        ad = ldsb + L_XB + (w & 1) * 16384 + rr * 128 + ((((w >> 1) * 4 + p4) ^ fr) * 8);
        PIN(ad);
    };
    auto sd_reads = [&]() {
        unsigned sa = ldsb + L_SD + g * 8;
        PIN(sa);
#pragma unroll
        for (int s = 0; s < 4; s++) {
            const v2u a = *(const LAS v2u*)(sa + s * 64), b = *(const LAS v2u*)(sa + s * 64 + 32);
            sdv[s] = mk8(a[0], a[1], b[0], b[1]);
        }
    };
    bf16x8 v0[4], v1[4];
    auto v_reads = [&]() {
        unsigned vd = ldsb + L_V + (w & 1) * 8192 + l * 16;
        PIN(vd);
#pragma unroll
        for (int s = 0; s < 4; s++) { v0[s] = lds128(vd + s * 2048); v1[s] = lds128(vd + s * 2048 + 1024); }
    };
    auto kv_tile = [&](auto hook6) {
        const unsigned h2 = w & 1, ft = w >> 1;
        WAITL0();
        ARR_MARK(18);
        BAR();
        SCHED();
        bf16x8 pf[4];
#pragma unroll
        for (int s = 0; s < 4; s++) pf[s] = cat8(tr_read(ad + s * 4096), tr_read(ad + s * 4096 + 2048));
        SCHED();
        hook6();
        SCHED();
        f32x4 k0 = zero4, k1 = zero4, kz = zero4;
#pragma unroll
        for (int s = 0; s < 4; s++) { k0 = MFMA(pf[s], v0[s], k0); k1 = MFMA(pf[s], v1[s], k1); SCHED(); }
#pragma unroll
        for (int s = 0; s < 4; s++) kz = MFMA(pf[s], sdv[s], kz);
        const unsigned pbase = WS_P2 + (hp * 16 + rt) * P2_STRIDE;
        const v4u o0 = {pk2(k0[0], k0[1]), pk2(k0[2], k0[3]), pk2(k1[0], k1[1]), pk2(k1[2], k1[3])};
        __builtin_amdgcn_raw_buffer_store_b128(o0, rsWS, l * 16, pbase + h2 * 4096 + ft * 1024, AUX_SC1);
        asm volatile("s_nop 1" :: "v"(o0));
        __builtin_amdgcn_raw_buffer_store_b64((v2u){pk2(kz[0], kz[1]), pk2(kz[2], kz[3])}, rsWS, t16 == 0 ? g * 8 : OOB,
                                              pbase + 8192 + h2 * 128 + ft * 32, AUX_SC1);
        SCHED();
        ARR_MARK(19);
        BAR();
    };

    const bool zjob = lw == 0 && (rt & 7) == 0;
    v4u pp[4];
    auto pp_ld = [&](int k) {
        return LD16(kr * P2_STRIDE + (l & 7) * 16, WS_P2 + (hp * 16 + (k & 1) * 8) * P2_STRIDE +
                    (k < 2 ? (rt >> 2) * 2048 + (lw & 1) * 1024 + (rt & 3) * 256 + (lw >> 1) * 128 : 8192 + (rt >> 3) * 128));
    };
    auto kv_reduce = [&]() {
        unsigned v3 = (l & 8) ? OOB : (l & 7) * 16 + g * 4, res;
        unsigned s3 = WS_G3 + (hp * 4 + (rt >> 2)) * 2048 + (lw & 1) * 1024 + (rt & 3) * 256 + (lw >> 1) * 128;
        PIN(v3);
        asm volatile("" : "+s"(s3));
        auto red = [&](const v4u* p) {
            float lo = xrow_scatter<false>(bflo(p[0][0]) + bflo(p[1][0]), bflo(p[0][1]) + bflo(p[1][1]), bflo(p[0][2]) + bflo(p[1][2]), bflo(p[0][3]) + bflo(p[1][3]));
            float hi = xrow_scatter<false>(bfhi(p[0][0]) + bfhi(p[1][0]), bfhi(p[0][1]) + bfhi(p[1][1]), bfhi(p[0][2]) + bfhi(p[1][2]), bfhi(p[0][3]) + bfhi(p[1][3]));
            const float chk = lo;
            lo += dppf<0x128>(lo); hi += dppf<0x128>(hi);
            res = nsent(pk2(lo, hi));
            return chk;
        };
        SLEEP(2);
        if (RARE(zjob)) {
            unsigned rz;
            sweepn<4>(pp_ld, pp, [&]() { const float a = red(pp + 2); rz = res; return a + red(pp); });
            ST4(rz, (l & 8) ? OOB : ((l >> 2) & 1) * 64 + ((l & 1) * 2 + (g >> 1)) * 16 + ((l >> 1) & 1) * 8 + (g & 1) * 4, WS_GZ + (hp * 2 + (rt >> 3)) * 128);
        } else {
            v4u p2[2];
            sweepn<2>(pp_ld, p2, [&]() { return red(p2); });
        }
        STAMPW(25, 0);
        ST4(res, v3, s3);
    };

    const unsigned half = w >> 2;
    const unsigned R2 = rt2 * 128 + lw * 32 + half * 16 + t16;

    rsrc_t rsD = __builtin_amdgcn_make_buffer_rsrc((void*)WD, 0, 1024 * 3584 * 2, 0x00020000);
    unsigned vD, sD;
    auto dloads = [&](int i0, int i1) {
#pragma unroll
        for (int i = i0; i < i1; i++)
            wreg[16 + i] = __builtin_amdgcn_raw_buffer_load_b128(rsD, vD, sD + i * 2048, W_AUX);
        FRESH();
    };
    v4u wp1[2][8];
    auto perm1 = [&]() {
#pragma unroll
        for (int n = 0; n < 16; n++) PIN(wreg[n]);
#pragma unroll
        for (int ip = 0; ip < 4; ip++)
#pragma unroll
            for (int c = 0; c < 4; c++) {
#pragma unroll
                for (int m = 0; m < 2; m++) {
                    const unsigned a = wreg[m * 8 + 2 * ip][c], b = wreg[m * 8 + 2 * ip + 1][c];
                    wp1[m][2 * c][ip] = __builtin_amdgcn_perm(b, a, 0x05040100);
                    wp1[m][2 * c + 1][ip] = __builtin_amdgcn_perm(b, a, 0x07060302);
                }
            }
#pragma unroll
        for (int c = 0; c < 8; c++) { PIN(wp1[0][c]); PIN(wp1[1][c]); }
    };
    v4u wrv[2];
    auto res_loads = [&]() {
#pragma unroll
        for (int i = 0; i < 2; i++) {
            dma16(ldsb + L_XB + w * 4096 + i * 1024, kr * 2048 + (((l & 7) ^ kr) * 16), rsX, (rt2 * 128 + lw * 32 + half * 16 + i * 8) * 2048 + ct * 128);
            wrv[i] = *(const v4u*)(WR + ct * 64 + i * 32 + 8 * g);
        }
        FRESH();
    };

    if (w < 4) {
        const unsigned wm = w & 1, hh = w >> 1;
        f32x4 acc[4][6];
        bf16x8 wf[2], wfq[2];
        float mbt[4], rst[4], csv[2];
        f32x4 mbv[4], csq[2], csk[2];
#pragma unroll
        for (int i = 0; i < 4; i++)
#pragma unroll
            for (int j = 0; j < 6; j++) { acc[i][j] = zero4; PIN(acc[i][j]); }
        {
            const unsigned aoff0 = ldsb + (wm * 64 + t16) * 128 + (((0 + g) ^ (l & 7)) * 16);
            const unsigned aoff1 = ldsb + (wm * 64 + t16) * 128 + (((4 + g) ^ (l & 7)) * 16);
            const unsigned sk = ((q4 >> 1) & 1) | ((g & 1) << 1);
            const unsigned boffA = ldsb + 16384 + (8 * g + q4) * 128 + (((2 * hh) ^ sk) * 32) + p4 * 8;
            const unsigned boffB = ldsb + 16384 + (8 * g + q4) * 128 + (((2 * hh + 1) ^ sk) * 32) + p4 * 8;
            bf16x8 A[2][4], B[3][2];
            s16x4 lo;
            unsigned a0[3], a1[3], bA[3], bB[3];
#pragma unroll
            for (int i = 0; i < 3; i++) {
                a0[i] = aoff0 + i * STAGE_B; a1[i] = aoff1 + i * STAGE_B; bA[i] = boffA + i * STAGE_B; bB[i] = boffB + i * STAGE_B;
                PIN(a0[i]); PIN(a1[i]); PIN(bA[i]); PIN(bB[i]);
            }
            unsigned aT = ldsb + L_MB + (wm * 64 + t16) * 4, aS = ldsb + L_SD + (wm * 64 + t16) * 2, aM = ldsb + L_MB + wm * 256 + g * 16;
            unsigned aC = ldsb + L_CS + hh * 128 + g * 16, aV = ldsb + L_CS + (128 + hh * 32 + t16) * 4;
            unsigned aW = ldsb + L_WPHI + hh * 2048 + (4 * g + q4) * 64 + p4 * 8, aK = ldsb + L_KH + w * 2048 + l * 16;
            PIN(aT); PIN(aS); PIN(aM); PIN(aC); PIN(aV); PIN(aW); PIN(aK);
            pre_addr();
            READ_TAG();
            waitv<5>();
            PIN(xr[0]); PIN(xr[1]); PIN(xr[2]); PIN(xr[3]);
            {
                float s[2], q[2], x0[2];
#pragma unroll
                for (int rr = 0; rr < 2; rr++) {
                    x0[rr] = bflo(rfl(xr[2 * rr][0]));
                    s[rr] = 0.f; q[rr] = 0.f;
#pragma unroll
                    for (int i = 0; i < 4; i++) {
                        const float a = bflo(xr[2 * rr][i]) - x0[rr], b = bfhi(xr[2 * rr][i]) - x0[rr];
                        const float c = bflo(xr[2 * rr + 1][i]) - x0[rr], d = bfhi(xr[2 * rr + 1][i]) - x0[rr];
                        s[rr] += (a + b) + (c + d);
                        q[rr] += (a * a + b * b) + (c * c + d * d);
                    }
                }
                const float sq = row_sum(xrow_scatter<false>(s[0], s[1], q[0], q[1]));
#define RLF(x, n) __uint_as_float(__builtin_amdgcn_readlane(__float_as_uint(x), (n)))
                const float r0 = RLF(sq, 0), r1 = RLF(sq, 16), r2 = RLF(sq, 32), r3 = RLF(sq, 48);
                const float sm = (l ? r1 : r0) * (1.0f / 1024.0f), qm = (l ? r3 : r2) * (1.0f / 1024.0f);
                const float var = fmaxf(qm - sm * sm, 0.f);
                if (l < 2) st_gran(ws, WS_G0 + (srow1 + l) * 8, tag, pk2((l ? x0[1] : x0[0]) + sm, sqrtf(rbf(rbf(var) + 1e-5f))));
            }
            waitv<1>();
            WAITL0();
            ARR_MARK(0);
            BAR();
            SCHED();
            STAMPW(1, 0);
            auto rdB = [&](int u, int j, int i) {
                if (i & 1) B[j % 3][i >> 1] = cat8(lo, bq(bA[u], bB[u], j, i)); else lo = bq(bA[u], bB[u], j, i);
            };
            auto rdA = [&](int u, int i) { A[i & 1][i >> 1] = lds128(((i & 1) ? a1[u] : a0[u]) + (i >> 1) * 2048); };
            auto head = [&](int u) {
                group(acc, A, B[0], 0, [&](int i) { rdB(u, 1 + (i >> 2), i & 3); });
                group(acc, A, B[1], 1, [&](int i) { if (i < 4) rdB(u, 3, i); });
                group(acc, A, B[2], 2, [&](int i) { if (i < 4) rdB(u, 4, i); });
                group(acc, A, B[0], 3, [&](int i) { if (i < 4) rdB(u, 5, i); });
                WL0();
            };
#pragma unroll
            for (int i = 0; i < 4; i++) rdB(0, 0, i);
#pragma unroll
            for (int i = 0; i < 8; i++) rdA(0, i);
            SCHED();
#pragma nounroll
            for (unsigned t = 0; t < 5; t++) {
#pragma unroll
                for (int u = 0; u < 3; u++) {
                    head(u);
                    ARR_MARK(1 + t * 3 + u);
                    BAR();
                    SCHED();
                    group(acc, A, B[1], 4, [&](int i) { if (i < 4) rdB((u + 1) % 3, 0, i); });
                    group(acc, A, B[2], 5, [&](int i) { rdA((u + 1) % 3, i); });
                }
            }
            unsigned kw[4][4], rsu[4];
            f32x2 kx;
            auto kpiece = [&](int n) {
                if (n > 0) { kw[((n - 1 >> 2) + 2) & 3][(n - 1) & 3] = pk2(kx[0], kx[1]); PIN(kw[((n - 1 >> 2) + 2) & 3][(n - 1) & 3]); }
                if (n < 16) {
                    const int mi = ((n >> 2) + 2) & 3, j = n & 3;
                    const f32x4 &c = acc[mi][2 + (j >> 1)], &s_ = csk[j >> 1];
                    float x0 = __builtin_fmaf(mbt[mi], s_[2 * (j & 1)], c[2 * (j & 1)]); PIN(x0);
                    float x1 = __builtin_fmaf(mbt[mi], s_[2 * (j & 1) + 1], c[2 * (j & 1) + 1]); PIN(x1);
                    kx = (f32x2){x0, x1};
                }
                if (n == 5 || n == 9) *(LAS v4u*)(aK + (n == 9) * 1024) = (v4u){kw[2 + (n == 9)][0], kw[2 + (n == 9)][1], kw[2 + (n == 9)][2], kw[2 + (n == 9)][3]};
            };
            group(acc, A, B[0], 0, [&](int i) { rdB(0, 1 + (i >> 2), i & 3); });
            group(acc, A, B[1], 1, [&](int i) {
                if (i < 4) { rdB(0, 3, i); mbt[(i + 2) & 3] = ldsf(aT + ((i + 2) & 3) * 64); }
                else if (i < 6) csk[i - 4] = ldsf4(aC + 256 + (i - 4) * 64);
                else wf[i - 6] = cat8(tr_read(aW + (i - 6) * 32), tr_read(aW + (i - 6) * 32 + 1024));
            });
            group(acc, A, B[2], 2, [&](int i) { if (i < 4) rdB(0, 4, i); else mbv[i - 4] = ldsf4(aM + (i - 4) * 64); kpiece(i); });
            group(acc, A, B[0], 3, [&](int i) {
                if (i < 4) rdB(0, 5, i); else rsu[i - 4] = *(const LAS u16*)(aS + (i - 4) * 32);
                if (i == 2 || i == 3) csv[i - 2] = ldsf(aV + (i - 2) * 64);
                kpiece(8 + i);
            });
            WL0();
            ARR_MARK(16);
            BAR();
            SCHED();
            kpiece(16);
            f32x4 p[2][2];
#pragma unroll
            for (int i = 0; i < 4; i++) p[i >> 1][i & 1] = MFMA(wf[i & 1], mk8(kw[i >> 1][0], kw[i >> 1][1], kw[i >> 1][2], kw[i >> 1][3]), zero4);
            SCHED();
            STAMPW(2, 0);
#pragma unroll
            for (int mi = 0; mi < 4; mi++) rst[mi] = bf2f(rsu[mi]);
#pragma unroll
            for (int a = 0; a < 2; a++)
#pragma unroll
                for (int dt = 0; dt < 2; dt++) {
                    const f32x4 x0 = acc[2 * a][4 + dt] + mbv[2 * a] * csv[dt], x1 = acc[2 * a + 1][4 + dt] + mbv[2 * a + 1] * csv[dt];
                    *(LAS v4u*)(aK + (L_V - L_KH) + w * 2048 + (a * 2 + dt) * 1024) = (v4u){pk2(x0[0], x0[1]), pk2(x0[2], x0[3]), pk2(x1[0], x1[1]), pk2(x1[2], x1[3])};
                }
            SCHED();
            rst[0] = __builtin_amdgcn_rcpf(rst[0]); rst[1] = __builtin_amdgcn_rcpf(rst[1]);
            bf16x8 f[4];
            struct HookC {
                decltype(v_reads)& vr; decltype(sd_reads)& sr;
                DI void operator()(int k, float& x) const { if (k == 10) { PIN(x); WAITL0(); ARR_MARK(17); BAR(); PIN(x); } if (k == 3) { PIN(x); vr(); sr(); PIN(x); } }
                DI void operator()(int, float&, float&) const {}
            };
            softmax2<true>(p, rst[0], rst[1], g >= 2, f, HookC{v_reads, sd_reads}, ImgPut{ia});
#pragma unroll
            for (int ft = 0; ft < 2; ft++) wfq[ft] = cat8(tr_read(aW + ft * 32 + 4096), tr_read(aW + ft * 32 + 5120));
            csq[0] = ldsf4(aC); csq[1] = ldsf4(aC + 64);
            STAMPW(3, 0);
            kv_tile([]() {});
            STAMPW(4, 0);
            SCHED();
            group(acc, A, B[1], 4, [&](int i) { if (i == 2 || i == 3) { rst[i] = __builtin_amdgcn_rcpf(rst[i]); PIN(rst[i]); } });
            group(acc, A, B[2], 5, [&](int) {});
            asm volatile("s_nop 7\n\ts_nop 1");
            SCHED();
        }
        bf16x8 phq[2][4];
        v4u kg[6];
        unsigned sG3 = WS_G3 + (hq * 2 + hh) * 4096, sGZ = WS_GZ + (hq * 2 + hh) * 128;
        {
            f32x4 pq[2][2][2];
#pragma unroll
            for (int mi = 0; mi < 4; mi++) {
                const f32x4 x0 = acc[mi][0] + mbt[mi] * csq[0], x1 = acc[mi][1] + mbt[mi] * csq[1];
                const bf16x8 qf = mk8(pk2(x0[0], x0[1]), pk2(x0[2], x0[3]), pk2(x1[0], x1[1]), pk2(x1[2], x1[3]));
                pq[mi >> 1][mi & 1][0] = MFMA(wfq[0], qf, zero4); pq[mi >> 1][mi & 1][1] = MFMA(wfq[1], qf, zero4);
            }
            MFMA_SETTLE2(pq[0]); MFMA_SETTLE2(pq[1]);
            softmax2<false>(pq[0], rst[0], rst[1], g >= 2, phq[0]);
#pragma unroll
            for (int i = 0; i < 4; i++) PIN(phq[0][i]);
            SCHED();
            kv_reduce();
            SCHED();
            res_loads();
            SCHED();
            BAR();
            SCHED();
            softmax2<false>(pq[1], rst[2], rst[3], g >= 2, phq[1]);
#pragma unroll
            for (int i = 0; i < 4; i++) { PIN(phq[0][i]); PIN(phq[1][i]); }
        }
        STAMPW(5, 0);
        STAMPW(26, 0);
        v2u fv = {tag ^ 0x5bd1e995u, tag};
        unsigned sF = WS_F4 + ((rt * 4 + w) * 16 + hq) * 8, vF = l == 0 ? 0 : OOB;
        {
            unsigned vO = l * 16 + hh * 1024, sO = WS_P4 + ((rt * 8 + wm * 4) * 32 + hq * 2) * 1024;
            asm volatile("" : "+s"(sO), "+s"(sF), "+s"(sG3), "+s"(sGZ));
            PIN(fv); PIN(vF);
            SLEEP(8);
            v4u ov[4];
#define PH(mi, s) phq[(mi) >> 1][2 * ((mi) & 1) + (s)]
#define KVA(s, dt) __builtin_bit_cast(bf16x8, kg[2 + 2 * (s) + (dt)])
            auto pairup = [&](v4u& a, v4u& b) {
                unsigned a2 = a[2], a3 = a[3], b0 = b[0], b1 = b[1];
                asm volatile("v_swap_b32 %0, %2\n\tv_swap_b32 %1, %3" : "+v"(a2), "+v"(a3), "+v"(b0), "+v"(b1));
                a[2] = a2; a[3] = a3; b[0] = b0; b[1] = b1;
            };
            f32x4 dn[4], n0[4], n1[4];
            float id[4];
            unsigned spins = 0, lim, m;
#pragma nounroll
            do {
                FRESH();
#pragma unroll
                for (int k = 0; k < 6; k++) { kg[k] = k >= 2 ? LD16(l * 16 + (k - 2) * 1024, sG3) : LD16(g * 16 + k * 64, sGZ); SCHED(); }
                lim = budget<-2, -1>(spins);
                m = 0;
#pragma unroll
                for (int i = 0; i < 8; i++) {
                    dn[i & 3] = MFMA(__builtin_bit_cast(bf16x8, kg[i >> 2]), PH(i & 3, i >> 2), i < 4 ? zero4 : dn[i & 3]);
                    if (i >= 1 && i < 7) { m = umx(umx(m, kg[i - 1][0]), kg[i - 1][1]); PIN(m); m = umx(umx(m, kg[i - 1][2]), kg[i - 1][3]); }
                    if (i == 5) pairup(kg[2], kg[3]);
                    if (i == 7) pairup(kg[4], kg[5]);
                    SCHED();
                }
            } while (__any(m > lim));
            STAMPW(6, 0);
#pragma unroll
            for (int mi = 0; mi <= 4; mi++) {
                if (mi < 4) {
                    n0[mi] = MFMA(KVA(0, 0), PH(mi, 0), zero4); n1[mi] = MFMA(KVA(0, 1), PH(mi, 0), zero4);
                    n0[mi] = MFMA(KVA(1, 0), PH(mi, 1), n0[mi]); n1[mi] = MFMA(KVA(1, 1), PH(mi, 1), n1[mi]);
                    id[mi] = __builtin_amdgcn_rcpf(rbf(rbf(dn[mi][0]) + 1e-6f));
                    asm volatile("" :: "v"(dn[mi]));
                }
                if (mi > 0) {
                    const f32x4 a = n0[mi - 1] * id[mi - 1], b = n1[mi - 1] * id[mi - 1];
                    ov[mi - 1] = (v4u){pk2(a[0], a[1]), pk2(a[2], a[3]), pk2(b[0], b[1]), pk2(b[2], b[3])};
                    ST16(ov[mi - 1], vO ^ ((mi - 1) * 32768 + ((mi - 1) & 1) * 1024), sO);
                }
                if (mi < 4) { SGB(0x8, 1); SGB(0x2, 6); SGB(0x8, 1); SGB(0x2, 3); SGB(0x8, 1); SGB(0x2, 3); SGB(0x8, 1); SGB(0x2, 4); }
                SCHED();
            }
#pragma unroll
            for (int mi = 0; mi < 4; mi++) PIN(ov[mi]);
        }
        BAR();
        waitv<0>();
        __builtin_amdgcn_raw_buffer_store_b64(fv, rsWS, vF, sF, AUX_SC1);
        STAMPW(7, 0);
    } else {
        const unsigned pw = lw & 1;
        const rsrc_t rsG = __builtin_amdgcn_make_buffer_rsrc((void*)(mat ? WU : WG), 0, 1024 * 3584 * 2, 0x00020000);
        unsigned vwl = kr * 57344 + (l & 7) * 16;
        unsigned sg = (kq * 256 + lw * 64) * 7168 + cg1 * 128;
        auto wloads = [&](int n0, int n1) {
#pragma unroll
            for (int j = n0; j < n1; j++) {
                const int n = (j & 1) * 8 + (j >> 1);
                asm volatile("buffer_load_dwordx4 %0, %1, %2, %3 offen nt" : "=v"(wreg[n]) : "v"(vwl), "s"(rsG), "s"(sg + (n >> 3) * 128 + (n & 7) * 7168));
            }
        };
        auto repack = [&](unsigned gI) {
            const unsigned s2 = (2 * g + (q4 >> 1)) & 3;
            bf16x8 f[4];
#pragma unroll
            for (int nt = 0; nt < 4; nt++) {
                const unsigned a = ldsb + L_WO + gI * 4096 + (4 * g + q4) * 128 + ((((nt >> 1) * 2 + (p4 >> 1)) ^ s2) * 32) + (p4 & 1) * 16 + (nt & 1) * 8;
                f[nt] = cat8(tr_read(a), tr_read(a + 2048));
            }
            WAITL0();
#pragma unroll
            for (int nt = 0; nt < 4; nt++) *(LAS bf16x8*)(ldsb + L_WO + gI * 4096 + nt * 1024 + l * 16) = f[nt];
        };
        unsigned aK, aW, aS;
        auto k_pre = [&]() {
            pre_addr();
            aK = ldsb + L_KH + lw * 2048 + l * 16; aW = ldsb + L_WPHI + (lw >> 1) * 2048 + (4 * g + q4) * 64 + p4 * 8; aS = ldsb + L_SD + (pw * 64 + 32 + t16) * 2;
            PIN(aK); PIN(aW); PIN(aS);
        };
        auto kfeat = [&](auto hook) {
            bf16x8 wf[2];
            float rst[2];
#pragma unroll
            for (int ft = 0; ft < 2; ft++) {
                wf[ft] = cat8(tr_read(aW + ft * 32), tr_read(aW + ft * 32 + 1024));
                rst[ft] = __builtin_amdgcn_rcpf(lds_bf(aS + ft * 32));
            }
            sd_reads();
            PIN(wf[0]); PIN(wf[1]); PIN(rst[0]); PIN(rst[1]);
            SCHED();
            ARR_MARK(16);
            BAR();
            SCHED();
            const bf16x8 kf0 = lds128(aK), kf1 = lds128(aK + 1024);
            SCHED();
            hook(-1, rst[0]);
            hook(0, rst[0]);
            SCHED();
            f32x4 p[2][2];
            p[0][0] = MFMA(wf[0], kf0, zero4); p[0][1] = MFMA(wf[1], kf0, zero4);
            p[1][0] = MFMA(wf[0], kf1, zero4); p[1][1] = MFMA(wf[1], kf1, zero4);
            MFMA_SETTLE2(p);
            bf16x8 f[4];
            softmax2<true>(p, rst[0], rst[1], g >= 2, f, hook, ImgPut{ia});
            SCHED();
            hook(5, rst[0]);
            SCHED();
            STAMPW(3, 4); STAMPW(3, 7);
            kv_tile([&]() { hook(6, rst[0]); });
            STAMPW(4, 4); STAMPW(4, 7);
        };
        auto wblk = [&](int k) {
            constexpr unsigned T[8] = {0x19181716, 0x041c1b1a, 0x08070605, 0x0c0b0a09, 0x1413120d, 0x11100015, 0x1d030201, 0x1f1e0f0e};
            return (T[k] >> (8 * lw)) & 31;
        };
        auto wo_init = [&]() {
#pragma unroll
            for (int i = 0; i < 4; i++) { vwo[i] = vWO + i * (16384 - 1024); PIN(vwo[i]); }
        };
        unsigned hs[8];
        auto wl_pre = [&]() { asm volatile("" : "+s"(sg)); PIN(vwl); };
        auto wo_pre = [&]() {
            wo_init();
#pragma unroll
            for (int k = 0; k < 8; k++) { hs[k] = wo_so(wblk(k)); asm volatile("" : "+s"(hs[k])); }
        };
        auto wo_rng = [&](int k0, int k1) {
#pragma unroll
            for (int k = k0; k < k1; k++) dma16x4(ldsb + L_WO + wblk(k) * 4096, vwo, rsWO, hs[k]);
        };
        auto pf = [&](int i) {
            if (i == 0) { wloads(0, 6); SCHED(); if (lw < 2) wo_pre(); SCHED(); }
            if (i < 3 && !ffn) wo_rng(i, i + 1);
            if (i == 1 || i == 2) wloads(6 * i, i == 1 ? 12 : 16);
        };
        struct Hook {
            decltype(pf)& f; decltype(v_reads)& vr;
            DI void operator()(int k, float& x) const { if (k == 10) return; PIN(x); if (k == 3) { ARR_MARK(17); BAR(); vr(); } f(3 + k); PIN(x); }
            DI void operator()(int k, float& x, float& y) const { PIN(y); f(3 + k); PIN(x); }
        };
        auto tail16 = [&]() {
            kfeat(Hook{pf, v_reads});
            if (ffn) wo_rng(0, 3);
            wo_rng(3, 5);
            static_for<3>([&](auto kc_) {
                constexpr int k = decltype(kc_)::value;
                waitv<4 * (4 - k)>();
                repack(wblk(k));
            });
            BAR();
            res_loads();
            wo_rng(5, 7);
            static_for<4>([&](auto kc_) {
                constexpr int k = decltype(kc_)::value + 3;
                waitv<4 * (6 - k)>();
                repack(wblk(k));
            });
            PIN(wrv[0]); PIN(wrv[1]);
            perm1();
            SCHED();
            BAR();
            wo_rng(7, 8);
        };
        auto vT23 = [&]() {
            const unsigned n = (idx * 2 + pw) * 64 + l, r = n / 12, c = n % 12;
            return idx < 24 ? ((r & 127) + (r >> 7) * 256 + 128) * 6144 + (c >> 2) * 2048 + (((xcd >> 1) * 4 + (c >> 2 ? 0 : 4) + (c & 3)) & 15) * 128 : OOB;
        };
        if (lw < 2) {
            const unsigned sA = (rt * 128 + lw * 64) * 2048, sB = lw * 32 * 6144 + hp * 128, sBq = lw * 32 * 6144 + hq * 128;
            unsigned buf = 1;
            unsigned va[4], vb[4];
#pragma unroll
            for (int i = 0; i < 4; i++) { va[i] = vA + i * (16384 - 1024); vb[i] = ((i & 1) ? vB1 : vB0) + i * (49152 - 1024); PIN(va[i]); PIN(vb[i]); }
            auto stage = [&](unsigned s, unsigned bf, int i0, int i1) {
                const unsigned kt = (s + rot) & 15;
                const unsigned lb = ldsb + bf * STAGE_B;
                const unsigned sa = sA + kt * 128, sb = kt * 393216;
                if (i0 <= 0 && 0 < i1) dma16x4(lb + lw * 8192, va, rsX, sa);
                if (i0 <= 1 && 1 < i1) dma16x4(lb + lw * 8192 + 4096, va, rsX, sa + 65536);
                if (i0 <= 2 && 2 < i1) dma16x4(lb + 16384 + 8192 + lw * 4096, vb, rsW, sb + sB + 2048);
                if (i0 <= 3 && 3 < i1) dma16x4(lb + 16384 + 16384 + lw * 4096, vb, rsW, sb + sB + 4096);
                if (i0 <= 4 && 4 < i1) dma16x4(lb + 16384 + lw * 4096, vb, rsW, sb + sBq);
            };
            stage(1, 1, 0, 4);
            SCHED();
            unsigned vt = vT23();
            PIN(vt);
            wl_pre(); k_pre();
            waitv<16>(); ARR_MARK(0); BAR();
            stage(1, 1, 4, 5);
            touch(vt, rsW, (xcd & 1) * 512 * 6144);
            waitv<1>(); ARR_MARK(1); BAR();
            stage(2, 2, 0, 5); stage(3, 0, 0, 3);
            waitv<12>(); ARR_MARK(2); BAR();
            stage(3, 0, 3, 5); stage(4, 1, 0, 4);
            waitv<16>(); ARR_MARK(3); BAR();
            stage(4, 1, 4, 5);
            buf = 2;
#pragma nounroll
            for (unsigned s = 5; s < 16; s++) {
                if (s > 5) { waitv<20>(); ARR_MARK(s - 2); BAR(); }
                stage(s, buf, 0, 5);
                buf = buf == 2 ? 0 : buf + 1;
            }
            waitv<20>(); ARR_MARK(14); BAR();
            pf(0);
            if (ffn) waitv<6>(); else waitv<10>();
            ARR_MARK(15); BAR();
            pf(1);
            tail16();
            READ_TAG();
        } else {
            SCHED();
            unsigned v3;
            const unsigned h3 = (xcd >> 1) * 4 + 3, kv3 = !(xcd & 2), wv = idx * 2 + pw;
            auto line3 = [&](unsigned per, unsigned rows, auto rowf) {
                const unsigned n = wv * (kv3 ? per : (per + 2) / 3) + l, r3 = kv3 ? n / 3 : n, c3 = kv3 ? n % 3 : 0;
                return (idx < 24 && l < (kv3 ? per : (per + 2) / 3) && r3 < rows) ? rowf(r3) * 6144 + c3 * 2048 + ((c3 ? h3 : h3 + 4) & 15) * 128 : OOB;
            };
            v3 = line3(32, 512, [](unsigned r) { return r; });
            touch(line3(16, 256, [](unsigned r) { return (r & 127) + (r >> 7) * 256; }), rsW, (xcd & 1) * 512 * 6144);
            {
                const rsrc_t rsP = __builtin_amdgcn_make_buffer_rsrc((void*)WPHI, 0, 32 * 1024 * 2, 0x00020000);
#pragma unroll
                for (int i = 0; i < 4; i++) dma16(ldsb + L_WPHI + pw * 4096 + i * 1024, l * 16, rsP, (pw ? hq : hp) * 4096 + i * 1024);
            }
            v4u sv = {SENT, SENT, SENT, SENT};
            unsigned vc4 = l < 16 ? WS_G3 + (hp * 4 + (rt >> 2)) * 2048 + pw * 1024 + (rt & 3) * 256 + l * 16
                         : l < 24 ? WS_P2 + (hp * 16 + rt) * P2_STRIDE + 8192 + pw * 128 + (l - 16) * 16
                         : (l < 32 && pw == 0 && (rt & 7) == 0) ? WS_GZ + (hp * 2 + (rt >> 3)) * 128 + (l - 24) * 16 : OOB;
            auto clears = [&](int i0, int i1) {
#pragma unroll
                for (int i = i0; i < i1; i++) {
                    if (i < 4) ST16(sv, l * 16, WS_P2 + (hp * 16 + rt) * P2_STRIDE + pw * 4096 + i * 1024);
                    if (i == 4) ST16(sv, vc4, 0);
                    if (i > 4) ST16(sv, l * 16, WS_TL + (bid * 4 + pw * 2 + i - 5) * 1024);
                }
                FRESH();
            };
            PIN(vc4); PIN(sv);
            unsigned ca = ldsb + 16384 + pw * 8192 + (8 * g + q4) * 128 + (((rt & 3) ^ (((q4 >> 1) & 1) | ((g & 1) << 1))) * 32) + p4 * 8;
            PIN(ca);
            bf16x8 cf[4];
            f32x4 cd[2] = {zero4, zero4};
            auto cs_read = [&](int st) {
#pragma unroll
                for (int i = 0; i < 4; i++) cf[i] = cat8(tr_read(ca + (st % 3) * STAGE_B + i * 4096), tr_read(ca + (st % 3) * STAGE_B + i * 4096 + 512));
                SCHED();
            };
            READ_TAG();
            waitv<5>();
            ARR_MARK(0);
            BAR();
            SCHED();
            cs_read(0);
            FRESH();
            const rsrc_t rsC = __builtin_amdgcn_make_buffer_rsrc((void*)pc0, 0, clen, 0x00020000);
            auto code_pf = [&]() { touch(l < 6 ? l * 128 : OOB, rsC, (idx * 2 + pw) * 768); };
            v4u gl[4];
            float s0 = 0.f, s1 = 0.f;
            unsigned bad;
            v2u gs;
            unsigned vgl = (l < 48 ? l : 0) * 16;
            unsigned aw0 = ldsb + L_CS + pw * 384 + (vgl >> 1), aw1 = ldsb + L_MB + (pw * 64 + l) * 4, aw2 = ldsb + L_SD + (pw * 64 + l) * 2;
            static_for<15>([&](auto tc) {
                constexpr int t = decltype(tc)::value;
                WAITL0();
                ARR_MARK(t + 1);
                BAR();
                SCHED();
                if constexpr (t < 4) {
                    __builtin_amdgcn_s_setprio(2);
#pragma unroll
                    for (int i = 0; i < 4; i++) cd[i & 1] = MFMA(ones, cf[(i & 1) * 2 + (i >> 1)], cd[i & 1]);
                    SCHED();
                    __builtin_amdgcn_s_setprio(0);
                    if constexpr (t < 3) cs_read(t + 1);
                    else __builtin_amdgcn_raw_buffer_store_b64((v2u){__float_as_uint(g ? cd[1][0] : cd[0][0]), tag}, rsWS, (g == 1 || g + pw == 0) ? g * 512 + t16 * 8 : OOB,
                                                               WS_G1 + ((hp * 4 + (rt >> 2)) * 192 + pw * 64 + (rt & 3) * 16) * 8, AUX_SC1);
                }
                if constexpr (t == 0) touch(v3, rsW, (~xcd & 1) * 512 * 6144);
                if constexpr (t == 12) code_pf();
                if constexpr (t == 8) {
                    waitv<0>();
                    __builtin_amdgcn_raw_buffer_store_b64((v2u){0u, tag}, rsWS, l == 0 ? 0 : OOB, WS_GC + ((hp * 2 + pw) * 16 + rt) * 8, AUX_SC1);
                }
                if constexpr (t == 8 || t == 9) {
#pragma unroll
                    for (int r = (t - 8) * 2; r < (t - 8) * 2 + 2; r++)
                        asm volatile("buffer_load_dwordx4 %0, %1, %2, %3 offen sc1" : "=v"(gl[r]) : "v"(vgl), "s"(rsWS), "s"(WS_G1 + ((hp * 4 + r) * 192 + pw * 96) * 8) : "memory");
                }
                if constexpr (t == 6) {
                    asm volatile("buffer_load_dwordx2 %0, %1, %2, %3 offen sc1" : "=v"(gs) : "v"(l * 8), "s"(rsWS), "s"(WS_G0 + (rt * 128 + pw * 64) * 8) : "memory");
                }
                if constexpr (t == 11) {
                    waitv<0>();
                    PIN(gs);
                    bad = gs[1] ^ tag;
#pragma unroll
                    for (int r = 0; r < 4; r++) {
                        PIN(gl[r]);
                        bad |= (gl[r][1] ^ tag) | (gl[r][3] ^ tag);
                        s0 += __uint_as_float(gl[r][0]); s1 += __uint_as_float(gl[r][2]);
                    }
                    PIN(s0); PIN(s1); PIN(bad);
                }
                if constexpr (t == 12) {
                    if (RARE(!__all(bad == 0))) {
                        s0 = 0.f; s1 = 0.f;
#pragma nounroll
                        for (unsigned r = 0; r < 4; r++) {
                            unsigned a[2];
                            sweep<2>(rsWS, vgl, WS_G1 + ((hp * 4 + r) * 192 + pw * 96) * 8, 8, tag, a);
                            s0 += __uint_as_float(a[0]); s1 += __uint_as_float(a[1]);
                        }
                        unsigned c[1];
                        sweep<1>(rsWS, l * 8, WS_G0 + (rt * 128 + pw * 64) * 8, 0, tag, c);
                        gs[0] = c[0];
                    }
                    *(LAS f32x2*)(aw0) = (f32x2){-s0, -s1};
                    stsf(aw1, bflo(gs[0]));
                    *(LAS u16*)(aw2) = (u16)(gs[0] >> 16);
                    SCHED();
                }
                if constexpr (t == 5) k_pre();
                if constexpr (t == 6) { wl_pre(); wo_pre(); }
                if constexpr (t == 7) { PIN(vgl); PIN(aw0); PIN(aw1); PIN(aw2); }
                clears(t < 7 ? t : 7, t < 7 ? t + 1 : 7);
                if constexpr (t == 12) asm volatile("buffer_load_dwordx2 %0, %1, %2, %3 offen sc1" : "=v"(gs) : "v"((l & 31) * 8), "s"(rsWS), "s"(WS_GC + (pw ? hq : hp) * 256) : "memory");
                if constexpr (t == 13) {
                    waitv<0>();
                    PIN(gs);
                    if (RARE(!__all(gs[1] == tag))) { unsigned c[1]; sweep<1>(rsWS, (l & 31) * 8, WS_GC + (pw ? hq : hp) * 256, 0, tag, c); }
                    SCHED();
                    pf(0);
                }
                if constexpr (t == 14) pf(1);
            });
            tail16();
        }
        WAITL0();
        STAMPW(7, 4); STAMPW(7, 7);
    }
    const unsigned sFl = WS_F4 + (rt2 * 4 + (lw >> 1) + 2 * half) * 128;
    vD = kr * 16384 + (l & 7) * 16; sD = (kc * 256 + lw * 64) * 2048 + cg * 128;
    PIN(vD);
    asm volatile("" : "+s"(sD), "+s"(rsD));
    if (half) dloads(0, 1);
    SCHED();
    ARR_MARK(20);
    BAR();
    ARR_END();
    const unsigned wb = ldsb + L_WO + half * 65536 + l * 16;
    bf16x8 bb[2][4];
    v4u xres[2];
#pragma unroll
    for (int nt = 0; nt < 4; nt++) {
        if (nt < 2) xres[nt] = *(const LAS v4u*)(ldsb + L_XB + w * 4096 + t16 * 128 + (((nt * 4 + g) ^ (l & 7)) * 16));
        bb[0][nt] = lds128(wb + nt * 1024);
        PIN(bb[0][nt]);
    }
    PIN(xres[0]); PIN(xres[1]);
    unsigned so[16], va0 = l * 16 + half * 32768, va1 = l * 16 + (half ^ 1) * 32768 + 1024;
    unsigned aXw = ldsb + L_XB + w * 4096 + l * 16, aXr = ldsb + L_XB + (w ^ 4) * 4096 + l * 16, sG5 = WS_G5 + ((rt2 * 8 + lw * 2 + half) * 16 + ct) * 256;
    unsigned vG5 = g == q4 ? t16 * 16 : OOB;
#pragma unroll
    for (int st = 0; st < 16; st++) { so[st] = WS_P4 + (rt2 * 8 + lw * 2) * 32768 + ((2 * st + rot2) & 31) * 1024; asm volatile("" : "+s"(so[st])); }
    unsigned wt[2];
#pragma unroll
    for (int i = 0; i < 2; i++) { wt[i] = ldsb + L_WO + half * 65536 + (4 * g + q4) * 128 + (((i * 2 + (p4 >> 1)) ^ ((2 * g + (q4 >> 1)) & 3)) * 32) + (p4 & 1) * 16; PIN(wt[i]); }
    PIN(va0); PIN(va1); PIN(aXw); PIN(aXr); PIN(vG5);
    asm volatile("" : "+s"(sG5));
    SLEEP(13);
    poll16(rsWS, sFl, tag, l);
    FRESH();
    SCHED();
    STAMPW(8, 0); STAMPW(8, 4);
    unsigned swc;
    f32x4 acc2[2][4];
    bf16x8 sop[2];
    {
        auto aload = [&](int st, int m2) { return LD16(m2 ? va1 : va0, so[st]); };
        constexpr int PD = 2;
        v4u af[PD][2];
#pragma unroll
        for (int i = 0; i < PD; i++) {
#pragma unroll
            for (int m2 = 1; m2 >= 0; m2--) af[i][m2] = aload(i, m2);
            SCHED();
        }
#pragma unroll
        for (int nt = 0; nt < 4; nt++) {
            const unsigned x0 = xres[nt >> 1][2 * (nt & 1)], x1 = xres[nt >> 1][2 * (nt & 1) + 1];
            acc2[0][nt] = (f32x4){bflo(x0), bfhi(x0), bflo(x1), bfhi(x1)};
            acc2[1][nt] = zero4;
        }
        unsigned m0 = p4 == 0 ? 0x3F803F80u : 0u, m1 = p4 == 1 ? ~0u : 0u;
        PIN(m0); PIN(m1);
#pragma unroll
        for (int i = 0; i < 2; i++) {
            v4u u;
#pragma unroll
            for (int j = 0; j < 4; j++) u[j] = (wrv[i][j] & m1) | m0;
            sop[i] = __builtin_bit_cast(bf16x8, u);
            PIN(sop[i]);
        }
        {
            float sw = 0.f;
#pragma unroll
            for (int i = 0; i < 8; i++) sw = dot2(wrv[i >> 2][i & 3], 0x3F803F80u, sw);
            swc = __float_as_uint(xrow_sum(sw));
            PIN(swc);
        }
        SCHED();
#pragma unroll
        for (int st = 0; st < 16; st++) {
            if (st == 2) BAR();
            if (st + 1 < 16) {
#pragma unroll
                for (int nt = 0; nt < 4; nt++) {
                    if (st + 1 < 14) bb[(st + 1) % (2)][nt] = lds128(wb + (st + 1) * 4096 + nt * 1024);
                    else bb[(st + 1) % (2)][nt] = cat8(tr_read(wt[nt >> 1] + (st + 1) * 4096 + (nt & 1) * 8), tr_read(wt[nt >> 1] + (st + 1) * 4096 + (nt & 1) * 8 + 2048));
                }
            }
#pragma unroll
            for (int mm = 0; mm < 2; mm++) {
                const int m2 = mm ^ 1;
                const bf16x8 a = __builtin_bit_cast(bf16x8, af[st % PD][m2]);
#pragma unroll
                for (int nt = 0; nt < 4; nt++) acc2[m2][nt] = MFMA(bb[st % (2)][nt], a, acc2[m2][nt]);
            }
            if (st + PD < 16) {
#pragma unroll
                for (int m2 = 1; m2 >= 0; m2--) af[st % PD][m2] = aload(PD + st, m2);
            }
#pragma unroll
            for (int i = 0; i < (st + 1 < 14 ? 4 : 8); i++) { SGB(0x8, 1); SGB(0x100, 1); }
            SCHED();
        }
    }
    STAMPW(9, 0); STAMPW(9, 4);
#pragma unroll
    for (int nt = 0; nt < 4; nt++) stsf4(aXw + nt * 1024, acc2[1][nt]);
    SCHED();
    if (half) dloads(1, 4);
    SCHED();
    WAITL0();
    if (half) __builtin_amdgcn_s_setprio(1);
    BAR();
    SCHED();
    v4u hpk[2];
    {
        f32x4 pa[4];
#pragma unroll
        for (int nt = 0; nt < 4; nt++) pa[nt] = ldsf4(aXr + nt * 1024);
        SCHED();
#pragma unroll
        for (int i = 0; i < 8; i++) {
            const f32x4 a = acc2[0][i >> 1] + pa[i >> 1];
            hpk[i >> 2][i & 3] = pk2(a[2 * (i & 1)], a[2 * (i & 1) + 1]);
        }
        const bf16x8 h0 = __builtin_bit_cast(bf16x8, hpk[0]), h1 = __builtin_bit_cast(bf16x8, hpk[1]);
        f32x4 gq = MFMA(h0, h0, zero4), sd = MFMA(sop[0], h0, zero4);
        gq = MFMA(h1, h1, gq); sd = MFMA(sop[1], h1, sd);
        float q0 = (l & 1) ? gq[1] : gq[0], q1 = (l & 1) ? gq[3] : gq[2];
        PIN(q0); PIN(q1);
        const v4u pv = {__float_as_uint(sd[0]), __float_as_uint((l & 2) ? q1 : q0), __float_as_uint(sd[1]), swc};
        ST16(pv, vG5, sG5);
        asm volatile("s_nop 1" :: "v"(pv));
    }
    __builtin_amdgcn_s_setprio(0);
    STAMPW(10, 0); STAMPW(10, 4);
    v4u wp2[8];
    auto perm2 = [&]() {
#pragma unroll
        for (int i = 16; i < 24; i++) PIN(wreg[i]);
#pragma unroll
        for (int ip = 0; ip < 4; ip++)
#pragma unroll
            for (int c = 0; c < 4; c++) {
                const unsigned a = wreg[16 + 2 * ip][c], b = wreg[16 + 2 * ip + 1][c];
                wp2[2 * c][ip] = __builtin_amdgcn_perm(b, a, 0x05040100);
                wp2[2 * c + 1][ip] = __builtin_amdgcn_perm(b, a, 0x07060302);
            }
#pragma unroll
        for (int c = 0; c < 8; c++) PIN(wp2[c]);
    };
    const unsigned mt = rt2 * 8 + lw * 2 + half;
    {
        v4u pv[4];
        unsigned sK = WS_GK + (ct * 128 + mt) * 4, vK = l == 0 ? 0 : OOB, rk = 2047u - R2;
        asm volatile("" : "+s"(sK));
        PIN(vK); PIN(rk);
        unsigned sS = WS_G5 + mt * 4096;
        asm volatile("" : "+s"(sS));
        SLEEP(18);
        __builtin_amdgcn_s_setprio(1);
        float T[4];
        sweepn<4>([&](int k) { return LD16(l * 16 + k * 1024, sS); }, pv, [&]() {
            const f32x4 ps = (__builtin_bit_cast(f32x4, pv[0]) + __builtin_bit_cast(f32x4, pv[1])) + (__builtin_bit_cast(f32x4, pv[2]) + __builtin_bit_cast(f32x4, pv[3]));
            const float r = xrow_scatter<false>(ps[0], ps[1], ps[2], ps[3]);
            xrow_gather(r, T);
            return r;
        });
        STAMPW(21, 0); STAMPW(21, 4);
        const float mean = T[0] * (1.0f / 1024.0f), var = fmaxf(T[1] * (1.0f / 1024.0f) - mean * mean, 0.f);
        const unsigned sg = pk2(mean, __builtin_amdgcn_sqrtf(rbf(rbf(var) + 1e-5f)));
        const float mb2 = bflo(sg), isd2 = __builtin_amdgcn_rcpf(bfhi(sg));
        const float rsc = (T[2] - mb2 * T[3]) * isd2;
        const unsigned rb = pk2(rsc, rsc);
        const unsigned key = ((rb ^ ((unsigned)((int)rb >> 31) | 0x80000000u)) & 0xffff0000u) | rk;
        unsigned kb = umx(key, dppu<0x128>(key));
        kb = umx(kb, dppu<0x124>(kb)); kb = umx(kb, dppu<0x122>(kb)); kb = umx(kb, dppu<0x121>(kb));
        ST4(kb, vK, sK);
        SCHED();
        __builtin_amdgcn_s_setprio(0);
        if (half) { dloads(4, 8); SCHED(); }
        const bool cand = key == kb;
        v4u e[2], outv[2];
#pragma unroll
        for (int i = 0; i < 8; i++) {
            const float h0 = bflo(hpk[i >> 2][i & 3]), h1 = bfhi(hpk[i >> 2][i & 3]);
            const unsigned d = pk2(h0 - mb2, h1 - mb2);
            e[i >> 2][i & 3] = pk2(bflo(d) * isd2, bfhi(d) * isd2);
        }
        ST16(e[0], cand ? g * 16 : OOB, WS_C + (mt * 16 + ct) * 128);
        ST16(e[1], cand ? g * 16 + 64 : OOB, WS_C + (mt * 16 + ct) * 128);
        SCHED();
        STAMPW(22, 0); STAMPW(22, 4);
#pragma unroll
        for (int i = 0; i < 8; i++)
            outv[i >> 2][i & 3] = pk2(bflo(hpk[i >> 2][i & 3]) + bflo(e[i >> 2][i & 3]), bfhi(hpk[i >> 2][i & 3]) + bfhi(e[i >> 2][i & 3]));
        if (cand) {
#pragma unroll
            for (int i = 0; i < 2; i++) { *(LAS v4u*)(ldsb + L_STASH + w * 256 + i * 64 + g * 16) = hpk[i]; *(LAS v4u*)(ldsb + L_STASH + w * 256 + 128 + i * 64 + g * 16) = e[i]; }
        }
#pragma unroll
        for (int i = 0; i < 2; i++) *(LAS v4u*)(ldsb + L_OUT + (lw * 2 + half) * 2048 + t16 * 128 + (((i * 4 + g) ^ (l & 7)) * 16)) = outv[i];
    }
    const rsrc_t rsO = __builtin_amdgcn_make_buffer_rsrc((void*)OUT, 0, 2048 * 1024 * 2, 0x00020000);
    unsigned aG = ldsb + L_WO + 512;
    PIN(aG);
    unsigned vX = (2 * g + (l & 1)) * 16, aP = (2 * g + (l & 1)) * 32, aR1 = ldsb + L_RED + lw * 4352 + l * 16, vl4 = l * 4;
    unsigned aR2 = ldsb + L_RED + ((lw & 1) * 2 + ((l >> 2) & 1)) * 1088 + kr * 16 + p4 * 4;
    unsigned sX = WS_C + kq * 512 + lw * 128, sG8w = WS_G8 + ((kq * 2 + mat) * 3584 + (cg1 + lw) * 64) * 4, sG8r = WS_G8 + (kc * 256 + lw * 64) * 4;
    unsigned sG9 = WS_G9 + (cg * 14 + kc) * 256;
    unsigned aR3 = aR1 + 17408 - lw * 2176;
    PIN(vX); PIN(aP); PIN(aR1); PIN(aR2); PIN(aR3); PIN(vl4);
    asm volatile("" : "+s"(sX), "+s"(sG8w), "+s"(sG8r), "+s"(sG9));
    SCHED();
    if (w == 1) {
        unsigned sK = WS_GK + (bid & 15) * 512;
        v2u hb[1];
        asm volatile("" : "+s"(sK));
        SLEEP(13);
        stsu(aG, wave_maxu(sweepz<1>([&](int) { return ldg(rsWS, l * 8, sK); }, hb)));
        STAMPR(23, 2);
    }
    if (half) perm2();
    STAMPW(24, 7);
    WAITL0();
    if (half) __builtin_amdgcn_s_setprio(1);
    BAR();
    STAMPW(11, 0); STAMPW(11, 4);
    const unsigned gkv = ldsu(aG);
    if (w < 4) {
        if (bid == 0 && tid == 0) __hip_atomic_store((gu32*)(ws + WS_EPOCH), tag, RLX_AGENT);
        v4u o2[4];
        unsigned vo[4];
#pragma unroll
        for (int i = 0; i < 4; i++) o2[i] = *(const LAS v4u*)(ldsb + L_OUT + w * 4096 + i * 1024 + kr * 128 + (((l & 7) ^ kr) * 16));
#pragma unroll
        for (int i = 0; i < 4; i++) { const unsigned R = mt * 16 + i * 8 + kr; vo[i] = 2047u - R != (gkv & 2047u) ? R * 2048 + (l & 7) * 16 : OOB; PIN(vo[i]); PIN(o2[i]); }
#pragma unroll
        for (int s = 0; s < 3; s++) {
            if (s) { BAR(); SLEEP(3); }
            SCHED();
#pragma unroll
            for (int i = 0; i < 4; i++)
                if ((i + 1) / 2 == s) __builtin_amdgcn_raw_buffer_store_b128(o2[i], rsO, vo[i], ct * 128, AUX_SC1);
            SCHED();
        }
        STAMPW(12, 0);
        return;
    }

    auto gemv = [&](const v4u& x, const v4u* wp, int n, unsigned a) {
        f32x4 d[16], o[4];
#pragma unroll
        for (int c = 0; c < n + 4; c++) {
            if (c < n) d[c] = MFMA(__builtin_bit_cast(bf16x8, x), __builtin_bit_cast(bf16x8, wp[c]), zero4);
            SCHED();
            if (c >= 4) {
                const int j = c - 4;
                float t = d[j][0] + dppf<0x128>(d[j][1]);
                asm volatile("" : "+v"(t) : "v"(d[j]));
                o[j >> 2][j & 3] = t;
                if ((j & 3) == 3) stsf4(a + (j >> 2) * 1088, o[j >> 2]);
            }
            SCHED();
        }
        return o[0][0];
    };
    auto sum4 = [&](unsigned a, unsigned stride) {
        float u = ldsf(a) + ldsf(a + stride), v = ldsf(a + 2 * stride) + ldsf(a + 3 * stride);
        PIN(u); PIN(v);
        return u + v;
    };
    if (ffn) {
        v4u xq[1];
        const unsigned vx = ((~gkv & 0x7f0u) << 7) + vX;
        sweepn<1>([&](int) { return LD16(vx, sX); }, xq, [&]() { return gemv(xq[0], &wp1[0][0], 16, aR1); });
        STAMPW(13, 4);
    }
    WAITL0();
    BAR();
    if (ffn) {
        if (lw < 2) ST4(__float_as_uint(sum4(aR2, 4352)), vl4, sG8w);
        STAMPW(14, 4);
        unsigned pv[8];
        v4u ap;
        SLEEP(12);
        sweepn<8>([&](int k) { return LD4(vl4, sG8r + k * 3584 * 4); }, pv, [&]() {
            const float gg = rbf((__uint_as_float(pv[0]) + __uint_as_float(pv[2])) + (__uint_as_float(pv[4]) + __uint_as_float(pv[6])));
            const float uu = rbf((__uint_as_float(pv[1]) + __uint_as_float(pv[3])) + (__uint_as_float(pv[5]) + __uint_as_float(pv[7])));
            const float act = rbf(gg * __builtin_amdgcn_rcpf(1.0f + __expf(-gg))) * uu;
            const unsigned apk = pk2(act, dppf<0xB1>(act));
#pragma unroll
            for (int ip = 0; ip < 4; ip++) ap[ip] = __builtin_amdgcn_ds_bpermute(aP + ip * 8, apk);
            return act;
        });
        STAMPW(15, 4);
        gemv(ap, wp2, 8, aR3);
    }
    WAITL0();
    BAR();
    if (lw != 0) return;
    if (ffn) ST4(__float_as_uint(sum4(aR2 + 17408, 2176)), vl4, sG9);
    SCHED();
    STAMPW(16, 4);
    const unsigned gkey = rfl(gkv), srow = 2047u - (gkey & 2047u);
    if ((srow >> 7) == rt2) {
        const float rs = __uint_as_float(((gkey & 0x80000000u) ? gkey ^ 0x80000000u : ~gkey) & 0xffff0000u);
        float sig = rbf(1.0f / (1.0f + __expf(-rs)));
        const unsigned sb = ldsb + L_STASH + (((srow >> 5) & 3) + ((srow >> 4) & 1) * 4) * 256 + (t16 * 4 + g) * 2;
        float hc = lds_bf(sb), lc = lds_bf(sb + 128);
        unsigned vo = (t16 * 4 + g) * 2, so_ = srow * 2048 + ct * 128, vy = l < 32 ? l * 16 + 3072 : OOB, sY = WS_G9 + ct * 3584;
        PIN(sig); PIN(hc); PIN(lc); PIN(vo); PIN(vy);
        asm volatile("" : "+s"(so_), "+s"(sY));
        v4u yp[4];
        float y;
        SLEEP(7);
        sweepn<4>([&](int k) { return LD16(k < 3 ? l * 16 + k * 1024 : vy, sY); }, yp, [&]() {
            const f32x4 ys = (__builtin_bit_cast(f32x4, yp[0]) + __builtin_bit_cast(f32x4, yp[1])) + (__builtin_bit_cast(f32x4, yp[2]) + __builtin_bit_cast(f32x4, yp[3]));
            y = xrow_scatter<false>(ys[0], ys[1], ys[2], ys[3]);
            return y;
        });
        __builtin_amdgcn_raw_buffer_store_b16((u16)(pk2(hc + rbf(lc + rbf(rbf(y) * sig)), 0.f) & 0xffffu), rsO, vo, so_, 0);
        STAMPW(17, 4);
    }
}
__device__ __attribute__((used, noinline)) void fk_tail() { asm volatile(".Lfk_end:"); }

extern "C" void kernel_launch(void* const* d_in, const int* in_sizes, int n_in, void* d_out, int out_size, void* d_ws,
                              size_t ws_size, hipStream_t stream) {
    (void)in_sizes; (void)n_in; (void)out_size; (void)ws_size;
    (void)hipFuncSetAttribute((const void*)fused_kernel, hipFuncAttributeMaxDynamicSharedMemorySize, LDS_BYTES);
    fused_kernel<<<256, 512, LDS_BYTES, stream>>>((const u16*)d_in[0], (const u16*)d_in[1], (const u16*)d_in[2], (const u16*)d_in[3],
                                                  (const u16*)d_in[5], (const u16*)d_in[6], (const u16*)d_in[7], (const u16*)d_in[8],
                                                  (u16*)d_out, (char*)d_ws);
}
```

```cpp
#include <hip/hip_runtime.h>
#include <stdint.h>
#include <utility>

typedef unsigned short u16;
typedef unsigned long long u64;
typedef short bf16x8 __attribute__((ext_vector_type(8)));
typedef short s16x4 __attribute__((ext_vector_type(4)));
typedef float f32x4 __attribute__((ext_vector_type(4)));
typedef float f32x2 __attribute__((ext_vector_type(2)));
typedef __bf16 bf16x2 __attribute__((ext_vector_type(2)));
typedef unsigned v4u __attribute__((ext_vector_type(4)));
typedef unsigned v2u __attribute__((ext_vector_type(2)));
#define LAS __attribute__((address_space(3)))
#define GAS __attribute__((address_space(1)))
#define CAS __attribute__((address_space(4)))
typedef GAS u64 gu64;
typedef GAS unsigned gu32;
typedef __amdgpu_buffer_rsrc_t rsrc_t;
#define DI __device__ __forceinline__
#define RLX_AGENT __ATOMIC_RELAXED, __HIP_MEMORY_SCOPE_AGENT
#define BAR() __builtin_amdgcn_s_barrier()
#define SCHED() __builtin_amdgcn_sched_barrier(0)
#define SGB(m, n) __builtin_amdgcn_sched_group_barrier((m), (n), 0)
#define WAITL0() asm volatile("s_waitcnt lgkmcnt(0)" ::: "memory")
#define WL0() __builtin_amdgcn_s_waitcnt(0xc07f)
#define MFMA(a, b, c) __builtin_amdgcn_mfma_f32_16x16x32_bf16((a), (b), (c), 0, 0, 0)
#define ORDER_BEHIND_POLL() __builtin_amdgcn_fence(__ATOMIC_ACQUIRE, "wavefront")
#define AUX_SC1 16
template <int N> DI void waitv() { asm volatile("s_waitcnt vmcnt(%0)" ::"i"(N) : "memory"); }

#define WS_EPOCH 0
#define WS_G0 0x1000
#define WS_G1 0x8000
#define WS_F4 0x78000
#define WS_GC 0x70000
#define WS_G3 0x80000
#define WS_GZ 0xC0000
#define SENT 0xFFFFFFFFu
#define WS_TL 0x100000
#define WS_G5 0x100000
#define WS_G8 0x180000
#define WS_G9 0x1A0000
#define WS_GK 0x1B4000
#define WS_C 0x1C0000
#define WS_P2 0x200000
#define P2_STRIDE 0x2180
#define WS_P4 0x700000
#define WS_USED 0xB00000
#define WS_STAMP 0x1000000

#define STAGE_B 40960
#define LDS_BYTES 163840
#define L_WO 32768
#define L_MB 161792
#define L_WPHI (L_MB - 8192)
#define L_SD (L_MB + 512)
#define L_CS (L_MB + 1024)
#define L_V L_WO
#define L_KH (L_WO + 65536)
#define L_XB 0
#define L_STASH (L_WO + 1024)
#define L_OUT (L_WO + 8192)
#define L_RED 4096

#define SPIN_CAP 20000u
#define W_AUX 2
#define LOG2E 1.4426950408889634f

DI unsigned rfl(unsigned x) { return __builtin_amdgcn_readfirstlane(x); }
DI float bf2f(unsigned u) { return __uint_as_float(u << 16); }
DI float bflo(unsigned u) { return __uint_as_float(u << 16); }
DI float bfhi(unsigned u) { return __uint_as_float(u & 0xffff0000u); }
DI unsigned pk2(float lo, float hi) { f32x2 v = {lo, hi}; return __builtin_bit_cast(unsigned, __builtin_convertvector(v, bf16x2)); }
DI unsigned nsent(unsigned x) { return __builtin_elementwise_min(x, SENT - 1); }
DI unsigned umx(unsigned a, unsigned b) { return __builtin_elementwise_max(a, b); }
DI float rbf(float f) { return bflo(pk2(f, f) & 0xffffu); }
DI bf16x8 mk8(unsigned a, unsigned b, unsigned c, unsigned d) { v4u v = {a, b, c, d}; return __builtin_bit_cast(bf16x8, v); }
DI void dma16(unsigned ldsaddr, unsigned voff, rsrc_t rs, unsigned soff) {
    asm volatile("s_mov_b32 m0, %0\n\ts_nop 0\n\tbuffer_load_dwordx4 %1, %2, %3 offen lds" ::"s"(ldsaddr), "v"(voff), "s"(rs), "s"(soff) : "memory");
}
DI void dma16x4(unsigned ldsaddr, const unsigned* v, rsrc_t rs, unsigned soff) {
    asm volatile("s_mov_b32 m0, %0\n\ts_nop 0\n\tbuffer_load_dwordx4 %1, %5, %6 offen lds\n\tbuffer_load_dwordx4 %2, %5, %6 offen offset:1024 lds\n\t"
                 "buffer_load_dwordx4 %3, %5, %6 offen offset:2048 lds\n\tbuffer_load_dwordx4 %4, %5, %6 offen offset:3072 lds"
                 ::"s"(ldsaddr), "v"(v[0]), "v"(v[1]), "v"(v[2]), "v"(v[3]), "s"(rs), "s"(soff) : "memory");
}
DI void touch(unsigned voff, rsrc_t rs, unsigned soff) { asm volatile("buffer_load_dword v255, %0, %1, %2 offen" ::"v"(voff), "s"(rs), "s"(soff) : "memory", "v255"); }
DI v4u ld16_asm(unsigned voff, rsrc_t rs, unsigned soff) {
    v4u r; asm volatile("buffer_load_dwordx4 %0, %1, %2, %3 offen" : "=v"(r) : "v"(voff), "s"(rs), "s"(soff) : "memory"); return r;
}
#define PIN(x) asm volatile("" : "+v"(x))
DI s16x4 tr_read(unsigned a) { return __builtin_amdgcn_ds_read_tr16_b64_v4i16((LAS s16x4*)(a)); }
DI bf16x8 cat8(s16x4 a, s16x4 b) { return __builtin_shufflevector(a, b, 0, 1, 2, 3, 4, 5, 6, 7); }
DI bf16x8 lds128(unsigned a) { return *(const LAS bf16x8*)(a); }
DI f32x4 ldsf4(unsigned a) { return *(const LAS f32x4*)(a); }
DI float ldsf(unsigned a) { return *(const LAS float*)(a); }
DI unsigned ldsu(unsigned a) { return *(const LAS unsigned*)(a); }
DI void stsf4(unsigned a, f32x4 v) { *(LAS f32x4*)(a) = v; }
DI void stsf(unsigned a, float v) { *(LAS float*)(a) = v; }
DI void stsu(unsigned a, unsigned v) { *(LAS unsigned*)(a) = v; }
template <int C> DI float dppf(float x) { return __uint_as_float(__builtin_amdgcn_update_dpp(0, __float_as_uint(x), C, 0xf, 0xf, true)); }
template <int C> DI unsigned dppu(unsigned x) { return __builtin_amdgcn_update_dpp(0, x, C, 0xf, 0xf, true); }
DI float max2f(float a, float b) { float r; asm("v_max_f32_e32 %0, %1, %2" : "=v"(r) : "v"(a), "v"(b)); return r; }
DI float max3f(float a, float b, float c) { float r; asm("v_max3_f32 %0, %1, %2, %3" : "=v"(r) : "v"(a), "v"(b), "v"(c)); return r; }
DI float row_sum(float x) { x += dppf<0x128>(x); x += dppf<0x124>(x); x += dppf<0x122>(x); x += dppf<0x121>(x); return x; }
DI float xrow_sum(float x) {
    auto a = __builtin_amdgcn_permlane16_swap(__float_as_uint(x), __float_as_uint(x), false, false);
    x = __uint_as_float(a[0]) + __uint_as_float(a[1]);
    auto b = __builtin_amdgcn_permlane32_swap(__float_as_uint(x), __float_as_uint(x), false, false);
    return __uint_as_float(b[0]) + __uint_as_float(b[1]);
}
DI unsigned wave_maxu(unsigned x) {
    x = umx(x, dppu<0x128>(x)); x = umx(x, dppu<0x124>(x)); x = umx(x, dppu<0x122>(x)); x = umx(x, dppu<0x121>(x));
    auto a = __builtin_amdgcn_permlane16_swap(x, x, false, false);
    x = umx(a[0], a[1]);
    auto b = __builtin_amdgcn_permlane32_swap(x, x, false, false);
    return umx(b[0], b[1]);
}
DI float nmax3f(float a, float b, float c) { float r; asm("v_max3_f32 %0, -%1, -%2, -%3" : "=v"(r) : "v"(a), "v"(b), "v"(c)); return r; }
DI float nmax2f(float a, float b) { float r; asm("v_max_f32_e64 %0, -%1, -%2" : "=v"(r) : "v"(a), "v"(b)); return r; }
template <bool MAX> DI float xrow_scatter(float a, float b, float c, float d) {
    const auto s = __builtin_amdgcn_permlane16_swap(__float_as_uint(a), __float_as_uint(b), false, false);
    const auto t = __builtin_amdgcn_permlane16_swap(__float_as_uint(c), __float_as_uint(d), false, false);
    float u = MAX ? max2f(__uint_as_float(s[0]), __uint_as_float(s[1])) : __uint_as_float(s[0]) + __uint_as_float(s[1]);
    float v = MAX ? max2f(__uint_as_float(t[0]), __uint_as_float(t[1])) : __uint_as_float(t[0]) + __uint_as_float(t[1]);
    if (MAX) asm volatile("s_nop 1" : "+v"(u), "+v"(v));
    const auto r = __builtin_amdgcn_permlane32_swap(__float_as_uint(u), __float_as_uint(v), false, false);
    return MAX ? max2f(__uint_as_float(r[0]), __uint_as_float(r[1])) : __uint_as_float(r[0]) + __uint_as_float(r[1]);
}
DI void xrow_gather(float w, float (&o)[4]) {
    const auto r = __builtin_amdgcn_permlane32_swap(__float_as_uint(w), __float_as_uint(w), false, false);
    const auto s = __builtin_amdgcn_permlane16_swap(r[0], r[0], false, false);
    const auto t = __builtin_amdgcn_permlane16_swap(r[1], r[1], false, false);
    o[0] = __uint_as_float(s[0]); o[1] = __uint_as_float(s[1]); o[2] = __uint_as_float(t[0]); o[3] = __uint_as_float(t[1]);
}
struct NoHook { DI void operator()(int, float&) const {} DI void operator()(int, float&, float&) const {} };
struct NoPut { DI void operator()(int, const bf16x8&) const {} };
template <bool MUL, class H = NoHook, class P = NoPut> DI void softmax2(const f32x4 (&p)[2][2], float rs0, float rs1, bool hi, bf16x8 (&out)[4], H hook = H(), P put = P()) {
    float m[4];
#pragma unroll
    for (int i = 0; i < 2; i++) {
        const f32x4 &p0 = p[i][0], &p1 = p[i][1];
        m[2 * i] = max3f(max3f(p0[0], p0[1], p0[2]), max3f(p0[3], p1[0], p1[1]), max2f(p1[2], p1[3]));
        m[2 * i + 1] = max3f(nmax3f(p0[0], p0[1], p0[2]), nmax3f(p0[3], p1[0], p1[1]), nmax2f(p1[2], p1[3]));
    }
    asm volatile("s_nop 1" : "+v"(m[0]), "+v"(m[1]), "+v"(m[2]), "+v"(m[3]));
    hook(1, m[3]);
    const float rsr = hi ? rs1 : rs0;
    float a[4];
    xrow_gather(xrow_scatter<true>(m[0], m[1], m[2], m[3]) * (rsr * -LOG2E), a);
    hook(10, a[0]);
    f32x2 e[4][4];
    float s[4];
#pragma unroll
    for (int k = 0; k < 4; k++) {
        if (k == 2) hook(2, a[2], s[1]);
        const float sc = ((k >> 1) ? rs1 : rs0) * ((k & 1) ? -LOG2E : LOG2E);
#pragma unroll
        for (int j = 0; j < 4; j++) {
            const f32x4& q = p[k >> 1][j >> 1];
            const f32x2 t = __builtin_elementwise_fma((f32x2){q[2 * (j & 1)], q[2 * (j & 1) + 1]}, (f32x2){sc, sc}, (f32x2){a[k], a[k]});
            e[k][j] = (f32x2){__builtin_amdgcn_exp2f(t[0]), __builtin_amdgcn_exp2f(t[1])};
        }
        const f32x2 h = (e[k][0] + e[k][1]) + (e[k][2] + e[k][3]);
        s[k] = h[0] + h[1];
        if (k == 3) hook(3, s[k]);
    }
    float iv = __builtin_amdgcn_rcpf(xrow_scatter<false>(s[0], s[1], s[2], s[3]));
    if (MUL) iv *= rsr;
    xrow_gather(iv, a);
#pragma unroll
    for (int k = 0; k < 4; k++) {
        if (k == 2) hook(4, a[2]);
        const f32x2 c = {a[k], a[k]};
        const f32x2 x0 = e[k][0] * c, x1 = e[k][1] * c, x2 = e[k][2] * c, x3 = e[k][3] * c;
        out[k] = mk8(pk2(x0[0], x0[1]), pk2(x1[0], x1[1]), pk2(x2[0], x2[1]), pk2(x3[0], x3[1]));
        put(k, out[k]);
    }
}
DI float dot2(unsigned a, unsigned b, float c) {
    return __builtin_amdgcn_fdot2_f32_bf16(__builtin_bit_cast(bf16x2, a), __builtin_bit_cast(bf16x2, b), c, false);
}
DI void st_gran(char* ws, unsigned off, unsigned tag, unsigned val) {
    __hip_atomic_store((gu64*)(ws + off), ((u64)tag << 32) | val, RLX_AGENT);
}
DI v2u ldg(rsrc_t rs, unsigned voff, unsigned soff) { return __builtin_amdgcn_raw_buffer_load_b64(rs, voff, soff, AUX_SC1); }
#define RARE(c) __builtin_expect(!!(c), 0)
#define FRESH() asm volatile("" ::: "memory")
template <int N> DI void sweep(rsrc_t rs, unsigned voff, unsigned soff, unsigned S, unsigned tag, unsigned (&v)[N]) {
#pragma nounroll
    for (unsigned spins = 0;;) {
        unsigned bad = 0;
        FRESH();
#pragma unroll
        for (int k = 0; k < N; ++k) { const v2u x = ldg(rs, voff, soff + k * S); v[k] = x[0]; bad |= x[1] ^ tag; }
        if (__all(bad == 0)) break;
        if (++spins > SPIN_CAP) break;
        __builtin_amdgcn_s_sleep(1);
    }
}
template <int A, int B> DI unsigned budget(unsigned& spins) {
    unsigned r;
    asm volatile("s_add_i32 %0, %0, 1\n\ts_cmpk_lt_u32 %0, %2\n\ts_cselect_b32 %1, %3, %4" : "+s"(spins), "=s"(r) : "i"(SPIN_CAP), "i"(A), "i"(B) : "scc");
    SCHED();
    return r;
}
DI unsigned wmax(unsigned x) { return x; }
DI unsigned wmax(v2u x) { return umx(x[0], x[1]); }
DI unsigned wmax(v4u x) { return umx(umx(x[0], x[1]), umx(x[2], x[3])); }
template <int N, class T, class F> DI unsigned sweepz(F ld, T (&v)[N]) {
    unsigned spins = 0, lim, m;
#pragma nounroll
    do {
        FRESH();
#pragma unroll
        for (int k = 0; k < N; ++k) v[k] = ld(k);
        lim = budget<-2, -1>(spins);
        m = 0;
#pragma unroll
        for (int k = 0; k < N; ++k) m = umx(m, wmax(v[k]));
    } while (__any(m > lim));
    return m;
}
template <int N, class T, class F, class G> DI void sweepn(F ld, T (&v)[N], G use) {
    unsigned spins = 0;
    bool again;
#pragma nounroll
    do {
        FRESH();
#pragma unroll
        for (int k = 0; k < N; ++k) v[k] = ld(k);
        const int m = budget<3, 0>(spins);
        again = __any(__builtin_amdgcn_classf(use(), m));
    } while (again);
}
#define OOB 0x4000000u
#define LD16(v, s) __builtin_amdgcn_raw_buffer_load_b128(rsWS, (v), (s), AUX_SC1)
#define LD4(v, s) __builtin_amdgcn_raw_buffer_load_b32(rsWS, (v), (s), AUX_SC1)
#define ST16(x, v, s) __builtin_amdgcn_raw_buffer_store_b128((x), rsWS, (v), (s), AUX_SC1)
#define ST4(x, v, s) __builtin_amdgcn_raw_buffer_store_b32((x), rsWS, (v), (s), AUX_SC1)
#define SLEEP(n) __builtin_amdgcn_s_sleep(n)
DI void poll16(rsrc_t rs, unsigned off, unsigned tag, int l) {
    unsigned spins = 0, lim, bad;
#pragma nounroll
    do {
        FRESH();
        const v2u x = ldg(rs, (l & 15) * 8, off);
        lim = budget<0, -1>(spins);
        bad = (x[1] ^ tag) | (x[0] ^ tag ^ 0x5bd1e995u);
    } while (__any(bad > lim));
    ORDER_BEHIND_POLL();
}
#define STAMPR(i, role)
#define STAMP(i)
#define STAMPW(i, ww)

#define ARR_DECL
#define KATTR
#define ARR_MARK(i)
#define ARR_END()
template <class F, int... Is> DI void static_for_impl(F&& f, std::integer_sequence<int, Is...>) { (f(std::integral_constant<int, Is>{}), ...); }
template <int N, class F> DI void static_for(F&& f) { static_for_impl(f, std::make_integer_sequence<int, N>{}); }

DI s16x4 bq(unsigned b0, unsigned b1, int j, int i) {
    const int ni = (j + 2) % 6;
    return tr_read(((ni & 1) ? b1 : b0) + (ni >> 1) * 8192 + (i >> 1) * 4096 + (i & 1) * 512);
}
template <class F> DI void group(f32x4 (&acc)[4][6], const bf16x8 (&A)[2][4], const bf16x8 (&B)[2], int j, F gap) {
    const int ni = (j + 2) % 6;
#pragma unroll
    for (int i = 0; i < 8; i++) {
        const int mi = i >> 1, ks = i & 1;
        if (ni < 4) asm volatile("v_mfma_f32_16x16x32_bf16 %0, %1, %2, %0" : "+v"(acc[mi][ni]) : "v"(B[ks]), "v"(A[ks][mi]));
        else asm volatile("v_mfma_f32_16x16x32_bf16 %0, %1, %2, %0" : "+v"(acc[mi][ni]) : "v"(A[ks][mi]), "v"(B[ks]));
        gap(i);
        SCHED();
    }
}
struct ImgPut {
    const unsigned (&ia)[4];
    DI void operator()(int k, const bf16x8& f) const {
        const v4u u = __builtin_bit_cast(v4u, f);
        *(LAS v2u*)(ia[2 * (k & 1)] + (k >> 1) * 2048) = (v2u){u[0], u[1]};
        *(LAS v2u*)(ia[2 * (k & 1) + 1] + (k >> 1) * 2048) = (v2u){u[2], u[3]};
        SCHED();
    }
};
DI float lds_bf(unsigned a) { return bf2f(*(const LAS u16*)(a)); }
#define MFMA_SETTLE2(p) asm volatile("s_nop 7\n\ts_nop 1" : "+v"(p[0][0]), "+v"(p[0][1]), "+v"(p[1][0]), "+v"(p[1][1]))

__global__ KATTR __launch_bounds__(512, 2) void fused_kernel(const u16* __restrict__ X, const u16* __restrict__ WQKV, const u16* __restrict__ WPHI,
                                                       const u16* __restrict__ WO, const u16* __restrict__ WR, const u16* __restrict__ WG,
                                                       const u16* __restrict__ WU, const u16* __restrict__ WD, u16* __restrict__ OUT,
                                                       char* __restrict__ ws) {
    extern __shared__ __attribute__((aligned(16))) char lds[];
    u64 pc0; unsigned clen;
    const int tid = threadIdx.x, l = tid & 63;
    asm volatile("s_getpc_b64 %0\n.Lfk_beg:\n\ts_lshr_b32 %1, %3, 3\n\tv_and_b32 v255, 0x1e0, %2\n\tv_add_lshl_u32 v255, v255, %1, 7\n\ts_mov_b32 %1, .Lfk_end-.Lfk_beg\n\t"
                 "v_min_u32 v255, %1, v255\n\tglobal_load_dword v255, v255, %0" : "=&s"(pc0), "=&s"(clen) : "v"(tid), "s"(blockIdx.x) : "v255", "scc");
    SCHED();
    const unsigned w = rfl(tid >> 6);
    const unsigned bid = blockIdx.x, xcd = bid & 7, idx = bid >> 3;
    const unsigned rt = (xcd & 1) * 8 + (idx & 7), hp = (xcd >> 1) * 4 + (idx >> 3);
    const unsigned i16 = idx & 15, rt2 = xcd * 2 + (idx >> 4), ct = i16 == 4 ? 11 : i16 == 11 ? 4 : i16;
    const unsigned hq = (hp + 4) & 15;
    const unsigned rot = (rt >> 2) * 4;
    const unsigned rot2 = (2 * (rt2 + ct)) & 31;
    const unsigned ldsb = (unsigned)(size_t)lds;
    const int g = l >> 4, q4 = (l & 15) >> 2, p4 = l & 3, t16 = l & 15;
    const unsigned kr = l >> 3;
    asm volatile("" ::"s"(X), "s"(WQKV), "s"(WPHI), "s"(WO), "s"(WR), "s"(WG), "s"(WU), "s"(WD), "s"(OUT), "s"(ws));
    unsigned tag;
#define READ_TAG() do { SCHED(); tag = *(const volatile CAS unsigned*)(ws + WS_EPOCH) + 1u; asm volatile("" : "+s"(tag)); } while (0)
    const rsrc_t rsWS = __builtin_amdgcn_make_buffer_rsrc((void*)ws, 0, WS_USED, 0x00020000);
    const rsrc_t rsX = __builtin_amdgcn_make_buffer_rsrc((void*)X, 0, 2048 * 1024 * 2, 0x00020000);
    const rsrc_t rsW = __builtin_amdgcn_make_buffer_rsrc((void*)WQKV, 0, 1024 * 3072 * 2, 0x00020000);
    const unsigned vA = (kr * 1024 + (((l & 7) ^ kr) * 8)) * 2;
    const unsigned vB0 = (kr * 3072 + (((l & 7) ^ (((kr >> 1) & 1) << 1)) * 8)) * 2;
    const unsigned vB1 = (kr * 3072 + (((l & 7) ^ ((((kr >> 1) & 1) | 2) << 1)) * 8)) * 2;
    const unsigned srow1 = rt * 128 + hp * 8 + (w & 3) * 2;
    v4u xr[4];
    if (w < 4) {
#pragma unroll
        for (int i = 0; i < 4; i++) xr[i] = ld16_asm(l * 16, rsX, srow1 * 2048 + i * 1024);
    }
    {
        const unsigned vBw = (w & 1) ? vB1 : vB0;
#pragma unroll
        for (int i = 0; i < 2; i++) dma16(ldsb + (w * 2 + i) * 1024, vA, rsX, (rt * 128 + w * 16 + i * 8) * 2048 + rot * 128);
#pragma unroll
        for (int i = 0; i < 3; i++) dma16(ldsb + 16384 + i * 8192 + w * 1024, vBw, rsW, (rot * 64 + w * 8) * 6144 + (i ? hp : hq) * 128 + i * 2048);
    }
    const f32x4 zero4 = {0.f, 0.f, 0.f, 0.f};
    ARR_DECL;
    const bf16x8 ones = mk8(0x3F803F80u, 0x3F803F80u, 0x3F803F80u, 0x3F803F80u);
    const unsigned lw = w & 3;
    const bool ffn = (ct & 7) != 3;
    const unsigned fb = !ffn ? 255u : rt2 * 14 + (i16 < 3 ? i16 : i16 - 2);
    const unsigned cg = (fb + 5) & 15, kc = fb >> 4;
    const unsigned kq = fb / 56, mat = (fb % 56) / 28, cg1 = (fb % 28) * 2;
    v4u wreg[24];
    STAMPW(0, 0); STAMPW(0, 4);

    const rsrc_t rsWO = __builtin_amdgcn_make_buffer_rsrc((void*)WO, 0, 1024 * 1024 * 2, 0x00020000);
    const unsigned vWO = kr * 2048 + (((l & 7) ^ (((kr >> 1) & 3) << 1)) * 16);
    unsigned vwo[4];
    auto wo_so = [&](unsigned b) { return (((2 * b & 30) + (b >> 4) + rot2) & 31) * 65536 + ct * 128; };
    const unsigned fw = (((t16 >> 1) & 3) << 2) | (((t16 >> 3) & 1) << 1) | (t16 & 1);
    unsigned ad;
    unsigned ia[4];
    bf16x8 sdv[4];
    auto pre_addr = [&]() {
#pragma unroll
        for (int k = 0; k < 4; k++) { ia[k] = ldsb + L_XB + lw * 8192 + (w >> 2) * 4096 + t16 * 128 + (((4 * k + g) ^ fw) * 8); PIN(ia[k]); }
        const unsigned rr = 4 * g + q4, fr = (((rr >> 1) & 3) << 2) | (((rr >> 3) & 1) << 1) | (rr & 1);
        ad = ldsb + L_XB + (w & 1) * 16384 + rr * 128 + ((((w >> 1) * 4 + p4) ^ fr) * 8);
        PIN(ad);
    };
    auto sd_reads = [&]() {
        unsigned sa = ldsb + L_SD + g * 8;
        PIN(sa);
#pragma unroll
        for (int s = 0; s < 4; s++) {
            const v2u a = *(const LAS v2u*)(sa + s * 64), b = *(const LAS v2u*)(sa + s * 64 + 32);
            sdv[s] = mk8(a[0], a[1], b[0], b[1]);
        }
    };
    bf16x8 v0[4], v1[4];
    auto v_reads = [&]() {
        unsigned vd = ldsb + L_V + (w & 1) * 8192 + l * 16;
        PIN(vd);
#pragma unroll
        for (int s = 0; s < 4; s++) { v0[s] = lds128(vd + s * 2048); v1[s] = lds128(vd + s * 2048 + 1024); }
    };
    auto kv_tile = [&](auto hook6) {
        const unsigned h2 = w & 1, ft = w >> 1;
        WAITL0();
        ARR_MARK(18);
        BAR();
        SCHED();
        bf16x8 pf[4];
#pragma unroll
        for (int s = 0; s < 4; s++) pf[s] = cat8(tr_read(ad + s * 4096), tr_read(ad + s * 4096 + 2048));
        SCHED();
        hook6();
        SCHED();
        f32x4 k0 = zero4, k1 = zero4, kz = zero4;
#pragma unroll
        for (int s = 0; s < 4; s++) { k0 = MFMA(pf[s], v0[s], k0); k1 = MFMA(pf[s], v1[s], k1); SCHED(); }
#pragma unroll
        for (int s = 0; s < 4; s++) kz = MFMA(pf[s], sdv[s], kz);
        const unsigned pbase = WS_P2 + (hp * 16 + rt) * P2_STRIDE;
        const v4u o0 = {pk2(k0[0], k0[1]), pk2(k0[2], k0[3]), pk2(k1[0], k1[1]), pk2(k1[2], k1[3])};
        __builtin_amdgcn_raw_buffer_store_b128(o0, rsWS, l * 16, pbase + h2 * 4096 + ft * 1024, AUX_SC1);
        asm volatile("s_nop 1" :: "v"(o0));
        __builtin_amdgcn_raw_buffer_store_b64((v2u){pk2(kz[0], kz[1]), pk2(kz[2], kz[3])}, rsWS, t16 == 0 ? g * 8 : OOB,
                                              pbase + 8192 + h2 * 128 + ft * 32, AUX_SC1);
        SCHED();
        ARR_MARK(19);
        BAR();
    };

    const bool zjob = lw == 0 && (rt & 7) == 0;
    v4u pp[4];
    auto pp_ld = [&](int k) {
        return LD16(kr * P2_STRIDE + (l & 7) * 16, WS_P2 + (hp * 16 + (k & 1) * 8) * P2_STRIDE +
                    (k < 2 ? (rt >> 2) * 2048 + (lw & 1) * 1024 + (rt & 3) * 256 + (lw >> 1) * 128 : 8192 + (rt >> 3) * 128 + (zjob ? 0u : OOB)));
    };
    auto kv_reduce = [&]() {
        unsigned v3 = (l & 8) ? OOB : (l & 7) * 16 + g * 4, res;
        unsigned s3 = WS_G3 + (hp * 4 + (rt >> 2)) * 2048 + (lw & 1) * 1024 + (rt & 3) * 256 + (lw >> 1) * 128;
        PIN(v3);
        asm volatile("" : "+s"(s3));
        auto red = [&](const v4u* p) {
            float lo = xrow_scatter<false>(bflo(p[0][0]) + bflo(p[1][0]), bflo(p[0][1]) + bflo(p[1][1]), bflo(p[0][2]) + bflo(p[1][2]), bflo(p[0][3]) + bflo(p[1][3]));
            float hi = xrow_scatter<false>(bfhi(p[0][0]) + bfhi(p[1][0]), bfhi(p[0][1]) + bfhi(p[1][1]), bfhi(p[0][2]) + bfhi(p[1][2]), bfhi(p[0][3]) + bfhi(p[1][3]));
            const float chk = lo;
            lo += dppf<0x128>(lo); hi += dppf<0x128>(hi);
            res = nsent(pk2(lo, hi));
            return chk;
        };
        SLEEP(2);
        sweepn<4>(pp_ld, pp, [&]() {
            const float a = red(pp);
            return a + __uint_as_float(umx(wmax(pp[2]), wmax(pp[3])) == SENT ? SENT : 0u);
        });
        ST4(res, v3, s3);
        STAMPW(25, 0);
        if (RARE(zjob)) {
            red(pp + 2);
            ST4(res, (l & 8) ? OOB : ((l >> 2) & 1) * 64 + ((l & 1) * 2 + (g >> 1)) * 16 + ((l >> 1) & 1) * 8 + (g & 1) * 4, WS_GZ + (hp * 2 + (rt >> 3)) * 128);
        }
    };

    const unsigned half = w >> 2;
    const unsigned R2 = rt2 * 128 + lw * 32 + half * 16 + t16;

    rsrc_t rsD = __builtin_amdgcn_make_buffer_rsrc((void*)WD, 0, 1024 * 3584 * 2, 0x00020000);
    unsigned vD, sD;
    auto dloads = [&](int i0, int i1) {
#pragma unroll
        for (int i = i0; i < i1; i++)
            wreg[16 + i] = __builtin_amdgcn_raw_buffer_load_b128(rsD, vD, sD + i * 2048, W_AUX);
        FRESH();
    };
    v4u wp1[2][8];
    auto perm1 = [&]() {
#pragma unroll
        for (int n = 0; n < 16; n++) PIN(wreg[n]);
#pragma unroll
        for (int ip = 0; ip < 4; ip++)
#pragma unroll
            for (int c = 0; c < 4; c++) {
#pragma unroll
                for (int m = 0; m < 2; m++) {
                    const unsigned a = wreg[m * 8 + 2 * ip][c], b = wreg[m * 8 + 2 * ip + 1][c];
                    wp1[m][2 * c][ip] = __builtin_amdgcn_perm(b, a, 0x05040100);
                    wp1[m][2 * c + 1][ip] = __builtin_amdgcn_perm(b, a, 0x07060302);
                }
            }
#pragma unroll
        for (int c = 0; c < 8; c++) { PIN(wp1[0][c]); PIN(wp1[1][c]); }
    };
    v4u wrv[2];
    auto res_loads = [&]() {
#pragma unroll
        for (int i = 0; i < 2; i++) {
            dma16(ldsb + L_XB + w * 4096 + i * 1024, kr * 2048 + (((l & 7) ^ kr) * 16), rsX, (rt2 * 128 + lw * 32 + half * 16 + i * 8) * 2048 + ct * 128);
            wrv[i] = *(const v4u*)(WR + ct * 64 + i * 32 + 8 * g);
        }
        FRESH();
    };

    if (w < 4) {
        const unsigned wm = w & 1, hh = w >> 1;
        f32x4 acc[4][6];
        bf16x8 wf[2], wfq[2];
        float mbt[4], rst[4], csv[2];
        f32x4 mbv[4], csq[2], csk[2];
#pragma unroll
        for (int i = 0; i < 4; i++)
#pragma unroll
            for (int j = 0; j < 6; j++) { acc[i][j] = zero4; PIN(acc[i][j]); }
        {
            const unsigned aoff0 = ldsb + (wm * 64 + t16) * 128 + (((0 + g) ^ (l & 7)) * 16);
            const unsigned aoff1 = ldsb + (wm * 64 + t16) * 128 + (((4 + g) ^ (l & 7)) * 16);
            const unsigned sk = ((q4 >> 1) & 1) | ((g & 1) << 1);
            const unsigned boffA = ldsb + 16384 + (8 * g + q4) * 128 + (((2 * hh) ^ sk) * 32) + p4 * 8;
            const unsigned boffB = ldsb + 16384 + (8 * g + q4) * 128 + (((2 * hh + 1) ^ sk) * 32) + p4 * 8;
            bf16x8 A[2][4], B[3][2];
            s16x4 lo;
            unsigned a0[3], a1[3], bA[3], bB[3];
#pragma unroll
            for (int i = 0; i < 3; i++) {
                a0[i] = aoff0 + i * STAGE_B; a1[i] = aoff1 + i * STAGE_B; bA[i] = boffA + i * STAGE_B; bB[i] = boffB + i * STAGE_B;
                PIN(a0[i]); PIN(a1[i]); PIN(bA[i]); PIN(bB[i]);
            }
            unsigned aT = ldsb + L_MB + (wm * 64 + t16) * 4, aS = ldsb + L_SD + (wm * 64 + t16) * 2, aM = ldsb + L_MB + wm * 256 + g * 16;
            unsigned aC = ldsb + L_CS + hh * 128 + g * 16, aV = ldsb + L_CS + (128 + hh * 32 + t16) * 4;
            unsigned aW = ldsb + L_WPHI + hh * 2048 + (4 * g + q4) * 64 + p4 * 8, aK = ldsb + L_KH + w * 2048 + l * 16;
            PIN(aT); PIN(aS); PIN(aM); PIN(aC); PIN(aV); PIN(aW); PIN(aK);
            pre_addr();
            READ_TAG();
            waitv<5>();
            PIN(xr[0]); PIN(xr[1]); PIN(xr[2]); PIN(xr[3]);
            {
                float s[2], q[2], x0[2];
#pragma unroll
                for (int rr = 0; rr < 2; rr++) {
                    x0[rr] = bflo(rfl(xr[2 * rr][0]));
                    s[rr] = 0.f; q[rr] = 0.f;
#pragma unroll
                    for (int i = 0; i < 4; i++) {
                        const float a = bflo(xr[2 * rr][i]) - x0[rr], b = bfhi(xr[2 * rr][i]) - x0[rr];
                        const float c = bflo(xr[2 * rr + 1][i]) - x0[rr], d = bfhi(xr[2 * rr + 1][i]) - x0[rr];
                        s[rr] += (a + b) + (c + d);
                        q[rr] += (a * a + b * b) + (c * c + d * d);
                    }
                }
                const float sq = row_sum(xrow_scatter<false>(s[0], s[1], q[0], q[1]));
#define RLF(x, n) __uint_as_float(__builtin_amdgcn_readlane(__float_as_uint(x), (n)))
                const float r0 = RLF(sq, 0), r1 = RLF(sq, 16), r2 = RLF(sq, 32), r3 = RLF(sq, 48);
                const float sm = (l ? r1 : r0) * (1.0f / 1024.0f), qm = (l ? r3 : r2) * (1.0f / 1024.0f);
                const float var = fmaxf(qm - sm * sm, 0.f);
                if (l < 2) st_gran(ws, WS_G0 + (srow1 + l) * 8, tag, pk2((l ? x0[1] : x0[0]) + sm, sqrtf(rbf(rbf(var) + 1e-5f))));
            }
            waitv<1>();
            WAITL0();
            ARR_MARK(0);
            BAR();
            SCHED();
            STAMPW(1, 0);
            auto rdB = [&](int u, int j, int i) {
                if (i & 1) B[j % 3][i >> 1] = cat8(lo, bq(bA[u], bB[u], j, i)); else lo = bq(bA[u], bB[u], j, i);
            };
            auto rdA = [&](int u, int i) { A[i & 1][i >> 1] = lds128(((i & 1) ? a1[u] : a0[u]) + (i >> 1) * 2048); };
            auto head = [&](int u) {
                group(acc, A, B[0], 0, [&](int i) { rdB(u, 1 + (i >> 2), i & 3); });
                group(acc, A, B[1], 1, [&](int i) { if (i < 4) rdB(u, 3, i); });
                group(acc, A, B[2], 2, [&](int i) { if (i < 4) rdB(u, 4, i); });
                group(acc, A, B[0], 3, [&](int i) { if (i < 4) rdB(u, 5, i); });
                WL0();
            };
#pragma unroll
            for (int i = 0; i < 4; i++) rdB(0, 0, i);
#pragma unroll
            for (int i = 0; i < 8; i++) rdA(0, i);
            SCHED();
#pragma nounroll
            for (unsigned t = 0; t < 5; t++) {
#pragma unroll
                for (int u = 0; u < 3; u++) {
                    head(u);
                    ARR_MARK(1 + t * 3 + u);
                    BAR();
                    SCHED();
                    group(acc, A, B[1], 4, [&](int i) { if (i < 4) rdB((u + 1) % 3, 0, i); });
                    group(acc, A, B[2], 5, [&](int i) { rdA((u + 1) % 3, i); });
                }
            }
            unsigned kw[4][4], rsu[4];
            f32x2 kx;
            auto kpiece = [&](int n) {
                if (n > 0) { kw[((n - 1 >> 2) + 2) & 3][(n - 1) & 3] = pk2(kx[0], kx[1]); PIN(kw[((n - 1 >> 2) + 2) & 3][(n - 1) & 3]); }
                if (n < 16) {
                    const int mi = ((n >> 2) + 2) & 3, j = n & 3;
                    const f32x4 &c = acc[mi][2 + (j >> 1)], &s_ = csk[j >> 1];
                    float x0 = __builtin_fmaf(mbt[mi], s_[2 * (j & 1)], c[2 * (j & 1)]); PIN(x0);
                    float x1 = __builtin_fmaf(mbt[mi], s_[2 * (j & 1) + 1], c[2 * (j & 1) + 1]); PIN(x1);
                    kx = (f32x2){x0, x1};
                }
                if (n == 5 || n == 9) *(LAS v4u*)(aK + (n == 9) * 1024) = (v4u){kw[2 + (n == 9)][0], kw[2 + (n == 9)][1], kw[2 + (n == 9)][2], kw[2 + (n == 9)][3]};
            };
            group(acc, A, B[0], 0, [&](int i) { rdB(0, 1 + (i >> 2), i & 3); });
            group(acc, A, B[1], 1, [&](int i) {
                if (i < 4) { rdB(0, 3, i); mbt[(i + 2) & 3] = ldsf(aT + ((i + 2) & 3) * 64); }
                else if (i < 6) csk[i - 4] = ldsf4(aC + 256 + (i - 4) * 64);
                else wf[i - 6] = cat8(tr_read(aW + (i - 6) * 32), tr_read(aW + (i - 6) * 32 + 1024));
            });
            group(acc, A, B[2], 2, [&](int i) { if (i < 4) rdB(0, 4, i); else mbv[i - 4] = ldsf4(aM + (i - 4) * 64); kpiece(i); });
            group(acc, A, B[0], 3, [&](int i) {
                if (i < 4) rdB(0, 5, i); else rsu[i - 4] = *(const LAS u16*)(aS + (i - 4) * 32);
                if (i == 2 || i == 3) csv[i - 2] = ldsf(aV + (i - 2) * 64);
                kpiece(8 + i);
            });
            WL0();
            ARR_MARK(16);
            BAR();
            SCHED();
            kpiece(16);
            f32x4 p[2][2];
#pragma unroll
            for (int i = 0; i < 4; i++) p[i >> 1][i & 1] = MFMA(wf[i & 1], mk8(kw[i >> 1][0], kw[i >> 1][1], kw[i >> 1][2], kw[i >> 1][3]), zero4);
            SCHED();
            STAMPW(2, 0);
#pragma unroll
            for (int mi = 0; mi < 4; mi++) rst[mi] = bf2f(rsu[mi]);
#pragma unroll
            for (int a = 0; a < 2; a++)
#pragma unroll
                for (int dt = 0; dt < 2; dt++) {
                    const f32x4 x0 = acc[2 * a][4 + dt] + mbv[2 * a] * csv[dt], x1 = acc[2 * a + 1][4 + dt] + mbv[2 * a + 1] * csv[dt];
                    *(LAS v4u*)(aK + (L_V - L_KH) + w * 2048 + (a * 2 + dt) * 1024) = (v4u){pk2(x0[0], x0[1]), pk2(x0[2], x0[3]), pk2(x1[0], x1[1]), pk2(x1[2], x1[3])};
                }
            SCHED();
            rst[0] = __builtin_amdgcn_rcpf(rst[0]); rst[1] = __builtin_amdgcn_rcpf(rst[1]);
            bf16x8 f[4];
            struct HookC {
                decltype(v_reads)& vr; decltype(sd_reads)& sr;
                DI void operator()(int k, float& x) const { if (k == 10) { PIN(x); WAITL0(); ARR_MARK(17); BAR(); PIN(x); } if (k == 3) { PIN(x); vr(); sr(); PIN(x); } }
                DI void operator()(int, float&, float&) const {}
            };
            softmax2<true>(p, rst[0], rst[1], g >= 2, f, HookC{v_reads, sd_reads}, ImgPut{ia});
#pragma unroll
            for (int ft = 0; ft < 2; ft++) wfq[ft] = cat8(tr_read(aW + ft * 32 + 4096), tr_read(aW + ft * 32 + 5120));
            csq[0] = ldsf4(aC); csq[1] = ldsf4(aC + 64);
            STAMPW(3, 0);
            kv_tile([]() {});
            STAMPW(4, 0);
            SCHED();
            group(acc, A, B[1], 4, [&](int i) { if (i == 2 || i == 3) { rst[i] = __builtin_amdgcn_rcpf(rst[i]); PIN(rst[i]); } });
            group(acc, A, B[2], 5, [&](int) {});
            asm volatile("s_nop 7\n\ts_nop 1");
            SCHED();
        }
        bf16x8 phq[2][4];
        v4u kg[6];
        unsigned sG3 = WS_G3 + (hq * 2 + hh) * 4096, sGZ = WS_GZ + (hq * 2 + hh) * 128;
        {
            f32x4 pq[2][2][2];
#pragma unroll
            for (int mi = 0; mi < 4; mi++) {
                const f32x4 x0 = acc[mi][0] + mbt[mi] * csq[0], x1 = acc[mi][1] + mbt[mi] * csq[1];
                const bf16x8 qf = mk8(pk2(x0[0], x0[1]), pk2(x0[2], x0[3]), pk2(x1[0], x1[1]), pk2(x1[2], x1[3]));
                pq[mi >> 1][mi & 1][0] = MFMA(wfq[0], qf, zero4); pq[mi >> 1][mi & 1][1] = MFMA(wfq[1], qf, zero4);
            }
            MFMA_SETTLE2(pq[0]); MFMA_SETTLE2(pq[1]);
            softmax2<false>(pq[0], rst[0], rst[1], g >= 2, phq[0]);
#pragma unroll
            for (int i = 0; i < 4; i++) PIN(phq[0][i]);
            SCHED();
            kv_reduce();
            SCHED();
            res_loads();
            SCHED();
            BAR();
            SCHED();
            softmax2<false>(pq[1], rst[2], rst[3], g >= 2, phq[1]);
#pragma unroll
            for (int i = 0; i < 4; i++) { PIN(phq[0][i]); PIN(phq[1][i]); }
        }
        STAMPW(5, 0);
        STAMPW(26, 0);
        v2u fv = {tag ^ 0x5bd1e995u, tag};
        unsigned sF = WS_F4 + ((rt * 4 + w) * 16 + hq) * 8, vF = l == 0 ? 0 : OOB;
        {
            unsigned vO = l * 16 + hh * 1024, sO = WS_P4 + ((rt * 8 + wm * 4) * 32 + hq * 2) * 1024;
            asm volatile("" : "+s"(sO), "+s"(sF), "+s"(sG3), "+s"(sGZ));
            PIN(fv); PIN(vF);
            SLEEP(8);
            v4u ov[4];
#define PH(mi, s) phq[(mi) >> 1][2 * ((mi) & 1) + (s)]
#define KVA(s, dt) __builtin_bit_cast(bf16x8, kg[2 + 2 * (s) + (dt)])
            auto pairup = [&](v4u& a, v4u& b) {
                unsigned a2 = a[2], a3 = a[3], b0 = b[0], b1 = b[1];
                asm volatile("v_swap_b32 %0, %2\n\tv_swap_b32 %1, %3" : "+v"(a2), "+v"(a3), "+v"(b0), "+v"(b1));
                a[2] = a2; a[3] = a3; b[0] = b0; b[1] = b1;
            };
            f32x4 dn[4], n0[4], n1[4];
            float id[4];
            unsigned spins = 0, lim, m;
#pragma nounroll
            do {
                FRESH();
#pragma unroll
                for (int k = 0; k < 6; k++) { kg[k] = k >= 2 ? LD16(l * 16 + (k - 2) * 1024, sG3) : LD16(g * 16 + k * 64, sGZ); SCHED(); }
                lim = budget<-2, -1>(spins);
                m = 0;
#pragma unroll
                for (int i = 0; i < 8; i++) {
                    dn[i & 3] = MFMA(__builtin_bit_cast(bf16x8, kg[i >> 2]), PH(i & 3, i >> 2), i < 4 ? zero4 : dn[i & 3]);
                    if (i >= 1 && i < 7) { m = umx(umx(m, kg[i - 1][0]), kg[i - 1][1]); PIN(m); m = umx(umx(m, kg[i - 1][2]), kg[i - 1][3]); }
                    if (i == 5) pairup(kg[2], kg[3]);
                    if (i == 7) pairup(kg[4], kg[5]);
                    SCHED();
                }
            } while (__any(m > lim));
            STAMPW(6, 0);
#pragma unroll
            for (int mi = 0; mi <= 4; mi++) {
                if (mi < 4) {
                    n0[mi] = MFMA(KVA(0, 0), PH(mi, 0), zero4); n1[mi] = MFMA(KVA(0, 1), PH(mi, 0), zero4);
                    n0[mi] = MFMA(KVA(1, 0), PH(mi, 1), n0[mi]); n1[mi] = MFMA(KVA(1, 1), PH(mi, 1), n1[mi]);
                    id[mi] = __builtin_amdgcn_rcpf(rbf(rbf(dn[mi][0]) + 1e-6f));
                    asm volatile("" :: "v"(dn[mi]));
                }
                if (mi > 0) {
                    const f32x4 a = n0[mi - 1] * id[mi - 1], b = n1[mi - 1] * id[mi - 1];
                    ov[mi - 1] = (v4u){pk2(a[0], a[1]), pk2(a[2], a[3]), pk2(b[0], b[1]), pk2(b[2], b[3])};
                    ST16(ov[mi - 1], vO ^ ((mi - 1) * 32768 + ((mi - 1) & 1) * 1024), sO);
                }
                if (mi < 4) { SGB(0x8, 1); SGB(0x2, 6); SGB(0x8, 1); SGB(0x2, 3); SGB(0x8, 1); SGB(0x2, 3); SGB(0x8, 1); SGB(0x2, 4); }
                SCHED();
            }
#pragma unroll
            for (int mi = 0; mi < 4; mi++) PIN(ov[mi]);
        }
        BAR();
        waitv<0>();
        __builtin_amdgcn_raw_buffer_store_b64(fv, rsWS, vF, sF, AUX_SC1);
        STAMPW(7, 0);
    } else {
        const unsigned pw = lw & 1;
        const rsrc_t rsG = __builtin_amdgcn_make_buffer_rsrc((void*)(mat ? WU : WG), 0, 1024 * 3584 * 2, 0x00020000);
        unsigned vwl = kr * 57344 + (l & 7) * 16;
        unsigned sg = (kq * 256 + lw * 64) * 7168 + cg1 * 128;
        auto wloads = [&](int n0, int n1) {
#pragma unroll
            for (int j = n0; j < n1; j++) {
                const int n = (j & 1) * 8 + (j >> 1);
                asm volatile("buffer_load_dwordx4 %0, %1, %2, %3 offen nt" : "=v"(wreg[n]) : "v"(vwl), "s"(rsG), "s"(sg + (n >> 3) * 128 + (n & 7) * 7168));
            }
        };
        auto repack = [&](unsigned gI) {
            const unsigned s2 = (2 * g + (q4 >> 1)) & 3;
            bf16x8 f[4];
#pragma unroll
            for (int nt = 0; nt < 4; nt++) {
                const unsigned a = ldsb + L_WO + gI * 4096 + (4 * g + q4) * 128 + ((((nt >> 1) * 2 + (p4 >> 1)) ^ s2) * 32) + (p4 & 1) * 16 + (nt & 1) * 8;
                f[nt] = cat8(tr_read(a), tr_read(a + 2048));
            }
            WAITL0();
#pragma unroll
            for (int nt = 0; nt < 4; nt++) *(LAS bf16x8*)(ldsb + L_WO + gI * 4096 + nt * 1024 + l * 16) = f[nt];
        };
        unsigned aK, aW, aS;
        auto k_pre = [&]() {
            pre_addr();
            aK = ldsb + L_KH + lw * 2048 + l * 16; aW = ldsb + L_WPHI + (lw >> 1) * 2048 + (4 * g + q4) * 64 + p4 * 8; aS = ldsb + L_SD + (pw * 64 + 32 + t16) * 2;
            PIN(aK); PIN(aW); PIN(aS);
        };
        auto kfeat = [&](auto hook) {
            bf16x8 wf[2];
            float rst[2];
#pragma unroll
            for (int ft = 0; ft < 2; ft++) {
                wf[ft] = cat8(tr_read(aW + ft * 32), tr_read(aW + ft * 32 + 1024));
                rst[ft] = __builtin_amdgcn_rcpf(lds_bf(aS + ft * 32));
            }
            sd_reads();
            PIN(wf[0]); PIN(wf[1]); PIN(rst[0]); PIN(rst[1]);
            SCHED();
            ARR_MARK(16);
            BAR();
            SCHED();
            const bf16x8 kf0 = lds128(aK), kf1 = lds128(aK + 1024);
            SCHED();
            hook(-1, rst[0]);
            hook(0, rst[0]);
            SCHED();
            f32x4 p[2][2];
            p[0][0] = MFMA(wf[0], kf0, zero4); p[0][1] = MFMA(wf[1], kf0, zero4);
            p[1][0] = MFMA(wf[0], kf1, zero4); p[1][1] = MFMA(wf[1], kf1, zero4);
            MFMA_SETTLE2(p);
            bf16x8 f[4];
            softmax2<true>(p, rst[0], rst[1], g >= 2, f, hook, ImgPut{ia});
            SCHED();
            hook(5, rst[0]);
            SCHED();
            STAMPW(3, 4); STAMPW(3, 7);
            kv_tile([&]() { hook(6, rst[0]); });
            STAMPW(4, 4); STAMPW(4, 7);
        };
        auto wblk = [&](int k) {
            constexpr unsigned T[8] = {0x19181716, 0x041c1b1a, 0x08070605, 0x0c0b0a09, 0x1413120d, 0x11100015, 0x1d030201, 0x1f1e0f0e};
            return (T[k] >> (8 * lw)) & 31;
        };
        auto wo_init = [&]() {
#pragma unroll
            for (int i = 0; i < 4; i++) { vwo[i] = vWO + i * (16384 - 1024); PIN(vwo[i]); }
        };
        unsigned hs[8];
        auto wl_pre = [&]() { asm volatile("" : "+s"(sg)); PIN(vwl); };
        auto wo_pre = [&]() {
            wo_init();
#pragma unroll
            for (int k = 0; k < 8; k++) { hs[k] = wo_so(wblk(k)); asm volatile("" : "+s"(hs[k])); }
        };
        auto wo_rng = [&](int k0, int k1) {
#pragma unroll
            for (int k = k0; k < k1; k++) dma16x4(ldsb + L_WO + wblk(k) * 4096, vwo, rsWO, hs[k]);
        };
        auto pf = [&](int i) {
            if (i == 0) { wloads(0, 6); SCHED(); wo_pre(); SCHED(); }
            if (i < 3 && !ffn) wo_rng(i, i + 1);
            if (i == 1 || i == 2) wloads(6 * i, i == 1 ? 12 : 16);
        };
        struct Hook {
            decltype(pf)& f; decltype(v_reads)& vr;
            DI void operator()(int k, float& x) const { if (k == 10) return; PIN(x); if (k == 3) { ARR_MARK(17); BAR(); vr(); } f(3 + k); PIN(x); }
            DI void operator()(int k, float& x, float& y) const { PIN(y); f(3 + k); PIN(x); }
        };
        auto tail16 = [&]() {
            kfeat(Hook{pf, v_reads});
            if (ffn) wo_rng(0, 3);
            wo_rng(3, 5);
            static_for<3>([&](auto kc_) {
                constexpr int k = decltype(kc_)::value;
                waitv<4 * (4 - k)>();
                repack(wblk(k));
            });
            BAR();
            res_loads();
            wo_rng(5, 7);
            static_for<4>([&](auto kc_) {
                constexpr int k = decltype(kc_)::value + 3;
                waitv<4 * (6 - k)>();
                repack(wblk(k));
            });
            PIN(wrv[0]); PIN(wrv[1]);
            perm1();
            SCHED();
            BAR();
            wo_rng(7, 8);
        };
        auto vT23 = [&]() {
            const unsigned n = (idx * 2 + pw) * 64 + l, r = n / 12, c = n % 12;
            return idx < 24 ? ((r & 127) + (r >> 7) * 256 + 128) * 6144 + (c >> 2) * 2048 + (((xcd >> 1) * 4 + (c >> 2 ? 0 : 4) + (c & 3)) & 15) * 128 : OOB;
        };
        if (lw < 2) {
            const unsigned sA = (rt * 128 + lw * 64) * 2048, sB = lw * 32 * 6144 + hp * 128, sBq = lw * 32 * 6144 + hq * 128;
            unsigned buf = 1;
            unsigned va[4], vb[4];
#pragma unroll
            for (int i = 0; i < 4; i++) { va[i] = vA + i * (16384 - 1024); vb[i] = ((i & 1) ? vB1 : vB0) + i * (49152 - 1024); PIN(va[i]); PIN(vb[i]); }
            auto stage = [&](unsigned s, unsigned bf, int i0, int i1) {
                const unsigned kt = (s + rot) & 15;
                const unsigned lb = ldsb + bf * STAGE_B;
                const unsigned sa = sA + kt * 128, sb = kt * 393216;
                if (i0 <= 0 && 0 < i1) dma16x4(lb + lw * 8192, va, rsX, sa);
                if (i0 <= 1 && 1 < i1) dma16x4(lb + lw * 8192 + 4096, va, rsX, sa + 65536);
                if (i0 <= 2 && 2 < i1) dma16x4(lb + 16384 + 8192 + lw * 4096, vb, rsW, sb + sB + 2048);
                if (i0 <= 3 && 3 < i1) dma16x4(lb + 16384 + 16384 + lw * 4096, vb, rsW, sb + sB + 4096);
                if (i0 <= 4 && 4 < i1) dma16x4(lb + 16384 + lw * 4096, vb, rsW, sb + sBq);
            };
            stage(1, 1, 0, 4);
            SCHED();
            unsigned vt = vT23();
            PIN(vt);
            wl_pre(); k_pre();
            waitv<16>(); ARR_MARK(0); BAR();
            stage(1, 1, 4, 5);
            touch(vt, rsW, (xcd & 1) * 512 * 6144);
            waitv<1>(); ARR_MARK(1); BAR();
            stage(2, 2, 0, 5); stage(3, 0, 0, 3);
            waitv<12>(); ARR_MARK(2); BAR();
            stage(3, 0, 3, 5); stage(4, 1, 0, 4);
            waitv<16>(); ARR_MARK(3); BAR();
            stage(4, 1, 4, 5);
            buf = 2;
#pragma nounroll
            for (unsigned s = 5; s < 16; s++) {
                if (s > 5) { waitv<20>(); ARR_MARK(s - 2); BAR(); }
                stage(s, buf, 0, 5);
                buf = buf == 2 ? 0 : buf + 1;
            }
            READ_TAG();
            waitv<20>();
        } else {
            SCHED();
            unsigned v3;
            const unsigned h3 = (xcd >> 1) * 4 + 3, kv3 = !(xcd & 2), wv = idx * 2 + pw;
            auto line3 = [&](unsigned per, unsigned rows, auto rowf) {
                const unsigned n = wv * (kv3 ? per : (per + 2) / 3) + l, r3 = kv3 ? n / 3 : n, c3 = kv3 ? n % 3 : 0;
                return (idx < 24 && l < (kv3 ? per : (per + 2) / 3) && r3 < rows) ? rowf(r3) * 6144 + c3 * 2048 + ((c3 ? h3 : h3 + 4) & 15) * 128 : OOB;
            };
            v3 = line3(32, 512, [](unsigned r) { return r; });
            touch(line3(16, 256, [](unsigned r) { return (r & 127) + (r >> 7) * 256; }), rsW, (xcd & 1) * 512 * 6144);
            {
                const rsrc_t rsP = __builtin_amdgcn_make_buffer_rsrc((void*)WPHI, 0, 32 * 1024 * 2, 0x00020000);
#pragma unroll
                for (int i = 0; i < 4; i++) dma16(ldsb + L_WPHI + pw * 4096 + i * 1024, l * 16, rsP, (pw ? hq : hp) * 4096 + i * 1024);
            }
            v4u sv = {SENT, SENT, SENT, SENT};
            unsigned vc4 = l < 16 ? WS_G3 + (hp * 4 + (rt >> 2)) * 2048 + pw * 1024 + (rt & 3) * 256 + l * 16
                         : l < 24 ? WS_P2 + (hp * 16 + rt) * P2_STRIDE + 8192 + pw * 128 + (l - 16) * 16
                         : (l < 32 && pw == 0 && (rt & 7) == 0) ? WS_GZ + (hp * 2 + (rt >> 3)) * 128 + (l - 24) * 16 : OOB;
            auto clears = [&](int i0, int i1) {
#pragma unroll
                for (int i = i0; i < i1; i++) {
                    if (i < 4) ST16(sv, l * 16, WS_P2 + (hp * 16 + rt) * P2_STRIDE + pw * 4096 + i * 1024);
                    if (i == 4) ST16(sv, vc4, 0);
                    if (i > 4) ST16(sv, l * 16, WS_TL + (bid * 4 + pw * 2 + i - 5) * 1024);
                }
                FRESH();
            };
            PIN(vc4); PIN(sv);
            unsigned ca = ldsb + 16384 + pw * 8192 + (8 * g + q4) * 128 + (((rt & 3) ^ (((q4 >> 1) & 1) | ((g & 1) << 1))) * 32) + p4 * 8;
            PIN(ca);
            bf16x8 cf[4];
            f32x4 cd[2] = {zero4, zero4};
            auto cs_read = [&](int st) {
#pragma unroll
                for (int i = 0; i < 4; i++) cf[i] = cat8(tr_read(ca + (st % 3) * STAGE_B + i * 4096), tr_read(ca + (st % 3) * STAGE_B + i * 4096 + 512));
                SCHED();
            };
            READ_TAG();
            waitv<5>();
            ARR_MARK(0);
            BAR();
            SCHED();
            cs_read(0);
            FRESH();
            const rsrc_t rsC = __builtin_amdgcn_make_buffer_rsrc((void*)pc0, 0, clen, 0x00020000);
            auto code_pf = [&]() { touch(l < 4 ? l * 128 : OOB, rsC, (idx * 2 + pw) * 512); };
            v4u gl[4];
            float s0 = 0.f, s1 = 0.f;
            unsigned bad;
            v2u gs, gc;
            unsigned vgl = (l < 48 ? l : 0) * 16;
            unsigned aw0 = ldsb + L_CS + pw * 384 + (vgl >> 1), aw1 = ldsb + L_MB + (pw * 64 + l) * 4, aw2 = ldsb + L_SD + (pw * 64 + l) * 2;
            static_for<13>([&](auto tc) {
                constexpr int t = decltype(tc)::value;
                WAITL0();
                ARR_MARK(t + 1);
                BAR();
                SCHED();
                if constexpr (t < 4) {
                    __builtin_amdgcn_s_setprio(2);
#pragma unroll
                    for (int i = 0; i < 4; i++) cd[i & 1] = MFMA(ones, cf[(i & 1) * 2 + (i >> 1)], cd[i & 1]);
                    SCHED();
                    __builtin_amdgcn_s_setprio(0);
                    if constexpr (t < 3) cs_read(t + 1);
                    else __builtin_amdgcn_raw_buffer_store_b64((v2u){__float_as_uint(g ? cd[1][0] : cd[0][0]), tag}, rsWS, (g == 1 || g + pw == 0) ? g * 512 + t16 * 8 : OOB,
                                                               WS_G1 + ((hp * 4 + (rt >> 2)) * 192 + pw * 64 + (rt & 3) * 16) * 8, AUX_SC1);
                }
                if constexpr (t == 0) touch(v3, rsW, (~xcd & 1) * 512 * 6144);
                if constexpr (t == 12) code_pf();
                if constexpr (t == 8) {
                    waitv<0>();
                    __builtin_amdgcn_raw_buffer_store_b64((v2u){0u, tag}, rsWS, l == 0 ? 0 : OOB, WS_GC + ((hp * 2 + pw) * 16 + rt) * 8, AUX_SC1);
                }
                if constexpr (t == 8 || t == 9) {
#pragma unroll
                    for (int r = (t - 8) * 2; r < (t - 8) * 2 + 2; r++)
                        asm volatile("buffer_load_dwordx4 %0, %1, %2, %3 offen sc1" : "=v"(gl[r]) : "v"(vgl), "s"(rsWS), "s"(WS_G1 + ((hp * 4 + r) * 192 + pw * 96) * 8) : "memory");
                }
                if constexpr (t == 6) {
                    asm volatile("buffer_load_dwordx2 %0, %1, %2, %3 offen sc1" : "=v"(gs) : "v"(l * 8), "s"(rsWS), "s"(WS_G0 + (rt * 128 + pw * 64) * 8) : "memory");
                }
                if constexpr (t == 11) {
                    waitv<0>();
                    PIN(gs);
                    bad = gs[1] ^ tag;
#pragma unroll
                    for (int r = 0; r < 4; r++) {
                        PIN(gl[r]);
                        bad |= (gl[r][1] ^ tag) | (gl[r][3] ^ tag);
                        s0 += __uint_as_float(gl[r][0]); s1 += __uint_as_float(gl[r][2]);
                    }
                    PIN(s0); PIN(s1); PIN(bad);
                }
                if constexpr (t == 12) {
                    if (RARE(!__all(bad == 0))) {
                        s0 = 0.f; s1 = 0.f;
#pragma nounroll
                        for (unsigned r = 0; r < 4; r++) {
                            unsigned a[2];
                            sweep<2>(rsWS, vgl, WS_G1 + ((hp * 4 + r) * 192 + pw * 96) * 8, 8, tag, a);
                            s0 += __uint_as_float(a[0]); s1 += __uint_as_float(a[1]);
                        }
                        unsigned c[1];
                        sweep<1>(rsWS, l * 8, WS_G0 + (rt * 128 + pw * 64) * 8, 0, tag, c);
                        gs[0] = c[0];
                    }
                    *(LAS f32x2*)(aw0) = (f32x2){-s0, -s1};
                    stsf(aw1, bflo(gs[0]));
                    *(LAS u16*)(aw2) = (u16)(gs[0] >> 16);
                    SCHED();
                }
                if constexpr (t == 5) k_pre();
                if constexpr (t == 6) wl_pre();
                if constexpr (t == 7) { PIN(vgl); PIN(aw0); PIN(aw1); PIN(aw2); }
                clears(t < 7 ? t : 7, t < 7 ? t + 1 : 7);
                if constexpr (t == 11) asm volatile("buffer_load_dwordx2 %0, %1, %2, %3 offen sc1" : "=v"(gc) : "v"((l & 31) * 8), "s"(rsWS), "s"(WS_GC + (pw ? hq : hp) * 256) : "memory");
                if constexpr (t == 12) {
                    waitv<1>();
                    PIN(gc);
                    if (RARE(!__all(gc[1] == tag))) { unsigned c[1]; sweep<1>(rsWS, (l & 31) * 8, WS_GC + (pw ? hq : hp) * 256, 0, tag, c); }
                    SCHED();
                }
            });
            WAITL0();
        }
        ARR_MARK(14); BAR();
        SCHED();
        pf(0);
        if (ffn) waitv<6>(); else waitv<10>();
        ARR_MARK(15); BAR();
        pf(1);
        tail16();
        WAITL0();
        STAMPW(7, 4); STAMPW(7, 7);
    }
    const unsigned sFl = WS_F4 + (rt2 * 4 + (lw >> 1) + 2 * half) * 128;
    vD = kr * 16384 + (l & 7) * 16; sD = (kc * 256 + lw * 64) * 2048 + cg * 128;
    PIN(vD);
    asm volatile("" : "+s"(sD), "+s"(rsD));
    if (half) dloads(0, 1);
    SCHED();
    ARR_MARK(20);
    BAR();
    ARR_END();
    const unsigned wb = ldsb + L_WO + half * 65536 + l * 16;
    bf16x8 bb[2][4];
    v4u xres[2];
#pragma unroll
    for (int nt = 0; nt < 4; nt++) {
        if (nt < 2) xres[nt] = *(const LAS v4u*)(ldsb + L_XB + w * 4096 + t16 * 128 + (((nt * 4 + g) ^ (l & 7)) * 16));
        bb[0][nt] = lds128(wb + nt * 1024);
        PIN(bb[0][nt]);
    }
    PIN(xres[0]); PIN(xres[1]);
    unsigned so[16], va0 = l * 16 + half * 32768, va1 = l * 16 + (half ^ 1) * 32768 + 1024;
    unsigned aXw = ldsb + L_XB + w * 4096 + l * 16, aXr = ldsb + L_XB + (w ^ 4) * 4096 + l * 16, sG5 = WS_G5 + ((rt2 * 8 + lw * 2 + half) * 16 + ct) * 256;
    unsigned vG5 = g == q4 ? t16 * 16 : OOB;
#pragma unroll
    for (int st = 0; st < 16; st++) { so[st] = WS_P4 + (rt2 * 8 + lw * 2) * 32768 + ((2 * st + rot2) & 31) * 1024; asm volatile("" : "+s"(so[st])); }
    unsigned wt[2];
#pragma unroll
    for (int i = 0; i < 2; i++) { wt[i] = ldsb + L_WO + half * 65536 + (4 * g + q4) * 128 + (((i * 2 + (p4 >> 1)) ^ ((2 * g + (q4 >> 1)) & 3)) * 32) + (p4 & 1) * 16; PIN(wt[i]); }
    PIN(va0); PIN(va1); PIN(aXw); PIN(aXr); PIN(vG5);
    asm volatile("" : "+s"(sG5));
    SLEEP(13);
    poll16(rsWS, sFl, tag, l);
    FRESH();
    SCHED();
    STAMPW(8, 0); STAMPW(8, 4);
    unsigned swc;
    f32x4 acc2[2][4];
    bf16x8 sop[2];
    {
        auto aload = [&](int st, int m2) { return LD16(m2 ? va1 : va0, so[st]); };
        constexpr int PD = 2;
        v4u af[PD][2];
#pragma unroll
        for (int i = 0; i < PD; i++) {
#pragma unroll
            for (int m2 = 1; m2 >= 0; m2--) af[i][m2] = aload(i, m2);
            SCHED();
        }
#pragma unroll
        for (int nt = 0; nt < 4; nt++) {
            const unsigned x0 = xres[nt >> 1][2 * (nt & 1)], x1 = xres[nt >> 1][2 * (nt & 1) + 1];
            acc2[0][nt] = (f32x4){bflo(x0), bfhi(x0), bflo(x1), bfhi(x1)};
            acc2[1][nt] = zero4;
        }
        unsigned m0 = p4 == 0 ? 0x3F803F80u : 0u, m1 = p4 == 1 ? ~0u : 0u;
        PIN(m0); PIN(m1);
#pragma unroll
        for (int i = 0; i < 2; i++) {
            v4u u;
#pragma unroll
            for (int j = 0; j < 4; j++) u[j] = (wrv[i][j] & m1) | m0;
            sop[i] = __builtin_bit_cast(bf16x8, u);
            PIN(sop[i]);
        }
        {
            float sw = 0.f;
#pragma unroll
            for (int i = 0; i < 8; i++) sw = dot2(wrv[i >> 2][i & 3], 0x3F803F80u, sw);
            swc = __float_as_uint(xrow_sum(sw));
            PIN(swc);
        }
        SCHED();
#pragma unroll
        for (int st = 0; st < 16; st++) {
            if (st == 2) BAR();
            if (st + 1 < 16) {
#pragma unroll
                for (int nt = 0; nt < 4; nt++) {
                    if (st + 1 < 14) bb[(st + 1) % (2)][nt] = lds128(wb + (st + 1) * 4096 + nt * 1024);
                    else bb[(st + 1) % (2)][nt] = cat8(tr_read(wt[nt >> 1] + (st + 1) * 4096 + (nt & 1) * 8), tr_read(wt[nt >> 1] + (st + 1) * 4096 + (nt & 1) * 8 + 2048));
                }
            }
#pragma unroll
            for (int mm = 0; mm < 2; mm++) {
                const int m2 = mm ^ 1;
                const bf16x8 a = __builtin_bit_cast(bf16x8, af[st % PD][m2]);
#pragma unroll
                for (int nt = 0; nt < 4; nt++) acc2[m2][nt] = MFMA(bb[st % (2)][nt], a, acc2[m2][nt]);
            }
            if (st + PD < 16) {
#pragma unroll
                for (int m2 = 1; m2 >= 0; m2--) af[st % PD][m2] = aload(PD + st, m2);
            }
#pragma unroll
            for (int i = 0; i < (st + 1 < 14 ? 4 : 8); i++) { SGB(0x8, 1); SGB(0x100, 1); }
            SCHED();
        }
    }
    STAMPW(9, 0); STAMPW(9, 4);
#pragma unroll
    for (int nt = 0; nt < 4; nt++) stsf4(aXw + nt * 1024, acc2[1][nt]);
    SCHED();
    if (half) dloads(1, 4);
    SCHED();
    WAITL0();
    if (half) __builtin_amdgcn_s_setprio(1);
    BAR();
    SCHED();
    v4u hpk[2];
    {
        f32x4 pa[4];
#pragma unroll
        for (int nt = 0; nt < 4; nt++) pa[nt] = ldsf4(aXr + nt * 1024);
        SCHED();
#pragma unroll
        for (int i = 0; i < 8; i++) {
            const f32x4 a = acc2[0][i >> 1] + pa[i >> 1];
            hpk[i >> 2][i & 3] = pk2(a[2 * (i & 1)], a[2 * (i & 1) + 1]);
        }
        const bf16x8 h0 = __builtin_bit_cast(bf16x8, hpk[0]), h1 = __builtin_bit_cast(bf16x8, hpk[1]);
        f32x4 gq = MFMA(h0, h0, zero4), sd = MFMA(sop[0], h0, zero4);
        gq = MFMA(h1, h1, gq); sd = MFMA(sop[1], h1, sd);
        float q0 = (l & 1) ? gq[1] : gq[0], q1 = (l & 1) ? gq[3] : gq[2];
        PIN(q0); PIN(q1);
        const v4u pv = {__float_as_uint(sd[0]), __float_as_uint((l & 2) ? q1 : q0), __float_as_uint(sd[1]), swc};
        ST16(pv, vG5, sG5);
        asm volatile("s_nop 1" :: "v"(pv));
    }
    __builtin_amdgcn_s_setprio(0);
    STAMPW(10, 0); STAMPW(10, 4);
    v4u wp2[8];
    auto perm2 = [&]() {
#pragma unroll
        for (int i = 16; i < 24; i++) PIN(wreg[i]);
#pragma unroll
        for (int ip = 0; ip < 4; ip++)
#pragma unroll
            for (int c = 0; c < 4; c++) {
                const unsigned a = wreg[16 + 2 * ip][c], b = wreg[16 + 2 * ip + 1][c];
                wp2[2 * c][ip] = __builtin_amdgcn_perm(b, a, 0x05040100);
                wp2[2 * c + 1][ip] = __builtin_amdgcn_perm(b, a, 0x07060302);
            }
#pragma unroll
        for (int c = 0; c < 8; c++) PIN(wp2[c]);
    };
    const unsigned mt = rt2 * 8 + lw * 2 + half;
    {
        v4u pv[4];
        unsigned sK = WS_GK + (ct * 128 + mt) * 4, vK = l == 0 ? 0 : OOB, rk = 2047u - R2;
        asm volatile("" : "+s"(sK));
        PIN(vK); PIN(rk);
        unsigned sS = WS_G5 + mt * 4096;
        asm volatile("" : "+s"(sS));
        SLEEP(18);
        __builtin_amdgcn_s_setprio(1);
        float T[4];
        sweepn<4>([&](int k) { return LD16(l * 16 + k * 1024, sS); }, pv, [&]() {
            const f32x4 ps = (__builtin_bit_cast(f32x4, pv[0]) + __builtin_bit_cast(f32x4, pv[1])) + (__builtin_bit_cast(f32x4, pv[2]) + __builtin_bit_cast(f32x4, pv[3]));
            const float r = xrow_scatter<false>(ps[0], ps[1], ps[2], ps[3]);
            xrow_gather(r, T);
            return r;
        });
        STAMPW(21, 0); STAMPW(21, 4);
        const float mean = T[0] * (1.0f / 1024.0f), var = fmaxf(T[1] * (1.0f / 1024.0f) - mean * mean, 0.f);
        const unsigned sg = pk2(mean, __builtin_amdgcn_sqrtf(rbf(rbf(var) + 1e-5f)));
        const float mb2 = bflo(sg), isd2 = __builtin_amdgcn_rcpf(bfhi(sg));
        const float rsc = (T[2] - mb2 * T[3]) * isd2;
        const unsigned rb = pk2(rsc, rsc);
        const unsigned key = ((rb ^ ((unsigned)((int)rb >> 31) | 0x80000000u)) & 0xffff0000u) | rk;
        unsigned kb = umx(key, dppu<0x128>(key));
        kb = umx(kb, dppu<0x124>(kb)); kb = umx(kb, dppu<0x122>(kb)); kb = umx(kb, dppu<0x121>(kb));
        ST4(kb, vK, sK);
        SCHED();
        __builtin_amdgcn_s_setprio(0);
        if (half) { dloads(4, 8); SCHED(); }
        const bool cand = key == kb;
        v4u e[2], outv[2];
#pragma unroll
        for (int i = 0; i < 8; i++) {
            const float h0 = bflo(hpk[i >> 2][i & 3]), h1 = bfhi(hpk[i >> 2][i & 3]);
            const unsigned d = pk2(h0 - mb2, h1 - mb2);
            e[i >> 2][i & 3] = pk2(bflo(d) * isd2, bfhi(d) * isd2);
        }
        ST16(e[0], cand ? g * 16 : OOB, WS_C + (mt * 16 + ct) * 128);
        ST16(e[1], cand ? g * 16 + 64 : OOB, WS_C + (mt * 16 + ct) * 128);
        SCHED();
        STAMPW(22, 0); STAMPW(22, 4);
#pragma unroll
        for (int i = 0; i < 8; i++)
            outv[i >> 2][i & 3] = pk2(bflo(hpk[i >> 2][i & 3]) + bflo(e[i >> 2][i & 3]), bfhi(hpk[i >> 2][i & 3]) + bfhi(e[i >> 2][i & 3]));
        if (cand) {
#pragma unroll
            for (int i = 0; i < 2; i++) { *(LAS v4u*)(ldsb + L_STASH + w * 256 + i * 64 + g * 16) = hpk[i]; *(LAS v4u*)(ldsb + L_STASH + w * 256 + 128 + i * 64 + g * 16) = e[i]; }
        }
#pragma unroll
        for (int i = 0; i < 2; i++) *(LAS v4u*)(ldsb + L_OUT + (lw * 2 + half) * 2048 + t16 * 128 + (((i * 4 + g) ^ (l & 7)) * 16)) = outv[i];
    }
    const rsrc_t rsO = __builtin_amdgcn_make_buffer_rsrc((void*)OUT, 0, 2048 * 1024 * 2, 0x00020000);
    unsigned aG = ldsb + L_WO + 512;
    PIN(aG);
    unsigned vX = (2 * g + (l & 1)) * 16, aP = (2 * g + (l & 1)) * 32, aR1 = ldsb + L_RED + lw * 4352 + l * 16, vl4 = l * 4;
    unsigned aR2 = ldsb + L_RED + ((lw & 1) * 2 + ((l >> 2) & 1)) * 1088 + kr * 16 + p4 * 4;
    unsigned sX = WS_C + kq * 512 + lw * 128, sG8w = WS_G8 + ((kq * 2 + mat) * 3584 + (cg1 + lw) * 64) * 4, sG8r = WS_G8 + (kc * 256 + lw * 64) * 4;
    unsigned sG9 = WS_G9 + (cg * 14 + kc) * 256;
    unsigned aR3 = aR1 + 17408 - lw * 2176;
    PIN(vX); PIN(aP); PIN(aR1); PIN(aR2); PIN(aR3); PIN(vl4);
    asm volatile("" : "+s"(sX), "+s"(sG8w), "+s"(sG8r), "+s"(sG9));
    SCHED();
    if (w == 1) {
        unsigned sK = WS_GK + (bid & 15) * 512;
        v2u hb[1];
        asm volatile("" : "+s"(sK));
        SLEEP(13);
        stsu(aG, wave_maxu(sweepz<1>([&](int) { return ldg(rsWS, l * 8, sK); }, hb)));
        STAMPR(23, 2);
    }
    if (half) perm2();
    STAMPW(24, 7);
    WAITL0();
    if (half) __builtin_amdgcn_s_setprio(1);
    BAR();
    STAMPW(11, 0); STAMPW(11, 4);
    const unsigned gkv = ldsu(aG);
    if (w < 4) {
        if (bid == 0 && tid == 0) __hip_atomic_store((gu32*)(ws + WS_EPOCH), tag, RLX_AGENT);
        v4u o2[4];
        unsigned vo[4];
#pragma unroll
        for (int i = 0; i < 4; i++) o2[i] = *(const LAS v4u*)(ldsb + L_OUT + w * 4096 + i * 1024 + kr * 128 + (((l & 7) ^ kr) * 16));
#pragma unroll
        for (int i = 0; i < 4; i++) { const unsigned R = mt * 16 + i * 8 + kr; vo[i] = 2047u - R != (gkv & 2047u) ? R * 2048 + (l & 7) * 16 : OOB; PIN(vo[i]); PIN(o2[i]); }
#pragma unroll
        for (int s = 0; s < 3; s++) {
            if (s) { BAR(); SLEEP(3); }
            SCHED();
#pragma unroll
            for (int i = 0; i < 4; i++)
                if ((i + 1) / 2 == s) __builtin_amdgcn_raw_buffer_store_b128(o2[i], rsO, vo[i], ct * 128, AUX_SC1);
            SCHED();
        }
        STAMPW(12, 0);
        return;
    }

    auto gemv = [&](const v4u& x, const v4u* wp, int n, unsigned a) {
        f32x4 d[16], o[4];
#pragma unroll
        for (int c = 0; c < n + 4; c++) {
            if (c < n) d[c] = MFMA(__builtin_bit_cast(bf16x8, x), __builtin_bit_cast(bf16x8, wp[c]), zero4);
            SCHED();
            if (c >= 4) {
                const int j = c - 4;
                float t = d[j][0] + dppf<0x128>(d[j][1]);
                asm volatile("" : "+v"(t) : "v"(d[j]));
                o[j >> 2][j & 3] = t;
                if ((j & 3) == 3) stsf4(a + (j >> 2) * 1088, o[j >> 2]);
            }
            SCHED();
        }
        return o[0][0];
    };
    auto sum4 = [&](unsigned a, unsigned stride) {
        float u = ldsf(a) + ldsf(a + stride), v = ldsf(a + 2 * stride) + ldsf(a + 3 * stride);
        PIN(u); PIN(v);
        return u + v;
    };
    if (ffn) {
        v4u xq[1];
        const unsigned vx = ((~gkv & 0x7f0u) << 7) + vX;
        sweepn<1>([&](int) { return LD16(vx, sX); }, xq, [&]() { return gemv(xq[0], &wp1[0][0], 16, aR1); });
        STAMPW(13, 4);
    }
    WAITL0();
    BAR();
    if (ffn) {
        if (lw < 2) ST4(__float_as_uint(sum4(aR2, 4352)), vl4, sG8w);
        STAMPW(14, 4);
        unsigned pv[8];
        v4u ap;
        SLEEP(12);
        sweepn<8>([&](int k) { return LD4(vl4, sG8r + k * 3584 * 4); }, pv, [&]() {
            const float gg = rbf((__uint_as_float(pv[0]) + __uint_as_float(pv[2])) + (__uint_as_float(pv[4]) + __uint_as_float(pv[6])));
            const float uu = rbf((__uint_as_float(pv[1]) + __uint_as_float(pv[3])) + (__uint_as_float(pv[5]) + __uint_as_float(pv[7])));
            const float act = rbf(gg * __builtin_amdgcn_rcpf(1.0f + __expf(-gg))) * uu;
            const unsigned apk = pk2(act, dppf<0xB1>(act));
#pragma unroll
            for (int ip = 0; ip < 4; ip++) ap[ip] = __builtin_amdgcn_ds_bpermute(aP + ip * 8, apk);
            return act;
        });
        STAMPW(15, 4);
        gemv(ap, wp2, 8, aR3);
    }
    WAITL0();
    BAR();
    if (lw != 0) return;
    if (ffn) ST4(__float_as_uint(sum4(aR2 + 17408, 2176)), vl4, sG9);
    SCHED();
    STAMPW(16, 4);
    const unsigned gkey = rfl(gkv), srow = 2047u - (gkey & 2047u);
    if ((srow >> 7) == rt2) {
        const float rs = __uint_as_float(((gkey & 0x80000000u) ? gkey ^ 0x80000000u : ~gkey) & 0xffff0000u);
        float sig = rbf(1.0f / (1.0f + __expf(-rs)));
        const unsigned sb = ldsb + L_STASH + (((srow >> 5) & 3) + ((srow >> 4) & 1) * 4) * 256 + (t16 * 4 + g) * 2;
        float hc = lds_bf(sb), lc = lds_bf(sb + 128);
        unsigned vo = (t16 * 4 + g) * 2, so_ = srow * 2048 + ct * 128, vy = l < 32 ? l * 16 + 3072 : OOB, sY = WS_G9 + ct * 3584;
        PIN(sig); PIN(hc); PIN(lc); PIN(vo); PIN(vy);
        asm volatile("" : "+s"(so_), "+s"(sY));
        v4u yp[4];
        float y;
        SLEEP(7);
        sweepn<4>([&](int k) { return LD16(k < 3 ? l * 16 + k * 1024 : vy, sY); }, yp, [&]() {
            const f32x4 ys = (__builtin_bit_cast(f32x4, yp[0]) + __builtin_bit_cast(f32x4, yp[1])) + (__builtin_bit_cast(f32x4, yp[2]) + __builtin_bit_cast(f32x4, yp[3]));
            y = xrow_scatter<false>(ys[0], ys[1], ys[2], ys[3]);
            return y;
        });
        __builtin_amdgcn_raw_buffer_store_b16((u16)(pk2(hc + rbf(lc + rbf(rbf(y) * sig)), 0.f) & 0xffffu), rsO, vo, so_, 0);
        STAMPW(17, 4);
    }
}
__device__ __attribute__((used, noinline)) void fk_tail() { asm volatile(".Lfk_end:"); }

extern "C" void kernel_launch(void* const* d_in, const int* in_sizes, int n_in, void* d_out, int out_size, void* d_ws,
                              size_t ws_size, hipStream_t stream) {
    (void)in_sizes; (void)n_in; (void)out_size; (void)ws_size;
    (void)hipFuncSetAttribute((const void*)fused_kernel, hipFuncAttributeMaxDynamicSharedMemorySize, LDS_BYTES);
    fused_kernel<<<256, 512, LDS_BYTES, stream>>>((const u16*)d_in[0], (const u16*)d_in[1], (const u16*)d_in[2], (const u16*)d_in[3],
                                                  (const u16*)d_in[5], (const u16*)d_in[6], (const u16*)d_in[7], (const u16*)d_in[8],
                                                  (u16*)d_out, (char*)d_ws);
}
```

```cpp
#include <hip/hip_runtime.h>
#include <stdint.h>
#include <utility>

typedef unsigned short u16;
typedef unsigned long long u64;
typedef short bf16x8 __attribute__((ext_vector_type(8)));
typedef short s16x4 __attribute__((ext_vector_type(4)));
typedef float f32x4 __attribute__((ext_vector_type(4)));
typedef float f32x2 __attribute__((ext_vector_type(2)));
typedef __bf16 bf16x2 __attribute__((ext_vector_type(2)));
typedef unsigned v4u __attribute__((ext_vector_type(4)));
typedef unsigned v2u __attribute__((ext_vector_type(2)));
#define LAS __attribute__((address_space(3)))
#define GAS __attribute__((address_space(1)))
#define CAS __attribute__((address_space(4)))
typedef GAS u64 gu64;
typedef GAS unsigned gu32;
typedef __amdgpu_buffer_rsrc_t rsrc_t;
#define DI __device__ __forceinline__
#define RLX_AGENT __ATOMIC_RELAXED, __HIP_MEMORY_SCOPE_AGENT
#define BAR() __builtin_amdgcn_s_barrier()
#define SCHED() __builtin_amdgcn_sched_barrier(0)
#define SGB(m, n) __builtin_amdgcn_sched_group_barrier((m), (n), 0)
#define WAITL0() asm volatile("s_waitcnt lgkmcnt(0)" ::: "memory")
#define WL0() __builtin_amdgcn_s_waitcnt(0xc07f)
#define MFMA(a, b, c) __builtin_amdgcn_mfma_f32_16x16x32_bf16((a), (b), (c), 0, 0, 0)
#define ORDER_BEHIND_POLL() __builtin_amdgcn_fence(__ATOMIC_ACQUIRE, "wavefront")
#define AUX_SC1 16
template <int N> DI void waitv() { asm volatile("s_waitcnt vmcnt(%0)" ::"i"(N) : "memory"); }

#define WS_EPOCH 0
#define WS_G0 0x1000
#define WS_G1 0x8000
#define WS_F4 0x78000
#define WS_GC 0x70000
#define WS_G3 0x80000
#define SENT 0xFFFFFFFFu
#define WS_TL 0x100000
#define WS_G5 0x100000
#define WS_G8 0x180000
#define WS_G9 0x1A0000
#define WS_GK 0x1B4000
#define WS_C 0x1C0000
#define WS_P2 0x200000
#define P2_STRIDE 0x2180
#define WS_P4 0x700000
#define WS_USED 0xB00000
#define WS_STAMP 0x1000000

#define STAGE_B 40960
#define LDS_BYTES 163840
#define L_WO 32768
#define L_MB 161792
#define L_WPHI (L_MB - 8192)
#define L_SD (L_MB + 512)
#define L_CS (L_MB + 1024)
#define L_V L_WO
#define L_KH (L_WO + 65536)
#define L_XB 0
#define L_STASH (L_WO + 1024)
#define L_OUT (L_WO + 8192)
#define L_RED 4096

#define SPIN_CAP 20000u
#define W_AUX 2
#define LOG2E 1.4426950408889634f

DI unsigned rfl(unsigned x) { return __builtin_amdgcn_readfirstlane(x); }
DI float bf2f(unsigned u) { return __uint_as_float(u << 16); }
DI float bflo(unsigned u) { return __uint_as_float(u << 16); }
DI float bfhi(unsigned u) { return __uint_as_float(u & 0xffff0000u); }
DI unsigned pk2(float lo, float hi) { f32x2 v = {lo, hi}; return __builtin_bit_cast(unsigned, __builtin_convertvector(v, bf16x2)); }
DI unsigned nsent(unsigned x) { return __builtin_elementwise_min(x, SENT - 1); }
DI unsigned umx(unsigned a, unsigned b) { return __builtin_elementwise_max(a, b); }
DI float rbf(float f) { return bflo(pk2(f, f) & 0xffffu); }
DI bf16x8 mk8(unsigned a, unsigned b, unsigned c, unsigned d) { v4u v = {a, b, c, d}; return __builtin_bit_cast(bf16x8, v); }
DI void dma16(unsigned ldsaddr, unsigned voff, rsrc_t rs, unsigned soff) {
    asm volatile("s_mov_b32 m0, %0\n\ts_nop 0\n\tbuffer_load_dwordx4 %1, %2, %3 offen lds" ::"s"(ldsaddr), "v"(voff), "s"(rs), "s"(soff) : "memory");
}
DI void dma16x4(unsigned ldsaddr, const unsigned* v, rsrc_t rs, unsigned soff) {
    asm volatile("s_mov_b32 m0, %0\n\ts_nop 0\n\tbuffer_load_dwordx4 %1, %5, %6 offen lds\n\tbuffer_load_dwordx4 %2, %5, %6 offen offset:1024 lds\n\t"
                 "buffer_load_dwordx4 %3, %5, %6 offen offset:2048 lds\n\tbuffer_load_dwordx4 %4, %5, %6 offen offset:3072 lds"
                 ::"s"(ldsaddr), "v"(v[0]), "v"(v[1]), "v"(v[2]), "v"(v[3]), "s"(rs), "s"(soff) : "memory");
}
DI void touch(unsigned voff, rsrc_t rs, unsigned soff) { asm volatile("buffer_load_dword v255, %0, %1, %2 offen" ::"v"(voff), "s"(rs), "s"(soff) : "memory", "v255"); }
DI v4u ld16_asm(unsigned voff, rsrc_t rs, unsigned soff) {
    v4u r; asm volatile("buffer_load_dwordx4 %0, %1, %2, %3 offen" : "=v"(r) : "v"(voff), "s"(rs), "s"(soff) : "memory"); return r;
}
#define PIN(x) asm volatile("" : "+v"(x))
DI s16x4 tr_read(unsigned a) { return __builtin_amdgcn_ds_read_tr16_b64_v4i16((LAS s16x4*)(a)); }
DI bf16x8 cat8(s16x4 a, s16x4 b) { return __builtin_shufflevector(a, b, 0, 1, 2, 3, 4, 5, 6, 7); }
DI bf16x8 lds128(unsigned a) { return *(const LAS bf16x8*)(a); }
DI f32x4 ldsf4(unsigned a) { return *(const LAS f32x4*)(a); }
DI float ldsf(unsigned a) { return *(const LAS float*)(a); }
DI unsigned ldsu(unsigned a) { return *(const LAS unsigned*)(a); }
DI void stsf4(unsigned a, f32x4 v) { *(LAS f32x4*)(a) = v; }
DI void stsf(unsigned a, float v) { *(LAS float*)(a) = v; }
DI void stsu(unsigned a, unsigned v) { *(LAS unsigned*)(a) = v; }
template <int C> DI float dppf(float x) { return __uint_as_float(__builtin_amdgcn_update_dpp(0, __float_as_uint(x), C, 0xf, 0xf, true)); }
template <int C> DI unsigned dppu(unsigned x) { return __builtin_amdgcn_update_dpp(0, x, C, 0xf, 0xf, true); }
DI float max2f(float a, float b) { float r; asm("v_max_f32_e32 %0, %1, %2" : "=v"(r) : "v"(a), "v"(b)); return r; }
DI float max3f(float a, float b, float c) { float r; asm("v_max3_f32 %0, %1, %2, %3" : "=v"(r) : "v"(a), "v"(b), "v"(c)); return r; }
DI float row_sum(float x) { x += dppf<0x128>(x); x += dppf<0x124>(x); x += dppf<0x122>(x); x += dppf<0x121>(x); return x; }
DI float xrow_sum(float x) {
    auto a = __builtin_amdgcn_permlane16_swap(__float_as_uint(x), __float_as_uint(x), false, false);
    x = __uint_as_float(a[0]) + __uint_as_float(a[1]);
    auto b = __builtin_amdgcn_permlane32_swap(__float_as_uint(x), __float_as_uint(x), false, false);
    return __uint_as_float(b[0]) + __uint_as_float(b[1]);
}
DI unsigned wave_maxu(unsigned x) {
    x = umx(x, dppu<0x128>(x)); x = umx(x, dppu<0x124>(x)); x = umx(x, dppu<0x122>(x)); x = umx(x, dppu<0x121>(x));
    auto a = __builtin_amdgcn_permlane16_swap(x, x, false, false);
    x = umx(a[0], a[1]);
    auto b = __builtin_amdgcn_permlane32_swap(x, x, false, false);
    return umx(b[0], b[1]);
}
DI float nmax3f(float a, float b, float c) { float r; asm("v_max3_f32 %0, -%1, -%2, -%3" : "=v"(r) : "v"(a), "v"(b), "v"(c)); return r; }
DI float nmax2f(float a, float b) { float r; asm("v_max_f32_e64 %0, -%1, -%2" : "=v"(r) : "v"(a), "v"(b)); return r; }
template <bool MAX> DI float xrow_scatter(float a, float b, float c, float d) {
    const auto s = __builtin_amdgcn_permlane16_swap(__float_as_uint(a), __float_as_uint(b), false, false);
    const auto t = __builtin_amdgcn_permlane16_swap(__float_as_uint(c), __float_as_uint(d), false, false);
    float u = MAX ? max2f(__uint_as_float(s[0]), __uint_as_float(s[1])) : __uint_as_float(s[0]) + __uint_as_float(s[1]);
    float v = MAX ? max2f(__uint_as_float(t[0]), __uint_as_float(t[1])) : __uint_as_float(t[0]) + __uint_as_float(t[1]);
    if (MAX) asm volatile("s_nop 1" : "+v"(u), "+v"(v));
    const auto r = __builtin_amdgcn_permlane32_swap(__float_as_uint(u), __float_as_uint(v), false, false);
    return MAX ? max2f(__uint_as_float(r[0]), __uint_as_float(r[1])) : __uint_as_float(r[0]) + __uint_as_float(r[1]);
}
DI void xrow_gather(float w, float (&o)[4]) {
    const auto r = __builtin_amdgcn_permlane32_swap(__float_as_uint(w), __float_as_uint(w), false, false);
    const auto s = __builtin_amdgcn_permlane16_swap(r[0], r[0], false, false);
    const auto t = __builtin_amdgcn_permlane16_swap(r[1], r[1], false, false);
    o[0] = __uint_as_float(s[0]); o[1] = __uint_as_float(s[1]); o[2] = __uint_as_float(t[0]); o[3] = __uint_as_float(t[1]);
}
struct NoHook { DI void operator()(int, float&) const {} DI void operator()(int, float&, float&) const {} };
struct NoPut { DI void operator()(int, const bf16x8&) const {} };
template <bool MUL, class H = NoHook, class P = NoPut> DI void softmax2(const f32x4 (&p)[2][2], float rs0, float rs1, bool hi, bf16x8 (&out)[4], H hook = H(), P put = P()) {
    float m[4];
#pragma unroll
    for (int i = 0; i < 2; i++) {
        const f32x4 &p0 = p[i][0], &p1 = p[i][1];
        m[2 * i] = max3f(max3f(p0[0], p0[1], p0[2]), max3f(p0[3], p1[0], p1[1]), max2f(p1[2], p1[3]));
        m[2 * i + 1] = max3f(nmax3f(p0[0], p0[1], p0[2]), nmax3f(p0[3], p1[0], p1[1]), nmax2f(p1[2], p1[3]));
    }
    asm volatile("s_nop 1" : "+v"(m[0]), "+v"(m[1]), "+v"(m[2]), "+v"(m[3]));
    hook(1, m[3]);
    const float rsr = hi ? rs1 : rs0;
    float a[4];
    xrow_gather(xrow_scatter<true>(m[0], m[1], m[2], m[3]) * (rsr * -LOG2E), a);
    hook(10, a[0]);
    f32x2 e[4][4];
    float s[4];
#pragma unroll
    for (int k = 0; k < 4; k++) {
        if (k == 2) hook(2, a[2], s[1]);
        const float sc = ((k >> 1) ? rs1 : rs0) * ((k & 1) ? -LOG2E : LOG2E);
#pragma unroll
        for (int j = 0; j < 4; j++) {
            const f32x4& q = p[k >> 1][j >> 1];
            const f32x2 t = __builtin_elementwise_fma((f32x2){q[2 * (j & 1)], q[2 * (j & 1) + 1]}, (f32x2){sc, sc}, (f32x2){a[k], a[k]});
            e[k][j] = (f32x2){__builtin_amdgcn_exp2f(t[0]), __builtin_amdgcn_exp2f(t[1])};
        }
        const f32x2 h = (e[k][0] + e[k][1]) + (e[k][2] + e[k][3]);
        s[k] = h[0] + h[1];
        if (k == 3) hook(3, s[k]);
    }
    float iv = __builtin_amdgcn_rcpf(xrow_scatter<false>(s[0], s[1], s[2], s[3]));
    if (MUL) iv *= rsr;
    xrow_gather(iv, a);
#pragma unroll
    for (int k = 0; k < 4; k++) {
        if (k == 2) hook(4, a[2]);
        const f32x2 c = {a[k], a[k]};
        const f32x2 x0 = e[k][0] * c, x1 = e[k][1] * c, x2 = e[k][2] * c, x3 = e[k][3] * c;
        out[k] = mk8(pk2(x0[0], x0[1]), pk2(x1[0], x1[1]), pk2(x2[0], x2[1]), pk2(x3[0], x3[1]));
        put(k, out[k]);
    }
}
DI float dot2(unsigned a, unsigned b, float c) {
    return __builtin_amdgcn_fdot2_f32_bf16(__builtin_bit_cast(bf16x2, a), __builtin_bit_cast(bf16x2, b), c, false);
}
DI void st_gran(char* ws, unsigned off, unsigned tag, unsigned val) {
    __hip_atomic_store((gu64*)(ws + off), ((u64)tag << 32) | val, RLX_AGENT);
}
DI v2u ldg(rsrc_t rs, unsigned voff, unsigned soff) { return __builtin_amdgcn_raw_buffer_load_b64(rs, voff, soff, AUX_SC1); }
#define RARE(c) __builtin_expect(!!(c), 0)
#define FRESH() asm volatile("" ::: "memory")
template <int N> DI void sweep(rsrc_t rs, unsigned voff, unsigned soff, unsigned S, unsigned tag, unsigned (&v)[N]) {
#pragma nounroll
    for (unsigned spins = 0;;) {
        unsigned bad = 0;
        FRESH();
#pragma unroll
        for (int k = 0; k < N; ++k) { const v2u x = ldg(rs, voff, soff + k * S); v[k] = x[0]; bad |= x[1] ^ tag; }
        if (__all(bad == 0)) break;
        if (++spins > SPIN_CAP) break;
        __builtin_amdgcn_s_sleep(1);
    }
}
template <int A, int B> DI unsigned budget(unsigned& spins) {
    unsigned r;
    asm volatile("s_add_i32 %0, %0, 1\n\ts_cmpk_lt_u32 %0, %2\n\ts_cselect_b32 %1, %3, %4" : "+s"(spins), "=s"(r) : "i"(SPIN_CAP), "i"(A), "i"(B) : "scc");
    SCHED();
    return r;
}
DI unsigned wmax(unsigned x) { return x; }
DI unsigned wmax(v2u x) { return umx(x[0], x[1]); }
DI unsigned wmax(v4u x) { return umx(umx(x[0], x[1]), umx(x[2], x[3])); }
template <int N, class T, class F> DI unsigned sweepz(F ld, T (&v)[N]) {
    unsigned spins = 0, lim, m;
#pragma nounroll
    do {
        FRESH();
#pragma unroll
        for (int k = 0; k < N; ++k) v[k] = ld(k);
        lim = budget<-2, -1>(spins);
        m = 0;
#pragma unroll
        for (int k = 0; k < N; ++k) m = umx(m, wmax(v[k]));
    } while (__any(m > lim));
    return m;
}
template <int N, class T, class F, class G> DI void sweepn(F ld, T (&v)[N], G use) {
    unsigned spins = 0;
    bool again;
#pragma nounroll
    do {
        FRESH();
#pragma unroll
        for (int k = 0; k < N; ++k) v[k] = ld(k);
        const int m = budget<3, 0>(spins);
        again = __any(__builtin_amdgcn_classf(use(), m));
    } while (again);
}
#define OOB 0x4000000u
#define LD16(v, s) __builtin_amdgcn_raw_buffer_load_b128(rsWS, (v), (s), AUX_SC1)
#define LD4(v, s) __builtin_amdgcn_raw_buffer_load_b32(rsWS, (v), (s), AUX_SC1)
#define ST16(x, v, s) __builtin_amdgcn_raw_buffer_store_b128((x), rsWS, (v), (s), AUX_SC1)
#define ST4(x, v, s) __builtin_amdgcn_raw_buffer_store_b32((x), rsWS, (v), (s), AUX_SC1)
#define SLEEP(n) __builtin_amdgcn_s_sleep(n)
DI void poll16(rsrc_t rs, unsigned off, unsigned tag, int l) {
    unsigned spins = 0, lim, bad;
#pragma nounroll
    do {
        FRESH();
        const v2u x = ldg(rs, (l & 15) * 8, off);
        lim = budget<0, -1>(spins);
        bad = (x[1] ^ tag) | (x[0] ^ tag ^ 0x5bd1e995u);
    } while (__any(bad > lim));
    ORDER_BEHIND_POLL();
}
#define STAMPR(i, role)
#define STAMP(i)
#define STAMPW(i, ww)

#define ARR_DECL
#define KATTR
#define ARR_MARK(i)
#define ARR_END()
template <class F, int... Is> DI void static_for_impl(F&& f, std::integer_sequence<int, Is...>) { (f(std::integral_constant<int, Is>{}), ...); }
template <int N, class F> DI void static_for(F&& f) { static_for_impl(f, std::make_integer_sequence<int, N>{}); }

DI s16x4 bq(unsigned b0, unsigned b1, int j, int i) {
    const int ni = (j + 2) % 6;
    return tr_read(((ni & 1) ? b1 : b0) + (ni >> 1) * 8192 + (i >> 1) * 4096 + (i & 1) * 512);
}
template <class F> DI void group(f32x4 (&acc)[4][6], const bf16x8 (&A)[2][4], const bf16x8 (&B)[2], int j, F gap) {
    const int ni = (j + 2) % 6;
#pragma unroll
    for (int i = 0; i < 8; i++) {
        const int mi = i >> 1, ks = i & 1;
        if (ni < 4) asm volatile("v_mfma_f32_16x16x32_bf16 %0, %1, %2, %0" : "+v"(acc[mi][ni]) : "v"(B[ks]), "v"(A[ks][mi]));
        else asm volatile("v_mfma_f32_16x16x32_bf16 %0, %1, %2, %0" : "+v"(acc[mi][ni]) : "v"(A[ks][mi]), "v"(B[ks]));
        gap(i);
        SCHED();
    }
}
struct ImgPut {
    const unsigned (&ia)[4];
    DI void operator()(int k, const bf16x8& f) const {
        const v4u u = __builtin_bit_cast(v4u, f);
        *(LAS v2u*)(ia[2 * (k & 1)] + (k >> 1) * 2048) = (v2u){u[0], u[1]};
        *(LAS v2u*)(ia[2 * (k & 1) + 1] + (k >> 1) * 2048) = (v2u){u[2], u[3]};
        SCHED();
    }
};
DI float lds_bf(unsigned a) { return bf2f(*(const LAS u16*)(a)); }
#define MFMA_SETTLE2(p) asm volatile("s_nop 7\n\ts_nop 1" : "+v"(p[0][0]), "+v"(p[0][1]), "+v"(p[1][0]), "+v"(p[1][1]))

__global__ KATTR __launch_bounds__(512, 2) void fused_kernel(const u16* __restrict__ X, const u16* __restrict__ WQKV, const u16* __restrict__ WPHI,
                                                       const u16* __restrict__ WO, const u16* __restrict__ WR, const u16* __restrict__ WG,
                                                       const u16* __restrict__ WU, const u16* __restrict__ WD, u16* __restrict__ OUT,
                                                       char* __restrict__ ws) {
    extern __shared__ __attribute__((aligned(16))) char lds[];
    u64 pc0; unsigned clen;
    const int tid = threadIdx.x, l = tid & 63;
    asm volatile("s_getpc_b64 %0\n.Lfk_beg:\n\ts_lshr_b32 %1, %3, 3\n\tv_and_b32 v255, 0x1e0, %2\n\tv_add_lshl_u32 v255, v255, %1, 7\n\ts_mov_b32 %1, .Lfk_end-.Lfk_beg\n\t"
                 "v_min_u32 v255, %1, v255\n\tglobal_load_dword v255, v255, %0" : "=&s"(pc0), "=&s"(clen) : "v"(tid), "s"(blockIdx.x) : "v255", "scc");
    SCHED();
    const unsigned w = rfl(tid >> 6);
    const unsigned bid = blockIdx.x, xcd = bid & 7, idx = bid >> 3;
    const unsigned rt = (xcd & 1) * 8 + (idx & 7), hp = (xcd >> 1) * 4 + (idx >> 3);
    const unsigned i16 = idx & 15, rt2 = xcd * 2 + (idx >> 4), ct = i16 == 4 ? 11 : i16 == 11 ? 4 : i16;
    const unsigned hq = (hp + 4) & 15;
    const unsigned rot = (rt >> 2) * 4;
    const unsigned rot2 = (2 * (rt2 + ct)) & 31;
    const unsigned ldsb = (unsigned)(size_t)lds;
    const int g = l >> 4, q4 = (l & 15) >> 2, p4 = l & 3, t16 = l & 15;
    const unsigned kr = l >> 3;
    asm volatile("" ::"s"(X), "s"(WQKV), "s"(WPHI), "s"(WO), "s"(WR), "s"(WG), "s"(WU), "s"(WD), "s"(OUT), "s"(ws));
    unsigned tag;
#define READ_TAG() do { SCHED(); tag = *(const volatile CAS unsigned*)(ws + WS_EPOCH) + 1u; asm volatile("" : "+s"(tag)); } while (0)
    const rsrc_t rsWS = __builtin_amdgcn_make_buffer_rsrc((void*)ws, 0, WS_USED, 0x00020000);
    const rsrc_t rsX = __builtin_amdgcn_make_buffer_rsrc((void*)X, 0, 2048 * 1024 * 2, 0x00020000);
    const rsrc_t rsW = __builtin_amdgcn_make_buffer_rsrc((void*)WQKV, 0, 1024 * 3072 * 2, 0x00020000);
    const unsigned vA = (kr * 1024 + (((l & 7) ^ kr) * 8)) * 2;
    const unsigned vB0 = (kr * 3072 + (((l & 7) ^ (((kr >> 1) & 1) << 1)) * 8)) * 2;
    const unsigned vB1 = (kr * 3072 + (((l & 7) ^ ((((kr >> 1) & 1) | 2) << 1)) * 8)) * 2;
    const unsigned srow1 = rt * 128 + hp * 8 + (w & 3) * 2;
    v4u xr[4];
    if (w < 4) {
#pragma unroll
        for (int i = 0; i < 4; i++) xr[i] = ld16_asm(l * 16, rsX, srow1 * 2048 + i * 1024);
    }
    {
        const unsigned vBw = (w & 1) ? vB1 : vB0;
#pragma unroll
        for (int i = 0; i < 2; i++) dma16(ldsb + (w * 2 + i) * 1024, vA, rsX, (rt * 128 + w * 16 + i * 8) * 2048 + rot * 128);
#pragma unroll
        for (int i = 0; i < 3; i++) dma16(ldsb + 16384 + i * 8192 + w * 1024, vBw, rsW, (rot * 64 + w * 8) * 6144 + (i ? hp : hq) * 128 + i * 2048);
    }
    const f32x4 zero4 = {0.f, 0.f, 0.f, 0.f};
    ARR_DECL;
    const bf16x8 ones = mk8(0x3F803F80u, 0x3F803F80u, 0x3F803F80u, 0x3F803F80u);
    const unsigned lw = w & 3;
    const bool ffn = (ct & 7) != 3;
    const unsigned fb = !ffn ? 255u : rt2 * 14 + (i16 < 3 ? i16 : i16 - 2);
    const unsigned cg = (fb + 5) & 15, kc = fb >> 4;
    const unsigned kq = fb / 56, mat = (fb % 56) / 28, cg1 = (fb % 28) * 2;
    v4u wreg[24];
    STAMPW(0, 0); STAMPW(0, 4);

    const rsrc_t rsWO = __builtin_amdgcn_make_buffer_rsrc((void*)WO, 0, 1024 * 1024 * 2, 0x00020000);
    const unsigned vWO = kr * 2048 + (((l & 7) ^ (((kr >> 1) & 3) << 1)) * 16);
    unsigned vwo[4];
    auto wo_so = [&](unsigned b) { return (((2 * b & 30) + (b >> 4) + rot2) & 31) * 65536 + ct * 128; };
    const unsigned fw = (((t16 >> 1) & 3) << 2) | (((t16 >> 3) & 1) << 1) | (t16 & 1);
    unsigned ad;
    unsigned ia[4];
    bf16x8 sdv[4];
    auto pre_addr = [&]() {
#pragma unroll
        for (int k = 0; k < 4; k++) { ia[k] = ldsb + L_XB + lw * 8192 + (w >> 2) * 4096 + t16 * 128 + (((4 * k + g) ^ fw) * 8); PIN(ia[k]); }
        const unsigned rr = 4 * g + q4, fr = (((rr >> 1) & 3) << 2) | (((rr >> 3) & 1) << 1) | (rr & 1);
        ad = ldsb + L_XB + (w & 1) * 16384 + rr * 128 + ((((w >> 1) * 4 + p4) ^ fr) * 8);
        PIN(ad);
    };
    auto sd_reads = [&]() {
        unsigned sa = ldsb + L_SD + g * 8;
        PIN(sa);
#pragma unroll
        for (int s = 0; s < 4; s++) {
            const v2u a = *(const LAS v2u*)(sa + s * 64), b = *(const LAS v2u*)(sa + s * 64 + 32);
            sdv[s] = mk8(a[0], a[1], b[0], b[1]);
        }
    };
    bf16x8 v0[4], v1[4];
    auto v_reads = [&]() {
        unsigned vd = ldsb + L_V + (w & 1) * 8192 + l * 16;
        PIN(vd);
#pragma unroll
        for (int s = 0; s < 4; s++) { v0[s] = lds128(vd + s * 2048); v1[s] = lds128(vd + s * 2048 + 1024); }
    };
    auto kv_tile = [&](auto hook6) {
        const unsigned h2 = w & 1, ft = w >> 1;
        WAITL0();
        ARR_MARK(18);
        BAR();
        SCHED();
        bf16x8 pf[4];
#pragma unroll
        for (int s = 0; s < 4; s++) pf[s] = cat8(tr_read(ad + s * 4096), tr_read(ad + s * 4096 + 2048));
        SCHED();
        hook6();
        SCHED();
        f32x4 k0 = zero4, k1 = zero4, kz = zero4;
#pragma unroll
        for (int s = 0; s < 4; s++) { k0 = MFMA(pf[s], v0[s], k0); k1 = MFMA(pf[s], v1[s], k1); SCHED(); }
#pragma unroll
        for (int s = 0; s < 4; s++) kz = MFMA(pf[s], sdv[s], kz);
        const unsigned pbase = WS_P2 + (hp * 16 + rt) * P2_STRIDE;
        const v4u o0 = {pk2(k0[0], k0[1]), pk2(k0[2], k0[3]), pk2(k1[0], k1[1]), pk2(k1[2], k1[3])};
        __builtin_amdgcn_raw_buffer_store_b128(o0, rsWS, l * 16, pbase + h2 * 4096 + ft * 1024, AUX_SC1);
        asm volatile("s_nop 1" :: "v"(o0));
        __builtin_amdgcn_raw_buffer_store_b64((v2u){pk2(kz[0], kz[1]), pk2(kz[2], kz[3])}, rsWS, t16 == 0 ? g * 8 : OOB,
                                              pbase + 8192 + h2 * 128 + ft * 32, AUX_SC1);
        SCHED();
        ARR_MARK(19);
        BAR();
    };

    unsigned res;
    auto red = [&](const v4u* p) {
        float lo = xrow_scatter<false>(bflo(p[0][0]) + bflo(p[1][0]), bflo(p[0][1]) + bflo(p[1][1]), bflo(p[0][2]) + bflo(p[1][2]), bflo(p[0][3]) + bflo(p[1][3]));
        float hi = xrow_scatter<false>(bfhi(p[0][0]) + bfhi(p[1][0]), bfhi(p[0][1]) + bfhi(p[1][1]), bfhi(p[0][2]) + bfhi(p[1][2]), bfhi(p[0][3]) + bfhi(p[1][3]));
        const float chk = lo;
        lo += dppf<0x128>(lo); hi += dppf<0x128>(hi);
        res = pk2(lo, hi);
        return chk;
    };
    auto kv_reduce = [&]() {
        unsigned v3 = (l & 8) ? OOB : (l & 7) * 16 + g * 4;
        unsigned s3 = WS_G3 + (hp * 4 + (rt >> 2)) * 2048 + (lw & 1) * 1024 + (rt & 3) * 256 + (lw >> 1) * 128;
        PIN(v3);
        asm volatile("" : "+s"(s3));
        SLEEP(2);
        v4u pp[2];
        sweepn<2>([&](int k) { return LD16(kr * P2_STRIDE + (l & 7) * 16, WS_P2 + (hp * 16 + k * 8) * P2_STRIDE + (rt >> 2) * 2048 + (lw & 1) * 1024 + (rt & 3) * 256 + (lw >> 1) * 128); },
                  pp, [&]() { return red(pp); });
        ST4(nsent(res), v3, s3);
        STAMPW(25, 0);
    };

    const unsigned half = w >> 2;
    const unsigned R2 = rt2 * 128 + lw * 32 + half * 16 + t16;

    rsrc_t rsD = __builtin_amdgcn_make_buffer_rsrc((void*)WD, 0, 1024 * 3584 * 2, 0x00020000);
    unsigned vD, sD;
    auto dloads = [&](int i0, int i1) {
#pragma unroll
        for (int i = i0; i < i1; i++)
            wreg[16 + i] = __builtin_amdgcn_raw_buffer_load_b128(rsD, vD, sD + i * 2048, W_AUX);
        FRESH();
    };
    v4u wp1[2][8];
    auto perm1 = [&]() {
#pragma unroll
        for (int n = 0; n < 16; n++) PIN(wreg[n]);
#pragma unroll
        for (int ip = 0; ip < 4; ip++)
#pragma unroll
            for (int c = 0; c < 4; c++) {
#pragma unroll
                for (int m = 0; m < 2; m++) {
                    const unsigned a = wreg[m * 8 + 2 * ip][c], b = wreg[m * 8 + 2 * ip + 1][c];
                    wp1[m][2 * c][ip] = __builtin_amdgcn_perm(b, a, 0x05040100);
                    wp1[m][2 * c + 1][ip] = __builtin_amdgcn_perm(b, a, 0x07060302);
                }
            }
#pragma unroll
        for (int c = 0; c < 8; c++) { PIN(wp1[0][c]); PIN(wp1[1][c]); }
    };
    v4u wrv[2];
    auto res_loads = [&]() {
#pragma unroll
        for (int i = 0; i < 2; i++) {
            dma16(ldsb + L_XB + w * 4096 + i * 1024, kr * 2048 + (((l & 7) ^ kr) * 16), rsX, (rt2 * 128 + lw * 32 + half * 16 + i * 8) * 2048 + ct * 128);
            wrv[i] = *(const v4u*)(WR + ct * 64 + i * 32 + 8 * g);
        }
        FRESH();
    };

    if (w < 4) {
        const unsigned wm = w & 1, hh = w >> 1;
        f32x4 acc[4][6];
        bf16x8 wf[2], wfq[2];
        float mbt[4], rst[4], csv[2];
        f32x4 mbv[4], csq[2], csk[2];
#pragma unroll
        for (int i = 0; i < 4; i++)
#pragma unroll
            for (int j = 0; j < 6; j++) { acc[i][j] = zero4; PIN(acc[i][j]); }
        {
            const unsigned aoff0 = ldsb + (wm * 64 + t16) * 128 + (((0 + g) ^ (l & 7)) * 16);
            const unsigned aoff1 = ldsb + (wm * 64 + t16) * 128 + (((4 + g) ^ (l & 7)) * 16);
            const unsigned sk = ((q4 >> 1) & 1) | ((g & 1) << 1);
            const unsigned boffA = ldsb + 16384 + (8 * g + q4) * 128 + (((2 * hh) ^ sk) * 32) + p4 * 8;
            const unsigned boffB = ldsb + 16384 + (8 * g + q4) * 128 + (((2 * hh + 1) ^ sk) * 32) + p4 * 8;
            bf16x8 A[2][4], B[3][2];
            s16x4 lo;
            unsigned a0[3], a1[3], bA[3], bB[3];
#pragma unroll
            for (int i = 0; i < 3; i++) {
                a0[i] = aoff0 + i * STAGE_B; a1[i] = aoff1 + i * STAGE_B; bA[i] = boffA + i * STAGE_B; bB[i] = boffB + i * STAGE_B;
                PIN(a0[i]); PIN(a1[i]); PIN(bA[i]); PIN(bB[i]);
            }
            unsigned aT = ldsb + L_MB + (wm * 64 + t16) * 4, aS = ldsb + L_SD + (wm * 64 + t16) * 2, aM = ldsb + L_MB + wm * 256 + g * 16;
            unsigned aC = ldsb + L_CS + hh * 128 + g * 16, aV = ldsb + L_CS + (128 + hh * 32 + t16) * 4;
            unsigned aW = ldsb + L_WPHI + hh * 2048 + (4 * g + q4) * 64 + p4 * 8, aK = ldsb + L_KH + w * 2048 + l * 16;
            PIN(aT); PIN(aS); PIN(aM); PIN(aC); PIN(aV); PIN(aW); PIN(aK);
            pre_addr();
            READ_TAG();
            waitv<5>();
            PIN(xr[0]); PIN(xr[1]); PIN(xr[2]); PIN(xr[3]);
            {
                float s[2], q[2], x0[2];
#pragma unroll
                for (int rr = 0; rr < 2; rr++) {
                    x0[rr] = bflo(rfl(xr[2 * rr][0]));
                    s[rr] = 0.f; q[rr] = 0.f;
#pragma unroll
                    for (int i = 0; i < 4; i++) {
                        const float a = bflo(xr[2 * rr][i]) - x0[rr], b = bfhi(xr[2 * rr][i]) - x0[rr];
                        const float c = bflo(xr[2 * rr + 1][i]) - x0[rr], d = bfhi(xr[2 * rr + 1][i]) - x0[rr];
                        s[rr] += (a + b) + (c + d);
                        q[rr] += (a * a + b * b) + (c * c + d * d);
                    }
                }
                const float sq = row_sum(xrow_scatter<false>(s[0], s[1], q[0], q[1]));
#define RLF(x, n) __uint_as_float(__builtin_amdgcn_readlane(__float_as_uint(x), (n)))
                const float r0 = RLF(sq, 0), r1 = RLF(sq, 16), r2 = RLF(sq, 32), r3 = RLF(sq, 48);
                const float sm = (l ? r1 : r0) * (1.0f / 1024.0f), qm = (l ? r3 : r2) * (1.0f / 1024.0f);
                const float var = fmaxf(qm - sm * sm, 0.f);
                if (l < 2) st_gran(ws, WS_G0 + (srow1 + l) * 8, tag, pk2((l ? x0[1] : x0[0]) + sm, sqrtf(rbf(rbf(var) + 1e-5f))));
            }
            waitv<1>();
            WAITL0();
            ARR_MARK(0);
            BAR();
            SCHED();
            STAMPW(1, 0);
            auto rdB = [&](int u, int j, int i) {
                if (i & 1) B[j % 3][i >> 1] = cat8(lo, bq(bA[u], bB[u], j, i)); else lo = bq(bA[u], bB[u], j, i);
            };
            auto rdA = [&](int u, int i) { A[i & 1][i >> 1] = lds128(((i & 1) ? a1[u] : a0[u]) + (i >> 1) * 2048); };
            auto head = [&](int u) {
                group(acc, A, B[0], 0, [&](int i) { rdB(u, 1 + (i >> 2), i & 3); });
                group(acc, A, B[1], 1, [&](int i) { if (i < 4) rdB(u, 3, i); });
                group(acc, A, B[2], 2, [&](int i) { if (i < 4) rdB(u, 4, i); });
                group(acc, A, B[0], 3, [&](int i) { if (i < 4) rdB(u, 5, i); });
                WL0();
            };
#pragma unroll
            for (int i = 0; i < 4; i++) rdB(0, 0, i);
#pragma unroll
            for (int i = 0; i < 8; i++) rdA(0, i);
            SCHED();
#pragma nounroll
            for (unsigned t = 0; t < 5; t++) {
#pragma unroll
                for (int u = 0; u < 3; u++) {
                    head(u);
                    ARR_MARK(1 + t * 3 + u);
                    BAR();
                    SCHED();
                    group(acc, A, B[1], 4, [&](int i) { if (i < 4) rdB((u + 1) % 3, 0, i); });
                    group(acc, A, B[2], 5, [&](int i) { rdA((u + 1) % 3, i); });
                }
            }
            unsigned kw[4][4], rsu[4];
            f32x2 kx;
            auto kpiece = [&](int n) {
                if (n > 0) { kw[((n - 1 >> 2) + 2) & 3][(n - 1) & 3] = pk2(kx[0], kx[1]); PIN(kw[((n - 1 >> 2) + 2) & 3][(n - 1) & 3]); }
                if (n < 16) {
                    const int mi = ((n >> 2) + 2) & 3, j = n & 3;
                    const f32x4 &c = acc[mi][2 + (j >> 1)], &s_ = csk[j >> 1];
                    float x0 = __builtin_fmaf(mbt[mi], s_[2 * (j & 1)], c[2 * (j & 1)]); PIN(x0);
                    float x1 = __builtin_fmaf(mbt[mi], s_[2 * (j & 1) + 1], c[2 * (j & 1) + 1]); PIN(x1);
                    kx = (f32x2){x0, x1};
                }
                if (n == 5 || n == 9) *(LAS v4u*)(aK + (n == 9) * 1024) = (v4u){kw[2 + (n == 9)][0], kw[2 + (n == 9)][1], kw[2 + (n == 9)][2], kw[2 + (n == 9)][3]};
            };
            group(acc, A, B[0], 0, [&](int i) { rdB(0, 1 + (i >> 2), i & 3); });
            group(acc, A, B[1], 1, [&](int i) {
                if (i < 4) { rdB(0, 3, i); mbt[(i + 2) & 3] = ldsf(aT + ((i + 2) & 3) * 64); }
                else if (i < 6) csk[i - 4] = ldsf4(aC + 256 + (i - 4) * 64);
                else wf[i - 6] = cat8(tr_read(aW + (i - 6) * 32), tr_read(aW + (i - 6) * 32 + 1024));
            });
            group(acc, A, B[2], 2, [&](int i) { if (i < 4) rdB(0, 4, i); else mbv[i - 4] = ldsf4(aM + (i - 4) * 64); kpiece(i); });
            group(acc, A, B[0], 3, [&](int i) {
                if (i < 4) rdB(0, 5, i); else rsu[i - 4] = *(const LAS u16*)(aS + (i - 4) * 32);
                if (i == 2 || i == 3) csv[i - 2] = ldsf(aV + (i - 2) * 64);
                kpiece(8 + i);
            });
            WL0();
            ARR_MARK(16);
            BAR();
            SCHED();
            kpiece(16);
            f32x4 p[2][2];
#pragma unroll
            for (int i = 0; i < 4; i++) p[i >> 1][i & 1] = MFMA(wf[i & 1], mk8(kw[i >> 1][0], kw[i >> 1][1], kw[i >> 1][2], kw[i >> 1][3]), zero4);
            SCHED();
            STAMPW(2, 0);
#pragma unroll
            for (int mi = 0; mi < 4; mi++) rst[mi] = bf2f(rsu[mi]);
#pragma unroll
            for (int a = 0; a < 2; a++)
#pragma unroll
                for (int dt = 0; dt < 2; dt++) {
                    const f32x4 x0 = acc[2 * a][4 + dt] + mbv[2 * a] * csv[dt], x1 = acc[2 * a + 1][4 + dt] + mbv[2 * a + 1] * csv[dt];
                    *(LAS v4u*)(aK + (L_V - L_KH) + w * 2048 + (a * 2 + dt) * 1024) = (v4u){pk2(x0[0], x0[1]), pk2(x0[2], x0[3]), pk2(x1[0], x1[1]), pk2(x1[2], x1[3])};
                }
            SCHED();
            rst[0] = __builtin_amdgcn_rcpf(rst[0]); rst[1] = __builtin_amdgcn_rcpf(rst[1]);
            bf16x8 f[4];
            struct HookC {
                decltype(v_reads)& vr; decltype(sd_reads)& sr;
                DI void operator()(int k, float& x) const { if (k == 10) { PIN(x); WAITL0(); ARR_MARK(17); BAR(); PIN(x); } if (k == 3) { PIN(x); vr(); sr(); PIN(x); } }
                DI void operator()(int, float&, float&) const {}
            };
            softmax2<true>(p, rst[0], rst[1], g >= 2, f, HookC{v_reads, sd_reads}, ImgPut{ia});
#pragma unroll
            for (int ft = 0; ft < 2; ft++) wfq[ft] = cat8(tr_read(aW + ft * 32 + 4096), tr_read(aW + ft * 32 + 5120));
            csq[0] = ldsf4(aC); csq[1] = ldsf4(aC + 64);
            STAMPW(3, 0);
            kv_tile([]() {});
            STAMPW(4, 0);
            SCHED();
            group(acc, A, B[1], 4, [&](int i) { if (i == 2 || i == 3) { rst[i] = __builtin_amdgcn_rcpf(rst[i]); PIN(rst[i]); } });
            group(acc, A, B[2], 5, [&](int) {});
            asm volatile("s_nop 7\n\ts_nop 1");
            SCHED();
        }
        bf16x8 phq[2][4];
        v4u kg[4], pz[2];
        unsigned sG3 = WS_G3 + (hq * 2 + hh) * 4096;
        auto z_ld = [&](int k) { return LD16(kr * P2_STRIDE + (l & 7) * 16, WS_P2 + (hq * 16 + k * 8) * P2_STRIDE + 8192 + hh * 128); };
        {
            f32x4 pq[2][2][2];
#pragma unroll
            for (int mi = 0; mi < 4; mi++) {
                const f32x4 x0 = acc[mi][0] + mbt[mi] * csq[0], x1 = acc[mi][1] + mbt[mi] * csq[1];
                const bf16x8 qf = mk8(pk2(x0[0], x0[1]), pk2(x0[2], x0[3]), pk2(x1[0], x1[1]), pk2(x1[2], x1[3]));
                pq[mi >> 1][mi & 1][0] = MFMA(wfq[0], qf, zero4); pq[mi >> 1][mi & 1][1] = MFMA(wfq[1], qf, zero4);
            }
            MFMA_SETTLE2(pq[0]); MFMA_SETTLE2(pq[1]);
            softmax2<false>(pq[0], rst[0], rst[1], g >= 2, phq[0]);
#pragma unroll
            for (int i = 0; i < 4; i++) PIN(phq[0][i]);
            SCHED();
            kv_reduce();
            SCHED();
            pz[0] = z_ld(0); pz[1] = z_ld(1);
            FRESH();
            res_loads();
            SCHED();
            BAR();
            SCHED();
            softmax2<false>(pq[1], rst[2], rst[3], g >= 2, phq[1]);
#pragma unroll
            for (int i = 0; i < 4; i++) { PIN(phq[0][i]); PIN(phq[1][i]); }
        }
        STAMPW(5, 0);
        STAMPW(26, 0);
        v2u fv = {tag ^ 0x5bd1e995u, tag};
        unsigned sF = WS_F4 + ((rt * 4 + w) * 16 + hq) * 8, vF = l == 0 ? 0 : OOB;
        {
#define PH(mi, s) phq[(mi) >> 1][2 * ((mi) & 1) + (s)]
            float id[4];
            auto kg_ld = [&](int k) { return LD16(l * 16 + k * 1024, sG3); };
            asm volatile("" : "+s"(sG3));
            SCHED();
            SLEEP(5);
            FRESH();
#pragma unroll
            for (int k = 0; k < 4; k++) kg[k] = kg_ld(k);
            FRESH();
            {
                SCHED();
                const float r = red(pz);
                if (RARE(__any(r != r))) sweepn<2>(z_ld, pz, [&]() { return red(pz); });
                const unsigned az = ldsb + L_XB + w * 4096 + 2048;
                stsu(az + ((l >> 2) & 1) * 64 + ((l & 1) * 2 + (g >> 1)) * 16 + ((l >> 1) & 1) * 8 + (g & 1) * 4, res);
                FRESH();
                const bf16x8 z0 = lds128(az + g * 16), z1 = lds128(az + g * 16 + 64);
#pragma unroll
                for (int mi = 0; mi < 4; mi++) {
                    const f32x4 dn = MFMA(z1, PH(mi, 1), MFMA(z0, PH(mi, 0), zero4));
                    id[mi] = __builtin_amdgcn_rcpf(rbf(rbf(dn[0]) + 1e-6f));
                }
                PIN(id[0]); PIN(id[1]); PIN(id[2]); PIN(id[3]);
            }
            unsigned vO = l * 16 + hh * 1024, sO = WS_P4 + ((rt * 8 + wm * 4) * 32 + hq * 2) * 1024;
            asm volatile("" : "+s"(sO), "+s"(sF));
            unsigned vo4[4];
#pragma unroll
            for (int i = 0; i < 4; i++) { vo4[i] = vO ^ (i * 32768 + (i & 1) * 1024); PIN(vo4[i]); }
            PIN(fv); PIN(vF);
            SCHED();
            v4u ov[4];
#define KVA(s, dt) __builtin_bit_cast(bf16x8, kg[2 * (s) + (dt)])
            auto pairup = [&](v4u& a, v4u& b) {
                unsigned a2 = a[2], a3 = a[3], b0 = b[0], b1 = b[1];
                asm volatile("v_swap_b32 %0, %2\n\tv_swap_b32 %1, %3" : "+v"(a2), "+v"(a3), "+v"(b0), "+v"(b1));
                a[2] = a2; a[3] = a3; b[0] = b0; b[1] = b1;
            };
            f32x4 n0[4], n1[4];
            if (__any(umx(umx(wmax(kg[0]), wmax(kg[1])), umx(wmax(kg[2]), wmax(kg[3]))) == SENT)) sweepz<4>(kg_ld, kg);
            SCHED();
            pairup(kg[0], kg[1]); pairup(kg[2], kg[3]);
            asm volatile("s_nop 1");
            SCHED();
            STAMPW(6, 0);
#pragma unroll
            for (int mi = 0; mi <= 4; mi++) {
                if (mi < 4) {
                    n0[mi] = MFMA(KVA(0, 0), PH(mi, 0), zero4); n1[mi] = MFMA(KVA(0, 1), PH(mi, 0), zero4);
                    n0[mi] = MFMA(KVA(1, 0), PH(mi, 1), n0[mi]); n1[mi] = MFMA(KVA(1, 1), PH(mi, 1), n1[mi]);
                }
                if (mi > 0) {
                    const f32x4 a = n0[mi - 1] * id[mi - 1], b = n1[mi - 1] * id[mi - 1];
                    ov[mi - 1] = (v4u){pk2(a[0], a[1]), pk2(a[2], a[3]), pk2(b[0], b[1]), pk2(b[2], b[3])};
                    ST16(ov[mi - 1], vo4[mi - 1], sO);
                }
                if (mi < 4) { SGB(0x8, 2); SGB(0x2, 3); SGB(0x8, 1); SGB(0x2, 3); SGB(0x8, 1); SGB(0x2, 4); }
                SCHED();
            }
#pragma unroll
            for (int mi = 0; mi < 4; mi++) PIN(ov[mi]);
        }
        BAR();
        waitv<0>();
        __builtin_amdgcn_raw_buffer_store_b64(fv, rsWS, vF, sF, AUX_SC1);
        STAMPW(7, 0);
    } else {
        const unsigned pw = lw & 1;
        const rsrc_t rsG = __builtin_amdgcn_make_buffer_rsrc((void*)(mat ? WU : WG), 0, 1024 * 3584 * 2, 0x00020000);
        unsigned vwl = kr * 57344 + (l & 7) * 16;
        unsigned sg = (kq * 256 + lw * 64) * 7168 + cg1 * 128;
        auto wloads = [&](int n0, int n1) {
#pragma unroll
            for (int j = n0; j < n1; j++) {
                const int n = (j & 1) * 8 + (j >> 1);
                asm volatile("buffer_load_dwordx4 %0, %1, %2, %3 offen nt" : "=v"(wreg[n]) : "v"(vwl), "s"(rsG), "s"(sg + (n >> 3) * 128 + (n & 7) * 7168));
            }
        };
        auto repack = [&](unsigned gI) {
            const unsigned s2 = (2 * g + (q4 >> 1)) & 3;
            bf16x8 f[4];
#pragma unroll
            for (int nt = 0; nt < 4; nt++) {
                const unsigned a = ldsb + L_WO + gI * 4096 + (4 * g + q4) * 128 + ((((nt >> 1) * 2 + (p4 >> 1)) ^ s2) * 32) + (p4 & 1) * 16 + (nt & 1) * 8;
                f[nt] = cat8(tr_read(a), tr_read(a + 2048));
            }
            WAITL0();
#pragma unroll
            for (int nt = 0; nt < 4; nt++) *(LAS bf16x8*)(ldsb + L_WO + gI * 4096 + nt * 1024 + l * 16) = f[nt];
        };
        unsigned aK, aW, aS;
        auto k_pre = [&]() {
            pre_addr();
            aK = ldsb + L_KH + lw * 2048 + l * 16; aW = ldsb + L_WPHI + (lw >> 1) * 2048 + (4 * g + q4) * 64 + p4 * 8; aS = ldsb + L_SD + (pw * 64 + 32 + t16) * 2;
            PIN(aK); PIN(aW); PIN(aS);
        };
        auto kfeat = [&](auto hook) {
            bf16x8 wf[2];
            float rst[2];
#pragma unroll
            for (int ft = 0; ft < 2; ft++) {
                wf[ft] = cat8(tr_read(aW + ft * 32), tr_read(aW + ft * 32 + 1024));
                rst[ft] = __builtin_amdgcn_rcpf(lds_bf(aS + ft * 32));
            }
            sd_reads();
            PIN(wf[0]); PIN(wf[1]); PIN(rst[0]); PIN(rst[1]);
            SCHED();
            ARR_MARK(16);
            BAR();
            SCHED();
            const bf16x8 kf0 = lds128(aK), kf1 = lds128(aK + 1024);
            SCHED();
            hook(-1, rst[0]);
            hook(0, rst[0]);
            SCHED();
            f32x4 p[2][2];
            p[0][0] = MFMA(wf[0], kf0, zero4); p[0][1] = MFMA(wf[1], kf0, zero4);
            p[1][0] = MFMA(wf[0], kf1, zero4); p[1][1] = MFMA(wf[1], kf1, zero4);
            MFMA_SETTLE2(p);
            bf16x8 f[4];
            softmax2<true>(p, rst[0], rst[1], g >= 2, f, hook, ImgPut{ia});
            SCHED();
            hook(5, rst[0]);
            SCHED();
            STAMPW(3, 4); STAMPW(3, 7);
            kv_tile([&]() { hook(6, rst[0]); });
            STAMPW(4, 4); STAMPW(4, 7);
        };
        auto wblk = [&](int k) {
            constexpr unsigned T[8] = {0x19181716, 0x041c1b1a, 0x08070605, 0x0c0b0a09, 0x1413120d, 0x11100015, 0x1d030201, 0x1f1e0f0e};
            return (T[k] >> (8 * lw)) & 31;
        };
        auto wo_init = [&]() {
#pragma unroll
            for (int i = 0; i < 4; i++) { vwo[i] = vWO + i * (16384 - 1024); PIN(vwo[i]); }
        };
        unsigned hs[8];
        auto wl_pre = [&]() { asm volatile("" : "+s"(sg)); PIN(vwl); };
        auto wo_pre = [&]() {
            wo_init();
#pragma unroll
            for (int k = 0; k < 8; k++) { hs[k] = wo_so(wblk(k)); asm volatile("" : "+s"(hs[k])); }
        };
        auto wo_rng = [&](int k0, int k1) {
#pragma unroll
            for (int k = k0; k < k1; k++) dma16x4(ldsb + L_WO + wblk(k) * 4096, vwo, rsWO, hs[k]);
        };
        auto pf = [&](int i) {
            if (i == 0) { wloads(0, 6); SCHED(); wo_pre(); SCHED(); }
            if (i < 3 && !ffn) wo_rng(i, i + 1);
            if (i == 1 || i == 2) wloads(6 * i, i == 1 ? 12 : 16);
        };
        struct Hook {
            decltype(pf)& f; decltype(v_reads)& vr;
            DI void operator()(int k, float& x) const { if (k == 10) return; PIN(x); if (k == 3) { ARR_MARK(17); BAR(); vr(); } f(3 + k); PIN(x); }
            DI void operator()(int k, float& x, float& y) const { PIN(y); f(3 + k); PIN(x); }
        };
        auto tail16 = [&]() {
            kfeat(Hook{pf, v_reads});
            if (ffn) wo_rng(0, 3);
            wo_rng(3, 5);
            static_for<3>([&](auto kc_) {
                constexpr int k = decltype(kc_)::value;
                waitv<4 * (4 - k)>();
                repack(wblk(k));
            });
            BAR();
            res_loads();
            wo_rng(5, 7);
            static_for<4>([&](auto kc_) {
                constexpr int k = decltype(kc_)::value + 3;
                waitv<4 * (6 - k)>();
                repack(wblk(k));
            });
            PIN(wrv[0]); PIN(wrv[1]);
            perm1();
            SCHED();
            BAR();
            wo_rng(7, 8);
        };
        auto vT23 = [&]() {
            const unsigned n = (idx * 2 + pw) * 64 + l, r = n / 12, c = n % 12;
            return idx < 24 ? ((r & 127) + (r >> 7) * 256 + 128) * 6144 + (c >> 2) * 2048 + (((xcd >> 1) * 4 + (c >> 2 ? 0 : 4) + (c & 3)) & 15) * 128 : OOB;
        };
        if (lw < 2) {
            const unsigned sA = (rt * 128 + lw * 64) * 2048, sB = lw * 32 * 6144 + hp * 128, sBq = lw * 32 * 6144 + hq * 128;
            unsigned buf = 1;
            unsigned va[4], vb[4];
#pragma unroll
            for (int i = 0; i < 4; i++) { va[i] = vA + i * (16384 - 1024); vb[i] = ((i & 1) ? vB1 : vB0) + i * (49152 - 1024); PIN(va[i]); PIN(vb[i]); }
            auto stage = [&](unsigned s, unsigned bf, int i0, int i1) {
                const unsigned kt = (s + rot) & 15;
                const unsigned lb = ldsb + bf * STAGE_B;
                const unsigned sa = sA + kt * 128, sb = kt * 393216;
                if (i0 <= 0 && 0 < i1) dma16x4(lb + lw * 8192, va, rsX, sa);
                if (i0 <= 1 && 1 < i1) dma16x4(lb + lw * 8192 + 4096, va, rsX, sa + 65536);
                if (i0 <= 2 && 2 < i1) dma16x4(lb + 16384 + 8192 + lw * 4096, vb, rsW, sb + sB + 2048);
                if (i0 <= 3 && 3 < i1) dma16x4(lb + 16384 + 16384 + lw * 4096, vb, rsW, sb + sB + 4096);
                if (i0 <= 4 && 4 < i1) dma16x4(lb + 16384 + lw * 4096, vb, rsW, sb + sBq);
            };
            stage(1, 1, 0, 4);
            SCHED();
            unsigned vt = vT23();
            PIN(vt);
            wl_pre(); k_pre();
            waitv<16>(); ARR_MARK(0); BAR();
            stage(1, 1, 4, 5);
            touch(vt, rsW, (xcd & 1) * 512 * 6144);
            waitv<1>(); ARR_MARK(1); BAR();
            stage(2, 2, 0, 5); stage(3, 0, 0, 3);
            waitv<12>(); ARR_MARK(2); BAR();
            stage(3, 0, 3, 5); stage(4, 1, 0, 4);
            waitv<16>(); ARR_MARK(3); BAR();
            stage(4, 1, 4, 5);
            buf = 2;
#pragma nounroll
            for (unsigned s = 5; s < 16; s++) {
                if (s > 5) { waitv<20>(); ARR_MARK(s - 2); BAR(); }
                stage(s, buf, 0, 5);
                buf = buf == 2 ? 0 : buf + 1;
            }
            READ_TAG();
            waitv<20>();
        } else {
            SCHED();
            unsigned v3;
            const unsigned h3 = (xcd >> 1) * 4 + 3, kv3 = !(xcd & 2), wv = idx * 2 + pw;
            auto line3 = [&](unsigned per, unsigned rows, auto rowf) {
                const unsigned n = wv * (kv3 ? per : (per + 2) / 3) + l, r3 = kv3 ? n / 3 : n, c3 = kv3 ? n % 3 : 0;
                return (idx < 24 && l < (kv3 ? per : (per + 2) / 3) && r3 < rows) ? rowf(r3) * 6144 + c3 * 2048 + ((c3 ? h3 : h3 + 4) & 15) * 128 : OOB;
            };
            v3 = line3(32, 512, [](unsigned r) { return r; });
            touch(line3(16, 256, [](unsigned r) { return (r & 127) + (r >> 7) * 256; }), rsW, (xcd & 1) * 512 * 6144);
            {
                const rsrc_t rsP = __builtin_amdgcn_make_buffer_rsrc((void*)WPHI, 0, 32 * 1024 * 2, 0x00020000);
#pragma unroll
                for (int i = 0; i < 4; i++) dma16(ldsb + L_WPHI + pw * 4096 + i * 1024, l * 16, rsP, (pw ? hq : hp) * 4096 + i * 1024);
            }
            v4u sv = {SENT, SENT, SENT, SENT};
            unsigned vc4 = l < 16 ? WS_G3 + (hp * 4 + (rt >> 2)) * 2048 + pw * 1024 + (rt & 3) * 256 + l * 16
                         : l < 24 ? WS_P2 + (hp * 16 + rt) * P2_STRIDE + 8192 + pw * 128 + (l - 16) * 16 : OOB;
            auto clears = [&](int i0, int i1) {
#pragma unroll
                for (int i = i0; i < i1; i++) {
                    if (i < 4) ST16(sv, l * 16, WS_P2 + (hp * 16 + rt) * P2_STRIDE + pw * 4096 + i * 1024);
                    if (i == 4) ST16(sv, vc4, 0);
                    if (i > 4) ST16(sv, l * 16, WS_TL + (bid * 4 + pw * 2 + i - 5) * 1024);
                }
                FRESH();
            };
            PIN(vc4); PIN(sv);
            unsigned ca = ldsb + 16384 + pw * 8192 + (8 * g + q4) * 128 + (((rt & 3) ^ (((q4 >> 1) & 1) | ((g & 1) << 1))) * 32) + p4 * 8;
            PIN(ca);
            bf16x8 cf[4];
            f32x4 cd[2] = {zero4, zero4};
            auto cs_read = [&](int st) {
#pragma unroll
                for (int i = 0; i < 4; i++) cf[i] = cat8(tr_read(ca + (st % 3) * STAGE_B + i * 4096), tr_read(ca + (st % 3) * STAGE_B + i * 4096 + 512));
                SCHED();
            };
            READ_TAG();
            waitv<5>();
            ARR_MARK(0);
            BAR();
            SCHED();
            cs_read(0);
            FRESH();
            const rsrc_t rsC = __builtin_amdgcn_make_buffer_rsrc((void*)pc0, 0, clen, 0x00020000);
            auto code_pf = [&]() { touch(l < 4 ? l * 128 : OOB, rsC, (idx * 2 + pw) * 512); };
            v4u gl[4];
            float s0 = 0.f, s1 = 0.f;
            unsigned bad;
            v2u gs, gc;
            unsigned vgl = (l < 48 ? l : 0) * 16;
            unsigned aw0 = ldsb + L_CS + pw * 384 + (vgl >> 1), aw1 = ldsb + L_MB + (pw * 64 + l) * 4, aw2 = ldsb + L_SD + (pw * 64 + l) * 2;
            static_for<13>([&](auto tc) {
                constexpr int t = decltype(tc)::value;
                WAITL0();
                ARR_MARK(t + 1);
                BAR();
                SCHED();
                if constexpr (t < 4) {
                    __builtin_amdgcn_s_setprio(2);
#pragma unroll
                    for (int i = 0; i < 4; i++) cd[i & 1] = MFMA(ones, cf[(i & 1) * 2 + (i >> 1)], cd[i & 1]);
                    SCHED();
                    __builtin_amdgcn_s_setprio(0);
                    if constexpr (t < 3) cs_read(t + 1);
                    else __builtin_amdgcn_raw_buffer_store_b64((v2u){__float_as_uint(g ? cd[1][0] : cd[0][0]), tag}, rsWS, (g == 1 || g + pw == 0) ? g * 512 + t16 * 8 : OOB,
                                                               WS_G1 + ((hp * 4 + (rt >> 2)) * 192 + pw * 64 + (rt & 3) * 16) * 8, AUX_SC1);
                }
                if constexpr (t == 0) touch(v3, rsW, (~xcd & 1) * 512 * 6144);
                if constexpr (t == 12) code_pf();
                if constexpr (t == 8) {
                    waitv<0>();
                    __builtin_amdgcn_raw_buffer_store_b64((v2u){0u, tag}, rsWS, l == 0 ? 0 : OOB, WS_GC + ((hp * 2 + pw) * 16 + rt) * 8, AUX_SC1);
                }
                if constexpr (t == 8 || t == 9) {
#pragma unroll
                    for (int r = (t - 8) * 2; r < (t - 8) * 2 + 2; r++)
                        asm volatile("buffer_load_dwordx4 %0, %1, %2, %3 offen sc1" : "=v"(gl[r]) : "v"(vgl), "s"(rsWS), "s"(WS_G1 + ((hp * 4 + r) * 192 + pw * 96) * 8) : "memory");
                }
                if constexpr (t == 6) {
                    asm volatile("buffer_load_dwordx2 %0, %1, %2, %3 offen sc1" : "=v"(gs) : "v"(l * 8), "s"(rsWS), "s"(WS_G0 + (rt * 128 + pw * 64) * 8) : "memory");
                }
                if constexpr (t == 11) {
                    waitv<0>();
                    PIN(gs);
                    bad = gs[1] ^ tag;
#pragma unroll
                    for (int r = 0; r < 4; r++) {
                        PIN(gl[r]);
                        bad |= (gl[r][1] ^ tag) | (gl[r][3] ^ tag);
                        s0 += __uint_as_float(gl[r][0]); s1 += __uint_as_float(gl[r][2]);
                    }
                    PIN(s0); PIN(s1); PIN(bad);
                }
                if constexpr (t == 12) {
                    if (RARE(!__all(bad == 0))) {
                        s0 = 0.f; s1 = 0.f;
#pragma nounroll
                        for (unsigned r = 0; r < 4; r++) {
                            unsigned a[2];
                            sweep<2>(rsWS, vgl, WS_G1 + ((hp * 4 + r) * 192 + pw * 96) * 8, 8, tag, a);
                            s0 += __uint_as_float(a[0]); s1 += __uint_as_float(a[1]);
                        }
                        unsigned c[1];
                        sweep<1>(rsWS, l * 8, WS_G0 + (rt * 128 + pw * 64) * 8, 0, tag, c);
                        gs[0] = c[0];
                    }
                    *(LAS f32x2*)(aw0) = (f32x2){-s0, -s1};
                    stsf(aw1, bflo(gs[0]));
                    *(LAS u16*)(aw2) = (u16)(gs[0] >> 16);
                    SCHED();
                }
                if constexpr (t == 5) k_pre();
                if constexpr (t == 6) wl_pre();
                if constexpr (t == 7) { PIN(vgl); PIN(aw0); PIN(aw1); PIN(aw2); }
                clears(t < 7 ? t : 7, t < 7 ? t + 1 : 7);
                if constexpr (t == 11) asm volatile("buffer_load_dwordx2 %0, %1, %2, %3 offen sc1" : "=v"(gc) : "v"((l & 31) * 8), "s"(rsWS), "s"(WS_GC + (pw ? hq : hp) * 256) : "memory");
                if constexpr (t == 12) {
                    waitv<1>();
                    PIN(gc);
                    if (RARE(!__all(gc[1] == tag))) { unsigned c[1]; sweep<1>(rsWS, (l & 31) * 8, WS_GC + (pw ? hq : hp) * 256, 0, tag, c); }
                    SCHED();
                }
            });
            WAITL0();
        }
        ARR_MARK(14); BAR();
        SCHED();
        pf(0);
        if (ffn) waitv<6>(); else waitv<10>();
        ARR_MARK(15); BAR();
        pf(1);
        tail16();
        WAITL0();
        STAMPW(7, 4); STAMPW(7, 7);
    }
    const unsigned sFl = WS_F4 + (rt2 * 4 + (lw >> 1) + 2 * half) * 128;
    vD = kr * 16384 + (l & 7) * 16; sD = (kc * 256 + lw * 64) * 2048 + cg * 128;
    PIN(vD);
    asm volatile("" : "+s"(sD), "+s"(rsD));
    if (half) dloads(0, 1);
    SCHED();
    ARR_MARK(20);
    BAR();
    ARR_END();
    const unsigned wb = ldsb + L_WO + half * 65536 + l * 16;
    bf16x8 bb[2][4];
    v4u xres[2];
#pragma unroll
    for (int nt = 0; nt < 4; nt++) {
        if (nt < 2) xres[nt] = *(const LAS v4u*)(ldsb + L_XB + w * 4096 + t16 * 128 + (((nt * 4 + g) ^ (l & 7)) * 16));
        bb[0][nt] = lds128(wb + nt * 1024);
        PIN(bb[0][nt]);
    }
    PIN(xres[0]); PIN(xres[1]);
    unsigned so[16], va0 = l * 16 + half * 32768, va1 = l * 16 + (half ^ 1) * 32768 + 1024;
    unsigned aXw = ldsb + L_XB + w * 4096 + l * 16, aXr = ldsb + L_XB + (w ^ 4) * 4096 + l * 16, sG5 = WS_G5 + ((rt2 * 8 + lw * 2 + half) * 16 + ct) * 256;
    unsigned vG5 = g == q4 ? t16 * 16 : OOB;
#pragma unroll
    for (int st = 0; st < 16; st++) { so[st] = WS_P4 + (rt2 * 8 + lw * 2) * 32768 + ((2 * st + rot2) & 31) * 1024; asm volatile("" : "+s"(so[st])); }
    unsigned wt[2];
#pragma unroll
    for (int i = 0; i < 2; i++) { wt[i] = ldsb + L_WO + half * 65536 + (4 * g + q4) * 128 + (((i * 2 + (p4 >> 1)) ^ ((2 * g + (q4 >> 1)) & 3)) * 32) + (p4 & 1) * 16; PIN(wt[i]); }
    PIN(va0); PIN(va1); PIN(aXw); PIN(aXr); PIN(vG5);
    asm volatile("" : "+s"(sG5));
    SLEEP(13);
    poll16(rsWS, sFl, tag, l);
    FRESH();
    SCHED();
    STAMPW(8, 0); STAMPW(8, 4);
    unsigned swc;
    f32x4 acc2[2][4];
    bf16x8 sop[2];
    {
        auto aload = [&](int st, int m2) { return LD16(m2 ? va1 : va0, so[st]); };
        constexpr int PD = 2;
        v4u af[PD][2];
#pragma unroll
        for (int i = 0; i < PD; i++) {
#pragma unroll
            for (int m2 = 1; m2 >= 0; m2--) af[i][m2] = aload(i, m2);
            SCHED();
        }
#pragma unroll
        for (int nt = 0; nt < 4; nt++) {
            const unsigned x0 = xres[nt >> 1][2 * (nt & 1)], x1 = xres[nt >> 1][2 * (nt & 1) + 1];
            acc2[0][nt] = (f32x4){bflo(x0), bfhi(x0), bflo(x1), bfhi(x1)};
            acc2[1][nt] = zero4;
        }
        unsigned m0 = p4 == 0 ? 0x3F803F80u : 0u, m1 = p4 == 1 ? ~0u : 0u;
        PIN(m0); PIN(m1);
#pragma unroll
        for (int i = 0; i < 2; i++) {
            v4u u;
#pragma unroll
            for (int j = 0; j < 4; j++) u[j] = (wrv[i][j] & m1) | m0;
            sop[i] = __builtin_bit_cast(bf16x8, u);
            PIN(sop[i]);
        }
        {
            float sw = 0.f;
#pragma unroll
            for (int i = 0; i < 8; i++) sw = dot2(wrv[i >> 2][i & 3], 0x3F803F80u, sw);
            swc = __float_as_uint(xrow_sum(sw));
            PIN(swc);
        }
        SCHED();
#pragma unroll
        for (int st = 0; st < 16; st++) {
            if (st == 2) BAR();
            if (st + 1 < 16) {
#pragma unroll
                for (int nt = 0; nt < 4; nt++) {
                    if (st + 1 < 14) bb[(st + 1) % (2)][nt] = lds128(wb + (st + 1) * 4096 + nt * 1024);
                    else bb[(st + 1) % (2)][nt] = cat8(tr_read(wt[nt >> 1] + (st + 1) * 4096 + (nt & 1) * 8), tr_read(wt[nt >> 1] + (st + 1) * 4096 + (nt & 1) * 8 + 2048));
                }
            }
#pragma unroll
            for (int mm = 0; mm < 2; mm++) {
                const int m2 = mm ^ 1;
                const bf16x8 a = __builtin_bit_cast(bf16x8, af[st % PD][m2]);
#pragma unroll
                for (int nt = 0; nt < 4; nt++) acc2[m2][nt] = MFMA(bb[st % (2)][nt], a, acc2[m2][nt]);
            }
            if (st + PD < 16) {
#pragma unroll
                for (int m2 = 1; m2 >= 0; m2--) af[st % PD][m2] = aload(PD + st, m2);
            }
#pragma unroll
            for (int i = 0; i < (st + 1 < 14 ? 4 : 8); i++) { SGB(0x8, 1); SGB(0x100, 1); }
            SCHED();
        }
    }
    STAMPW(9, 0); STAMPW(9, 4);
#pragma unroll
    for (int nt = 0; nt < 4; nt++) stsf4(aXw + nt * 1024, acc2[1][nt]);
    SCHED();
    if (half) dloads(1, 4);
    SCHED();
    WAITL0();
    if (half) __builtin_amdgcn_s_setprio(1);
    BAR();
    SCHED();
    v4u hpk[2];
    {
        f32x4 pa[4];
#pragma unroll
        for (int nt = 0; nt < 4; nt++) pa[nt] = ldsf4(aXr + nt * 1024);
        SCHED();
#pragma unroll
        for (int i = 0; i < 8; i++) {
            const f32x4 a = acc2[0][i >> 1] + pa[i >> 1];
            hpk[i >> 2][i & 3] = pk2(a[2 * (i & 1)], a[2 * (i & 1) + 1]);
        }
        const bf16x8 h0 = __builtin_bit_cast(bf16x8, hpk[0]), h1 = __builtin_bit_cast(bf16x8, hpk[1]);
        f32x4 gq = MFMA(h0, h0, zero4), sd = MFMA(sop[0], h0, zero4);
        gq = MFMA(h1, h1, gq); sd = MFMA(sop[1], h1, sd);
        float q0 = (l & 1) ? gq[1] : gq[0], q1 = (l & 1) ? gq[3] : gq[2];
        PIN(q0); PIN(q1);
        const v4u pv = {__float_as_uint(sd[0]), __float_as_uint((l & 2) ? q1 : q0), __float_as_uint(sd[1]), swc};
        ST16(pv, vG5, sG5);
        asm volatile("s_nop 1" :: "v"(pv));
    }
    __builtin_amdgcn_s_setprio(0);
    STAMPW(10, 0); STAMPW(10, 4);
    v4u wp2[8];
    auto perm2 = [&]() {
#pragma unroll
        for (int i = 16; i < 24; i++) PIN(wreg[i]);
#pragma unroll
        for (int ip = 0; ip < 4; ip++)
#pragma unroll
            for (int c = 0; c < 4; c++) {
                const unsigned a = wreg[16 + 2 * ip][c], b = wreg[16 + 2 * ip + 1][c];
                wp2[2 * c][ip] = __builtin_amdgcn_perm(b, a, 0x05040100);
                wp2[2 * c + 1][ip] = __builtin_amdgcn_perm(b, a, 0x07060302);
            }
#pragma unroll
        for (int c = 0; c < 8; c++) PIN(wp2[c]);
    };
    const unsigned mt = rt2 * 8 + lw * 2 + half;
    {
        v4u pv[4];
        unsigned sK = WS_GK + (ct * 128 + mt) * 4, vK = l == 0 ? 0 : OOB, rk = 2047u - R2;
        asm volatile("" : "+s"(sK));
        PIN(vK); PIN(rk);
        unsigned sS = WS_G5 + mt * 4096;
        asm volatile("" : "+s"(sS));
        SLEEP(18);
        __builtin_amdgcn_s_setprio(1);
        float T[4];
        sweepn<4>([&](int k) { return LD16(l * 16 + k * 1024, sS); }, pv, [&]() {
            const f32x4 ps = (__builtin_bit_cast(f32x4, pv[0]) + __builtin_bit_cast(f32x4, pv[1])) + (__builtin_bit_cast(f32x4, pv[2]) + __builtin_bit_cast(f32x4, pv[3]));
            const float r = xrow_scatter<false>(ps[0], ps[1], ps[2], ps[3]);
            xrow_gather(r, T);
            return r;
        });
        STAMPW(21, 0); STAMPW(21, 4);
        const float mean = T[0] * (1.0f / 1024.0f), var = fmaxf(T[1] * (1.0f / 1024.0f) - mean * mean, 0.f);
        const unsigned sg = pk2(mean, __builtin_amdgcn_sqrtf(rbf(rbf(var) + 1e-5f)));
        const float mb2 = bflo(sg), isd2 = __builtin_amdgcn_rcpf(bfhi(sg));
        const float rsc = (T[2] - mb2 * T[3]) * isd2;
        const unsigned rb = pk2(rsc, rsc);
        const unsigned key = ((rb ^ ((unsigned)((int)rb >> 31) | 0x80000000u)) & 0xffff0000u) | rk;
        unsigned kb = umx(key, dppu<0x128>(key));
        kb = umx(kb, dppu<0x124>(kb)); kb = umx(kb, dppu<0x122>(kb)); kb = umx(kb, dppu<0x121>(kb));
        ST4(kb, vK, sK);
        SCHED();
        __builtin_amdgcn_s_setprio(0);
        if (half) { dloads(4, 8); SCHED(); }
        const bool cand = key == kb;
        v4u e[2], outv[2];
#pragma unroll
        for (int i = 0; i < 8; i++) {
            const float h0 = bflo(hpk[i >> 2][i & 3]), h1 = bfhi(hpk[i >> 2][i & 3]);
            const unsigned d = pk2(h0 - mb2, h1 - mb2);
            e[i >> 2][i & 3] = pk2(bflo(d) * isd2, bfhi(d) * isd2);
        }
        ST16(e[0], cand ? g * 16 : OOB, WS_C + (mt * 16 + ct) * 128);
        ST16(e[1], cand ? g * 16 + 64 : OOB, WS_C + (mt * 16 + ct) * 128);
        SCHED();
        STAMPW(22, 0); STAMPW(22, 4);
#pragma unroll
        for (int i = 0; i < 8; i++)
            outv[i >> 2][i & 3] = pk2(bflo(hpk[i >> 2][i & 3]) + bflo(e[i >> 2][i & 3]), bfhi(hpk[i >> 2][i & 3]) + bfhi(e[i >> 2][i & 3]));
        if (cand) {
#pragma unroll
            for (int i = 0; i < 2; i++) { *(LAS v4u*)(ldsb + L_STASH + w * 256 + i * 64 + g * 16) = hpk[i]; *(LAS v4u*)(ldsb + L_STASH + w * 256 + 128 + i * 64 + g * 16) = e[i]; }
        }
#pragma unroll
        for (int i = 0; i < 2; i++) *(LAS v4u*)(ldsb + L_OUT + (lw * 2 + half) * 2048 + t16 * 128 + (((i * 4 + g) ^ (l & 7)) * 16)) = outv[i];
    }
    const rsrc_t rsO = __builtin_amdgcn_make_buffer_rsrc((void*)OUT, 0, 2048 * 1024 * 2, 0x00020000);
    unsigned aG = ldsb + L_WO + 512;
    PIN(aG);
    unsigned vX = (2 * g + (l & 1)) * 16, aP = (2 * g + (l & 1)) * 32, aR1 = ldsb + L_RED + lw * 4352 + l * 16, vl4 = l * 4;
    unsigned aR2 = ldsb + L_RED + ((lw & 1) * 2 + ((l >> 2) & 1)) * 1088 + kr * 16 + p4 * 4;
    unsigned sX = WS_C + kq * 512 + lw * 128, sG8w = WS_G8 + ((kq * 2 + mat) * 3584 + (cg1 + lw) * 64) * 4, sG8r = WS_G8 + (kc * 256 + lw * 64) * 4;
    unsigned sG9 = WS_G9 + (cg * 14 + kc) * 256;
    unsigned aR3 = aR1 + 17408 - lw * 2176;
    PIN(vX); PIN(aP); PIN(aR1); PIN(aR2); PIN(aR3); PIN(vl4);
    asm volatile("" : "+s"(sX), "+s"(sG8w), "+s"(sG8r), "+s"(sG9));
    SCHED();
    if (w == 1) {
        unsigned sK = WS_GK + (bid & 15) * 512;
        v2u hb[1];
        asm volatile("" : "+s"(sK));
        SLEEP(13);
        stsu(aG, wave_maxu(sweepz<1>([&](int) { return ldg(rsWS, l * 8, sK); }, hb)));
        STAMPR(23, 2);
    }
    if (half) perm2();
    STAMPW(24, 7);
    WAITL0();
    if (half) __builtin_amdgcn_s_setprio(1);
    BAR();
    STAMPW(11, 0); STAMPW(11, 4);
    const unsigned gkv = ldsu(aG);
    if (w < 4) {
        if (bid == 0 && tid == 0) __hip_atomic_store((gu32*)(ws + WS_EPOCH), tag, RLX_AGENT);
        v4u o2[4];
        unsigned vo[4];
#pragma unroll
        for (int i = 0; i < 4; i++) o2[i] = *(const LAS v4u*)(ldsb + L_OUT + w * 4096 + i * 1024 + kr * 128 + (((l & 7) ^ kr) * 16));
#pragma unroll
        for (int i = 0; i < 4; i++) { const unsigned R = mt * 16 + i * 8 + kr; vo[i] = 2047u - R != (gkv & 2047u) ? R * 2048 + (l & 7) * 16 : OOB; PIN(vo[i]); PIN(o2[i]); }
#pragma unroll
        for (int s = 0; s < 3; s++) {
            if (s) { BAR(); SLEEP(3); }
            SCHED();
#pragma unroll
            for (int i = 0; i < 4; i++)
                if ((i + 1) / 2 == s) __builtin_amdgcn_raw_buffer_store_b128(o2[i], rsO, vo[i], ct * 128, AUX_SC1);
            SCHED();
        }
        STAMPW(12, 0);
        return;
    }

    auto gemv = [&](const v4u& x, const v4u* wp, int n, unsigned a) {
        f32x4 d[16], o[4];
#pragma unroll
        for (int c = 0; c < n + 4; c++) {
            if (c < n) d[c] = MFMA(__builtin_bit_cast(bf16x8, x), __builtin_bit_cast(bf16x8, wp[c]), zero4);
            SCHED();
            if (c >= 4) {
                const int j = c - 4;
                float t = d[j][0] + dppf<0x128>(d[j][1]);
                asm volatile("" : "+v"(t) : "v"(d[j]));
                o[j >> 2][j & 3] = t;
                if ((j & 3) == 3) stsf4(a + (j >> 2) * 1088, o[j >> 2]);
            }
            SCHED();
        }
        return o[0][0];
    };
    auto sum4 = [&](unsigned a, unsigned stride) {
        float u = ldsf(a) + ldsf(a + stride), v = ldsf(a + 2 * stride) + ldsf(a + 3 * stride);
        PIN(u); PIN(v);
        return u + v;
    };
    if (ffn) {
        v4u xq[1];
        const unsigned vx = ((~gkv & 0x7f0u) << 7) + vX;
        sweepn<1>([&](int) { return LD16(vx, sX); }, xq, [&]() { return gemv(xq[0], &wp1[0][0], 16, aR1); });
        STAMPW(13, 4);
    }
    WAITL0();
    BAR();
    if (ffn) {
        if (lw < 2) ST4(__float_as_uint(sum4(aR2, 4352)), vl4, sG8w);
        STAMPW(14, 4);
        unsigned pv[8];
        v4u ap;
        SLEEP(12);
        sweepn<8>([&](int k) { return LD4(vl4, sG8r + k * 3584 * 4); }, pv, [&]() {
            const float gg = rbf((__uint_as_float(pv[0]) + __uint_as_float(pv[2])) + (__uint_as_float(pv[4]) + __uint_as_float(pv[6])));
            const float uu = rbf((__uint_as_float(pv[1]) + __uint_as_float(pv[3])) + (__uint_as_float(pv[5]) + __uint_as_float(pv[7])));
            const float act = rbf(gg * __builtin_amdgcn_rcpf(1.0f + __expf(-gg))) * uu;
            const unsigned apk = pk2(act, dppf<0xB1>(act));
#pragma unroll
            for (int ip = 0; ip < 4; ip++) ap[ip] = __builtin_amdgcn_ds_bpermute(aP + ip * 8, apk);
            return act;
        });
        STAMPW(15, 4);
        gemv(ap, wp2, 8, aR3);
    }
    WAITL0();
    BAR();
    if (lw != 0) return;
    if (ffn) ST4(__float_as_uint(sum4(aR2 + 17408, 2176)), vl4, sG9);
    SCHED();
    STAMPW(16, 4);
    const unsigned gkey = rfl(gkv), srow = 2047u - (gkey & 2047u);
    if ((srow >> 7) == rt2) {
        const float rs = __uint_as_float(((gkey & 0x80000000u) ? gkey ^ 0x80000000u : ~gkey) & 0xffff0000u);
        float sig = rbf(1.0f / (1.0f + __expf(-rs)));
        const unsigned sb = ldsb + L_STASH + (((srow >> 5) & 3) + ((srow >> 4) & 1) * 4) * 256 + (t16 * 4 + g) * 2;
        float hc = lds_bf(sb), lc = lds_bf(sb + 128);
        unsigned vo = (t16 * 4 + g) * 2, so_ = srow * 2048 + ct * 128, vy = l < 32 ? l * 16 + 3072 : OOB, sY = WS_G9 + ct * 3584;
        PIN(sig); PIN(hc); PIN(lc); PIN(vo); PIN(vy);
        asm volatile("" : "+s"(so_), "+s"(sY));
        v4u yp[4];
        float y;
        SLEEP(7);
        sweepn<4>([&](int k) { return LD16(k < 3 ? l * 16 + k * 1024 : vy, sY); }, yp, [&]() {
            const f32x4 ys = (__builtin_bit_cast(f32x4, yp[0]) + __builtin_bit_cast(f32x4, yp[1])) + (__builtin_bit_cast(f32x4, yp[2]) + __builtin_bit_cast(f32x4, yp[3]));
            y = xrow_scatter<false>(ys[0], ys[1], ys[2], ys[3]);
            return y;
        });
        __builtin_amdgcn_raw_buffer_store_b16((u16)(pk2(hc + rbf(lc + rbf(rbf(y) * sig)), 0.f) & 0xffffu), rsO, vo, so_, 0);
        STAMPW(17, 4);
    }
}
__device__ __attribute__((used, noinline)) void fk_tail() { asm volatile(".Lfk_end:"); }

extern "C" void kernel_launch(void* const* d_in, const int* in_sizes, int n_in, void* d_out, int out_size, void* d_ws,
                              size_t ws_size, hipStream_t stream) {
    (void)in_sizes; (void)n_in; (void)out_size; (void)ws_size;
    (void)hipFuncSetAttribute((const void*)fused_kernel, hipFuncAttributeMaxDynamicSharedMemorySize, LDS_BYTES);
    fused_kernel<<<256, 512, LDS_BYTES, stream>>>((const u16*)d_in[0], (const u16*)d_in[1], (const u16*)d_in[2], (const u16*)d_in[3],
                                                  (const u16*)d_in[5], (const u16*)d_in[6], (const u16*)d_in[7], (const u16*)d_in[8],
                                                  (u16*)d_out, (char*)d_ws);
}
```

```cpp
#include <hip/hip_runtime.h>
#include <stdint.h>
#include <utility>

typedef unsigned short u16;
typedef unsigned long long u64;
typedef short bf16x8 __attribute__((ext_vector_type(8)));
typedef short s16x4 __attribute__((ext_vector_type(4)));
typedef float f32x4 __attribute__((ext_vector_type(4)));
typedef float f32x2 __attribute__((ext_vector_type(2)));
typedef __bf16 bf16x2 __attribute__((ext_vector_type(2)));
typedef unsigned v4u __attribute__((ext_vector_type(4)));
typedef unsigned v2u __attribute__((ext_vector_type(2)));
#define LAS __attribute__((address_space(3)))
#define GAS __attribute__((address_space(1)))
#define CAS __attribute__((address_space(4)))
typedef GAS u64 gu64;
typedef GAS unsigned gu32;
typedef __amdgpu_buffer_rsrc_t rsrc_t;
#define DI __device__ __forceinline__
#define RLX_AGENT __ATOMIC_RELAXED, __HIP_MEMORY_SCOPE_AGENT
#define BAR() __builtin_amdgcn_s_barrier()
#define SCHED() __builtin_amdgcn_sched_barrier(0)
#define SGB(m, n) __builtin_amdgcn_sched_group_barrier((m), (n), 0)
#define WAITL0() asm volatile("s_waitcnt lgkmcnt(0)" ::: "memory")
#define WL0() __builtin_amdgcn_s_waitcnt(0xc07f)
#define MFMA(a, b, c) __builtin_amdgcn_mfma_f32_16x16x32_bf16((a), (b), (c), 0, 0, 0)
#define ORDER_BEHIND_POLL() __builtin_amdgcn_fence(__ATOMIC_ACQUIRE, "wavefront")
#define AUX_SC1 16
template <int N> DI void waitv() { asm volatile("s_waitcnt vmcnt(%0)" ::"i"(N) : "memory"); }

#define WS_EPOCH 0
#define WS_G0 0x1000
#define WS_G1 0x8000
#define WS_F4 0x78000
#define WS_GC 0x70000
#define WS_G3 0x80000
#define SENT 0xFFFFFFFFu
#define WS_TL 0x100000
#define WS_G5 0x100000
#define WS_G8 0x180000
#define WS_G9 0x1A0000
#define WS_GK 0x1B4000
#define WS_C 0x1C0000
#define WS_P2 0x200000
#define P2_STRIDE 0x2180
#define WS_P4 0x700000
#define WS_USED 0xB00000
#define WS_STAMP 0x1000000

#define STAGE_B 40960
#define LDS_BYTES 163840
#define L_WO 32768
#define L_MB 161792
#define L_WPHI (L_MB - 8192)
#define L_SD (L_MB + 512)
#define L_CS (L_MB + 1024)
#define L_V L_WO
#define L_KH (L_WO + 65536)
#define L_XB 0
#define L_STASH (L_WO + 1024)
#define L_OUT (L_WO + 8192)
#define L_RED 4096

#define SPIN_CAP 20000u
#define W_AUX 2
#define LOG2E 1.4426950408889634f

DI unsigned rfl(unsigned x) { return __builtin_amdgcn_readfirstlane(x); }
DI float bf2f(unsigned u) { return __uint_as_float(u << 16); }
DI float bflo(unsigned u) { return __uint_as_float(u << 16); }
DI float bfhi(unsigned u) { return __uint_as_float(u & 0xffff0000u); }
DI unsigned pk2(float lo, float hi) { f32x2 v = {lo, hi}; return __builtin_bit_cast(unsigned, __builtin_convertvector(v, bf16x2)); }
DI unsigned nsent(unsigned x) { return __builtin_elementwise_min(x, SENT - 1); }
DI unsigned umx(unsigned a, unsigned b) { return __builtin_elementwise_max(a, b); }
DI float rbf(float f) { return bflo(pk2(f, f) & 0xffffu); }
DI bf16x8 mk8(unsigned a, unsigned b, unsigned c, unsigned d) { v4u v = {a, b, c, d}; return __builtin_bit_cast(bf16x8, v); }
DI void dma16(unsigned ldsaddr, unsigned voff, rsrc_t rs, unsigned soff) {
    asm volatile("s_mov_b32 m0, %0\n\ts_nop 0\n\tbuffer_load_dwordx4 %1, %2, %3 offen lds" ::"s"(ldsaddr), "v"(voff), "s"(rs), "s"(soff) : "memory");
}
DI void dma16x4(unsigned ldsaddr, const unsigned* v, rsrc_t rs, unsigned soff) {
    asm volatile("s_mov_b32 m0, %0\n\ts_nop 0\n\tbuffer_load_dwordx4 %1, %5, %6 offen lds\n\tbuffer_load_dwordx4 %2, %5, %6 offen offset:1024 lds\n\t"
                 "buffer_load_dwordx4 %3, %5, %6 offen offset:2048 lds\n\tbuffer_load_dwordx4 %4, %5, %6 offen offset:3072 lds"
                 ::"s"(ldsaddr), "v"(v[0]), "v"(v[1]), "v"(v[2]), "v"(v[3]), "s"(rs), "s"(soff) : "memory");
}
DI void touch(unsigned voff, rsrc_t rs, unsigned soff) { asm volatile("buffer_load_dword v255, %0, %1, %2 offen" ::"v"(voff), "s"(rs), "s"(soff) : "memory", "v255"); }
DI v4u ld16_asm(unsigned voff, rsrc_t rs, unsigned soff) {
    v4u r; asm volatile("buffer_load_dwordx4 %0, %1, %2, %3 offen" : "=v"(r) : "v"(voff), "s"(rs), "s"(soff) : "memory"); return r;
}
#define PIN(x) asm volatile("" : "+v"(x))
DI s16x4 tr_read(unsigned a) { return __builtin_amdgcn_ds_read_tr16_b64_v4i16((LAS s16x4*)(a)); }
DI bf16x8 cat8(s16x4 a, s16x4 b) { return __builtin_shufflevector(a, b, 0, 1, 2, 3, 4, 5, 6, 7); }
DI bf16x8 lds128(unsigned a) { return *(const LAS bf16x8*)(a); }
DI f32x4 ldsf4(unsigned a) { return *(const LAS f32x4*)(a); }
DI float ldsf(unsigned a) { return *(const LAS float*)(a); }
DI unsigned ldsu(unsigned a) { return *(const LAS unsigned*)(a); }
DI void stsf4(unsigned a, f32x4 v) { *(LAS f32x4*)(a) = v; }
DI void stsf(unsigned a, float v) { *(LAS float*)(a) = v; }
DI void stsu(unsigned a, unsigned v) { *(LAS unsigned*)(a) = v; }
template <int C> DI float dppf(float x) { return __uint_as_float(__builtin_amdgcn_update_dpp(0, __float_as_uint(x), C, 0xf, 0xf, true)); }
template <int C> DI unsigned dppu(unsigned x) { return __builtin_amdgcn_update_dpp(0, x, C, 0xf, 0xf, true); }
DI float max2f(float a, float b) { float r; asm("v_max_f32_e32 %0, %1, %2" : "=v"(r) : "v"(a), "v"(b)); return r; }
DI float max3f(float a, float b, float c) { float r; asm("v_max3_f32 %0, %1, %2, %3" : "=v"(r) : "v"(a), "v"(b), "v"(c)); return r; }
DI float row_sum(float x) { x += dppf<0x128>(x); x += dppf<0x124>(x); x += dppf<0x122>(x); x += dppf<0x121>(x); return x; }
DI float xrow_sum(float x) {
    auto a = __builtin_amdgcn_permlane16_swap(__float_as_uint(x), __float_as_uint(x), false, false);
    x = __uint_as_float(a[0]) + __uint_as_float(a[1]);
    auto b = __builtin_amdgcn_permlane32_swap(__float_as_uint(x), __float_as_uint(x), false, false);
    return __uint_as_float(b[0]) + __uint_as_float(b[1]);
}
DI unsigned wave_maxu(unsigned x) {
    x = umx(x, dppu<0x128>(x)); x = umx(x, dppu<0x124>(x)); x = umx(x, dppu<0x122>(x)); x = umx(x, dppu<0x121>(x));
    auto a = __builtin_amdgcn_permlane16_swap(x, x, false, false);
    x = umx(a[0], a[1]);
    auto b = __builtin_amdgcn_permlane32_swap(x, x, false, false);
    return umx(b[0], b[1]);
}
DI float nmax3f(float a, float b, float c) { float r; asm("v_max3_f32 %0, -%1, -%2, -%3" : "=v"(r) : "v"(a), "v"(b), "v"(c)); return r; }
DI float nmax2f(float a, float b) { float r; asm("v_max_f32_e64 %0, -%1, -%2" : "=v"(r) : "v"(a), "v"(b)); return r; }
template <bool MAX> DI float xrow_scatter(float a, float b, float c, float d) {
    const auto s = __builtin_amdgcn_permlane16_swap(__float_as_uint(a), __float_as_uint(b), false, false);
    const auto t = __builtin_amdgcn_permlane16_swap(__float_as_uint(c), __float_as_uint(d), false, false);
    float u = MAX ? max2f(__uint_as_float(s[0]), __uint_as_float(s[1])) : __uint_as_float(s[0]) + __uint_as_float(s[1]);
    float v = MAX ? max2f(__uint_as_float(t[0]), __uint_as_float(t[1])) : __uint_as_float(t[0]) + __uint_as_float(t[1]);
    if (MAX) asm volatile("s_nop 1" : "+v"(u), "+v"(v));
    const auto r = __builtin_amdgcn_permlane32_swap(__float_as_uint(u), __float_as_uint(v), false, false);
    return MAX ? max2f(__uint_as_float(r[0]), __uint_as_float(r[1])) : __uint_as_float(r[0]) + __uint_as_float(r[1]);
}
DI void xrow_gather(float w, float (&o)[4]) {
    const auto r = __builtin_amdgcn_permlane32_swap(__float_as_uint(w), __float_as_uint(w), false, false);
    const auto s = __builtin_amdgcn_permlane16_swap(r[0], r[0], false, false);
    const auto t = __builtin_amdgcn_permlane16_swap(r[1], r[1], false, false);
    o[0] = __uint_as_float(s[0]); o[1] = __uint_as_float(s[1]); o[2] = __uint_as_float(t[0]); o[3] = __uint_as_float(t[1]);
}
struct NoHook { DI void operator()(int, float&) const {} DI void operator()(int, float&, float&) const {} };
struct NoPut { DI void operator()(int, const bf16x8&) const {} };
template <bool MUL, class H = NoHook, class P = NoPut> DI void softmax2(const f32x4 (&p)[2][2], float rs0, float rs1, bool hi, bf16x8 (&out)[4], H hook = H(), P put = P()) {
    float m[4];
#pragma unroll
    for (int i = 0; i < 2; i++) {
        const f32x4 &p0 = p[i][0], &p1 = p[i][1];
        m[2 * i] = max3f(max3f(p0[0], p0[1], p0[2]), max3f(p0[3], p1[0], p1[1]), max2f(p1[2], p1[3]));
        m[2 * i + 1] = max3f(nmax3f(p0[0], p0[1], p0[2]), nmax3f(p0[3], p1[0], p1[1]), nmax2f(p1[2], p1[3]));
    }
    asm volatile("s_nop 1" : "+v"(m[0]), "+v"(m[1]), "+v"(m[2]), "+v"(m[3]));
    hook(1, m[3]);
    const float rsr = hi ? rs1 : rs0;
    float a[4];
    xrow_gather(xrow_scatter<true>(m[0], m[1], m[2], m[3]) * (rsr * -LOG2E), a);
    hook(10, a[0]);
    f32x2 e[4][4];
    float s[4];
#pragma unroll
    for (int k = 0; k < 4; k++) {
        if (k == 2) hook(2, a[2], s[1]);
        const float sc = ((k >> 1) ? rs1 : rs0) * ((k & 1) ? -LOG2E : LOG2E);
#pragma unroll
        for (int j = 0; j < 4; j++) {
            const f32x4& q = p[k >> 1][j >> 1];
            const f32x2 t = __builtin_elementwise_fma((f32x2){q[2 * (j & 1)], q[2 * (j & 1) + 1]}, (f32x2){sc, sc}, (f32x2){a[k], a[k]});
            e[k][j] = (f32x2){__builtin_amdgcn_exp2f(t[0]), __builtin_amdgcn_exp2f(t[1])};
        }
        const f32x2 h = (e[k][0] + e[k][1]) + (e[k][2] + e[k][3]);
        s[k] = h[0] + h[1];
        if (k == 3) hook(3, s[k]);
    }
    float iv = __builtin_amdgcn_rcpf(xrow_scatter<false>(s[0], s[1], s[2], s[3]));
    if (MUL) iv *= rsr;
    xrow_gather(iv, a);
#pragma unroll
    for (int k = 0; k < 4; k++) {
        if (k == 2) hook(4, a[2]);
        const f32x2 c = {a[k], a[k]};
        const f32x2 x0 = e[k][0] * c, x1 = e[k][1] * c, x2 = e[k][2] * c, x3 = e[k][3] * c;
        out[k] = mk8(pk2(x0[0], x0[1]), pk2(x1[0], x1[1]), pk2(x2[0], x2[1]), pk2(x3[0], x3[1]));
        put(k, out[k]);
    }
}
DI float dot2(unsigned a, unsigned b, float c) {
    return __builtin_amdgcn_fdot2_f32_bf16(__builtin_bit_cast(bf16x2, a), __builtin_bit_cast(bf16x2, b), c, false);
}
DI void st_gran(char* ws, unsigned off, unsigned tag, unsigned val) {
    __hip_atomic_store((gu64*)(ws + off), ((u64)tag << 32) | val, RLX_AGENT);
}
DI v2u ldg(rsrc_t rs, unsigned voff, unsigned soff) { return __builtin_amdgcn_raw_buffer_load_b64(rs, voff, soff, AUX_SC1); }
#define RARE(c) __builtin_expect(!!(c), 0)
#define FRESH() asm volatile("" ::: "memory")
template <int N> DI void sweep(rsrc_t rs, unsigned voff, unsigned soff, unsigned S, unsigned tag, unsigned (&v)[N]) {
#pragma nounroll
    for (unsigned spins = 0;;) {
        unsigned bad = 0;
        FRESH();
#pragma unroll
        for (int k = 0; k < N; ++k) { const v2u x = ldg(rs, voff, soff + k * S); v[k] = x[0]; bad |= x[1] ^ tag; }
        if (__all(bad == 0)) break;
        if (++spins > SPIN_CAP) break;
        __builtin_amdgcn_s_sleep(1);
    }
}
template <int A, int B> DI unsigned budget(unsigned& spins) {
    unsigned r;
    asm volatile("s_add_i32 %0, %0, 1\n\ts_cmpk_lt_u32 %0, %2\n\ts_cselect_b32 %1, %3, %4" : "+s"(spins), "=s"(r) : "i"(SPIN_CAP), "i"(A), "i"(B) : "scc");
    SCHED();
    return r;
}
DI unsigned wmax(unsigned x) { return x; }
DI unsigned wmax(v2u x) { return umx(x[0], x[1]); }
DI unsigned wmax(v4u x) { return umx(umx(x[0], x[1]), umx(x[2], x[3])); }
template <int N, class T, class F> DI unsigned sweepz(F ld, T (&v)[N]) {
    unsigned spins = 0, lim, m;
#pragma nounroll
    do {
        FRESH();
#pragma unroll
        for (int k = 0; k < N; ++k) v[k] = ld(k);
        lim = budget<-2, -1>(spins);
        m = 0;
#pragma unroll
        for (int k = 0; k < N; ++k) m = umx(m, wmax(v[k]));
    } while (__any(m > lim));
    return m;
}
template <int N, class T, class F, class G> DI void sweepn(F ld, T (&v)[N], G use) {
    unsigned spins = 0;
    bool again;
#pragma nounroll
    do {
        FRESH();
#pragma unroll
        for (int k = 0; k < N; ++k) v[k] = ld(k);
        const int m = budget<3, 0>(spins);
        again = __any(__builtin_amdgcn_classf(use(), m));
    } while (again);
}
#define OOB 0x4000000u
#define LD16(v, s) __builtin_amdgcn_raw_buffer_load_b128(rsWS, (v), (s), AUX_SC1)
#define LD4(v, s) __builtin_amdgcn_raw_buffer_load_b32(rsWS, (v), (s), AUX_SC1)
#define ST16(x, v, s) __builtin_amdgcn_raw_buffer_store_b128((x), rsWS, (v), (s), AUX_SC1)
#define ST4(x, v, s) __builtin_amdgcn_raw_buffer_store_b32((x), rsWS, (v), (s), AUX_SC1)
#define SLEEP(n) __builtin_amdgcn_s_sleep(n)
DI void poll16(rsrc_t rs, unsigned off, unsigned tag, int l) {
    unsigned spins = 0, lim, bad;
#pragma nounroll
    do {
        FRESH();
        const v2u x = ldg(rs, (l & 15) * 8, off);
        lim = budget<0, -1>(spins);
        bad = (x[1] ^ tag) | (x[0] ^ tag ^ 0x5bd1e995u);
    } while (__any(bad > lim));
    ORDER_BEHIND_POLL();
}
#define STAMPR(i, role)
#define STAMP(i)
#define STAMPW(i, ww)

#define ARR_DECL
#define KATTR
#define ARR_MARK(i)
#define ARR_END()
template <class F, int... Is> DI void static_for_impl(F&& f, std::integer_sequence<int, Is...>) { (f(std::integral_constant<int, Is>{}), ...); }
template <int N, class F> DI void static_for(F&& f) { static_for_impl(f, std::make_integer_sequence<int, N>{}); }

DI s16x4 bq(unsigned b0, unsigned b1, int j, int i) {
    const int ni = (j + 2) % 6;
    return tr_read(((ni & 1) ? b1 : b0) + (ni >> 1) * 8192 + (i >> 1) * 4096 + (i & 1) * 512);
}
template <class F> DI void group(f32x4 (&acc)[4][6], const bf16x8 (&A)[2][4], const bf16x8 (&B)[2], int j, F gap) {
    const int ni = (j + 2) % 6;
#pragma unroll
    for (int i = 0; i < 8; i++) {
        const int mi = i >> 1, ks = i & 1;
        if (ni < 4) asm volatile("v_mfma_f32_16x16x32_bf16 %0, %1, %2, %0" : "+v"(acc[mi][ni]) : "v"(B[ks]), "v"(A[ks][mi]));
        else asm volatile("v_mfma_f32_16x16x32_bf16 %0, %1, %2, %0" : "+v"(acc[mi][ni]) : "v"(A[ks][mi]), "v"(B[ks]));
        gap(i);
        SCHED();
    }
}
struct ImgPut {
    const unsigned (&ia)[4];
    DI void operator()(int k, const bf16x8& f) const {
        const v4u u = __builtin_bit_cast(v4u, f);
        *(LAS v2u*)(ia[2 * (k & 1)] + (k >> 1) * 2048) = (v2u){u[0], u[1]};
        *(LAS v2u*)(ia[2 * (k & 1) + 1] + (k >> 1) * 2048) = (v2u){u[2], u[3]};
        SCHED();
    }
};
DI float lds_bf(unsigned a) { return bf2f(*(const LAS u16*)(a)); }
#define MFMA_SETTLE2(p) asm volatile("s_nop 7\n\ts_nop 1" : "+v"(p[0][0]), "+v"(p[0][1]), "+v"(p[1][0]), "+v"(p[1][1]))

__global__ KATTR __launch_bounds__(512, 2) void fused_kernel(const u16* __restrict__ X, const u16* __restrict__ WQKV, const u16* __restrict__ WPHI,
                                                       const u16* __restrict__ WO, const u16* __restrict__ WR, const u16* __restrict__ WG,
                                                       const u16* __restrict__ WU, const u16* __restrict__ WD, u16* __restrict__ OUT,
                                                       char* __restrict__ ws) {
    extern __shared__ __attribute__((aligned(16))) char lds[];
    u64 pc0; unsigned clen;
    const int tid = threadIdx.x, l = tid & 63;
    asm volatile("s_getpc_b64 %0\n.Lfk_beg:\n\ts_lshr_b32 %1, %3, 3\n\tv_and_b32 v255, 0x1e0, %2\n\tv_add_lshl_u32 v255, v255, %1, 7\n\ts_mov_b32 %1, .Lfk_end-.Lfk_beg\n\t"
                 "v_min_u32 v255, %1, v255\n\tglobal_load_dword v255, v255, %0" : "=&s"(pc0), "=&s"(clen) : "v"(tid), "s"(blockIdx.x) : "v255", "scc");
    SCHED();
    const unsigned w = rfl(tid >> 6);
    const unsigned bid = blockIdx.x, xcd = bid & 7, idx = bid >> 3;
    const unsigned rt = (xcd & 1) * 8 + (idx & 7), hp = (xcd >> 1) * 4 + (idx >> 3);
    const unsigned i16 = idx & 15, rt2 = xcd * 2 + (idx >> 4), ct = i16 == 4 ? 11 : i16 == 11 ? 4 : i16;
    const unsigned hq = (hp + 4) & 15;
    const unsigned rot = (rt >> 2) * 4;
    const unsigned rot2 = (2 * (rt2 + ct)) & 31;
    const unsigned ldsb = (unsigned)(size_t)lds;
    const int g = l >> 4, q4 = (l & 15) >> 2, p4 = l & 3, t16 = l & 15;
    const unsigned kr = l >> 3;
    asm volatile("" ::"s"(X), "s"(WQKV), "s"(WPHI), "s"(WO), "s"(WR), "s"(WG), "s"(WU), "s"(WD), "s"(OUT), "s"(ws));
    unsigned tag;
#define READ_TAG() do { SCHED(); tag = *(const volatile CAS unsigned*)(ws + WS_EPOCH) + 1u; asm volatile("" : "+s"(tag)); } while (0)
    const rsrc_t rsWS = __builtin_amdgcn_make_buffer_rsrc((void*)ws, 0, WS_USED, 0x00020000);
    const rsrc_t rsX = __builtin_amdgcn_make_buffer_rsrc((void*)X, 0, 2048 * 1024 * 2, 0x00020000);
    const rsrc_t rsW = __builtin_amdgcn_make_buffer_rsrc((void*)WQKV, 0, 1024 * 3072 * 2, 0x00020000);
    const unsigned vA = (kr * 1024 + (((l & 7) ^ kr) * 8)) * 2;
    const unsigned vB0 = (kr * 3072 + (((l & 7) ^ (((kr >> 1) & 1) << 1)) * 8)) * 2;
    const unsigned vB1 = (kr * 3072 + (((l & 7) ^ ((((kr >> 1) & 1) | 2) << 1)) * 8)) * 2;
    const unsigned srow1 = rt * 128 + hp * 8 + (w & 3) * 2;
    v4u xr[4];
    if (w < 4) {
#pragma unroll
        for (int i = 0; i < 4; i++) xr[i] = ld16_asm(l * 16, rsX, srow1 * 2048 + i * 1024);
    }
    {
        const unsigned vBw = (w & 1) ? vB1 : vB0;
#pragma unroll
        for (int i = 0; i < 2; i++) dma16(ldsb + (w * 2 + i) * 1024, vA, rsX, (rt * 128 + w * 16 + i * 8) * 2048 + rot * 128);
#pragma unroll
        for (int i = 0; i < 3; i++) dma16(ldsb + 16384 + i * 8192 + w * 1024, vBw, rsW, (rot * 64 + w * 8) * 6144 + (i ? hp : hq) * 128 + i * 2048);
    }
    const f32x4 zero4 = {0.f, 0.f, 0.f, 0.f};
    ARR_DECL;
    const bf16x8 ones = mk8(0x3F803F80u, 0x3F803F80u, 0x3F803F80u, 0x3F803F80u);
    const unsigned lw = w & 3;
    const bool ffn = (ct & 7) != 3;
    const unsigned fb = !ffn ? 255u : rt2 * 14 + (i16 < 3 ? i16 : i16 - 2);
    const unsigned cg = (fb + 5) & 15, kc = fb >> 4;
    const unsigned kq = fb / 56, mat = (fb % 56) / 28, cg1 = (fb % 28) * 2;
    v4u wreg[24];
    STAMPW(0, 0); STAMPW(0, 4);

    const rsrc_t rsWO = __builtin_amdgcn_make_buffer_rsrc((void*)WO, 0, 1024 * 1024 * 2, 0x00020000);
    const unsigned vWO = kr * 2048 + (((l & 7) ^ (((kr >> 1) & 3) << 1)) * 16);
    unsigned vwo[4];
    auto wo_so = [&](unsigned b) { return (((2 * b & 30) + (b >> 4) + rot2) & 31) * 65536 + ct * 128; };
    const unsigned fw = (((t16 >> 1) & 3) << 2) | (((t16 >> 3) & 1) << 1) | (t16 & 1);
    unsigned ad;
    unsigned ia[4];
    bf16x8 sdv[4];
    auto pre_addr = [&]() {
#pragma unroll
        for (int k = 0; k < 4; k++) { ia[k] = ldsb + L_XB + lw * 8192 + (w >> 2) * 4096 + t16 * 128 + (((4 * k + g) ^ fw) * 8); PIN(ia[k]); }
        const unsigned rr = 4 * g + q4, fr = (((rr >> 1) & 3) << 2) | (((rr >> 3) & 1) << 1) | (rr & 1);
        ad = ldsb + L_XB + (w & 1) * 16384 + rr * 128 + ((((w >> 1) * 4 + p4) ^ fr) * 8);
        PIN(ad);
    };
    auto sd_reads = [&]() {
        unsigned sa = ldsb + L_SD + g * 8;
        PIN(sa);
#pragma unroll
        for (int s = 0; s < 4; s++) {
            const v2u a = *(const LAS v2u*)(sa + s * 64), b = *(const LAS v2u*)(sa + s * 64 + 32);
            sdv[s] = mk8(a[0], a[1], b[0], b[1]);
        }
    };
    bf16x8 v0[4], v1[4];
    auto v_reads = [&]() {
        unsigned vd = ldsb + L_V + (w & 1) * 8192 + l * 16;
        PIN(vd);
#pragma unroll
        for (int s = 0; s < 4; s++) { v0[s] = lds128(vd + s * 2048); v1[s] = lds128(vd + s * 2048 + 1024); }
    };
    auto kv_tile = [&](auto hook6) {
        const unsigned h2 = w & 1, ft = w >> 1;
        WAITL0();
        ARR_MARK(18);
        BAR();
        SCHED();
        bf16x8 pf[4];
#pragma unroll
        for (int s = 0; s < 4; s++) pf[s] = cat8(tr_read(ad + s * 4096), tr_read(ad + s * 4096 + 2048));
        SCHED();
        hook6();
        SCHED();
        f32x4 k0 = zero4, k1 = zero4, kz = zero4;
#pragma unroll
        for (int s = 0; s < 4; s++) { k0 = MFMA(pf[s], v0[s], k0); k1 = MFMA(pf[s], v1[s], k1); SCHED(); }
#pragma unroll
        for (int s = 0; s < 4; s++) kz = MFMA(pf[s], sdv[s], kz);
        const unsigned pbase = WS_P2 + (hp * 16 + rt) * P2_STRIDE;
        const v4u o0 = {pk2(k0[0], k0[1]), pk2(k0[2], k0[3]), pk2(k1[0], k1[1]), pk2(k1[2], k1[3])};
        __builtin_amdgcn_raw_buffer_store_b128(o0, rsWS, l * 16, pbase + h2 * 4096 + ft * 1024, AUX_SC1);
        asm volatile("s_nop 1" :: "v"(o0));
        __builtin_amdgcn_raw_buffer_store_b64((v2u){pk2(kz[0], kz[1]), pk2(kz[2], kz[3])}, rsWS, t16 == 0 ? g * 8 : OOB,
                                              pbase + 8192 + h2 * 128 + ft * 32, AUX_SC1);
        SCHED();
        ARR_MARK(19);
        BAR();
    };

    unsigned res;
    auto red = [&](const v4u* p) {
        float lo = xrow_scatter<false>(bflo(p[0][0]) + bflo(p[1][0]), bflo(p[0][1]) + bflo(p[1][1]), bflo(p[0][2]) + bflo(p[1][2]), bflo(p[0][3]) + bflo(p[1][3]));
        float hi = xrow_scatter<false>(bfhi(p[0][0]) + bfhi(p[1][0]), bfhi(p[0][1]) + bfhi(p[1][1]), bfhi(p[0][2]) + bfhi(p[1][2]), bfhi(p[0][3]) + bfhi(p[1][3]));
        const float chk = lo;
        lo += dppf<0x128>(lo); hi += dppf<0x128>(hi);
        res = pk2(lo, hi);
        return chk;
    };
    auto kv_reduce = [&]() {
        unsigned v3 = (l & 8) ? OOB : (l & 7) * 16 + g * 4;
        unsigned s3 = WS_G3 + (hp * 4 + (rt >> 2)) * 2048 + (lw & 1) * 1024 + (rt & 3) * 256 + (lw >> 1) * 128;
        PIN(v3);
        asm volatile("" : "+s"(s3));
        SLEEP(2);
        v4u pp[2];
        sweepn<2>([&](int k) { return LD16(kr * P2_STRIDE + (l & 7) * 16, WS_P2 + (hp * 16 + k * 8) * P2_STRIDE + (rt >> 2) * 2048 + (lw & 1) * 1024 + (rt & 3) * 256 + (lw >> 1) * 128); },
                  pp, [&]() { return red(pp); });
        ST4(nsent(res), v3, s3);
        STAMPW(25, 0);
    };

    const unsigned half = w >> 2;
    const unsigned R2 = rt2 * 128 + lw * 32 + half * 16 + t16;

    rsrc_t rsD = __builtin_amdgcn_make_buffer_rsrc((void*)WD, 0, 1024 * 3584 * 2, 0x00020000);
    unsigned vD, sD;
    auto dloads = [&](int i0, int i1) {
#pragma unroll
        for (int i = i0; i < i1; i++)
            wreg[16 + i] = __builtin_amdgcn_raw_buffer_load_b128(rsD, vD, sD + i * 2048, W_AUX);
        FRESH();
    };
    v4u wp1[2][8];
    auto perm1 = [&]() {
#pragma unroll
        for (int n = 0; n < 16; n++) PIN(wreg[n]);
#pragma unroll
        for (int ip = 0; ip < 4; ip++)
#pragma unroll
            for (int c = 0; c < 4; c++) {
#pragma unroll
                for (int m = 0; m < 2; m++) {
                    const unsigned a = wreg[m * 8 + 2 * ip][c], b = wreg[m * 8 + 2 * ip + 1][c];
                    wp1[m][2 * c][ip] = __builtin_amdgcn_perm(b, a, 0x05040100);
                    wp1[m][2 * c + 1][ip] = __builtin_amdgcn_perm(b, a, 0x07060302);
                }
            }
#pragma unroll
        for (int c = 0; c < 8; c++) { PIN(wp1[0][c]); PIN(wp1[1][c]); }
    };
    auto res_loads = [&]() {
        dma16(ldsb + L_XB + w * 4096, kr * 2048 + (((l & 7) ^ kr) * 16), rsX, (rt2 * 128 + lw * 32 + half * 16) * 2048 + ct * 128);
        dma16(ldsb + L_XB + w * 4096 + 3072, (l & 7) * 16, __builtin_amdgcn_make_buffer_rsrc((void*)WR, 0, 2048, 0x00020000), ct * 128);
        dma16(ldsb + L_XB + w * 4096 + 1024, kr * 2048 + (((l & 7) ^ kr) * 16), rsX, (rt2 * 128 + lw * 32 + half * 16 + 8) * 2048 + ct * 128);
    };

    if (w < 4) {
        const unsigned wm = w & 1, hh = w >> 1;
        f32x4 acc[4][6];
        bf16x8 wf[2], wfq[2];
        float mbt[4], rst[4], csv[2];
        f32x4 mbv[4], csq[2], csk[2];
#pragma unroll
        for (int i = 0; i < 4; i++)
#pragma unroll
            for (int j = 0; j < 6; j++) { acc[i][j] = zero4; PIN(acc[i][j]); }
        {
            const unsigned aoff0 = ldsb + (wm * 64 + t16) * 128 + (((0 + g) ^ (l & 7)) * 16);
            const unsigned aoff1 = ldsb + (wm * 64 + t16) * 128 + (((4 + g) ^ (l & 7)) * 16);
            const unsigned sk = ((q4 >> 1) & 1) | ((g & 1) << 1);
            const unsigned boffA = ldsb + 16384 + (8 * g + q4) * 128 + (((2 * hh) ^ sk) * 32) + p4 * 8;
            const unsigned boffB = ldsb + 16384 + (8 * g + q4) * 128 + (((2 * hh + 1) ^ sk) * 32) + p4 * 8;
            bf16x8 A[2][4], B[3][2];
            s16x4 lo;
            unsigned a0[3], a1[3], bA[3], bB[3];
#pragma unroll
            for (int i = 0; i < 3; i++) {
                a0[i] = aoff0 + i * STAGE_B; a1[i] = aoff1 + i * STAGE_B; bA[i] = boffA + i * STAGE_B; bB[i] = boffB + i * STAGE_B;
                PIN(a0[i]); PIN(a1[i]); PIN(bA[i]); PIN(bB[i]);
            }
            unsigned aT = ldsb + L_MB + (wm * 64 + t16) * 4, aS = ldsb + L_SD + (wm * 64 + t16) * 2, aM = ldsb + L_MB + wm * 256 + g * 16;
            unsigned aC = ldsb + L_CS + hh * 128 + g * 16, aV = ldsb + L_CS + (128 + hh * 32 + t16) * 4;
            unsigned aW = ldsb + L_WPHI + hh * 2048 + (4 * g + q4) * 64 + p4 * 8, aK = ldsb + L_KH + w * 2048 + l * 16;
            PIN(aT); PIN(aS); PIN(aM); PIN(aC); PIN(aV); PIN(aW); PIN(aK);
            pre_addr();
            READ_TAG();
            waitv<5>();
            PIN(xr[0]); PIN(xr[1]); PIN(xr[2]); PIN(xr[3]);
            {
                float s[2], q[2], x0[2];
#pragma unroll
                for (int rr = 0; rr < 2; rr++) {
                    x0[rr] = bflo(rfl(xr[2 * rr][0]));
                    s[rr] = 0.f; q[rr] = 0.f;
#pragma unroll
                    for (int i = 0; i < 4; i++) {
                        const float a = bflo(xr[2 * rr][i]) - x0[rr], b = bfhi(xr[2 * rr][i]) - x0[rr];
                        const float c = bflo(xr[2 * rr + 1][i]) - x0[rr], d = bfhi(xr[2 * rr + 1][i]) - x0[rr];
                        s[rr] += (a + b) + (c + d);
                        q[rr] += (a * a + b * b) + (c * c + d * d);
                    }
                }
                const float sq = row_sum(xrow_scatter<false>(s[0], s[1], q[0], q[1]));
#define RLF(x, n) __uint_as_float(__builtin_amdgcn_readlane(__float_as_uint(x), (n)))
                const float r0 = RLF(sq, 0), r1 = RLF(sq, 16), r2 = RLF(sq, 32), r3 = RLF(sq, 48);
                const float sm = (l ? r1 : r0) * (1.0f / 1024.0f), qm = (l ? r3 : r2) * (1.0f / 1024.0f);
                const float var = fmaxf(qm - sm * sm, 0.f);
                if (l < 2) st_gran(ws, WS_G0 + (srow1 + l) * 8, tag, pk2((l ? x0[1] : x0[0]) + sm, sqrtf(rbf(rbf(var) + 1e-5f))));
            }
            waitv<1>();
            WAITL0();
            ARR_MARK(0);
            BAR();
            SCHED();
            STAMPW(1, 0);
            auto rdB = [&](int u, int j, int i) {
                if (i & 1) B[j % 3][i >> 1] = cat8(lo, bq(bA[u], bB[u], j, i)); else lo = bq(bA[u], bB[u], j, i);
            };
            auto rdA = [&](int u, int i) { A[i & 1][i >> 1] = lds128(((i & 1) ? a1[u] : a0[u]) + (i >> 1) * 2048); };
            auto head = [&](int u) {
                group(acc, A, B[0], 0, [&](int i) { rdB(u, 1 + (i >> 2), i & 3); });
                group(acc, A, B[1], 1, [&](int i) { if (i < 4) rdB(u, 3, i); });
                group(acc, A, B[2], 2, [&](int i) { if (i < 4) rdB(u, 4, i); });
                group(acc, A, B[0], 3, [&](int i) { if (i < 4) rdB(u, 5, i); });
                WL0();
            };
#pragma unroll
            for (int i = 0; i < 4; i++) rdB(0, 0, i);
#pragma unroll
            for (int i = 0; i < 8; i++) rdA(0, i);
            SCHED();
#pragma nounroll
            for (unsigned t = 0; t < 5; t++) {
#pragma unroll
                for (int u = 0; u < 3; u++) {
                    head(u);
                    ARR_MARK(1 + t * 3 + u);
                    BAR();
                    SCHED();
                    group(acc, A, B[1], 4, [&](int i) { if (i < 4) rdB((u + 1) % 3, 0, i); });
                    group(acc, A, B[2], 5, [&](int i) { rdA((u + 1) % 3, i); });
                }
            }
            unsigned kw[4][4], rsu[4];
            f32x2 kx;
            auto kpiece = [&](int n) {
                if (n > 0) { kw[((n - 1 >> 2) + 2) & 3][(n - 1) & 3] = pk2(kx[0], kx[1]); PIN(kw[((n - 1 >> 2) + 2) & 3][(n - 1) & 3]); }
                if (n < 16) {
                    const int mi = ((n >> 2) + 2) & 3, j = n & 3;
                    const f32x4 &c = acc[mi][2 + (j >> 1)], &s_ = csk[j >> 1];
                    float x0 = __builtin_fmaf(mbt[mi], s_[2 * (j & 1)], c[2 * (j & 1)]); PIN(x0);
                    float x1 = __builtin_fmaf(mbt[mi], s_[2 * (j & 1) + 1], c[2 * (j & 1) + 1]); PIN(x1);
                    kx = (f32x2){x0, x1};
                }
                if (n == 5 || n == 9) *(LAS v4u*)(aK + (n == 9) * 1024) = (v4u){kw[2 + (n == 9)][0], kw[2 + (n == 9)][1], kw[2 + (n == 9)][2], kw[2 + (n == 9)][3]};
            };
            group(acc, A, B[0], 0, [&](int i) { rdB(0, 1 + (i >> 2), i & 3); });
            group(acc, A, B[1], 1, [&](int i) {
                if (i < 4) { rdB(0, 3, i); mbt[(i + 2) & 3] = ldsf(aT + ((i + 2) & 3) * 64); }
                else if (i < 6) csk[i - 4] = ldsf4(aC + 256 + (i - 4) * 64);
                else wf[i - 6] = cat8(tr_read(aW + (i - 6) * 32), tr_read(aW + (i - 6) * 32 + 1024));
            });
            group(acc, A, B[2], 2, [&](int i) { if (i < 4) rdB(0, 4, i); else mbv[i - 4] = ldsf4(aM + (i - 4) * 64); kpiece(i); });
            group(acc, A, B[0], 3, [&](int i) {
                if (i < 4) rdB(0, 5, i); else rsu[i - 4] = *(const LAS u16*)(aS + (i - 4) * 32);
                if (i == 2 || i == 3) csv[i - 2] = ldsf(aV + (i - 2) * 64);
                kpiece(8 + i);
            });
            WL0();
            ARR_MARK(16);
            BAR();
            SCHED();
            kpiece(16);
            f32x4 p[2][2];
#pragma unroll
            for (int i = 0; i < 4; i++) p[i >> 1][i & 1] = MFMA(wf[i & 1], mk8(kw[i >> 1][0], kw[i >> 1][1], kw[i >> 1][2], kw[i >> 1][3]), zero4);
            SCHED();
            STAMPW(2, 0);
#pragma unroll
            for (int mi = 0; mi < 4; mi++) rst[mi] = bf2f(rsu[mi]);
#pragma unroll
            for (int a = 0; a < 2; a++)
#pragma unroll
                for (int dt = 0; dt < 2; dt++) {
                    const f32x4 x0 = acc[2 * a][4 + dt] + mbv[2 * a] * csv[dt], x1 = acc[2 * a + 1][4 + dt] + mbv[2 * a + 1] * csv[dt];
                    *(LAS v4u*)(aK + (L_V - L_KH) + w * 2048 + (a * 2 + dt) * 1024) = (v4u){pk2(x0[0], x0[1]), pk2(x0[2], x0[3]), pk2(x1[0], x1[1]), pk2(x1[2], x1[3])};
                }
            SCHED();
            rst[0] = __builtin_amdgcn_rcpf(rst[0]); rst[1] = __builtin_amdgcn_rcpf(rst[1]);
            bf16x8 f[4];
            struct HookC {
                decltype(v_reads)& vr; decltype(sd_reads)& sr;
                DI void operator()(int k, float& x) const { if (k == 10) { PIN(x); WAITL0(); ARR_MARK(17); BAR(); PIN(x); } if (k == 3) { PIN(x); vr(); sr(); PIN(x); } }
                DI void operator()(int, float&, float&) const {}
            };
            softmax2<true>(p, rst[0], rst[1], g >= 2, f, HookC{v_reads, sd_reads}, ImgPut{ia});
#pragma unroll
            for (int ft = 0; ft < 2; ft++) wfq[ft] = cat8(tr_read(aW + ft * 32 + 4096), tr_read(aW + ft * 32 + 5120));
            csq[0] = ldsf4(aC); csq[1] = ldsf4(aC + 64);
            STAMPW(3, 0);
            kv_tile([]() {});
            STAMPW(4, 0);
            SCHED();
            group(acc, A, B[1], 4, [&](int i) { if (i == 2 || i == 3) { rst[i] = __builtin_amdgcn_rcpf(rst[i]); PIN(rst[i]); } });
            group(acc, A, B[2], 5, [&](int) {});
            asm volatile("s_nop 7\n\ts_nop 1");
            SCHED();
        }
        bf16x8 phq[2][4];
        v4u kg[4], pz[2];
        unsigned sG3 = WS_G3 + (hq * 2 + hh) * 4096;
        auto z_ld = [&](int k) { return LD16(kr * P2_STRIDE + (l & 7) * 16, WS_P2 + (hq * 16 + k * 8) * P2_STRIDE + 8192 + hh * 128); };
        {
            f32x4 pq[2][2][2];
#pragma unroll
            for (int mi = 0; mi < 4; mi++) {
                const f32x4 x0 = acc[mi][0] + mbt[mi] * csq[0], x1 = acc[mi][1] + mbt[mi] * csq[1];
                const bf16x8 qf = mk8(pk2(x0[0], x0[1]), pk2(x0[2], x0[3]), pk2(x1[0], x1[1]), pk2(x1[2], x1[3]));
                pq[mi >> 1][mi & 1][0] = MFMA(wfq[0], qf, zero4); pq[mi >> 1][mi & 1][1] = MFMA(wfq[1], qf, zero4);
            }
            MFMA_SETTLE2(pq[0]); MFMA_SETTLE2(pq[1]);
            softmax2<false>(pq[0], rst[0], rst[1], g >= 2, phq[0]);
#pragma unroll
            for (int i = 0; i < 4; i++) PIN(phq[0][i]);
            SCHED();
            kv_reduce();
            SCHED();
            pz[0] = z_ld(0); pz[1] = z_ld(1);
            FRESH();
            res_loads();
            SCHED();
            BAR();
            SCHED();
            softmax2<false>(pq[1], rst[2], rst[3], g >= 2, phq[1]);
#pragma unroll
            for (int i = 0; i < 4; i++) { PIN(phq[0][i]); PIN(phq[1][i]); }
        }
        STAMPW(5, 0);
        STAMPW(26, 0);
        v2u fv = {tag ^ 0x5bd1e995u, tag};
        unsigned sF = WS_F4 + ((rt * 4 + w) * 16 + hq) * 8, vF = l == 0 ? 0 : OOB;
        {
#define PH(mi, s) phq[(mi) >> 1][2 * ((mi) & 1) + (s)]
            float id[4];
            auto kg_ld = [&](int k) { return LD16(l * 16 + k * 1024, sG3); };
            asm volatile("" : "+s"(sG3));
            SCHED();
            SLEEP(2);
            FRESH();
#pragma unroll
            for (int k = 0; k < 4; k++) kg[k] = kg_ld(k);
            FRESH();
            {
                SCHED();
                const float r = red(pz);
                if (RARE(__any(r != r))) sweepn<2>(z_ld, pz, [&]() { return red(pz); });
                const unsigned az = ldsb + L_XB + w * 4096 + 2048;
                stsu(az + ((l >> 2) & 1) * 64 + ((l & 1) * 2 + (g >> 1)) * 16 + ((l >> 1) & 1) * 8 + (g & 1) * 4, res);
                FRESH();
                const bf16x8 z0 = lds128(az + g * 16), z1 = lds128(az + g * 16 + 64);
#pragma unroll
                for (int mi = 0; mi < 4; mi++) {
                    const f32x4 dn = MFMA(z1, PH(mi, 1), MFMA(z0, PH(mi, 0), zero4));
                    id[mi] = __builtin_amdgcn_rcpf(rbf(rbf(dn[0]) + 1e-6f));
                }
                PIN(id[0]); PIN(id[1]); PIN(id[2]); PIN(id[3]);
            }
            unsigned vO = l * 16 + hh * 1024, sO = WS_P4 + ((rt * 8 + wm * 4) * 32 + hq * 2) * 1024;
            asm volatile("" : "+s"(sO), "+s"(sF));
            unsigned vo4[4];
#pragma unroll
            for (int i = 0; i < 4; i++) { vo4[i] = vO ^ (i * 32768 + (i & 1) * 1024); PIN(vo4[i]); }
            PIN(fv); PIN(vF);
            SCHED();
            v4u ov[4];
#define KVA(s, dt) __builtin_bit_cast(bf16x8, kg[2 * (s) + (dt)])
            auto pairup = [&](v4u& a, v4u& b) {
                unsigned a2 = a[2], a3 = a[3], b0 = b[0], b1 = b[1];
                asm volatile("v_swap_b32 %0, %2\n\tv_swap_b32 %1, %3" : "+v"(a2), "+v"(a3), "+v"(b0), "+v"(b1));
                a[2] = a2; a[3] = a3; b[0] = b0; b[1] = b1;
            };
            f32x4 n0[4], n1[4];
            if (__any(umx(umx(wmax(kg[0]), wmax(kg[1])), umx(wmax(kg[2]), wmax(kg[3]))) == SENT)) sweepz<4>(kg_ld, kg);
            SCHED();
            pairup(kg[0], kg[1]); pairup(kg[2], kg[3]);
            asm volatile("s_nop 1");
            SCHED();
            STAMPW(6, 0);
#pragma unroll
            for (int mi = 0; mi <= 4; mi++) {
                if (mi < 4) {
                    n0[mi] = MFMA(KVA(0, 0), PH(mi, 0), zero4); n1[mi] = MFMA(KVA(0, 1), PH(mi, 0), zero4);
                    n0[mi] = MFMA(KVA(1, 0), PH(mi, 1), n0[mi]); n1[mi] = MFMA(KVA(1, 1), PH(mi, 1), n1[mi]);
                }
                if (mi > 0) {
                    const f32x4 a = n0[mi - 1] * id[mi - 1], b = n1[mi - 1] * id[mi - 1];
                    ov[mi - 1] = (v4u){pk2(a[0], a[1]), pk2(a[2], a[3]), pk2(b[0], b[1]), pk2(b[2], b[3])};
                    ST16(ov[mi - 1], vo4[mi - 1], sO);
                }
                if (mi < 4) { SGB(0x8, 2); SGB(0x2, 3); SGB(0x8, 1); SGB(0x2, 3); SGB(0x8, 1); SGB(0x2, 4); }
                SCHED();
            }
#pragma unroll
            for (int mi = 0; mi < 4; mi++) PIN(ov[mi]);
        }
        BAR();
        waitv<0>();
        __builtin_amdgcn_raw_buffer_store_b64(fv, rsWS, vF, sF, AUX_SC1);
        STAMPW(7, 0);
    } else {
        const unsigned pw = lw & 1;
        const rsrc_t rsG = __builtin_amdgcn_make_buffer_rsrc((void*)(mat ? WU : WG), 0, 1024 * 3584 * 2, 0x00020000);
        unsigned vwl = kr * 57344 + (l & 7) * 16;
        unsigned sg = (kq * 256 + lw * 64) * 7168 + cg1 * 128;
        auto wloads = [&](int n0, int n1) {
#pragma unroll
            for (int j = n0; j < n1; j++) {
                const int n = (j & 1) * 8 + (j >> 1);
                asm volatile("buffer_load_dwordx4 %0, %1, %2, %3 offen nt" : "=v"(wreg[n]) : "v"(vwl), "s"(rsG), "s"(sg + (n >> 3) * 128 + (n & 7) * 7168));
            }
        };
        auto repack = [&](unsigned gI) {
            const unsigned s2 = (2 * g + (q4 >> 1)) & 3;
            bf16x8 f[4];
#pragma unroll
            for (int nt = 0; nt < 4; nt++) {
                const unsigned a = ldsb + L_WO + gI * 4096 + (4 * g + q4) * 128 + ((((nt >> 1) * 2 + (p4 >> 1)) ^ s2) * 32) + (p4 & 1) * 16 + (nt & 1) * 8;
                f[nt] = cat8(tr_read(a), tr_read(a + 2048));
            }
            WAITL0();
#pragma unroll
            for (int nt = 0; nt < 4; nt++) *(LAS bf16x8*)(ldsb + L_WO + gI * 4096 + nt * 1024 + l * 16) = f[nt];
        };
        unsigned aK, aW, aS;
        auto k_pre = [&]() {
            pre_addr();
            aK = ldsb + L_KH + lw * 2048 + l * 16; aW = ldsb + L_WPHI + (lw >> 1) * 2048 + (4 * g + q4) * 64 + p4 * 8; aS = ldsb + L_SD + (pw * 64 + 32 + t16) * 2;
            PIN(aK); PIN(aW); PIN(aS);
        };
        auto kfeat = [&](auto hook) {
            bf16x8 wf[2];
            float rst[2];
#pragma unroll
            for (int ft = 0; ft < 2; ft++) {
                wf[ft] = cat8(tr_read(aW + ft * 32), tr_read(aW + ft * 32 + 1024));
                rst[ft] = __builtin_amdgcn_rcpf(lds_bf(aS + ft * 32));
            }
            sd_reads();
            PIN(wf[0]); PIN(wf[1]); PIN(rst[0]); PIN(rst[1]);
            SCHED();
            ARR_MARK(16);
            BAR();
            SCHED();
            const bf16x8 kf0 = lds128(aK), kf1 = lds128(aK + 1024);
            SCHED();
            hook(-1, rst[0]);
            hook(0, rst[0]);
            SCHED();
            f32x4 p[2][2];
            p[0][0] = MFMA(wf[0], kf0, zero4); p[0][1] = MFMA(wf[1], kf0, zero4);
            p[1][0] = MFMA(wf[0], kf1, zero4); p[1][1] = MFMA(wf[1], kf1, zero4);
            MFMA_SETTLE2(p);
            bf16x8 f[4];
            softmax2<true>(p, rst[0], rst[1], g >= 2, f, hook, ImgPut{ia});
            SCHED();
            hook(5, rst[0]);
            SCHED();
            STAMPW(3, 4); STAMPW(3, 7);
            kv_tile([&]() { hook(6, rst[0]); });
            STAMPW(4, 4); STAMPW(4, 7);
        };
        auto wblk = [&](int k) {
            constexpr unsigned T[8] = {0x19181716, 0x07060504, 0x13120908, 0x11101514, 0x03020100, 0x1b1a0b0a, 0x1d1c0d0c, 0x1f1e0f0e};
            return (T[k] >> (8 * lw)) & 31;
        };
        auto wo_init = [&]() {
#pragma unroll
            for (int i = 0; i < 4; i++) { vwo[i] = vWO + i * (16384 - 1024); PIN(vwo[i]); }
        };
        unsigned hs[8];
        auto wl_pre = [&]() { asm volatile("" : "+s"(sg)); PIN(vwl); };
        auto wo_pre = [&]() {
            wo_init();
#pragma unroll
            for (int k = 0; k < 8; k++) { hs[k] = wo_so(wblk(k)); asm volatile("" : "+s"(hs[k])); }
        };
        auto wo_rng = [&](int k0, int k1) {
#pragma unroll
            for (int k = k0; k < k1; k++) dma16x4(ldsb + L_WO + wblk(k) * 4096, vwo, rsWO, hs[k]);
        };
        auto pf = [&](int i) {
            if (i == 0) { wloads(0, 6); SCHED(); wo_pre(); SCHED(); }
            if (i < 3 && !ffn) wo_rng(i, i + 1);
            if (i == 1 || i == 2) wloads(6 * i, i == 1 ? 12 : 16);
            if (i == 8 && ffn) wo_rng(0, 1);
        };
        struct Hook {
            decltype(pf)& f; decltype(v_reads)& vr;
            DI void operator()(int k, float& x) const { if (k == 10) return; PIN(x); if (k == 3) { ARR_MARK(17); BAR(); vr(); } f(3 + k); PIN(x); }
            DI void operator()(int k, float& x, float& y) const { PIN(y); f(3 + k); PIN(x); }
        };
        auto tail16 = [&]() {
            kfeat(Hook{pf, v_reads});
            if (ffn) wo_rng(1, 3);
            wo_rng(3, 5);
            static_for<3>([&](auto kc_) {
                constexpr int k = decltype(kc_)::value;
                waitv<4 * (4 - k)>();
                repack(wblk(k));
            });
            BAR();
            res_loads();
            wo_rng(5, 7);
            static_for<2>([&](auto kc_) {
                constexpr int k = decltype(kc_)::value + 3;
                waitv<4 * (6 - k)>();
                repack(wblk(k));
            });
            FRESH();
            perm1();
            SCHED();
            BAR();
            wo_rng(7, 8);
        };
        auto vT23 = [&]() {
            const unsigned n = (idx * 2 + pw) * 64 + l, r = n / 12, c = n % 12;
            return idx < 24 ? ((r & 127) + (r >> 7) * 256 + 128) * 6144 + (c >> 2) * 2048 + (((xcd >> 1) * 4 + (c >> 2 ? 0 : 4) + (c & 3)) & 15) * 128 : OOB;
        };
        if (lw < 2) {
            const unsigned sA = (rt * 128 + lw * 64) * 2048, sB = lw * 32 * 6144 + hp * 128, sBq = lw * 32 * 6144 + hq * 128;
            unsigned buf = 1;
            unsigned va[4], vb[4];
#pragma unroll
            for (int i = 0; i < 4; i++) { va[i] = vA + i * (16384 - 1024); vb[i] = ((i & 1) ? vB1 : vB0) + i * (49152 - 1024); PIN(va[i]); PIN(vb[i]); }
            auto stage = [&](unsigned s, unsigned bf, int i0, int i1) {
                const unsigned kt = (s + rot) & 15;
                const unsigned lb = ldsb + bf * STAGE_B;
                const unsigned sa = sA + kt * 128, sb = kt * 393216;
                if (i0 <= 0 && 0 < i1) dma16x4(lb + lw * 8192, va, rsX, sa);
                if (i0 <= 1 && 1 < i1) dma16x4(lb + lw * 8192 + 4096, va, rsX, sa + 65536);
                if (i0 <= 2 && 2 < i1) dma16x4(lb + 16384 + 8192 + lw * 4096, vb, rsW, sb + sB + 2048);
                if (i0 <= 3 && 3 < i1) dma16x4(lb + 16384 + 16384 + lw * 4096, vb, rsW, sb + sB + 4096);
                if (i0 <= 4 && 4 < i1) dma16x4(lb + 16384 + lw * 4096, vb, rsW, sb + sBq);
            };
            stage(1, 1, 0, 4);
            SCHED();
            unsigned vt = vT23();
            PIN(vt);
            wl_pre(); k_pre();
            waitv<16>(); ARR_MARK(0); BAR();
            stage(1, 1, 4, 5);
            touch(vt, rsW, (xcd & 1) * 512 * 6144);
            waitv<1>(); ARR_MARK(1); BAR();
            stage(2, 2, 0, 5); stage(3, 0, 0, 3);
            waitv<12>(); ARR_MARK(2); BAR();
            stage(3, 0, 3, 5); stage(4, 1, 0, 4);
            waitv<16>(); ARR_MARK(3); BAR();
            stage(4, 1, 4, 5);
            buf = 2;
#pragma nounroll
            for (unsigned s = 5; s < 16; s++) {
                if (s > 5) { waitv<20>(); ARR_MARK(s - 2); BAR(); }
                stage(s, buf, 0, 5);
                buf = buf == 2 ? 0 : buf + 1;
            }
            READ_TAG();
            waitv<20>();
        } else {
            SCHED();
            unsigned v3;
            const unsigned h3 = (xcd >> 1) * 4 + 3, kv3 = !(xcd & 2), wv = idx * 2 + pw;
            auto line3 = [&](unsigned per, unsigned rows, auto rowf) {
                const unsigned n = wv * (kv3 ? per : (per + 2) / 3) + l, r3 = kv3 ? n / 3 : n, c3 = kv3 ? n % 3 : 0;
                return (idx < 24 && l < (kv3 ? per : (per + 2) / 3) && r3 < rows) ? rowf(r3) * 6144 + c3 * 2048 + ((c3 ? h3 : h3 + 4) & 15) * 128 : OOB;
            };
            v3 = line3(32, 512, [](unsigned r) { return r; });
            touch(line3(16, 256, [](unsigned r) { return (r & 127) + (r >> 7) * 256; }), rsW, (xcd & 1) * 512 * 6144);
            {
                const rsrc_t rsP = __builtin_amdgcn_make_buffer_rsrc((void*)WPHI, 0, 32 * 1024 * 2, 0x00020000);
#pragma unroll
                for (int i = 0; i < 4; i++) dma16(ldsb + L_WPHI + pw * 4096 + i * 1024, l * 16, rsP, (pw ? hq : hp) * 4096 + i * 1024);
            }
            v4u sv = {SENT, SENT, SENT, SENT};
            unsigned vc4 = l < 16 ? WS_G3 + (hp * 4 + (rt >> 2)) * 2048 + pw * 1024 + (rt & 3) * 256 + l * 16
                         : l < 24 ? WS_P2 + (hp * 16 + rt) * P2_STRIDE + 8192 + pw * 128 + (l - 16) * 16 : OOB;
            auto clears = [&](int i0, int i1) {
#pragma unroll
                for (int i = i0; i < i1; i++) {
                    if (i < 4) ST16(sv, l * 16, WS_P2 + (hp * 16 + rt) * P2_STRIDE + pw * 4096 + i * 1024);
                    if (i == 4) ST16(sv, vc4, 0);
                    if (i > 4) ST16(sv, l * 16, WS_TL + (bid * 4 + pw * 2 + i - 5) * 1024);
                }
                FRESH();
            };
            PIN(vc4); PIN(sv);
            unsigned ca = ldsb + 16384 + pw * 8192 + (8 * g + q4) * 128 + (((rt & 3) ^ (((q4 >> 1) & 1) | ((g & 1) << 1))) * 32) + p4 * 8;
            PIN(ca);
            bf16x8 cf[4];
            f32x4 cd[2] = {zero4, zero4};
            auto cs_read = [&](int st) {
#pragma unroll
                for (int i = 0; i < 4; i++) cf[i] = cat8(tr_read(ca + (st % 3) * STAGE_B + i * 4096), tr_read(ca + (st % 3) * STAGE_B + i * 4096 + 512));
                SCHED();
            };
            READ_TAG();
            waitv<5>();
            ARR_MARK(0);
            BAR();
            SCHED();
            cs_read(0);
            FRESH();
            const rsrc_t rsC = __builtin_amdgcn_make_buffer_rsrc((void*)pc0, 0, clen, 0x00020000);
            auto code_pf = [&]() { touch(l < 4 ? l * 128 : OOB, rsC, (idx * 2 + pw) * 512); };
            v4u gl[4];
            float s0 = 0.f, s1 = 0.f;
            unsigned bad;
            v2u gs, gc;
            unsigned vgl = (l < 48 ? l : 0) * 16;
            unsigned aw0 = ldsb + L_CS + pw * 384 + (vgl >> 1), aw1 = ldsb + L_MB + (pw * 64 + l) * 4, aw2 = ldsb + L_SD + (pw * 64 + l) * 2;
            static_for<13>([&](auto tc) {
                constexpr int t = decltype(tc)::value;
                WAITL0();
                ARR_MARK(t + 1);
                BAR();
                SCHED();
                if constexpr (t < 4) {
                    __builtin_amdgcn_s_setprio(2);
#pragma unroll
                    for (int i = 0; i < 4; i++) cd[i & 1] = MFMA(ones, cf[(i & 1) * 2 + (i >> 1)], cd[i & 1]);
                    SCHED();
                    __builtin_amdgcn_s_setprio(0);
                    if constexpr (t < 3) cs_read(t + 1);
                    else __builtin_amdgcn_raw_buffer_store_b64((v2u){__float_as_uint(g ? cd[1][0] : cd[0][0]), tag}, rsWS, (g == 1 || g + pw == 0) ? g * 512 + t16 * 8 : OOB,
                                                               WS_G1 + ((hp * 4 + (rt >> 2)) * 192 + pw * 64 + (rt & 3) * 16) * 8, AUX_SC1);
                }
                if constexpr (t == 0) touch(v3, rsW, (~xcd & 1) * 512 * 6144);
                if constexpr (t == 12) code_pf();
                if constexpr (t == 8) {
                    waitv<0>();
                    __builtin_amdgcn_raw_buffer_store_b64((v2u){0u, tag}, rsWS, l == 0 ? 0 : OOB, WS_GC + ((hp * 2 + pw) * 16 + rt) * 8, AUX_SC1);
                }
                if constexpr (t == 8 || t == 9) {
#pragma unroll
                    for (int r = (t - 8) * 2; r < (t - 8) * 2 + 2; r++)
                        asm volatile("buffer_load_dwordx4 %0, %1, %2, %3 offen sc1" : "=v"(gl[r]) : "v"(vgl), "s"(rsWS), "s"(WS_G1 + ((hp * 4 + r) * 192 + pw * 96) * 8) : "memory");
                }
                if constexpr (t == 6) {
                    asm volatile("buffer_load_dwordx2 %0, %1, %2, %3 offen sc1" : "=v"(gs) : "v"(l * 8), "s"(rsWS), "s"(WS_G0 + (rt * 128 + pw * 64) * 8) : "memory");
                }
                if constexpr (t == 11) {
                    waitv<0>();
                    PIN(gs);
                    bad = gs[1] ^ tag;
#pragma unroll
                    for (int r = 0; r < 4; r++) {
                        PIN(gl[r]);
                        bad |= (gl[r][1] ^ tag) | (gl[r][3] ^ tag);
                        s0 += __uint_as_float(gl[r][0]); s1 += __uint_as_float(gl[r][2]);
                    }
                    PIN(s0); PIN(s1); PIN(bad);
                }
                if constexpr (t == 12) {
                    if (RARE(!__all(bad == 0))) {
                        s0 = 0.f; s1 = 0.f;
#pragma nounroll
                        for (unsigned r = 0; r < 4; r++) {
                            unsigned a[2];
                            sweep<2>(rsWS, vgl, WS_G1 + ((hp * 4 + r) * 192 + pw * 96) * 8, 8, tag, a);
                            s0 += __uint_as_float(a[0]); s1 += __uint_as_float(a[1]);
                        }
                        unsigned c[1];
                        sweep<1>(rsWS, l * 8, WS_G0 + (rt * 128 + pw * 64) * 8, 0, tag, c);
                        gs[0] = c[0];
                    }
                    *(LAS f32x2*)(aw0) = (f32x2){-s0, -s1};
                    stsf(aw1, bflo(gs[0]));
                    *(LAS u16*)(aw2) = (u16)(gs[0] >> 16);
                    SCHED();
                }
                if constexpr (t == 5) k_pre();
                if constexpr (t == 6) wl_pre();
                if constexpr (t == 7) { PIN(vgl); PIN(aw0); PIN(aw1); PIN(aw2); }
                clears(t < 7 ? t : 7, t < 7 ? t + 1 : 7);
                if constexpr (t == 11) asm volatile("buffer_load_dwordx2 %0, %1, %2, %3 offen sc1" : "=v"(gc) : "v"((l & 31) * 8), "s"(rsWS), "s"(WS_GC + (pw ? hq : hp) * 256) : "memory");
                if constexpr (t == 12) {
                    waitv<1>();
                    PIN(gc);
                    if (RARE(!__all(gc[1] == tag))) { unsigned c[1]; sweep<1>(rsWS, (l & 31) * 8, WS_GC + (pw ? hq : hp) * 256, 0, tag, c); }
                    SCHED();
                }
            });
            WAITL0();
        }
        ARR_MARK(14); BAR();
        SCHED();
        pf(0);
        if (ffn) waitv<6>(); else waitv<10>();
        ARR_MARK(15); BAR();
        pf(1);
        tail16();
        WAITL0();
        STAMPW(7, 4); STAMPW(7, 7);
    }
    const unsigned sFl = WS_F4 + (rt2 * 4 + (lw >> 1) + 2 * half) * 128;
    vD = kr * 16384 + (l & 7) * 16; sD = (kc * 256 + lw * 64) * 2048 + cg * 128;
    PIN(vD);
    asm volatile("" : "+s"(sD), "+s"(rsD));
    if (half) dloads(0, 1);
    SCHED();
    ARR_MARK(20);
    BAR();
    ARR_END();
    const unsigned wb = ldsb + L_WO + half * 65536 + l * 16;
    bf16x8 bb[2][4];
    v4u xres[2], wrv[2];
#pragma unroll
    for (int nt = 0; nt < 4; nt++) {
        if (nt < 2) xres[nt] = *(const LAS v4u*)(ldsb + L_XB + w * 4096 + t16 * 128 + (((nt * 4 + g) ^ (l & 7)) * 16));
        bb[0][nt] = lds128(wb + nt * 1024);
        PIN(bb[0][nt]);
    }
#pragma unroll
    for (int i = 0; i < 2; i++) wrv[i] = *(const LAS v4u*)(ldsb + L_XB + w * 4096 + 3072 + i * 64 + g * 16);
    PIN(xres[0]); PIN(xres[1]);
    unsigned so[16], va0 = l * 16 + half * 32768, va1 = l * 16 + (half ^ 1) * 32768 + 1024;
    unsigned aXw = ldsb + L_XB + w * 4096 + l * 16, aXr = ldsb + L_XB + (w ^ 4) * 4096 + l * 16, sG5 = WS_G5 + ((rt2 * 8 + lw * 2 + half) * 16 + ct) * 256;
    unsigned vG5 = g == q4 ? t16 * 16 : OOB;
#pragma unroll
    for (int st = 0; st < 16; st++) { so[st] = WS_P4 + (rt2 * 8 + lw * 2) * 32768 + ((2 * st + rot2) & 31) * 1024; asm volatile("" : "+s"(so[st])); }
    unsigned wt[2];
#pragma unroll
    for (int i = 0; i < 2; i++) { wt[i] = ldsb + L_WO + half * 65536 + (4 * g + q4) * 128 + (((i * 2 + (p4 >> 1)) ^ ((2 * g + (q4 >> 1)) & 3)) * 32) + (p4 & 1) * 16; PIN(wt[i]); }
    PIN(va0); PIN(va1); PIN(aXw); PIN(aXr); PIN(vG5);
    asm volatile("" : "+s"(sG5));
    SLEEP(13);
    poll16(rsWS, sFl, tag, l);
    FRESH();
    SCHED();
    STAMPW(8, 0); STAMPW(8, 4);
    unsigned swc;
    f32x4 acc2[2][4];
    bf16x8 sop[2];
    {
        auto aload = [&](int st, int m2) { return LD16(m2 ? va1 : va0, so[st]); };
        constexpr int PD = 2;
        v4u af[PD][2];
#pragma unroll
        for (int i = 0; i < PD; i++) {
#pragma unroll
            for (int m2 = 1; m2 >= 0; m2--) af[i][m2] = aload(i, m2);
            SCHED();
        }
#pragma unroll
        for (int nt = 0; nt < 4; nt++) {
            const unsigned x0 = xres[nt >> 1][2 * (nt & 1)], x1 = xres[nt >> 1][2 * (nt & 1) + 1];
            acc2[0][nt] = (f32x4){bflo(x0), bfhi(x0), bflo(x1), bfhi(x1)};
            acc2[1][nt] = zero4;
        }
        unsigned m0 = p4 == 0 ? 0x3F803F80u : 0u, m1 = p4 == 1 ? ~0u : 0u;
        PIN(m0); PIN(m1);
#pragma unroll
        for (int i = 0; i < 2; i++) {
            v4u u;
#pragma unroll
            for (int j = 0; j < 4; j++) u[j] = (wrv[i][j] & m1) | m0;
            sop[i] = __builtin_bit_cast(bf16x8, u);
            PIN(sop[i]);
        }
        {
            float sw = 0.f;
#pragma unroll
            for (int i = 0; i < 8; i++) sw = dot2(wrv[i >> 2][i & 3], 0x3F803F80u, sw);
            swc = __float_as_uint(xrow_sum(sw));
            PIN(swc);
        }
        SCHED();
#pragma unroll
        for (int st = 0; st < 16; st++) {
            if (st == 2) BAR();
            if (st + 1 < 16) {
#pragma unroll
                for (int nt = 0; nt < 4; nt++) {
                    if (st + 1 < 10) bb[(st + 1) % (2)][nt] = lds128(wb + (st + 1) * 4096 + nt * 1024);
                    else bb[(st + 1) % (2)][nt] = cat8(tr_read(wt[nt >> 1] + (st + 1) * 4096 + (nt & 1) * 8), tr_read(wt[nt >> 1] + (st + 1) * 4096 + (nt & 1) * 8 + 2048));
                }
            }
#pragma unroll
            for (int mm = 0; mm < 2; mm++) {
                const int m2 = mm ^ 1;
                const bf16x8 a = __builtin_bit_cast(bf16x8, af[st % PD][m2]);
#pragma unroll
                for (int nt = 0; nt < 4; nt++) acc2[m2][nt] = MFMA(bb[st % (2)][nt], a, acc2[m2][nt]);
            }
            if (st + PD < 16) {
#pragma unroll
                for (int m2 = 1; m2 >= 0; m2--) af[st % PD][m2] = aload(PD + st, m2);
            }
#pragma unroll
            for (int i = 0; i < (st + 1 < 10 ? 4 : 8); i++) { SGB(0x8, 1); SGB(0x100, 1); }
            SCHED();
        }
    }
    STAMPW(9, 0); STAMPW(9, 4);
#pragma unroll
    for (int nt = 0; nt < 4; nt++) stsf4(aXw + nt * 1024, acc2[1][nt]);
    SCHED();
    if (half) dloads(1, 4);
    SCHED();
    WAITL0();
    if (half) __builtin_amdgcn_s_setprio(1);
    BAR();
    SCHED();
    v4u hpk[2];
    {
        f32x4 pa[4];
#pragma unroll
        for (int nt = 0; nt < 4; nt++) pa[nt] = ldsf4(aXr + nt * 1024);
        SCHED();
#pragma unroll
        for (int i = 0; i < 8; i++) {
            const f32x4 a = acc2[0][i >> 1] + pa[i >> 1];
            hpk[i >> 2][i & 3] = pk2(a[2 * (i & 1)], a[2 * (i & 1) + 1]);
        }
        const bf16x8 h0 = __builtin_bit_cast(bf16x8, hpk[0]), h1 = __builtin_bit_cast(bf16x8, hpk[1]);
        f32x4 gq = MFMA(h0, h0, zero4), sd = MFMA(sop[0], h0, zero4);
        gq = MFMA(h1, h1, gq); sd = MFMA(sop[1], h1, sd);
        float q0 = (l & 1) ? gq[1] : gq[0], q1 = (l & 1) ? gq[3] : gq[2];
        PIN(q0); PIN(q1);
        const v4u pv = {__float_as_uint(sd[0]), __float_as_uint((l & 2) ? q1 : q0), __float_as_uint(sd[1]), swc};
        ST16(pv, vG5, sG5);
        asm volatile("s_nop 1" :: "v"(pv));
    }
    __builtin_amdgcn_s_setprio(0);
    STAMPW(10, 0); STAMPW(10, 4);
    v4u wp2[8];
    auto perm2 = [&]() {
#pragma unroll
        for (int i = 16; i < 24; i++) PIN(wreg[i]);
#pragma unroll
        for (int ip = 0; ip < 4; ip++)
#pragma unroll
            for (int c = 0; c < 4; c++) {
                const unsigned a = wreg[16 + 2 * ip][c], b = wreg[16 + 2 * ip + 1][c];
                wp2[2 * c][ip] = __builtin_amdgcn_perm(b, a, 0x05040100);
                wp2[2 * c + 1][ip] = __builtin_amdgcn_perm(b, a, 0x07060302);
            }
#pragma unroll
        for (int c = 0; c < 8; c++) PIN(wp2[c]);
    };
    const unsigned mt = rt2 * 8 + lw * 2 + half;
    {
        v4u pv[4];
        unsigned sK = WS_GK + (ct * 128 + mt) * 4, vK = l == 0 ? 0 : OOB, rk = 2047u - R2;
        asm volatile("" : "+s"(sK));
        PIN(vK); PIN(rk);
        unsigned sS = WS_G5 + mt * 4096;
        asm volatile("" : "+s"(sS));
        SLEEP(18);
        __builtin_amdgcn_s_setprio(1);
        float T[4];
        sweepn<4>([&](int k) { return LD16(l * 16 + k * 1024, sS); }, pv, [&]() {
            const f32x4 ps = (__builtin_bit_cast(f32x4, pv[0]) + __builtin_bit_cast(f32x4, pv[1])) + (__builtin_bit_cast(f32x4, pv[2]) + __builtin_bit_cast(f32x4, pv[3]));
            const float r = xrow_scatter<false>(ps[0], ps[1], ps[2], ps[3]);
            xrow_gather(r, T);
            return r;
        });
        STAMPW(21, 0); STAMPW(21, 4);
        const float mean = T[0] * (1.0f / 1024.0f), var = fmaxf(T[1] * (1.0f / 1024.0f) - mean * mean, 0.f);
        const unsigned sg = pk2(mean, __builtin_amdgcn_sqrtf(rbf(rbf(var) + 1e-5f)));
        const float mb2 = bflo(sg), isd2 = __builtin_amdgcn_rcpf(bfhi(sg));
        const float rsc = (T[2] - mb2 * T[3]) * isd2;
        const unsigned rb = pk2(rsc, rsc);
        const unsigned key = ((rb ^ ((unsigned)((int)rb >> 31) | 0x80000000u)) & 0xffff0000u) | rk;
        unsigned kb = umx(key, dppu<0x128>(key));
        kb = umx(kb, dppu<0x124>(kb)); kb = umx(kb, dppu<0x122>(kb)); kb = umx(kb, dppu<0x121>(kb));
        ST4(kb, vK, sK);
        SCHED();
        __builtin_amdgcn_s_setprio(0);
        if (half) { dloads(4, 8); SCHED(); }
        const bool cand = key == kb;
        v4u e[2], outv[2];
#pragma unroll
        for (int i = 0; i < 8; i++) {
            const float h0 = bflo(hpk[i >> 2][i & 3]), h1 = bfhi(hpk[i >> 2][i & 3]);
            const unsigned d = pk2(h0 - mb2, h1 - mb2);
            e[i >> 2][i & 3] = pk2(bflo(d) * isd2, bfhi(d) * isd2);
        }
        ST16(e[0], cand ? g * 16 : OOB, WS_C + (mt * 16 + ct) * 128);
        ST16(e[1], cand ? g * 16 + 64 : OOB, WS_C + (mt * 16 + ct) * 128);
        SCHED();
        STAMPW(22, 0); STAMPW(22, 4);
#pragma unroll
        for (int i = 0; i < 8; i++)
            outv[i >> 2][i & 3] = pk2(bflo(hpk[i >> 2][i & 3]) + bflo(e[i >> 2][i & 3]), bfhi(hpk[i >> 2][i & 3]) + bfhi(e[i >> 2][i & 3]));
        if (cand) {
#pragma unroll
            for (int i = 0; i < 2; i++) { *(LAS v4u*)(ldsb + L_STASH + w * 256 + i * 64 + g * 16) = hpk[i]; *(LAS v4u*)(ldsb + L_STASH + w * 256 + 128 + i * 64 + g * 16) = e[i]; }
        }
#pragma unroll
        for (int i = 0; i < 2; i++) *(LAS v4u*)(ldsb + L_OUT + (lw * 2 + half) * 2048 + t16 * 128 + (((i * 4 + g) ^ (l & 7)) * 16)) = outv[i];
    }
    const rsrc_t rsO = __builtin_amdgcn_make_buffer_rsrc((void*)OUT, 0, 2048 * 1024 * 2, 0x00020000);
    unsigned aG = ldsb + L_WO + 512;
    PIN(aG);
    unsigned vX = (2 * g + (l & 1)) * 16, aP = (2 * g + (l & 1)) * 32, aR1 = ldsb + L_RED + lw * 4352 + l * 16, vl4 = l * 4;
    unsigned aR2 = ldsb + L_RED + ((lw & 1) * 2 + ((l >> 2) & 1)) * 1088 + kr * 16 + p4 * 4;
    unsigned sX = WS_C + kq * 512 + lw * 128, sG8w = WS_G8 + ((kq * 2 + mat) * 3584 + (cg1 + lw) * 64) * 4, sG8r = WS_G8 + (kc * 256 + lw * 64) * 4;
    unsigned sG9 = WS_G9 + (cg * 14 + kc) * 256;
    unsigned aR3 = aR1 + 17408 - lw * 2176;
    PIN(vX); PIN(aP); PIN(aR1); PIN(aR2); PIN(aR3); PIN(vl4);
    asm volatile("" : "+s"(sX), "+s"(sG8w), "+s"(sG8r), "+s"(sG9));
    SCHED();
    if (w == 1) {
        unsigned sK = WS_GK + (bid & 15) * 512;
        v2u hb[1];
        asm volatile("" : "+s"(sK));
        SLEEP(13);
        stsu(aG, wave_maxu(sweepz<1>([&](int) { return ldg(rsWS, l * 8, sK); }, hb)));
        STAMPR(23, 2);
    }
    if (half) perm2();
    STAMPW(24, 7);
    WAITL0();
    if (half) __builtin_amdgcn_s_setprio(1);
    BAR();
    STAMPW(11, 0); STAMPW(11, 4);
    const unsigned gkv = ldsu(aG);
    if (w < 4) {
        if (bid == 0 && tid == 0) __hip_atomic_store((gu32*)(ws + WS_EPOCH), tag, RLX_AGENT);
        v4u o2[4];
        unsigned vo[4];
#pragma unroll
        for (int i = 0; i < 4; i++) o2[i] = *(const LAS v4u*)(ldsb + L_OUT + w * 4096 + i * 1024 + kr * 128 + (((l & 7) ^ kr) * 16));
#pragma unroll
        for (int i = 0; i < 4; i++) { const unsigned R = mt * 16 + i * 8 + kr; vo[i] = 2047u - R != (gkv & 2047u) ? R * 2048 + (l & 7) * 16 : OOB; PIN(vo[i]); PIN(o2[i]); }
#pragma unroll
        for (int s = 0; s < 3; s++) {
            if (s) { BAR(); SLEEP(3); }
            SCHED();
#pragma unroll
            for (int i = 0; i < 4; i++)
                if ((i + 1) / 2 == s) __builtin_amdgcn_raw_buffer_store_b128(o2[i], rsO, vo[i], ct * 128, AUX_SC1);
            SCHED();
        }
        STAMPW(12, 0);
        return;
    }

    auto gemv = [&](const v4u& x, const v4u* wp, int n, unsigned a) {
        f32x4 d[16], o[4];
#pragma unroll
        for (int c = 0; c < n + 4; c++) {
            if (c < n) d[c] = MFMA(__builtin_bit_cast(bf16x8, x), __builtin_bit_cast(bf16x8, wp[c]), zero4);
            SCHED();
            if (c >= 4) {
                const int j = c - 4;
                float t = d[j][0] + dppf<0x128>(d[j][1]);
                asm volatile("" : "+v"(t) : "v"(d[j]));
                o[j >> 2][j & 3] = t;
                if ((j & 3) == 3) stsf4(a + (j >> 2) * 1088, o[j >> 2]);
            }
            SCHED();
        }
        return o[0][0];
    };
    auto sum4 = [&](unsigned a, unsigned stride) {
        float u = ldsf(a) + ldsf(a + stride), v = ldsf(a + 2 * stride) + ldsf(a + 3 * stride);
        PIN(u); PIN(v);
        return u + v;
    };
    if (ffn) {
        v4u xq[1];
        const unsigned vx = ((~gkv & 0x7f0u) << 7) + vX;
        sweepn<1>([&](int) { return LD16(vx, sX); }, xq, [&]() { return gemv(xq[0], &wp1[0][0], 16, aR1); });
        STAMPW(13, 4);
    }
    WAITL0();
    BAR();
    if (ffn) {
        if (lw < 2) ST4(__float_as_uint(sum4(aR2, 4352)), vl4, sG8w);
        STAMPW(14, 4);
        unsigned pv[8];
        v4u ap;
        SLEEP(12);
        sweepn<8>([&](int k) { return LD4(vl4, sG8r + k * 3584 * 4); }, pv, [&]() {
            const float gg = rbf((__uint_as_float(pv[0]) + __uint_as_float(pv[2])) + (__uint_as_float(pv[4]) + __uint_as_float(pv[6])));
            const float uu = rbf((__uint_as_float(pv[1]) + __uint_as_float(pv[3])) + (__uint_as_float(pv[5]) + __uint_as_float(pv[7])));
            const float act = rbf(gg * __builtin_amdgcn_rcpf(1.0f + __expf(-gg))) * uu;
            const unsigned apk = pk2(act, dppf<0xB1>(act));
#pragma unroll
            for (int ip = 0; ip < 4; ip++) ap[ip] = __builtin_amdgcn_ds_bpermute(aP + ip * 8, apk);
            return act;
        });
        STAMPW(15, 4);
        gemv(ap, wp2, 8, aR3);
    }
    WAITL0();
    BAR();
    if (lw != 0) return;
    if (ffn) ST4(__float_as_uint(sum4(aR2 + 17408, 2176)), vl4, sG9);
    SCHED();
    STAMPW(16, 4);
    const unsigned gkey = rfl(gkv), srow = 2047u - (gkey & 2047u);
    if ((srow >> 7) == rt2) {
        const float rs = __uint_as_float(((gkey & 0x80000000u) ? gkey ^ 0x80000000u : ~gkey) & 0xffff0000u);
        float sig = rbf(1.0f / (1.0f + __expf(-rs)));
        const unsigned sb = ldsb + L_STASH + (((srow >> 5) & 3) + ((srow >> 4) & 1) * 4) * 256 + (t16 * 4 + g) * 2;
        float hc = lds_bf(sb), lc = lds_bf(sb + 128);
        unsigned vo = (t16 * 4 + g) * 2, so_ = srow * 2048 + ct * 128, vy = l < 32 ? l * 16 + 3072 : OOB, sY = WS_G9 + ct * 3584;
        PIN(sig); PIN(hc); PIN(lc); PIN(vo); PIN(vy);
        asm volatile("" : "+s"(so_), "+s"(sY));
        v4u yp[4];
        float y;
        SLEEP(7);
        sweepn<4>([&](int k) { return LD16(k < 3 ? l * 16 + k * 1024 : vy, sY); }, yp, [&]() {
            const f32x4 ys = (__builtin_bit_cast(f32x4, yp[0]) + __builtin_bit_cast(f32x4, yp[1])) + (__builtin_bit_cast(f32x4, yp[2]) + __builtin_bit_cast(f32x4, yp[3]));
            y = xrow_scatter<false>(ys[0], ys[1], ys[2], ys[3]);
            return y;
        });
        __builtin_amdgcn_raw_buffer_store_b16((u16)(pk2(hc + rbf(lc + rbf(rbf(y) * sig)), 0.f) & 0xffffu), rsO, vo, so_, 0);
        STAMPW(17, 4);
    }
}
__device__ __attribute__((used, noinline)) void fk_tail() { asm volatile(".Lfk_end:"); }

extern "C" void kernel_launch(void* const* d_in, const int* in_sizes, int n_in, void* d_out, int out_size, void* d_ws,
                              size_t ws_size, hipStream_t stream) {
    (void)in_sizes; (void)n_in; (void)out_size; (void)ws_size;
    (void)hipFuncSetAttribute((const void*)fused_kernel, hipFuncAttributeMaxDynamicSharedMemorySize, LDS_BYTES);
    fused_kernel<<<256, 512, LDS_BYTES, stream>>>((const u16*)d_in[0], (const u16*)d_in[1], (const u16*)d_in[2], (const u16*)d_in[3],
                                                  (const u16*)d_in[5], (const u16*)d_in[6], (const u16*)d_in[7], (const u16*)d_in[8],
                                                  (u16*)d_out, (char*)d_ws);
}
```
